```python
import torch

def _softmax(x, axis=-1):
    return torch.softmax(x, dim=axis)

def _gelu(x):
    return torch.nn.functional.gelu(x, approximate='tanh')

def _silu(x):
    return torch.nn.functional.silu(x)

def _sigmoid(x):
    return torch.sigmoid(x)

def _layernorm(x, axis=-1, eps=1e-05):
    m = x.mean(dim=axis, keepdim=True)
    v = x.var(dim=axis, keepdim=True, unbiased=False)
    return (x - m) / torch.sqrt(v + eps)

def _scatter_add(tgt, idx, src, axis=0):
    return tgt.clone().index_add_(axis, idx, src)

def _segment_sum(src, seg, n):
    out = torch.zeros((n,) + src.shape[1:], dtype=src.dtype)
    return out.index_add_(0, seg, src)

def _pad(x, pw):
    flat = tuple((v for pair in reversed(pw) for v in pair))
    return torch.nn.functional.pad(x, flat)
kernel_name = 'adaLN_cond__memory_aug_recurrent_mem_tr__mixer_channel_mlp__prompt'

def setup_inputs():

    _w = lambda a: (lambda t: t.to(torch.bfloat16) if t.is_floating_point() else t)(torch.as_tensor(a))

    def _A_setup():
        B, S, Dm = (1, 2048, 512)
        H, Dh = (32, 128)
        M, seg = (8, 32)
        seg = min(seg, S)
        S = S // seg * seg
        return {'x': _w((lambda *s: torch.randn(*s, dtype=torch.bfloat16) * 0.05)(B, S, Dm)), 'mem0': _w((lambda *s: torch.randn(*s, dtype=torch.bfloat16) * 0.4)(B, M, Dm)), 'Wq': _w((lambda *s: torch.randn(*s, dtype=torch.bfloat16) * 3.2)(Dm, H, Dh) * Dm ** (-0.5)), 'Wk': _w((lambda *s: torch.randn(*s, dtype=torch.bfloat16) * 3.2)(Dm, H, Dh) * Dm ** (-0.5)), 'Wv': _w((lambda *s: torch.randn(*s, dtype=torch.bfloat16) * 0.4)(Dm, H, Dh) * Dm ** (-0.5)), 'Wo': _w((lambda *s: torch.randn(*s, dtype=torch.bfloat16) * 0.4)(H, Dh, Dm) * (H * Dh) ** (-0.5)), 'seg_len': seg}
    _A_in = _A_setup()

    def _B_setup():
        B, S, D, F = (1, 2048, 512, 1792)
        return {'x': _w((lambda *s: torch.randn(*s, dtype=torch.bfloat16) * 0.05)(B, S, D)), 'w1': _w((lambda *s: torch.randn(*s, dtype=torch.bfloat16) * 3.2)(D, F) * D ** (-0.5)), 'b1': _w((lambda *s: torch.randn(*s, dtype=torch.bfloat16) * 0.1)(F)), 'w2': _w((lambda *s: torch.randn(*s, dtype=torch.bfloat16) * 0.1)(F, D) * F ** (-0.5)), 'b2': _w((lambda *s: torch.randn(*s, dtype=torch.bfloat16) * 0.1)(D))}
    _B_in = _B_setup()
    return {'A_x': _A_in['x'], 'A_mem0': _A_in['mem0'], 'A_Wq': _A_in['Wq'], 'A_Wk': _A_in['Wk'], 'A_Wv': _A_in['Wv'], 'A_Wo': _A_in['Wo'], 'A_seg_len': _A_in['seg_len'], 'B_x': _B_in['x'], 'B_w1': _B_in['w1'], 'B_b1': _B_in['b1'], 'B_w2': _B_in['w2'], 'B_b2': _B_in['b2'], 'c': _w((lambda *s: torch.randn(*s, dtype=torch.bfloat16) * 0.05)(1, 512) * 8.0), 'w_mod': _w((lambda *s: torch.randn(*s, dtype=torch.bfloat16) * 0.05)(512, 6 * 512) * 512 ** (-0.5) * 8.0)}

def reference(A_x, A_mem0, A_Wq, A_Wk, A_Wv, A_Wo, A_seg_len, B_x, B_w1, B_b1, B_w2, B_b2, c, w_mod):

    def _A_ref(x, mem0, Wq, Wk, Wv, Wo, seg_len):
        B, S, Dm = x.shape
        H, Dh = (Wq.shape[1], Wq.shape[2])
        M = mem0.shape[1]
        scale = Dh ** (-0.5)
        n_seg = S // seg_len
        L = M + seg_len
        idx = torch.arange(L)
        mask = (idx[:, None] < M) | (idx[:, None] >= idx[None, :])
        mem = mem0
        outs = []
        for s in range(n_seg):
            xs = x[:, s * seg_len:(s + 1) * seg_len, :].clone()
            h = torch.concatenate([mem, xs], axis=1)
            q = torch.einsum('btd,dhe->bhte', h, Wq)
            k = torch.einsum('btd,dhe->bhte', h, Wk)
            v = torch.einsum('btd,dhe->bhte', h, Wv)
            scores = torch.einsum('bhqe,bhke->bhqk', q, k) * scale
            scores = torch.where(mask[None, None], scores, -1000000000.0)
            p = _softmax(scores, axis=-1)
            o = torch.einsum('bhqk,bhke->bhqe', p, v)
            o = torch.einsum('bhte,hed->btd', o, Wo)
            mem = o[:, :M, :].clone()
            outs.append(o[:, M:, :])
        return torch.concatenate(outs, axis=1)

    def _B_ref(x, w1, b1, w2, b2):
        y = _layernorm(x, axis=-1)
        h = _gelu(torch.einsum('bsd,df->bsf', y, w1) + b1)
        return torch.einsum('bsf,fd->bsd', h, w2) + b2
    x = A_x
    mod = (_silu(c) @ w_mod).reshape(c.shape[0], 6, 1, -1)
    sa, ca, ga, sb, cb, gb = (mod[:, 0].clone(), mod[:, 1].clone(), mod[:, 2].clone(), mod[:, 3].clone(), mod[:, 4].clone(), mod[:, 5].clone())
    h = _layernorm(x, axis=-1) * (1 + ca) + sa
    x = x + ga * _A_ref(h, A_mem0, A_Wq, A_Wk, A_Wv, A_Wo, A_seg_len)
    h = _layernorm(x, axis=-1) * (1 + cb) + sb
    x = x + gb * _B_ref(h, B_w1, B_b1, B_w2, B_b2)
    return x
```

```cpp
#include <hip/hip_runtime.h>
#include <hip/hip_bf16.h>
#include <stdint.h>

typedef unsigned int u32;
typedef unsigned short u16;
typedef unsigned long long u64;
typedef __attribute__((ext_vector_type(2))) __bf16 bf16x2;
typedef __attribute__((ext_vector_type(8))) __bf16 bf16x8;
typedef __attribute__((ext_vector_type(16))) __bf16 bf16x16;
typedef __attribute__((ext_vector_type(4))) float f32x4;
typedef __attribute__((ext_vector_type(16))) float f32x16;
typedef __attribute__((ext_vector_type(2))) u32 u32x2;
typedef __attribute__((ext_vector_type(4))) u32 u32x4;
typedef __attribute__((ext_vector_type(8))) u32 u32x8;

#define NTOK 2048
#define DM 512
#define NH 32
#define DH 128
#define NSEG 64
#define FF 1792
#define SENT 0xFFFFFFFFu

#define OFF_FLAG 0ull
#define OFF_MOD 8192ull
#define OFF_MEMX 32768ull
#define OFF_PEX 0x90000ull
#define OFF_H (2ull << 20)
#define OFF_KMEM (4ull << 20)
#define OFF_WVO (8ull << 20)
#define OFF_KX (24ull << 20)
#define OFF_QX (40ull << 20)
#define OFF_Y (56ull << 20)
#define OFF_UF (72ull << 20)
#define OFF_PX (136ull << 20)
#define OFF_X1 (142ull << 20)
#define OFF_Y2 (146ull << 20)
#define OFF_HID (148ull << 20)
#define OFF_W1F (156ull << 20)
#define OFF_W2F (158ull << 20)

__device__ __forceinline__ u32 pk2(float a, float b) {
  bf16x2 v; v[0] = (__bf16)a; v[1] = (__bf16)b; return __builtin_bit_cast(u32, v);
}
__device__ __forceinline__ u32 f2u(float x) { return __builtin_bit_cast(u32, x); }
__device__ __forceinline__ float u2f(u32 x) { return __builtin_bit_cast(float, x); }
__device__ __forceinline__ float bf_lo(u32 x) { return __builtin_bit_cast(float, x << 16); }
__device__ __forceinline__ float bf_hi(u32 x) { return __builtin_bit_cast(float, x & 0xffff0000u); }
__device__ __forceinline__ float bfr(float a) { return (float)(__bf16)a; }
__device__ __forceinline__ __amdgpu_buffer_rsrc_t mkrsrc(const void* p) {
  return __builtin_amdgcn_make_buffer_rsrc((void*)p, 0, 0x7fffffff, 0x00020000);
}
__device__ __forceinline__ u32x4 ld16_sc1(__amdgpu_buffer_rsrc_t r, u32 off) { return __builtin_amdgcn_raw_buffer_load_b128(r, off, 0, 16); }
__device__ __forceinline__ u32 ld4_sc1(__amdgpu_buffer_rsrc_t r, u32 off) { return __builtin_amdgcn_raw_buffer_load_b32(r, off, 0, 16); }
__device__ __forceinline__ void st16_sc1(u32x4 v, __amdgpu_buffer_rsrc_t r, u32 off) { __builtin_amdgcn_raw_buffer_store_b128(v, r, off, 0, 16); }
__device__ __forceinline__ void st8_sc1(u32x2 v, __amdgpu_buffer_rsrc_t r, u32 off) { __builtin_amdgcn_raw_buffer_store_b64(v, r, off, 0, 16); }
__device__ __forceinline__ void st4_sc1(u32 v, __amdgpu_buffer_rsrc_t r, u32 off) { __builtin_amdgcn_raw_buffer_store_b32(v, r, off, 0, 16); }
__device__ __forceinline__ u32 badm(u32x4 a) { return (u32)(a.x == SENT) | (u32)(a.y == SENT) | (u32)(a.z == SENT) | (u32)(a.w == SENT); }
__device__ __forceinline__ u32 sane(u32 x) { return x < 0xFFFFFFFEu ? x : 0xFFFFFFFEu; }
__device__ __forceinline__ u32 now32() { return (u32)__builtin_amdgcn_s_memtime(); }
__device__ __forceinline__ void wait_until(u32 t) { while ((int)(t - now32()) > 0) { } }
__device__ __forceinline__ bf16x8 as_bf8(u32x4 v) { return __builtin_bit_cast(bf16x8, v); }
__device__ __forceinline__ bf16x16 as_bf16(u32x4 a, u32x4 b) {
  u32x8 v = {a.x, a.y, a.z, a.w, b.x, b.y, b.z, b.w}; return __builtin_bit_cast(bf16x16, v);
}
#define MFMA16(a, b, c) __builtin_amdgcn_mfma_f32_16x16x32_bf16((a), (b), (c), 0, 0, 0)
#define MFMA32(a, b, c) __builtin_amdgcn_mfma_f32_32x32x16_bf16((a), (b), (c), 0, 0, 0)
#define SMF(a, b, c, idx) __builtin_amdgcn_smfmac_f32_16x16x64_bf16((a), (b), (c), (idx), 0, 0)

template <int N>
__device__ __forceinline__ u32 poll_chunks(__amdgpu_buffer_rsrc_t r, const u32 (&off)[N], u32x4 (&a)[N], u32 lane16) {
  u32 spins = 0;
  while (true) {
    asm volatile("" ::: "memory");
    u32 bad = 0;
#pragma unroll
    for (int j = 0; j < N; j++) a[j] = ld16_sc1(r, off[j] + lane16);
#pragma unroll
    for (int j = 0; j < N; j++) bad |= badm(a[j]);
    ++spins;
    if (!__any(bad)) return spins;
    if (spins > 400000u) return 0;
  }
}

__device__ __forceinline__ void stage_tile(char* lds, const u16* src, int rows, int ncols, int gstride, int pitch) {
  const int per_row = ncols >> 3;
  const int total = rows * per_row;
  for (int i = threadIdx.x; i < total; i += 256) {
    int r = i / per_row, c = i - r * per_row;
    u32x4 v = *(const u32x4*)(src + (size_t)r * gstride + c * 8);
    *(u32x4*)(lds + r * pitch + c * 16) = v;
  }
}
__device__ __forceinline__ u32 lds_u16(const char* lds, int byteoff) { return *(const u16*)(lds + byteoff); }
__device__ __forceinline__ bf16x8 gather8(const char* lds, int k0, int n, int pitch) {
  u32x4 v;
  v.x = lds_u16(lds, (k0 + 0) * pitch + n * 2) | (lds_u16(lds, (k0 + 1) * pitch + n * 2) << 16);
  v.y = lds_u16(lds, (k0 + 2) * pitch + n * 2) | (lds_u16(lds, (k0 + 3) * pitch + n * 2) << 16);
  v.z = lds_u16(lds, (k0 + 4) * pitch + n * 2) | (lds_u16(lds, (k0 + 5) * pitch + n * 2) << 16);
  v.w = lds_u16(lds, (k0 + 6) * pitch + n * 2) | (lds_u16(lds, (k0 + 7) * pitch + n * 2) << 16);
  return as_bf8(v);
}
__device__ __forceinline__ u32x4 gather8s(const char* lds, int k0, int n, int pitch) {
  u32x4 v;
  v.x = lds_u16(lds, (k0 + 0) * pitch + n * 2) | (lds_u16(lds, (k0 + 1) * pitch + n * 2) << 16);
  v.y = lds_u16(lds, (k0 + 8) * pitch + n * 2) | (lds_u16(lds, (k0 + 9) * pitch + n * 2) << 16);
  v.z = lds_u16(lds, (k0 + 2) * pitch + n * 2) | (lds_u16(lds, (k0 + 3) * pitch + n * 2) << 16);
  v.w = lds_u16(lds, (k0 + 10) * pitch + n * 2) | (lds_u16(lds, (k0 + 11) * pitch + n * 2) << 16);
  return v;
}
#define PACK_S(acc, b) (u32x4){pk2((acc)[(b)], (acc)[(b) + 1]), pk2((acc)[(b) + 4], (acc)[(b) + 5]), pk2((acc)[(b) + 2], (acc)[(b) + 3]), pk2((acc)[(b) + 6], (acc)[(b) + 7])}

__global__ void __launch_bounds__(256, 1) k0_prep(const u16* __restrict__ Wv, const u16* __restrict__ Wo,
                                                   const u16* __restrict__ cvec, const u16* __restrict__ wmod,
                                                   const u16* __restrict__ mem0, const u16* __restrict__ w1, const u16* __restrict__ w2,
                                                   char* __restrict__ ws) {
  extern __shared__ __attribute__((aligned(16))) char lds[];
  const int b = blockIdx.x, tid = threadIdx.x, lane = tid & 63, w = tid >> 6;
  if (b < 128) {
    const int h = b >> 2, q = b & 3, x = lane & 31, hh = lane >> 5;
    stage_tile(lds, Wo + (size_t)h * DH * DM, DH, DM, DM, 1024);
    __syncthreads();
    bf16x8 bw[4][8];
#pragma unroll
    for (int nt = 0; nt < 4; nt++)
#pragma unroll
      for (int ks = 0; ks < 8; ks++) bw[nt][ks] = gather8(lds, 16 * ks + 8 * hh, 128 * w + 32 * nt + x, 1024);
    u32x4* dst = (u32x4*)(ws + OFF_WVO);
    for (int mt = 0; mt < 4; mt++) {
      const int drow = 128 * q + 32 * mt;
      bf16x8 a[8];
#pragma unroll
      for (int ks = 0; ks < 8; ks++) a[ks] = *(const bf16x8*)(Wv + ((size_t)(drow + x) * NH + h) * DH + 16 * ks + 8 * hh);
      f32x16 acc[4];
#pragma unroll
      for (int nt = 0; nt < 4; nt++) {
#pragma unroll
        for (int i = 0; i < 16; i++) acc[nt][i] = 0.f;
#pragma unroll
        for (int ks = 0; ks < 8; ks++) acc[nt] = MFMA32(a[ks], bw[nt][ks], acc[nt]);
      }
#pragma unroll
      for (int nt = 0; nt < 4; nt++) {
        const int d = 128 * w + 32 * nt + x, c = d >> 3, cc = d & 7;
#pragma unroll
        for (int xb = 0; xb < 2; xb++) {
          const int dp = drow + 16 * xb, t = dp >> 6, ga = (dp >> 4) & 3, z = ga >> 1, gb = 2 * (ga & 1) + hh;
          const int lp = (h & 1) * 8 + cc + 16 * gb;
          size_t o = ((((size_t)(c * 4 + (h >> 3)) * 4 + ((h >> 1) & 3)) * 8 + t) * 2 + z) * 64 + lp;
          dst[o] = PACK_S(acc[nt], 8 * xb);
        }
      }
    }
  } else if (b < 128 + 48) {
    float* red = (float*)lds;
    const int cb = b - 128, cp = tid & 31, ksl = tid >> 5;
    float a0 = 0.f, a1 = 0.f;
#pragma unroll 16
    for (int k = 0; k < 64; k++) {
      const int kk = ksl * 64 + k;
      float cv = bf_lo((u32)cvec[kk]);
      float sv = bfr(cv / (1.f + __expf(-cv)));
      u32 wv = *(const u32*)(wmod + (size_t)kk * 3072 + cb * 64 + cp * 2);
      a0 += sv * bf_lo(wv); a1 += sv * bf_hi(wv);
    }
    red[(ksl * 32 + cp) * 2] = a0; red[(ksl * 32 + cp) * 2 + 1] = a1;
    __syncthreads();
    if (tid < 64) {
      float s = 0.f;
#pragma unroll
      for (int k = 0; k < 8; k++) s += red[k * 64 + tid];
      ((float*)(ws + OFF_MOD))[cb * 64 + tid] = bfr(s);
    }
  } else if (b < 176 + 28) {
    const int cb = b - 176;
    stage_tile(lds, w1 + cb * 64, DM, 64, FF, 128);
    __syncthreads();
    u32x4* dst = (u32x4*)(ws + OFF_W1F);
    for (int ks = 0; ks < 16; ks++) {
      bf16x8 v = gather8(lds, 32 * ks + 8 * (lane >> 4), 16 * w + (lane & 15), 128);
      dst[((size_t)(cb * 4 + w) * 16 + ks) * 64 + lane] = __builtin_bit_cast(u32x4, v);
    }
  } else if (b < 204 + 32) {
    const int cb = (b - 204) >> 2, kq = (b - 204) & 3;
    stage_tile(lds, w2 + (size_t)kq * 448 * DM + cb * 64, 448, 64, DM, 128);
    __syncthreads();
    u32x4* dst = (u32x4*)(ws + OFF_W2F);
    for (int ks = 0; ks < 14; ks++) {
      bf16x8 v = gather8(lds, 32 * ks + 8 * (lane >> 4), 16 * w + (lane & 15), 128);
      dst[((size_t)(cb * 4 + w) * 56 + kq * 14 + ks) * 64 + lane] = __builtin_bit_cast(u32x4, v);
    }
  } else {
    const int ib = b - 236;
    u32x4 sv = {SENT, SENT, SENT, SENT};
    u32x4* mx = (u32x4*)(ws + OFF_MEMX + 8192);
    for (int i = ib * 256 + tid; i < 64 * 512; i += 32 * 256) mx[i] = sv;
    u32x4* px = (u32x4*)(ws + OFF_PEX);
    for (int i = ib * 256 + tid; i < 64 * 1280; i += 32 * 256) px[i] = sv;
    if (ib == 0) {
      u32x4 z = {0u, 0u, 0u, 0u};
      for (int i = tid; i < 512; i += 256) ((u32x4*)(ws + OFF_FLAG))[i] = z;
      for (int i = tid; i < 512; i += 256) {
        int cbk = i >> 3, r = i & 7;
        u32x4 v = *(const u32x4*)(mem0 + r * DM + cbk * 8);
        v.x = sane(v.x); v.y = sane(v.y); v.z = sane(v.z); v.w = sane(v.w);
        ((u32x4*)(ws + OFF_MEMX))[i] = v;
      }
    }
  }
}

__device__ __forceinline__ float wave_sum(float v) {
#pragma unroll
  for (int o = 32; o > 0; o >>= 1) v += __shfl_xor(v, o);
  return v;
}
__global__ void __launch_bounds__(256) k1_ln(const u16* __restrict__ x, char* __restrict__ ws) {
  const int lane = threadIdx.x & 63, row = blockIdx.x * 4 + (threadIdx.x >> 6);
  const float* mod = (const float*)(ws + OFF_MOD);
  u32x4 v = *(const u32x4*)(x + (size_t)row * DM + lane * 8);
  float f[8] = {bf_lo(v.x), bf_hi(v.x), bf_lo(v.y), bf_hi(v.y), bf_lo(v.z), bf_hi(v.z), bf_lo(v.w), bf_hi(v.w)};
  float s = 0.f;
#pragma unroll
  for (int i = 0; i < 8; i++) s += f[i];
  const float m = wave_sum(s) * (1.f / DM);
  float q = 0.f;
#pragma unroll
  for (int i = 0; i < 8; i++) { f[i] -= m; q += f[i] * f[i]; }
  const float rs = rsqrtf(wave_sum(q) * (1.f / DM) + 1e-5f);
  float o[8];
#pragma unroll
  for (int i = 0; i < 8; i++) o[i] = f[i] * rs * (1.f + mod[512 + lane * 8 + i]) + mod[lane * 8 + i];
  u32x4 r = {pk2(o[0], o[1]), pk2(o[2], o[3]), pk2(o[4], o[5]), pk2(o[6], o[7])};
  *(u32x4*)((u16*)(ws + OFF_H) + (size_t)row * DM + lane * 8) = r;
}

#define AD_DEC 10
#define AD_INC 200
#define ADAPT(D, n) do { if ((n) == 1) D -= AD_DEC; else D += AD_INC; if (D < 0) D = 0; } while (0)

__device__ __forceinline__ bool wait_progress(__amdgpu_buffer_rsrc_t rf, u32 off, u32 need) {
  u32 spins = 0;
  while (true) {
    asm volatile("" ::: "memory");
    u32 v = ld4_sc1(rf, off);
    if (__all(v >= need)) return true;
    __builtin_amdgcn_s_sleep(2);
    if (++spins > 400000u) return false;
  }
}

__device__ __forceinline__ void role_score(const u16* __restrict__ Wq, const u16* __restrict__ Wk, char* __restrict__ ws,
                                           char* lds, int h) {
  const int tid = threadIdx.x, lane = tid & 63, w = tid >> 6;
  const int n = lane & 15, gb = lane >> 4;
  bf16x16 wf[4][8];
#pragma unroll
  for (int mat = 0; mat < 2; mat++) {
    __syncthreads();
    stage_tile(lds, (mat ? Wk : Wq) + h * DH, DM, DH, NH * DH, 256);
    __syncthreads();
#pragma unroll
    for (int tt = 0; tt < 2; tt++)
#pragma unroll
      for (int t = 0; t < 8; t++) {
        const int col = 32 * w + 16 * tt + n;
        u32x4 lo = gather8s(lds, 64 * t + 16 * (gb >> 1) + 4 * (gb & 1), col, 256);
        u32x4 hi = gather8s(lds, 64 * t + 16 * (2 + (gb >> 1)) + 4 * (gb & 1), col, 256);
        wf[mat * 2 + tt][t] = as_bf16(lo, hi);
      }
  }
  __syncthreads();
  char* memL = lds;
  char* qs = lds + 8192;
  char* ks_ = lds + 8192 + 2304;
  const int idx = (lane & 8) ? (int)0xEEEEEEEE : 0x44444444;
  const __amdgpu_buffer_rsrc_t rflag = mkrsrc(ws + OFF_FLAG);
  const __amdgpu_buffer_rsrc_t rkx = mkrsrc(ws + OFF_KX);
  const __amdgpu_buffer_rsrc_t rmem = mkrsrc(ws + OFF_MEMX);
  const __amdgpu_buffer_rsrc_t rpex = mkrsrc(ws + OFF_PEX);
  const int rho = lane & 15;
  const int keyp = 8 * ((rho >> 2) & 1) + 4 * (rho >> 3) + (rho & 3);
  int D1 = 0; u32 tref = now32();
  for (int s = 0; s < NSEG; s++) {
    u32x4 kx[2][4];
    if (w == 0) {
      if (!wait_progress(rflag, 4096u + h * 128u, s + 1)) return;
#pragma unroll
      for (int tt = 0; tt < 2; tt++)
#pragma unroll
        for (int k4 = 0; k4 < 4; k4++)
          kx[tt][k4] = ld16_sc1(rkx, (u32)(((32 * s + 16 * tt + keyp) * NH + h) * DH + 32 * k4 + 8 * gb) * 2u);
    }
    wait_until(tref + D1);
    {
      u32 off[2] = {(u32)s * 8192u + w * 2048u, (u32)s * 8192u + w * 2048u + 1024u};
      u32x4 a[2];
      u32 nsw = poll_chunks<2>(rmem, off, a, lane * 16u);
      if (!nsw) return;
      if (s > 0) ADAPT(D1, nsw);
      *(u32x4*)(memL + w * 2048 + lane * 16) = a[0];
      *(u32x4*)(memL + w * 2048 + 1024 + lane * 16) = a[1];
    }
    __syncthreads();
    f32x4 acc[4];
#pragma unroll
    for (int i = 0; i < 4; i++) acc[i] = (f32x4){0.f, 0.f, 0.f, 0.f};
#pragma unroll
    for (int t = 0; t < 8; t++) {
      bf16x8 af = *(const bf16x8*)(memL + t * 1024 + lane * 16);
#pragma unroll
      for (int i = 0; i < 4; i++) acc[i] = SMF(af, wf[i][t], acc[i], idx);
    }
    {
      const int col = 32 * w + 16 * (lane >> 5) + n, r0 = 4 * ((lane >> 4) & 1);
#pragma unroll
      for (int mat = 0; mat < 2; mat++) {
        char* dstp = mat ? ks_ : qs;
#pragma unroll
        for (int j = 0; j < 4; j++) {
          auto r = __builtin_amdgcn_permlane32_swap(f2u(acc[2 * mat][j]), f2u(acc[2 * mat + 1][j]), false, false);
          float v = u2f(r[0]) + u2f(r[1]);
          *(__bf16*)(dstp + (r0 + j) * 272 + col * 2) = (__bf16)v;
        }
      }
    }
    __syncthreads();
    if (w == 0) {
      f32x4 sc[3];
#pragma unroll
      for (int i = 0; i < 3; i++) sc[i] = (f32x4){0.f, 0.f, 0.f, 0.f};
#pragma unroll
      for (int k4 = 0; k4 < 4; k4++) {
        bf16x8 qf = *(const bf16x8*)(qs + (lane & 7) * 272 + (32 * k4 + 8 * gb) * 2);
        bf16x8 kf = *(const bf16x8*)(ks_ + (lane & 7) * 272 + (32 * k4 + 8 * gb) * 2);
        sc[0] = MFMA16(as_bf8(kx[0][k4]), qf, sc[0]);
        sc[1] = MFMA16(as_bf8(kx[1][k4]), qf, sc[1]);
        sc[2] = MFMA16(kf, qf, sc[2]);
      }
      const float sl = 0.08838834764831845f * 1.4426950408889634f;
      float mx = -3.0e38f;
#pragma unroll
      for (int j = 0; j < 4; j++) {
        if (gb >= 2) sc[2][j] = -3.0e38f;
        mx = fmaxf(mx, fmaxf(fmaxf(sc[0][j], sc[1][j]), sc[2][j]));
      }
      { auto r = __builtin_amdgcn_permlane16_swap(f2u(mx), f2u(mx), false, false);
        mx = fmaxf(u2f(r[0]), u2f(r[1])); }
      { auto r = __builtin_amdgcn_permlane32_swap(f2u(mx), f2u(mx), false, false);
        mx = fmaxf(u2f(r[0]), u2f(r[1])); }
      float sum = 0.f;
#pragma unroll
      for (int i = 0; i < 3; i++)
#pragma unroll
        for (int j = 0; j < 4; j++) {
          float p = __builtin_amdgcn_exp2f((sc[i][j] - mx) * sl);
          if (i == 2 && gb >= 2) p = 0.f;
          sc[i][j] = p; sum += p;
        }
      { auto r = __builtin_amdgcn_permlane16_swap(f2u(sum), f2u(sum), false, false);
        sum = u2f(r[0]) + u2f(r[1]); }
      { auto r = __builtin_amdgcn_permlane32_swap(f2u(sum), f2u(sum), false, false);
        sum = u2f(r[0]) + u2f(r[1]); }
      const float inv = 1.f / sum;
      u32x4 px = {sane(pk2(sc[0][0] * inv, sc[0][1] * inv)), sane(pk2(sc[0][2] * inv, sc[0][3] * inv)),
                  sane(pk2(sc[1][0] * inv, sc[1][1] * inv)), sane(pk2(sc[1][2] * inv, sc[1][3] * inv))};
      u32x2 pm = {sane(pk2(sc[2][0] * inv, sc[2][1] * inv)), sane(pk2(sc[2][2] * inv, sc[2][3] * inv))};
      if (n < 8) {
        st16_sc1(px, rpex, (u32)s * 20480u + h * 512u + gb * 128u + n * 16u);
        if (gb < 2) st8_sc1(pm, rpex, (u32)s * 20480u + 16384u + h * 128u + n * 16u + gb * 8u);
      }
    } else if (w == 1) {
      u16* km = (u16*)(ws + OFF_KMEM) + ((size_t)s * NH + h) * 1024;
#pragma unroll
      for (int j = 0; j < 2; j++) {
        int u = j * 64 + lane, r = u >> 4, c = u & 15;
        *(u32x4*)(km + r * 128 + c * 8) = *(const u32x4*)(ks_ + r * 272 + c * 16);
      }
    }
    tref = now32();
  }
}

__device__ __forceinline__ void role_column(char* __restrict__ ws, char* lds, int c) {
  const int tid = threadIdx.x, lane = tid & 63, w = tid >> 6;
  const int n = lane & 15, gb = lane >> 4, cc = lane & 7;
  bf16x16 wf[4][8];
  {
    const u32x4* src = (const u32x4*)(ws + OFF_WVO) + (size_t)(c * 4 + w) * 4096;
#pragma unroll
    for (int tl = 0; tl < 4; tl++)
#pragma unroll
      for (int t = 0; t < 8; t++) wf[tl][t] = as_bf16(src[((tl * 8 + t) * 2) * 64 + lane], src[((tl * 8 + t) * 2 + 1) * 64 + lane]);
  }
  char* memL = lds;
  char* yl = lds + 8192 + w * 1024;
  float* red = (float*)(lds + 12288);
  const int idx = (lane & 8) ? (int)0xEEEEEEEE : 0x44444444;
  const __amdgpu_buffer_rsrc_t rflag = mkrsrc(ws + OFF_FLAG);
  const __amdgpu_buffer_rsrc_t rmem = mkrsrc(ws + OFF_MEMX);
  const __amdgpu_buffer_rsrc_t rpex = mkrsrc(ws + OFF_PEX);
  int D1 = 0, D2 = 0; u32 tref = now32();
  for (int s = 0; s < NSEG; s++) {
    u32x4 uf[4][2];
    {
      if (!wait_progress(rflag, (u32)(8 * w + (lane & 7)) * 128u, s + 1)) return;
      const __amdgpu_buffer_rsrc_t ru = mkrsrc(ws + OFF_UF + ((size_t)s * 64 + c) * 16384);
#pragma unroll
      for (int m = 0; m < 4; m++)
#pragma unroll
        for (int z = 0; z < 2; z++) uf[m][z] = ld16_sc1(ru, (u32)((((4 * w + m) * 2 + z) * 4 + gb) * 8 + cc) * 16u);
    }
    wait_until(tref + D1);
    {
      u32 off[2] = {(u32)s * 8192u + w * 2048u, (u32)s * 8192u + w * 2048u + 1024u};
      u32x4 a[2];
      u32 nsw = poll_chunks<2>(rmem, off, a, lane * 16u);
      if (!nsw) return;
      if (s > 0) ADAPT(D1, nsw);
      tref = now32();
      *(u32x4*)(memL + w * 2048 + lane * 16) = a[0];
      *(u32x4*)(memL + w * 2048 + 1024 + lane * 16) = a[1];
    }
    __syncthreads();
    bf16x16 yb;
    {
      f32x4 acc[4];
#pragma unroll
      for (int i = 0; i < 4; i++) acc[i] = (f32x4){0.f, 0.f, 0.f, 0.f};
#pragma unroll
      for (int t = 0; t < 8; t++) {
        bf16x8 af = *(const bf16x8*)(memL + t * 1024 + lane * 16);
#pragma unroll
        for (int i = 0; i < 4; i++) acc[i] = SMF(af, wf[i][t], acc[i], idx);
      }
#pragma unroll
      for (int pr = 0; pr < 2; pr++) {
        float v[4];
#pragma unroll
        for (int j = 0; j < 4; j++) {
          auto r = __builtin_amdgcn_permlane32_swap(f2u(acc[2 * pr][j]), f2u(acc[2 * pr + 1][j]), false, false);
          v[j] = u2f(r[0]) + u2f(r[1]);
        }
        const int hl = 2 * (2 * pr + (lane >> 5)) + (n >> 3);
        u32x2 pv = {pk2(v[0], v[1]), pk2(v[2], v[3])};
        *(u32x2*)(yl + (hl * 8 + cc) * 16 + 8 * ((lane >> 4) & 1)) = pv;
      }
      asm volatile("s_waitcnt lgkmcnt(0)" ::: "memory");
      u32x4 hz[2];
#pragma unroll
      for (int z = 0; z < 2; z++) {
        const int ga = 2 * z + (gb >> 1);
        u32x2 a = *(const u32x2*)(yl + ((2 * ga) * 8 + cc) * 16 + 8 * (gb & 1));
        u32x2 b = *(const u32x2*)(yl + ((2 * ga + 1) * 8 + cc) * 16 + 8 * (gb & 1));
        hz[z] = (u32x4){a.x, b.x, a.y, b.y};
      }
      yb = as_bf16(hz[0], hz[1]);
      *(u32x4*)(ws + OFF_Y + ((size_t)s * 64 + c) * 4096 + w * 1024 + lane * 16) = *(const u32x4*)(yl + lane * 16);
    }
    wait_until(tref + D2);
    {
      u32 off[5];
#pragma unroll
      for (int j = 0; j < 4; j++) off[j] = (u32)s * 20480u + (4 * w + j) * 1024u;
      off[4] = (u32)s * 20480u + 16384u + w * 1024u;
      u32x4 g[5];
      u32 nsw = poll_chunks<5>(rpex, off, g, lane * 16u);
      if (!nsw) return;
      ADAPT(D2, nsw);
      f32x4 d0 = {0.f, 0.f, 0.f, 0.f}, d1 = {0.f, 0.f, 0.f, 0.f};
      d0 = SMF(as_bf8(g[0]), as_bf16(uf[0][0], uf[0][1]), d0, idx);
      d1 = SMF(as_bf8(g[1]), as_bf16(uf[1][0], uf[1][1]), d1, idx);
      d0 = SMF(as_bf8(g[2]), as_bf16(uf[2][0], uf[2][1]), d0, idx);
      d1 = SMF(as_bf8(g[3]), as_bf16(uf[3][0], uf[3][1]), d1, idx);
      d0 = SMF(as_bf8(g[4]), yb, d0, idx);
      float* rd = red + (s & 1) * 512;
      if (n < 8) {
#pragma unroll
        for (int j = 0; j < 4; j++) {
          const int rowp = 4 * gb + j;
          rd[((rowp & 7) * 8 + cc) * 8 + 2 * w + (rowp >> 3)] = d0[j] + d1[j];
        }
      }
    }
    __syncthreads();
    if (w == 0) {
      const float* rd = red + (s & 1) * 512 + lane * 8;
      f32x4 a = *(const f32x4*)rd, b = *(const f32x4*)(rd + 4);
      float v = ((a[0] + a[1]) + (a[2] + a[3])) + ((b[0] + b[1]) + (b[2] + b[3]));
      float o = __builtin_bit_cast(float, __builtin_amdgcn_mov_dpp(__builtin_bit_cast(int, v), 0xB1, 0xF, 0xF, true));
      if (!(lane & 1)) st4_sc1(sane(pk2(v, o)), rmem, (u32)(s + 1) * 8192u + c * 128u + (lane >> 1) * 4u);
    }
    tref = now32();
  }
}

__device__ __forceinline__ void stage_h(char* dst, const char* ws, int s) {
  const u16* hsrc = (const u16*)(ws + OFF_H) + (size_t)s * 32 * DM;
#pragma unroll
  for (int j = 0; j < 8; j++) {
    int u = j * 256 + threadIdx.x, r = u >> 6, c = u & 63;
    *(u32x4*)(dst + r * 1040 + c * 16) = *(const u32x4*)(hsrc + r * DM + c * 8);
  }
}

template <bool SC1>
__device__ __forceinline__ void store_T_tile(const f32x16& acc, char* base, int tokrow, int h, int w, int lane) {
  const __amdgpu_buffer_rsrc_t r = mkrsrc(base);
#pragma unroll
  for (int qp = 0; qp < 2; qp++) {
    u32 ax = pk2(acc[8 * qp + 0], acc[8 * qp + 1]), ay = pk2(acc[8 * qp + 2], acc[8 * qp + 3]);
    u32 bx = pk2(acc[8 * qp + 4], acc[8 * qp + 5]), by = pk2(acc[8 * qp + 6], acc[8 * qp + 7]);
    auto r0 = __builtin_amdgcn_permlane32_swap(ax, bx, false, false);
    auto r1 = __builtin_amdgcn_permlane32_swap(ay, by, false, false);
    u32x4 v = {(u32)r0[0], (u32)r1[0], (u32)r0[1], (u32)r1[1]};
    u32 off = (u32)(((tokrow + (lane & 31)) * NH + h) * DH + 32 * w + 16 * qp + 8 * (lane >> 5)) * 2u;
    if (SC1) __builtin_amdgcn_raw_buffer_store_b128(v, r, off, 0, 16);
    else __builtin_amdgcn_raw_buffer_store_b128(v, r, off, 0, 0);
  }
}

__device__ __forceinline__ void role_vu(const u16* __restrict__ Wv, const u16* __restrict__ Wo,
                                        char* __restrict__ ws, char* lds, int h) {
  const int tid = threadIdx.x, lane = tid & 63, w = tid >> 6, x = lane & 31, hh = lane >> 5;
  bf16x8 wv[32], wo[4][8];
  stage_tile(lds, Wv + h * DH, DM, DH, NH * DH, 256);
  __syncthreads();
#pragma unroll
  for (int ks = 0; ks < 32; ks++) wv[ks] = gather8(lds, 16 * ks + 8 * hh, 32 * w + x, 256);
  __syncthreads();
  stage_tile(lds, Wo + (size_t)h * DH * DM, DH, DM, DM, 1024);
  __syncthreads();
#pragma unroll
  for (int nt = 0; nt < 4; nt++)
#pragma unroll
    for (int ks = 0; ks < 8; ks++) wo[nt][ks] = gather8(lds, 16 * ks + 8 * hh, 128 * w + 32 * nt + x, 1024);
  __syncthreads();
  char* hb = lds;
  char* vs = lds + 33280;
  const __amdgpu_buffer_rsrc_t rflag = mkrsrc(ws + OFF_FLAG);
  for (int s = 0; s < NSEG; s++) {
    stage_h(hb, ws, s);
    __syncthreads();
    f32x16 av;
#pragma unroll
    for (int i = 0; i < 16; i++) av[i] = 0.f;
#pragma unroll
    for (int ks = 0; ks < 32; ks++) {
      bf16x8 hf = *(const bf16x8*)(hb + x * 1040 + (16 * ks + 8 * hh) * 2);
      av = MFMA32(wv[ks], hf, av);
    }
#pragma unroll
    for (int q = 0; q < 4; q++) {
      u32x2 pv = {pk2(av[4 * q], av[4 * q + 1]), pk2(av[4 * q + 2], av[4 * q + 3])};
      *(u32x2*)(vs + x * 272 + (32 * w + 8 * q + 4 * hh) * 2) = pv;
    }
    __syncthreads();
    bf16x8 vf[8];
#pragma unroll
    for (int ks = 0; ks < 8; ks++) vf[ks] = *(const bf16x8*)(vs + x * 272 + (16 * ks + 8 * hh) * 2);
    const __amdgpu_buffer_rsrc_t ru = mkrsrc(ws + OFF_UF + (size_t)s * 64 * 16384);
#pragma unroll
    for (int nt = 0; nt < 4; nt++) {
      f32x16 au;
#pragma unroll
      for (int i = 0; i < 16; i++) au[i] = 0.f;
#pragma unroll
      for (int ks = 0; ks < 8; ks++) au = MFMA32(vf[ks], wo[nt][ks], au);
      const int d = 128 * w + 32 * nt + x, c = d >> 3, cc = d & 7;
#pragma unroll
      for (int xb = 0; xb < 2; xb++)
        st16_sc1(PACK_S(au, 8 * xb), ru, (u32)((((c * 16 + (h >> 1)) * 2 + (h & 1)) * 4 + 2 * hh + xb) * 8 + cc) * 16u);
    }
    asm volatile("s_waitcnt vmcnt(0)" ::: "memory");
    __syncthreads();
    if (tid == 0) st4_sc1((u32)(s + 1), rflag, h * 128u);
  }
}

__device__ __forceinline__ void role_qk(const u16* __restrict__ Wq, const u16* __restrict__ Wk, char* __restrict__ ws, char* lds, int h) {
  const int tid = threadIdx.x, lane = tid & 63, w = tid >> 6, x = lane & 31, hh = lane >> 5;
  bf16x8 wq[32], wk[32];
  stage_tile(lds, Wk + h * DH, DM, DH, NH * DH, 256);
  __syncthreads();
#pragma unroll
  for (int ks = 0; ks < 32; ks++) wk[ks] = gather8(lds, 16 * ks + 8 * hh, 32 * w + x, 256);
  __syncthreads();
  stage_tile(lds, Wq + h * DH, DM, DH, NH * DH, 256);
  __syncthreads();
#pragma unroll
  for (int ks = 0; ks < 32; ks++) wq[ks] = gather8(lds, 16 * ks + 8 * hh, 32 * w + x, 256);
  __syncthreads();
  const __amdgpu_buffer_rsrc_t rflag = mkrsrc(ws + OFF_FLAG);
  char* hb = lds;
  for (int s = 0; s < NSEG; s++) {
    stage_h(hb, ws, s);
    __syncthreads();
    f32x16 aq, ak;
#pragma unroll
    for (int i = 0; i < 16; i++) { aq[i] = 0.f; ak[i] = 0.f; }
#pragma unroll
    for (int ks = 0; ks < 32; ks++) {
      bf16x8 hf = *(const bf16x8*)(hb + x * 1040 + (16 * ks + 8 * hh) * 2);
      ak = MFMA32(wk[ks], hf, ak);
      aq = MFMA32(wq[ks], hf, aq);
    }
    store_T_tile<true>(ak, ws + OFF_KX, 32 * s, h, w, lane);
    store_T_tile<false>(aq, ws + OFF_QX, 32 * s, h, w, lane);
    asm volatile("s_waitcnt vmcnt(0)" ::: "memory");
    __syncthreads();
    if (tid == 0) st4_sc1((u32)(s + 1), rflag, 4096u + h * 128u);
  }
}

__global__ void __launch_bounds__(256, 1) k2_rec(const u16* __restrict__ Wq, const u16* __restrict__ Wk, const u16* __restrict__ Wv,
                                                  const u16* __restrict__ Wo, char* __restrict__ ws) {
  extern __shared__ __attribute__((aligned(16))) char lds[];
  const int b = blockIdx.x;
  if (b < 32) role_score(Wq, Wk, ws, lds, b);
  else if (b < 96) role_column(ws, lds, b - 32);
  else if (b < 128) role_vu(Wv, Wo, ws, lds, b - 96);
  else role_qk(Wq, Wk, ws, lds, b - 128);
}

__device__ __forceinline__ float xg_max(float v) {
  auto r = __builtin_amdgcn_permlane16_swap(f2u(v), f2u(v), false, false);
  v = fmaxf(u2f(r[0]), u2f(r[1]));
  auto r2 = __builtin_amdgcn_permlane32_swap(f2u(v), f2u(v), false, false);
  return fmaxf(u2f(r2[0]), u2f(r2[1]));
}
__device__ __forceinline__ float xg_sum(float v) {
  auto r = __builtin_amdgcn_permlane16_swap(f2u(v), f2u(v), false, false);
  v = u2f(r[0]) + u2f(r[1]);
  auto r2 = __builtin_amdgcn_permlane32_swap(f2u(v), f2u(v), false, false);
  return u2f(r2[0]) + u2f(r2[1]);
}
__global__ void __launch_bounds__(256, 1) k3_attn(const u16* __restrict__ x, char* __restrict__ ws) {
  extern __shared__ __attribute__((aligned(16))) char lds[];
  const int b = blockIdx.x, tid = threadIdx.x, lane = tid & 63, w = tid >> 6;
  const int s = (b & 7) * 8 + (b >> 5), nq = (b >> 3) & 3;
  const int n = lane & 15, g = lane >> 4, cc = lane & 7;
  const u16* Qx = (const u16*)(ws + OFF_QX);
  const u16* Kx = (const u16*)(ws + OFF_KX);
  const u16* Km = (const u16*)(ws + OFF_KMEM);
  const u32x4* UF = (const u32x4*)(ws + OFF_UF) + (size_t)s * 64 * 1024;
  const char* Yg = ws + OFF_Y + (size_t)s * 64 * 4096;
  const int keyrow = 16 * ((n >> 2) & 1) + 4 * (n >> 3) + (n & 3);
  const int keyb = 16 * (g & 1) + 4 * (g >> 1);
  const float sl = 0.08838834764831845f * 1.4426950408889634f;
  f32x4 acc[2][8];
#pragma unroll
  for (int i = 0; i < 2; i++)
#pragma unroll
    for (int j = 0; j < 8; j++) acc[i][j] = (f32x4){0.f, 0.f, 0.f, 0.f};
  for (int grp = 0; grp < 2; grp++) {
    u32x4 pm[2] = {(u32x4){0u, 0u, 0u, 0u}, (u32x4){0u, 0u, 0u, 0u}};
#pragma unroll
    for (int hl = 0; hl < 4; hl++) {
      const int hd = 8 * w + 4 * grp + hl;
      bf16x8 qf[2][4], kf[2][4], mf[4];
      u32x4 ub[8];
#pragma unroll
      for (int ks = 0; ks < 4; ks++) {
#pragma unroll
        for (int t = 0; t < 2; t++) {
          qf[t][ks] = *(const bf16x8*)(Qx + ((size_t)(32 * s + 16 * t + n) * NH + hd) * DH + 32 * ks + 8 * g);
          kf[t][ks] = *(const bf16x8*)(Kx + ((size_t)(32 * s + 8 * t + keyrow) * NH + hd) * DH + 32 * ks + 8 * g);
        }
        mf[ks] = *(const bf16x8*)(Km + ((size_t)s * NH + hd) * 1024 + (n & 7) * 128 + 32 * ks + 8 * g);
      }
#pragma unroll
      for (int nt = 0; nt < 8; nt++) {
        const int c = 16 * nq + 2 * nt + (n >> 3);
        ub[nt] = UF[(size_t)c * 1024 + (((hd >> 1) * 2 + (hd & 1)) * 4 + g) * 8 + cc];
      }
      const bool mvalid = (g >> 1) == (hl >> 1);
#pragma unroll
      for (int qt = 0; qt < 2; qt++) {
        f32x4 sc[3];
#pragma unroll
        for (int i = 0; i < 3; i++) sc[i] = (f32x4){0.f, 0.f, 0.f, 0.f};
#pragma unroll
        for (int ks = 0; ks < 4; ks++) {
          sc[0] = MFMA16(kf[0][ks], qf[qt][ks], sc[0]);
          sc[1] = MFMA16(kf[1][ks], qf[qt][ks], sc[1]);
          sc[2] = MFMA16(mf[ks], qf[qt][ks], sc[2]);
        }
        const int qi = 16 * qt + n;
        float mx = -3.0e38f;
#pragma unroll
        for (int j = 0; j < 4; j++) {
          if (keyb + j > qi) sc[0][j] = -3.0e38f;
          if (keyb + 8 + j > qi) sc[1][j] = -3.0e38f;
          if (!mvalid) sc[2][j] = -3.0e38f;
          mx = fmaxf(mx, fmaxf(fmaxf(sc[0][j], sc[1][j]), sc[2][j]));
        }
        mx = xg_max(mx);
        float sum = 0.f;
#pragma unroll
        for (int i = 0; i < 3; i++)
#pragma unroll
          for (int j = 0; j < 4; j++) {
            float p = sc[i][j] > -1.0e38f ? __builtin_amdgcn_exp2f((sc[i][j] - mx) * sl) : 0.f;
            sc[i][j] = p; sum += p;
          }
        const float inv = 1.f / xg_sum(sum);
        u32x4 pa = {pk2(sc[0][0] * inv, sc[0][1] * inv), pk2(sc[1][0] * inv, sc[1][1] * inv),
                    pk2(sc[0][2] * inv, sc[0][3] * inv), pk2(sc[1][2] * inv, sc[1][3] * inv)};
        const u32 m0 = pk2(sc[2][0] * inv, sc[2][1] * inv), m1 = pk2(sc[2][2] * inv, sc[2][3] * inv);
        if (hl & 1) { pm[qt].z = mvalid ? m0 : pm[qt].z; pm[qt].w = mvalid ? m1 : pm[qt].w; }
        else        { pm[qt].x = mvalid ? m0 : pm[qt].x; pm[qt].y = mvalid ? m1 : pm[qt].y; }
#pragma unroll
        for (int nt = 0; nt < 8; nt++) acc[qt][nt] = MFMA16(as_bf8(pa), as_bf8(ub[nt]), acc[qt][nt]);
      }
    }
    {
      const int ha = 8 * w + 4 * grp + 2 * (g >> 1);
#pragma unroll
      for (int nt = 0; nt < 8; nt++) {
        const int c = 16 * nq + 2 * nt + (n >> 3);
        u32x2 ya = *(const u32x2*)(Yg + (size_t)c * 4096 + (ha * 8 + cc) * 16 + 8 * (g & 1));
        u32x2 yb = *(const u32x2*)(Yg + (size_t)c * 4096 + ((ha + 1) * 8 + cc) * 16 + 8 * (g & 1));
        u32x4 yf = {ya.x, ya.y, yb.x, yb.y};
        acc[0][nt] = MFMA16(as_bf8(pm[0]), as_bf8(yf), acc[0][nt]);
        acc[1][nt] = MFMA16(as_bf8(pm[1]), as_bf8(yf), acc[1][nt]);
      }
    }
  }
  float* red = (float*)lds;
#pragma unroll
  for (int qt = 0; qt < 2; qt++)
#pragma unroll
    for (int nt = 0; nt < 8; nt++)
#pragma unroll
      for (int j = 0; j < 4; j++) red[(w * 32 + 16 * qt + 4 * g + j) * 132 + 16 * nt + n] = acc[qt][nt][j];
  __syncthreads();
  const float* mod = (const float*)(ws + OFF_MOD);
  u16* x1 = (u16*)(ws + OFF_X1);
#pragma unroll
  for (int it = 0; it < 2; it++) {
    const int u = it * 256 + tid, r = u >> 4, c8 = (u & 15) * 8;
    float o[8];
#pragma unroll
    for (int j = 0; j < 8; j++) o[j] = 0.f;
#pragma unroll
    for (int ww = 0; ww < 4; ww++) {
      f32x4 a = *(const f32x4*)(red + (ww * 32 + r) * 132 + c8), bq = *(const f32x4*)(red + (ww * 32 + r) * 132 + c8 + 4);
#pragma unroll
      for (int j = 0; j < 4; j++) { o[j] += a[j]; o[4 + j] += bq[j]; }
    }
    const int col = 128 * nq + c8;
    const size_t go = (size_t)(32 * s + r) * DM + col;
    u32x4 xv = *(const u32x4*)(x + go);
    float xf[8] = {bf_lo(xv.x), bf_hi(xv.x), bf_lo(xv.y), bf_hi(xv.y), bf_lo(xv.z), bf_hi(xv.z), bf_lo(xv.w), bf_hi(xv.w)};
#pragma unroll
    for (int j = 0; j < 8; j++) o[j] = xf[j] + bfr(mod[1024 + col + j] * bfr(o[j]));
    u32x4 rv = {pk2(o[0], o[1]), pk2(o[2], o[3]), pk2(o[4], o[5]), pk2(o[6], o[7])};
    *(u32x4*)(x1 + go) = rv;
  }
}

__global__ void __launch_bounds__(256, 1) k4_mlp1(const u16* __restrict__ b1, char* __restrict__ ws) {
  extern __shared__ __attribute__((aligned(16))) char lds[];
  const int b = blockIdx.x, tid = threadIdx.x, lane = tid & 63, w = tid >> 6;
  const int mt0 = b / 7, nb = b - mt0 * 7;
  const float* mod = (const float*)(ws + OFF_MOD);
  const u16* x1 = (const u16*)(ws + OFF_X1);
  const int n = lane & 15, g = lane >> 4;
  {
    float cbv[8], sbv[8];
#pragma unroll
    for (int i = 0; i < 8; i++) { cbv[i] = 1.f + mod[2048 + lane * 8 + i]; sbv[i] = mod[1536 + lane * 8 + i]; }
#pragma unroll 4
    for (int rr = 0; rr < 16; rr++) {
      const int r = w * 16 + rr;
      u32x4 v = *(const u32x4*)(x1 + (size_t)(mt0 * 64 + r) * DM + lane * 8);
      float f[8] = {bf_lo(v.x), bf_hi(v.x), bf_lo(v.y), bf_hi(v.y), bf_lo(v.z), bf_hi(v.z), bf_lo(v.w), bf_hi(v.w)};
      float sm = 0.f;
#pragma unroll
      for (int i = 0; i < 8; i++) sm += f[i];
      float m = wave_sum(sm) * (1.f / DM), q = 0.f;
#pragma unroll
      for (int i = 0; i < 8; i++) { f[i] -= m; q += f[i] * f[i]; }
      float rs = rsqrtf(wave_sum(q) * (1.f / DM) + 1e-5f);
      sm = 0.f;
#pragma unroll
      for (int i = 0; i < 8; i++) { f[i] = bfr(f[i] * rs * cbv[i] + sbv[i]); sm += f[i]; }
      m = wave_sum(sm) * (1.f / DM); q = 0.f;
#pragma unroll
      for (int i = 0; i < 8; i++) { f[i] -= m; q += f[i] * f[i]; }
      rs = rsqrtf(wave_sum(q) * (1.f / DM) + 1e-5f);
      u32x4 o = {pk2(f[0] * rs, f[1] * rs), pk2(f[2] * rs, f[3] * rs), pk2(f[4] * rs, f[5] * rs), pk2(f[6] * rs, f[7] * rs)};
      *(u32x4*)(lds + r * 1040 + lane * 16) = o;
    }
  }
  __syncthreads();
  const u32x4* WF = (const u32x4*)(ws + OFF_W1F) + (size_t)(nb * 16 + w * 4) * 16 * 64 + lane;
  f32x4 acc[4][4];
#pragma unroll
  for (int i = 0; i < 4; i++)
#pragma unroll
    for (int j = 0; j < 4; j++) acc[i][j] = (f32x4){0.f, 0.f, 0.f, 0.f};
#pragma unroll 4
  for (int ks = 0; ks < 16; ks++) {
    u32x4 bfm[4]; bf16x8 af[4];
#pragma unroll
    for (int nt = 0; nt < 4; nt++) bfm[nt] = WF[(nt * 16 + ks) * 64];
#pragma unroll
    for (int mt = 0; mt < 4; mt++) af[mt] = *(const bf16x8*)(lds + (16 * mt + n) * 1040 + (32 * ks + 8 * g) * 2);
#pragma unroll
    for (int mt = 0; mt < 4; mt++)
#pragma unroll
      for (int nt = 0; nt < 4; nt++) acc[mt][nt] = MFMA16(as_bf8(bfm[nt]), af[mt], acc[mt][nt]);
  }
  u16* hid = (u16*)(ws + OFF_HID);
#pragma unroll
  for (int nt = 0; nt < 4; nt++) {
    const int f0 = nb * 256 + w * 64 + 16 * nt + 4 * g;
    u32x2 bv = *(const u32x2*)(b1 + f0);
    const float bb[4] = {bf_lo(bv.x), bf_hi(bv.x), bf_lo(bv.y), bf_hi(bv.y)};
#pragma unroll
    for (int mt = 0; mt < 4; mt++) {
      float o[4];
#pragma unroll
      for (int j = 0; j < 4; j++) {
        float u = bfr(bfr(acc[mt][nt][j]) + bb[j]);
        float z = 0.7978845608028654f * (u + 0.044715f * u * u * u);
        float e = __expf(2.f * z);
        float th = 1.f - 2.f / (e + 1.f);
        o[j] = 0.5f * u * (1.f + th);
      }
      u32x2 ov = {pk2(o[0], o[1]), pk2(o[2], o[3])};
      *(u32x2*)(hid + (size_t)(mt0 * 64 + 16 * mt + n) * FF + f0) = ov;
    }
  }
}

__global__ void __launch_bounds__(256, 1) k5_mlp2(const u16* __restrict__ b2, char* __restrict__ ws, u16* __restrict__ out) {
  extern __shared__ __attribute__((aligned(16))) char lds[];
  const int b = blockIdx.x, tid = threadIdx.x, lane = tid & 63, w = tid >> 6;
  const int mt0 = (b & 7) * 4 + (b >> 6), nb = (b >> 3) & 7;
  const int n = lane & 15, g = lane >> 4;
  const u16* hid = (const u16*)(ws + OFF_HID);
  const u32x4* WF = (const u32x4*)(ws + OFF_W2F) + ((size_t)(nb * 4) * 56 + w * 14) * 64 + lane;
  f32x4 acc[4][4];
#pragma unroll
  for (int i = 0; i < 4; i++)
#pragma unroll
    for (int j = 0; j < 4; j++) acc[i][j] = (f32x4){0.f, 0.f, 0.f, 0.f};
#pragma unroll 2
  for (int ks = 0; ks < 14; ks++) {
    u32x4 bfm[4]; bf16x8 af[4];
#pragma unroll
    for (int nt = 0; nt < 4; nt++) bfm[nt] = WF[(nt * 56 + ks) * 64];
#pragma unroll
    for (int mt = 0; mt < 4; mt++) af[mt] = *(const bf16x8*)(hid + (size_t)(mt0 * 64 + 16 * mt + n) * FF + (w * 14 + ks) * 32 + 8 * g);
#pragma unroll
    for (int mt = 0; mt < 4; mt++)
#pragma unroll
      for (int nt = 0; nt < 4; nt++) acc[mt][nt] = MFMA16(as_bf8(bfm[nt]), af[mt], acc[mt][nt]);
  }
  float* red = (float*)lds;
#pragma unroll
  for (int mt = 0; mt < 4; mt++)
#pragma unroll
    for (int nt = 0; nt < 4; nt++) *(f32x4*)(red + (w * 64 + 16 * mt + n) * 68 + 16 * nt + 4 * g) = acc[mt][nt];
  __syncthreads();
  const float* mod = (const float*)(ws + OFF_MOD);
  const u16* x1 = (const u16*)(ws + OFF_X1);
#pragma unroll
  for (int it = 0; it < 2; it++) {
    const int u = it * 256 + tid, r = u >> 3, c8 = (u & 7) * 8;
    float o[8];
#pragma unroll
    for (int j = 0; j < 8; j++) o[j] = 0.f;
#pragma unroll
    for (int ww = 0; ww < 4; ww++) {
      f32x4 a = *(const f32x4*)(red + (ww * 64 + r) * 68 + c8), bq = *(const f32x4*)(red + (ww * 64 + r) * 68 + c8 + 4);
#pragma unroll
      for (int j = 0; j < 4; j++) { o[j] += a[j]; o[4 + j] += bq[j]; }
    }
    const int col = 64 * nb + c8;
    const size_t go = (size_t)(mt0 * 64 + r) * DM + col;
    u32x4 xv = *(const u32x4*)(x1 + go), bv = *(const u32x4*)(b2 + col);
    float xf[8] = {bf_lo(xv.x), bf_hi(xv.x), bf_lo(xv.y), bf_hi(xv.y), bf_lo(xv.z), bf_hi(xv.z), bf_lo(xv.w), bf_hi(xv.w)};
    float bb[8] = {bf_lo(bv.x), bf_hi(bv.x), bf_lo(bv.y), bf_hi(bv.y), bf_lo(bv.z), bf_hi(bv.z), bf_lo(bv.w), bf_hi(bv.w)};
#pragma unroll
    for (int j = 0; j < 8; j++) o[j] = xf[j] + bfr(mod[2560 + col + j] * bfr(bfr(o[j]) + bb[j]));
    u32x4 rv = {pk2(o[0], o[1]), pk2(o[2], o[3]), pk2(o[4], o[5]), pk2(o[6], o[7])};
    *(u32x4*)(out + go) = rv;
  }
}

extern "C" void kernel_launch(void* const* d_in, const int* in_sizes, int n_in, void* d_out, int out_size, void* d_ws,
                              size_t ws_size, hipStream_t stream) {
  const u16* x = (const u16*)d_in[0];
  const u16* mem0 = (const u16*)d_in[1];
  const u16* Wq = (const u16*)d_in[2];
  const u16* Wk = (const u16*)d_in[3];
  const u16* Wv = (const u16*)d_in[4];
  const u16* Wo = (const u16*)d_in[5];
  const u16* w1 = (const u16*)d_in[8];
  const u16* b1 = (const u16*)d_in[9];
  const u16* w2 = (const u16*)d_in[10];
  const u16* b2 = (const u16*)d_in[11];
  const u16* cvec = (const u16*)d_in[12];
  const u16* wmod = (const u16*)d_in[13];
  char* ws = (char*)d_ws;
  hipFuncSetAttribute((const void*)k0_prep, hipFuncAttributeMaxDynamicSharedMemorySize, 131072);
  hipFuncSetAttribute((const void*)k2_rec, hipFuncAttributeMaxDynamicSharedMemorySize, 131072);
  k0_prep<<<268, 256, 131072, stream>>>(Wv, Wo, cvec, wmod, mem0, w1, w2, ws);
  k1_ln<<<512, 256, 0, stream>>>(x, ws);
  k2_rec<<<160, 256, 131072, stream>>>(Wq, Wk, Wv, Wo, ws);
  hipFuncSetAttribute((const void*)k3_attn, hipFuncAttributeMaxDynamicSharedMemorySize, 67584);
  hipFuncSetAttribute((const void*)k4_mlp1, hipFuncAttributeMaxDynamicSharedMemorySize, 66560);
  hipFuncSetAttribute((const void*)k5_mlp2, hipFuncAttributeMaxDynamicSharedMemorySize, 69632);
  k3_attn<<<256, 256, 67584, stream>>>(x, ws);
  k4_mlp1<<<224, 256, 66560, stream>>>(b1, ws);
  k5_mlp2<<<256, 256, 69632, stream>>>(b2, ws, (u16*)d_out);
}
```

```cpp
#include <hip/hip_runtime.h>
#include <hip/hip_bf16.h>
#include <stdint.h>

typedef unsigned int u32;
typedef unsigned short u16;
typedef unsigned long long u64;
typedef __attribute__((ext_vector_type(2))) __bf16 bf16x2;
typedef __attribute__((ext_vector_type(8))) __bf16 bf16x8;
typedef __attribute__((ext_vector_type(16))) __bf16 bf16x16;
typedef __attribute__((ext_vector_type(4))) float f32x4;
typedef __attribute__((ext_vector_type(16))) float f32x16;
typedef __attribute__((ext_vector_type(2))) u32 u32x2;
typedef __attribute__((ext_vector_type(4))) u32 u32x4;
typedef __attribute__((ext_vector_type(8))) u32 u32x8;

#define NTOK 2048
#define DM 512
#define NH 32
#define DH 128
#define NSEG 64
#define FF 1792
#define SENT 0xFFFFFFFFu

#define OFF_FLAG 0ull
#define OFF_DONE 8192u
#define OFF_MOD 16384ull
#define OFF_MEMX 32768ull
#define OFF_PEX 0x90000ull
#define OFF_H (2ull << 20)
#define OFF_KMEM (4ull << 20)
#define OFF_WVO (8ull << 20)
#define OFF_KX (24ull << 20)
#define OFF_QX (40ull << 20)
#define OFF_Y (56ull << 20)
#define OFF_UF (72ull << 20)
#define OFF_PX (136ull << 20)
#define OFF_X1 (142ull << 20)
#define OFF_Y2 (146ull << 20)
#define OFF_HID (148ull << 20)
#define OFF_W1F (156ull << 20)
#define OFF_W2F (158ull << 20)
#define OFF_DBG (200ull << 20)
#define STAMP(slot) do { } while (0)

__device__ __forceinline__ u32 pk2(float a, float b) {
  bf16x2 v; v[0] = (__bf16)a; v[1] = (__bf16)b; return __builtin_bit_cast(u32, v);
}
__device__ __forceinline__ u32 f2u(float x) { return __builtin_bit_cast(u32, x); }
__device__ __forceinline__ float u2f(u32 x) { return __builtin_bit_cast(float, x); }
__device__ __forceinline__ float bf_lo(u32 x) { return __builtin_bit_cast(float, x << 16); }
__device__ __forceinline__ float bf_hi(u32 x) { return __builtin_bit_cast(float, x & 0xffff0000u); }
__device__ __forceinline__ float bfr(float a) { return (float)(__bf16)a; }
__device__ __forceinline__ __amdgpu_buffer_rsrc_t mkrsrc(const void* p) {
  return __builtin_amdgcn_make_buffer_rsrc((void*)p, 0, 0x7fffffff, 0x00020000);
}
__device__ __forceinline__ u32x4 ld16_sc1(__amdgpu_buffer_rsrc_t r, u32 off) { return __builtin_amdgcn_raw_buffer_load_b128(r, off, 0, 16); }
__device__ __forceinline__ u32 ld4_sc1(__amdgpu_buffer_rsrc_t r, u32 off) { return __builtin_amdgcn_raw_buffer_load_b32(r, off, 0, 16); }
__device__ __forceinline__ void st16_sc1(u32x4 v, __amdgpu_buffer_rsrc_t r, u32 off) { __builtin_amdgcn_raw_buffer_store_b128(v, r, off, 0, 16); }
__device__ __forceinline__ void st8_sc1(u32x2 v, __amdgpu_buffer_rsrc_t r, u32 off) { __builtin_amdgcn_raw_buffer_store_b64(v, r, off, 0, 16); }
__device__ __forceinline__ void st4_sc1(u32 v, __amdgpu_buffer_rsrc_t r, u32 off) { __builtin_amdgcn_raw_buffer_store_b32(v, r, off, 0, 16); }
__device__ __forceinline__ u32 badm(u32x4 a) { return (u32)(a.x == SENT) | (u32)(a.y == SENT) | (u32)(a.z == SENT) | (u32)(a.w == SENT); }
__device__ __forceinline__ u32 sane(u32 x) { return x < 0xFFFFFFFEu ? x : 0xFFFFFFFEu; }
__device__ __forceinline__ u32 now32() { return (u32)__builtin_amdgcn_s_memtime(); }
__device__ __forceinline__ void wait_until(u32 t) { while ((int)(t - now32()) > 0) { } }
__device__ __forceinline__ bf16x8 as_bf8(u32x4 v) { return __builtin_bit_cast(bf16x8, v); }
__device__ __forceinline__ bf16x16 as_bf16(u32x4 a, u32x4 b) {
  u32x8 v = {a.x, a.y, a.z, a.w, b.x, b.y, b.z, b.w}; return __builtin_bit_cast(bf16x16, v);
}
#define MFMA16(a, b, c) __builtin_amdgcn_mfma_f32_16x16x32_bf16((a), (b), (c), 0, 0, 0)
#define MFMA32(a, b, c) __builtin_amdgcn_mfma_f32_32x32x16_bf16((a), (b), (c), 0, 0, 0)
#define SMF(a, b, c, idx) __builtin_amdgcn_smfmac_f32_16x16x64_bf16((a), (b), (c), (idx), 0, 0)

template <int N>
__device__ __forceinline__ u32 poll_chunks(__amdgpu_buffer_rsrc_t r, const u32 (&off)[N], u32x4 (&a)[N], u32 lane16, u32& tiss) {
  u32 spins = 0;
  while (true) {
    asm volatile("" : "+v"(lane16) :: "memory");
    if (spins) tiss = now32();
    u32 bad = 0;
#pragma unroll
    for (int j = 0; j < N; j++) a[j] = ld16_sc1(r, off[j] + lane16);
#pragma unroll
    for (int j = 0; j < N; j++) bad |= badm(a[j]);
    ++spins;
    if (!__any(bad)) return spins;
    if (spins > 400000u) return 0;
  }
}

typedef __attribute__((ext_vector_type(4))) short s16x4;
typedef __attribute__((address_space(3))) s16x4 lds_s16x4;
template <int ROWB>
__device__ __forceinline__ void dma_tile(char* lds, const u16* src, int rows, int gstride) {
  constexpr int LPR = ROWB / 16;
  const int total = rows * LPR;
  for (int u = threadIdx.x; u < total; u += 256) {
    const int r = u / LPR, c = u % LPR;
    __builtin_amdgcn_global_load_lds((const u32*)(src + (size_t)r * gstride + c * 8), (u32*)(lds + u * 16), 16, 0, 0);
  }
}
__device__ __forceinline__ void dma_wait() { asm volatile("s_waitcnt vmcnt(0)" ::: "memory"); __syncthreads(); }
__device__ __forceinline__ u32x2 tr4(const char* lds, int row0, int col0, int pitch, int lane) {
  const char* p = lds + (row0 + ((lane & 15) >> 2)) * pitch + (col0 + 4 * (lane & 3)) * 2;
  s16x4 v = __builtin_amdgcn_ds_read_tr16_b64_v4i16((lds_s16x4*)p);
  return __builtin_bit_cast(u32x2, v);
}
__device__ __forceinline__ bf16x8 frag8(const char* lds, int k0, int col0, int pitch, int lane) {
  u32x2 a = tr4(lds, k0, col0, pitch, lane), b = tr4(lds, k0 + 4, col0, pitch, lane);
  return as_bf8((u32x4){a.x, a.y, b.x, b.y});
}
__device__ __forceinline__ u32x4 frag8s(const char* lds, int k0, int col0, int pitch, int lane) {
  u32x2 a = tr4(lds, k0, col0, pitch, lane), b = tr4(lds, k0 + 8, col0, pitch, lane);
  return (u32x4){a.x, b.x, a.y, b.y};
}
#define PACK_S(acc, b) (u32x4){pk2((acc)[(b)], (acc)[(b) + 1]), pk2((acc)[(b) + 4], (acc)[(b) + 5]), pk2((acc)[(b) + 2], (acc)[(b) + 3]), pk2((acc)[(b) + 6], (acc)[(b) + 7])}

__global__ void __launch_bounds__(256, 1) k0_prep(const u16* __restrict__ Wv, const u16* __restrict__ Wo,
                                                   const u16* __restrict__ cvec, const u16* __restrict__ wmod,
                                                   const u16* __restrict__ mem0, const u16* __restrict__ w1, const u16* __restrict__ w2,
                                                   char* __restrict__ ws) {
  extern __shared__ __attribute__((aligned(16))) char lds[];
  const int b = blockIdx.x, tid = threadIdx.x, lane = tid & 63, w = tid >> 6;
  if (b < 128) {
    const int h = b >> 2, q = b & 3, x = lane & 31, hh = lane >> 5;
    dma_tile<1024>(lds, Wo + (size_t)h * DH * DM, DH, DM);
    dma_wait();
    bf16x8 bw[4][8];
#pragma unroll
    for (int nt = 0; nt < 4; nt++)
#pragma unroll
      for (int ks = 0; ks < 8; ks++) bw[nt][ks] = frag8(lds, 16 * ks + 8 * hh, 128 * w + 32 * nt + (x & 16), 1024, lane);
    u32x4* dst = (u32x4*)(ws + OFF_WVO);
    for (int mt = 0; mt < 4; mt++) {
      const int drow = 128 * q + 32 * mt;
      bf16x8 a[8];
#pragma unroll
      for (int ks = 0; ks < 8; ks++) a[ks] = *(const bf16x8*)(Wv + ((size_t)(drow + x) * NH + h) * DH + 16 * ks + 8 * hh);
      f32x16 acc[4];
#pragma unroll
      for (int nt = 0; nt < 4; nt++) {
#pragma unroll
        for (int i = 0; i < 16; i++) acc[nt][i] = 0.f;
#pragma unroll
        for (int ks = 0; ks < 8; ks++) acc[nt] = MFMA32(a[ks], bw[nt][ks], acc[nt]);
      }
#pragma unroll
      for (int nt = 0; nt < 4; nt++) {
        const int d = 128 * w + 32 * nt + x, c = d >> 3, cc = d & 7;
#pragma unroll
        for (int xb = 0; xb < 2; xb++) {
          const int dp = drow + 16 * xb, t = dp >> 6, ga = (dp >> 4) & 3, z = ga >> 1, gb = 2 * (ga & 1) + hh;
          const int lp = (h & 1) * 8 + cc + 16 * gb;
          size_t o = ((((size_t)(c * 4 + (h >> 3)) * 4 + ((h >> 1) & 3)) * 8 + t) * 2 + z) * 64 + lp;
          dst[o] = PACK_S(acc[nt], 8 * xb);
        }
      }
    }
  } else if (b < 128 + 48) {
    float* red = (float*)lds;
    const int cb = b - 128, cp = tid & 31, ksl = tid >> 5;
    float a0 = 0.f, a1 = 0.f;
#pragma unroll 16
    for (int k = 0; k < 64; k++) {
      const int kk = ksl * 64 + k;
      float cv = bf_lo((u32)cvec[kk]);
      float sv = bfr(cv / (1.f + __expf(-cv)));
      u32 wv = *(const u32*)(wmod + (size_t)kk * 3072 + cb * 64 + cp * 2);
      a0 += sv * bf_lo(wv); a1 += sv * bf_hi(wv);
    }
    red[(ksl * 32 + cp) * 2] = a0; red[(ksl * 32 + cp) * 2 + 1] = a1;
    __syncthreads();
    if (tid < 64) {
      float s = 0.f;
#pragma unroll
      for (int k = 0; k < 8; k++) s += red[k * 64 + tid];
      ((float*)(ws + OFF_MOD))[cb * 64 + tid] = bfr(s);
    }
  } else if (b < 176 + 28) {
    const int cb = b - 176;
    dma_tile<128>(lds, w1 + cb * 64, DM, FF);
    dma_wait();
    u32x4* dst = (u32x4*)(ws + OFF_W1F);
    for (int ks = 0; ks < 16; ks++) {
      bf16x8 v = frag8(lds, 32 * ks + 8 * (lane >> 4), 16 * w, 128, lane);
      dst[((size_t)(cb * 4 + w) * 16 + ks) * 64 + lane] = __builtin_bit_cast(u32x4, v);
    }
  } else if (b < 204 + 32) {
    const int cb = (b - 204) >> 2, kq = (b - 204) & 3;
    dma_tile<128>(lds, w2 + (size_t)kq * 448 * DM + cb * 64, 448, DM);
    dma_wait();
    u32x4* dst = (u32x4*)(ws + OFF_W2F);
    for (int ks = 0; ks < 14; ks++) {
      bf16x8 v = frag8(lds, 32 * ks + 8 * (lane >> 4), 16 * w, 128, lane);
      dst[((size_t)(cb * 4 + w) * 56 + kq * 14 + ks) * 64 + lane] = __builtin_bit_cast(u32x4, v);
    }
  } else {
    const int ib = b - 236;
    u32x4 sv = {SENT, SENT, SENT, SENT};
    u32x4* mx = (u32x4*)(ws + OFF_MEMX + 8192);
    for (int i = ib * 256 + tid; i < 64 * 512; i += 32 * 256) mx[i] = sv;
    u32x4* px = (u32x4*)(ws + OFF_PEX);
    for (int i = ib * 256 + tid; i < 64 * 1280; i += 32 * 256) px[i] = sv;
    if (ib == 0) {
      u32x4 z = {0u, 0u, 0u, 0u};
      for (int i = tid; i < 1024; i += 256) ((u32x4*)(ws + OFF_FLAG))[i] = z;
      for (int i = tid; i < 512; i += 256) {
        int cbk = i >> 3, r = i & 7;
        u32x4 v = *(const u32x4*)(mem0 + r * DM + cbk * 8);
        v.x = sane(v.x); v.y = sane(v.y); v.z = sane(v.z); v.w = sane(v.w);
        ((u32x4*)(ws + OFF_MEMX))[i] = v;
      }
    }
  }
}

__device__ __forceinline__ float wave_sum(float v) {
#pragma unroll
  for (int o = 32; o > 0; o >>= 1) v += __shfl_xor(v, o);
  return v;
}
__global__ void __launch_bounds__(256) k1_ln(const u16* __restrict__ x, char* __restrict__ ws) {
  const int lane = threadIdx.x & 63, row = blockIdx.x * 4 + (threadIdx.x >> 6);
  const float* mod = (const float*)(ws + OFF_MOD);
  u32x4 v = *(const u32x4*)(x + (size_t)row * DM + lane * 8);
  float f[8] = {bf_lo(v.x), bf_hi(v.x), bf_lo(v.y), bf_hi(v.y), bf_lo(v.z), bf_hi(v.z), bf_lo(v.w), bf_hi(v.w)};
  float s = 0.f;
#pragma unroll
  for (int i = 0; i < 8; i++) s += f[i];
  const float m = wave_sum(s) * (1.f / DM);
  float q = 0.f;
#pragma unroll
  for (int i = 0; i < 8; i++) { f[i] -= m; q += f[i] * f[i]; }
  const float rs = rsqrtf(wave_sum(q) * (1.f / DM) + 1e-5f);
  float o[8];
#pragma unroll
  for (int i = 0; i < 8; i++) o[i] = f[i] * rs * (1.f + mod[512 + lane * 8 + i]) + mod[lane * 8 + i];
  u32x4 r = {pk2(o[0], o[1]), pk2(o[2], o[3]), pk2(o[4], o[5]), pk2(o[6], o[7])};
  *(u32x4*)((u16*)(ws + OFF_H) + (size_t)row * DM + lane * 8) = r;
}

#define AD_DEC 8
#define AD_INC 100
#define D1S_INIT 3000
#define D1C_INIT 600
#define D2C_INIT 2800
#define ADAPT(D, n) do { if ((n) == 1) D -= AD_DEC; else D += AD_INC; if (D < 0) D = 0; } while (0)

__device__ __forceinline__ bool wait_progress(__amdgpu_buffer_rsrc_t rf, u32 off, u32 need, u32& seen) {
  u32 spins = 0;
  if (__all(seen >= need)) return true;
  while (true) {
    asm volatile("" : "+v"(off) :: "memory");
    seen = ld4_sc1(rf, off);
    if (__all(seen >= need)) return true;
    __builtin_amdgcn_s_sleep(2);
    if (++spins > 400000u) return false;
  }
}

__device__ __forceinline__ void role_score(const u16* __restrict__ Wq, const u16* __restrict__ Wk, char* ws,
                                           char* lds, int h) {
  const int tid = threadIdx.x, lane = tid & 63, w = tid >> 6;
  const int n = lane & 15, gb = lane >> 4;
  bf16x16 wf[4][8];
#pragma unroll
  for (int mat = 0; mat < 2; mat++) {
    __syncthreads();
    dma_tile<256>(lds, (mat ? Wk : Wq) + h * DH, DM, NH * DH);
    dma_wait();
#pragma unroll
    for (int tt = 0; tt < 2; tt++)
#pragma unroll
      for (int t = 0; t < 8; t++) {
        const int col = 32 * w + 16 * tt;
        u32x4 lo = frag8s(lds, 64 * t + 16 * (gb >> 1) + 4 * (gb & 1), col, 256, lane);
        u32x4 hi = frag8s(lds, 64 * t + 16 * (2 + (gb >> 1)) + 4 * (gb & 1), col, 256, lane);
        wf[mat * 2 + tt][t] = as_bf16(lo, hi);
      }
  }
  __syncthreads();
  char* memL = lds;
  char* qs = lds + 8192;
  char* ks_ = lds + 8192 + 2304;
  const int idx = (lane & 8) ? (int)0xEEEEEEEE : 0x44444444;
  const __amdgpu_buffer_rsrc_t rflag = mkrsrc(ws + OFF_FLAG);
  const __amdgpu_buffer_rsrc_t rkx = mkrsrc(ws + OFF_KX);
  const __amdgpu_buffer_rsrc_t rmem = mkrsrc(ws + OFF_MEMX);
  const __amdgpu_buffer_rsrc_t rpex = mkrsrc(ws + OFF_PEX);
  const int rho = lane & 15;
  const int keyp = 8 * ((rho >> 2) & 1) + 4 * (rho >> 3) + (rho & 3);
  const u32 pxoff = h * 512u + gb * 128u + (n & 7) * 16u + (n >> 3) * 8u;
  const u32 pmoff = 16384u + h * 128u + (n & 7) * 16u + (gb & 1) * 8u + (gb >> 1) * 4u;
  int D1 = D1S_INIT; u32 tref = now32() - D1S_INIT, seen = 0;
  STAMP(1);
  for (int s = 0; s < NSEG; s++) {
    u32x4 kx[2][4];
    if (w == 0) {
      if (!wait_progress(rflag, 4096u + h * 128u, s + 1, seen)) return;
#pragma unroll
      for (int tt = 0; tt < 2; tt++)
#pragma unroll
        for (int k4 = 0; k4 < 4; k4++)
          kx[tt][k4] = ld16_sc1(rkx, (u32)(((32 * s + 16 * tt + keyp) * NH + h) * DH + 32 * k4 + 8 * gb) * 2u);
    }
    f32x4 acc[4];
#pragma unroll
    for (int i = 0; i < 4; i++) { acc[i] = (f32x4){0.f, 0.f, 0.f, 0.f}; asm volatile("" : "+v"(acc[i])); }
    wait_until(tref + D1);
    {
      u32 off[2] = {(u32)s * 8192u + w * 2048u, (u32)s * 8192u + w * 2048u + 1024u};
      u32x4 a[2];
      u32 tiss = 0;
      u32 nsw = poll_chunks<2>(rmem, off, a, lane * 16u, tiss);
      if (!nsw) return;
      if (s > 2) ADAPT(D1, nsw);
      *(u32x4*)(memL + w * 2048 + lane * 16) = a[0];
      *(u32x4*)(memL + w * 2048 + 1024 + lane * 16) = a[1];
    }
    __syncthreads();
    STAMP(16 + 4 * s);
    bf16x8 af[8];
#pragma unroll
    for (int t = 0; t < 8; t++) af[t] = *(const bf16x8*)(memL + t * 1024 + lane * 16);
    __builtin_amdgcn_sched_barrier(0);
#pragma unroll
    for (int t = 0; t < 8; t++) { acc[0] = SMF(af[t], wf[0][t], acc[0], idx); acc[1] = SMF(af[t], wf[1][t], acc[1], idx); }
#pragma unroll
    for (int t = 0; t < 8; t++) { acc[2] = SMF(af[t], wf[2][t], acc[2], idx); acc[3] = SMF(af[t], wf[3][t], acc[3], idx); }
    {
      const int col = 32 * w + 16 * (lane >> 5) + n, r0 = 4 * ((lane >> 4) & 1);
#pragma unroll
      for (int mat = 0; mat < 2; mat++) {
        char* dstp = mat ? ks_ : qs;
#pragma unroll
        for (int j = 0; j < 4; j++) {
          auto r = __builtin_amdgcn_permlane32_swap(f2u(acc[2 * mat][j]), f2u(acc[2 * mat + 1][j]), false, false);
          float v = u2f(r[0]) + u2f(r[1]);
          *(__bf16*)(dstp + (r0 + j) * 272 + col * 2) = (__bf16)v;
        }
      }
    }
    __syncthreads();
    STAMP(17 + 4 * s);
    if (w == 0) {
      f32x4 sc[3];
#pragma unroll
      for (int i = 0; i < 3; i++) sc[i] = (f32x4){0.f, 0.f, 0.f, 0.f};
      bf16x8 qf[4], kf[4];
#pragma unroll
      for (int k4 = 0; k4 < 4; k4++) {
        qf[k4] = *(const bf16x8*)(qs + (lane & 7) * 272 + (32 * k4 + 8 * gb) * 2);
        kf[k4] = *(const bf16x8*)(ks_ + (lane & 7) * 272 + (32 * k4 + 8 * gb) * 2);
      }
      __builtin_amdgcn_sched_barrier(0);
#pragma unroll
      for (int k4 = 0; k4 < 4; k4++) {
        sc[0] = MFMA16(as_bf8(kx[0][k4]), qf[k4], sc[0]);
        sc[1] = MFMA16(as_bf8(kx[1][k4]), qf[k4], sc[1]);
        sc[2] = MFMA16(kf[k4], qf[k4], sc[2]);
      }
      const float sl = 0.08838834764831845f * 1.4426950408889634f;
      float mx = fmaxf(fmaxf(fmaxf(sc[0][0], sc[0][1]), fmaxf(sc[0][2], sc[0][3])),
                       fmaxf(fmaxf(fmaxf(sc[1][0], sc[1][1]), fmaxf(sc[1][2], sc[1][3])),
                             fmaxf(fmaxf(sc[2][0], sc[2][1]), fmaxf(sc[2][2], sc[2][3]))));
      { auto r = __builtin_amdgcn_permlane16_swap(f2u(mx), f2u(mx), false, false);
        mx = fmaxf(u2f(r[0]), u2f(r[1])); }
      { auto r = __builtin_amdgcn_permlane32_swap(f2u(mx), f2u(mx), false, false);
        mx = fmaxf(u2f(r[0]), u2f(r[1])); }
      const float nb = -mx * sl;
      const bool hiq = n >= 8, hig = gb >= 2;
      float p[6];
#pragma unroll
      for (int j = 0; j < 4; j++) p[j] = __builtin_amdgcn_exp2f(__builtin_fmaf(hiq ? sc[1][j] : sc[0][j], sl, nb));
      p[4] = __builtin_amdgcn_exp2f(__builtin_fmaf(hig ? sc[2][2] : sc[2][0], sl, nb));
      p[5] = __builtin_amdgcn_exp2f(__builtin_fmaf(hig ? sc[2][3] : sc[2][1], sl, nb));
      float sum = (p[0] + p[1]) + (p[2] + p[3]) + (hiq ? 0.f : (p[4] + p[5]));
      sum += u2f((u32)__builtin_amdgcn_mov_dpp((int)f2u(sum), 0x128, 0xF, 0xF, true));
      { auto r = __builtin_amdgcn_permlane16_swap(f2u(sum), f2u(sum), false, false);
        sum = u2f(r[0]) + u2f(r[1]); }
      { auto r = __builtin_amdgcn_permlane32_swap(f2u(sum), f2u(sum), false, false);
        sum = u2f(r[0]) + u2f(r[1]); }
      const float inv = __builtin_amdgcn_rcpf(sum);
      u32x2 px = {sane(pk2(p[0] * inv, p[1] * inv)), sane(pk2(p[2] * inv, p[3] * inv))};
      st8_sc1(px, rpex, (u32)s * 20480u + pxoff);
      if (!hiq) st4_sc1(sane(pk2(p[4] * inv, p[5] * inv)), rpex, (u32)s * 20480u + pmoff);
      STAMP(18 + 4 * s);
    } else if (w == 1) {
      const __amdgpu_buffer_rsrc_t rkm = mkrsrc(ws + OFF_KMEM);
#pragma unroll
      for (int j = 0; j < 2; j++) {
        int u = j * 64 + lane, r = u >> 4, c = u & 15;
        __builtin_amdgcn_raw_buffer_store_b128(*(const u32x4*)(ks_ + r * 272 + c * 16), rkm, (u32)((s * NH + h) * 1024 + r * 128 + c * 8) * 2u, 0, 0);
      }
    }
    tref = now32();
  }
}

__device__ __forceinline__ void role_column(char* ws, char* lds, int c) {
  const int tid = threadIdx.x, lane = tid & 63, w = tid >> 6;
  const int n = lane & 15, gb = lane >> 4, cc = lane & 7;
  bf16x16 wf[4][8];
  {
    const u32x4* src = (const u32x4*)(ws + OFF_WVO) + (size_t)(c * 4 + w) * 4096;
#pragma unroll
    for (int tl = 0; tl < 4; tl++)
#pragma unroll
      for (int t = 0; t < 8; t++) wf[tl][t] = as_bf16(src[((tl * 8 + t) * 2) * 64 + lane], src[((tl * 8 + t) * 2 + 1) * 64 + lane]);
  }
  char* memL = lds;
  char* yl = lds + 8192 + w * 1024;
  float* red = (float*)(lds + 12288);
  const int idx = (lane & 8) ? (int)0xEEEEEEEE : 0x44444444;
  const __amdgpu_buffer_rsrc_t rflag = mkrsrc(ws + OFF_FLAG);
  const __amdgpu_buffer_rsrc_t rmem = mkrsrc(ws + OFF_MEMX);
  const __amdgpu_buffer_rsrc_t rpex = mkrsrc(ws + OFF_PEX);
  int D1 = D1C_INIT, D2 = D2C_INIT; u32 tref = now32(), seen = 0;
  STAMP(1);
  for (int s = 0; s < NSEG; s++) {
    wait_until(tref + D1);
    {
      u32 off[2] = {(u32)s * 8192u + w * 2048u, (u32)s * 8192u + w * 2048u + 1024u};
      u32x4 a[2];
      u32 tiss = 0;
      u32 nsw = poll_chunks<2>(rmem, off, a, lane * 16u, tiss);
      if (!nsw) return;
      if (s > 2) ADAPT(D1, nsw);
      tref = now32();
      *(u32x4*)(memL + w * 2048 + lane * 16) = a[0];
      *(u32x4*)(memL + w * 2048 + 1024 + lane * 16) = a[1];
    }
    u32x4 uf[4][2];
    {
      if (!wait_progress(rflag, (u32)(8 * w + (lane & 7)) * 128u, s + 1, seen)) return;
      const __amdgpu_buffer_rsrc_t ru = mkrsrc(ws + OFF_UF + ((size_t)s * 64 + c) * 16384);
#pragma unroll
      for (int m = 0; m < 4; m++)
#pragma unroll
        for (int z = 0; z < 2; z++) uf[m][z] = ld16_sc1(ru, (u32)((((4 * w + m) * 2 + z) * 4 + gb) * 8 + cc) * 16u);
    }
    __syncthreads();
    STAMP(16 + 4 * s);
    bf16x16 yb;
    {
      f32x4 acc[4];
#pragma unroll
      for (int i = 0; i < 4; i++) acc[i] = (f32x4){0.f, 0.f, 0.f, 0.f};
      bf16x8 af[8];
#pragma unroll
      for (int t = 0; t < 8; t++) af[t] = *(const bf16x8*)(memL + t * 1024 + lane * 16);
      __builtin_amdgcn_sched_barrier(0);
#pragma unroll
      for (int t = 0; t < 8; t++)
#pragma unroll
        for (int i = 0; i < 4; i++) acc[i] = SMF(af[t], wf[i][t], acc[i], idx);
#pragma unroll
      for (int pr = 0; pr < 2; pr++) {
        float v[4];
#pragma unroll
        for (int j = 0; j < 4; j++) {
          auto r = __builtin_amdgcn_permlane32_swap(f2u(acc[2 * pr][j]), f2u(acc[2 * pr + 1][j]), false, false);
          v[j] = u2f(r[0]) + u2f(r[1]);
        }
        const int hl = 2 * (2 * pr + (lane >> 5)) + (n >> 3);
        u32x2 pv = {pk2(v[0], v[1]), pk2(v[2], v[3])};
        *(u32x2*)(yl + (hl * 8 + cc) * 16 + 8 * ((lane >> 4) & 1)) = pv;
      }
      asm volatile("s_waitcnt lgkmcnt(0)" ::: "memory");
      u32x4 hz[2];
#pragma unroll
      for (int z = 0; z < 2; z++) {
        const int ga = 2 * z + (gb >> 1);
        u32x2 a = *(const u32x2*)(yl + ((2 * ga) * 8 + cc) * 16 + 8 * (gb & 1));
        u32x2 b = *(const u32x2*)(yl + ((2 * ga + 1) * 8 + cc) * 16 + 8 * (gb & 1));
        hz[z] = (u32x4){a.x, b.x, a.y, b.y};
      }
      yb = as_bf16(hz[0], hz[1]);
      __builtin_amdgcn_raw_buffer_store_b128(*(const u32x4*)(yl + lane * 16), mkrsrc(ws + OFF_Y), (u32)(s * 64 + c) * 4096u + w * 1024u + lane * 16u, 0, 0);
    }
    STAMP(17 + 4 * s);
    wait_until(tref + D2);
    {
      u32 off[5];
#pragma unroll
      for (int j = 0; j < 4; j++) off[j] = (u32)s * 20480u + (4 * w + j) * 1024u;
      off[4] = (u32)s * 20480u + 16384u + w * 1024u;
      u32x4 g[5];
      u32 tiss = 0;
      u32 nsw = poll_chunks<5>(rpex, off, g, lane * 16u, tiss);
      if (!nsw) return;
      asm volatile("s_waitcnt vmcnt(0)" ::: "memory");
      if (s > 2) ADAPT(D2, nsw);
      f32x4 d0 = {0.f, 0.f, 0.f, 0.f}, d1 = {0.f, 0.f, 0.f, 0.f};
      d0 = SMF(as_bf8(g[0]), as_bf16(uf[0][0], uf[0][1]), d0, idx);
      d1 = SMF(as_bf8(g[1]), as_bf16(uf[1][0], uf[1][1]), d1, idx);
      d0 = SMF(as_bf8(g[2]), as_bf16(uf[2][0], uf[2][1]), d0, idx);
      d1 = SMF(as_bf8(g[3]), as_bf16(uf[3][0], uf[3][1]), d1, idx);
      d0 = SMF(as_bf8(g[4]), yb, d0, idx);
      float* rd = red + (s & 1) * 512;
      if (n < 8) {
#pragma unroll
        for (int j = 0; j < 4; j++) {
          const int rowp = 4 * gb + j;
          rd[((rowp & 7) * 8 + cc) * 8 + 2 * w + (rowp >> 3)] = d0[j] + d1[j];
        }
      }
    }
    __syncthreads();
    STAMP(18 + 4 * s);
    if (w == 0) {
      const float* rd = red + (s & 1) * 512 + lane * 8;
      f32x4 a = *(const f32x4*)rd, b = *(const f32x4*)(rd + 4);
      float v = ((a[0] + a[1]) + (a[2] + a[3])) + ((b[0] + b[1]) + (b[2] + b[3]));
      float o = __builtin_bit_cast(float, __builtin_amdgcn_mov_dpp(__builtin_bit_cast(int, v), 0xB1, 0xF, 0xF, true));
      if (!(lane & 1)) st4_sc1(sane(pk2(v, o)), rmem, (u32)(s + 1) * 8192u + c * 128u + (lane >> 1) * 4u);
      STAMP(19 + 4 * s);
    }
    tref = now32();
  }
}

__device__ __forceinline__ void load_h(u32x4 (&r)[8], const char* ws, int s) {
  const u16* hsrc = (const u16*)(ws + OFF_H) + (size_t)s * 32 * DM;
#pragma unroll
  for (int j = 0; j < 8; j++) r[j] = *(const u32x4*)(hsrc + (j * 256 + threadIdx.x) * 8);
}
__device__ __forceinline__ void put_h(char* dst, const u32x4 (&r)[8]) {
#pragma unroll
  for (int j = 0; j < 8; j++) {
    int u = j * 256 + threadIdx.x;
    *(u32x4*)(dst + (u >> 6) * 1040 + (u & 63) * 16) = r[j];
  }
}

template <bool SC1>
__device__ __forceinline__ void store_T_tile(const f32x16& acc, char* base, int tokrow, int h, int w, int lane) {
  const __amdgpu_buffer_rsrc_t r = mkrsrc(base);
#pragma unroll
  for (int qp = 0; qp < 2; qp++) {
    u32 ax = pk2(acc[8 * qp + 0], acc[8 * qp + 1]), ay = pk2(acc[8 * qp + 2], acc[8 * qp + 3]);
    u32 bx = pk2(acc[8 * qp + 4], acc[8 * qp + 5]), by = pk2(acc[8 * qp + 6], acc[8 * qp + 7]);
    auto r0 = __builtin_amdgcn_permlane32_swap(ax, bx, false, false);
    auto r1 = __builtin_amdgcn_permlane32_swap(ay, by, false, false);
    u32x4 v = {(u32)r0[0], (u32)r1[0], (u32)r0[1], (u32)r1[1]};
    u32 off = (u32)(((tokrow + (lane & 31)) * NH + h) * DH + 32 * w + 16 * qp + 8 * (lane >> 5)) * 2u;
    if (SC1) __builtin_amdgcn_raw_buffer_store_b128(v, r, off, 0, 16);
    else __builtin_amdgcn_raw_buffer_store_b128(v, r, off, 0, 0);
  }
}

__device__ __forceinline__ void role_vu(const u16* __restrict__ Wv, const u16* __restrict__ Wo,
                                        char* ws, char* lds, int h) {
  const int tid = threadIdx.x, lane = tid & 63, w = tid >> 6, x = lane & 31, hh = lane >> 5;
  bf16x8 wv[32], wo[4][8];
  dma_tile<256>(lds, Wv + h * DH, DM, NH * DH);
  dma_wait();
#pragma unroll
  for (int ks = 0; ks < 32; ks++) wv[ks] = frag8(lds, 16 * ks + 8 * hh, 32 * w + (x & 16), 256, lane);
  __syncthreads();
  dma_tile<1024>(lds, Wo + (size_t)h * DH * DM, DH, DM);
  dma_wait();
#pragma unroll
  for (int nt = 0; nt < 4; nt++)
#pragma unroll
    for (int ks = 0; ks < 8; ks++) wo[nt][ks] = frag8(lds, 16 * ks + 8 * hh, 128 * w + 32 * nt + (x & 16), 1024, lane);
  __syncthreads();
  char* vs = lds + 66560;
  const __amdgpu_buffer_rsrc_t rflag = mkrsrc(ws + OFF_FLAG);
  u32x4 hr[8];
  load_h(hr, ws, 0);
  put_h(lds, hr);
  load_h(hr, ws, 1);
  __syncthreads();
  for (int s = 0; s < NSEG; s++) {
    const char* hb = lds + (s & 1) * 33280;
    f32x16 av;
#pragma unroll
    for (int i = 0; i < 16; i++) av[i] = 0.f;
#pragma unroll
    for (int ks = 0; ks < 32; ks++) {
      bf16x8 hf = *(const bf16x8*)(hb + x * 1040 + (16 * ks + 8 * hh) * 2);
      av = MFMA32(wv[ks], hf, av);
    }
#pragma unroll
    for (int q = 0; q < 4; q++) {
      u32x2 pv = {pk2(av[4 * q], av[4 * q + 1]), pk2(av[4 * q + 2], av[4 * q + 3])};
      *(u32x2*)(vs + x * 272 + (32 * w + 8 * q + 4 * hh) * 2) = pv;
    }
    put_h(lds + ((s + 1) & 1) * 33280, hr);
    asm volatile("s_waitcnt vmcnt(0)" ::: "memory");
    __syncthreads();
    if (tid == 0 && s > 1) st4_sc1((u32)s, rflag, h * 128u);
    bf16x8 vf[8];
#pragma unroll
    for (int ks = 0; ks < 8; ks++) vf[ks] = *(const bf16x8*)(vs + x * 272 + (16 * ks + 8 * hh) * 2);
    const __amdgpu_buffer_rsrc_t ru = mkrsrc(ws + OFF_UF + (size_t)s * 64 * 16384);
#pragma unroll
    for (int nt = 0; nt < 4; nt++) {
      f32x16 au;
#pragma unroll
      for (int i = 0; i < 16; i++) au[i] = 0.f;
#pragma unroll
      for (int ks = 0; ks < 8; ks++) au = MFMA32(vf[ks], wo[nt][ks], au);
      const int d = 128 * w + 32 * nt + x, c = d >> 3, cc = d & 7;
#pragma unroll
      for (int xb = 0; xb < 2; xb++)
        st16_sc1(PACK_S(au, 8 * xb), ru, (u32)((((c * 16 + (h >> 1)) * 2 + (h & 1)) * 4 + 2 * hh + xb) * 8 + cc) * 16u);
    }
    if (s == 0 || s == NSEG - 1) asm volatile("s_waitcnt vmcnt(0)" ::: "memory");
    load_h(hr, ws, s + 2 < NSEG ? s + 2 : NSEG - 1);
    __syncthreads();
    if (tid == 0 && (s == 0 || s == NSEG - 1)) st4_sc1((u32)(s + 1), rflag, h * 128u);
    STAMP(16 + 4 * s);
  }
}

__device__ __forceinline__ void role_qk(const u16* __restrict__ Wq, const u16* __restrict__ Wk, char* ws, char* lds, int h) {
  const int tid = threadIdx.x, lane = tid & 63, w = tid >> 6, x = lane & 31, hh = lane >> 5;
  bf16x8 wq[32], wk[32];
  dma_tile<256>(lds, Wk + h * DH, DM, NH * DH);
  dma_wait();
#pragma unroll
  for (int ks = 0; ks < 32; ks++) wk[ks] = frag8(lds, 16 * ks + 8 * hh, 32 * w + (x & 16), 256, lane);
  __syncthreads();
  dma_tile<256>(lds, Wq + h * DH, DM, NH * DH);
  dma_wait();
#pragma unroll
  for (int ks = 0; ks < 32; ks++) wq[ks] = frag8(lds, 16 * ks + 8 * hh, 32 * w + (x & 16), 256, lane);
  __syncthreads();
  const __amdgpu_buffer_rsrc_t rflag = mkrsrc(ws + OFF_FLAG);
  u32x4 hr[8];
  load_h(hr, ws, 0);
  put_h(lds, hr);
  load_h(hr, ws, 1);
  __syncthreads();
  for (int s = 0; s < NSEG; s++) {
    const char* hb = lds + (s & 1) * 33280;
    f32x16 aq, ak;
#pragma unroll
    for (int i = 0; i < 16; i++) { aq[i] = 0.f; ak[i] = 0.f; }
#pragma unroll
    for (int ks = 0; ks < 32; ks++) {
      bf16x8 hf = *(const bf16x8*)(hb + x * 1040 + (16 * ks + 8 * hh) * 2);
      ak = MFMA32(wk[ks], hf, ak);
      aq = MFMA32(wq[ks], hf, aq);
    }
    put_h(lds + ((s + 1) & 1) * 33280, hr);
    asm volatile("s_waitcnt vmcnt(0)" ::: "memory");
    __syncthreads();
    if (tid == 0 && s > 1) st4_sc1((u32)s, rflag, 4096u + h * 128u);
    store_T_tile<true>(ak, ws + OFF_KX, 32 * s, h, w, lane);
    store_T_tile<false>(aq, ws + OFF_QX, 32 * s, h, w, lane);
    if (s == 0 || s == NSEG - 1) {
      asm volatile("s_waitcnt vmcnt(0)" ::: "memory");
      __syncthreads();
      if (tid == 0) st4_sc1((u32)(s + 1), rflag, 4096u + h * 128u);
    }
    load_h(hr, ws, s + 2 < NSEG ? s + 2 : NSEG - 1);
    STAMP(16 + 4 * s);
  }
}

__device__ __forceinline__ float xg_max(float v) {
  auto r = __builtin_amdgcn_permlane16_swap(f2u(v), f2u(v), false, false);
  v = fmaxf(u2f(r[0]), u2f(r[1]));
  auto r2 = __builtin_amdgcn_permlane32_swap(f2u(v), f2u(v), false, false);
  return fmaxf(u2f(r2[0]), u2f(r2[1]));
}
__device__ __forceinline__ float xg_sum(float v) {
  auto r = __builtin_amdgcn_permlane16_swap(f2u(v), f2u(v), false, false);
  v = u2f(r[0]) + u2f(r[1]);
  auto r2 = __builtin_amdgcn_permlane32_swap(f2u(v), f2u(v), false, false);
  return u2f(r2[0]) + u2f(r2[1]);
}
__global__ void __launch_bounds__(256, 1) k2_rec(const u16* __restrict__ Wq, const u16* __restrict__ Wk, const u16* __restrict__ Wv,
                                                  const u16* __restrict__ Wo, const u16* __restrict__ x, char* ws) {
  extern __shared__ __attribute__((aligned(16))) char lds[];
  const int b = blockIdx.x;
  STAMP(0);
  if (b < 32) role_score(Wq, Wk, ws, lds, b);
  else if (b < 96) role_column(ws, lds, b - 32);
  else if (b < 128) role_vu(Wv, Wo, ws, lds, b - 96);
  else role_qk(Wq, Wk, ws, lds, b - 128);
}

__global__ void __launch_bounds__(256) k3a_prob(char* __restrict__ ws) {
  const int b = blockIdx.x, tid = threadIdx.x, lane = tid & 63, w = tid >> 6;
  const int s = b >> 3, hd = (b & 7) * 4 + w, hl = w;
  const int n = lane & 15, g = lane >> 4;
  const u16* Qx = (const u16*)(ws + OFF_QX);
  const u16* Kx = (const u16*)(ws + OFF_KX);
  const u16* Km = (const u16*)(ws + OFF_KMEM);
  const int keyrow = 16 * ((n >> 2) & 1) + 4 * (n >> 3) + (n & 3);
  const int keyb = 16 * (g & 1) + 4 * (g >> 1);
  const float sl = 0.08838834764831845f * 1.4426950408889634f;
  bf16x8 qf[2][4], kf[2][4], mf[4];
#pragma unroll
  for (int ks = 0; ks < 4; ks++) {
#pragma unroll
    for (int t = 0; t < 2; t++) {
      qf[t][ks] = *(const bf16x8*)(Qx + ((size_t)(32 * s + 16 * t + n) * NH + hd) * DH + 32 * ks + 8 * g);
      kf[t][ks] = *(const bf16x8*)(Kx + ((size_t)(32 * s + 8 * t + keyrow) * NH + hd) * DH + 32 * ks + 8 * g);
    }
    mf[ks] = *(const bf16x8*)(Km + ((size_t)s * NH + hd) * 1024 + (n & 7) * 128 + 32 * ks + 8 * g);
  }
  const bool mvalid = (g >> 1) == (hl >> 1);
  u32x4* PXx = (u32x4*)(ws + OFF_PX);
  char* PXm = ws + OFF_PX + (4ull << 20);
#pragma unroll
  for (int qt = 0; qt < 2; qt++) {
    f32x4 sc[3];
#pragma unroll
    for (int i = 0; i < 3; i++) sc[i] = (f32x4){0.f, 0.f, 0.f, 0.f};
#pragma unroll
    for (int ks = 0; ks < 4; ks++) {
      sc[0] = MFMA16(kf[0][ks], qf[qt][ks], sc[0]);
      sc[1] = MFMA16(kf[1][ks], qf[qt][ks], sc[1]);
      sc[2] = MFMA16(mf[ks], qf[qt][ks], sc[2]);
    }
    const int qi = 16 * qt + n;
    float mx = -3.0e38f;
#pragma unroll
    for (int j = 0; j < 4; j++) {
      if (keyb + j > qi) sc[0][j] = -3.0e38f;
      if (keyb + 8 + j > qi) sc[1][j] = -3.0e38f;
      if (!mvalid) sc[2][j] = -3.0e38f;
      mx = fmaxf(mx, fmaxf(fmaxf(sc[0][j], sc[1][j]), sc[2][j]));
    }
    mx = xg_max(mx);
    float sum = 0.f;
#pragma unroll
    for (int i = 0; i < 3; i++)
#pragma unroll
      for (int j = 0; j < 4; j++) {
        float p = sc[i][j] > -1.0e38f ? __builtin_amdgcn_exp2f((sc[i][j] - mx) * sl) : 0.f;
        sc[i][j] = p; sum += p;
      }
    const float inv = __builtin_amdgcn_rcpf(xg_sum(sum));
    u32x4 pa = {pk2(sc[0][0] * inv, sc[0][1] * inv), pk2(sc[1][0] * inv, sc[1][1] * inv),
                pk2(sc[0][2] * inv, sc[0][3] * inv), pk2(sc[1][2] * inv, sc[1][3] * inv)};
    PXx[(((size_t)s * NH + hd) * 2 + qt) * 64 + lane] = pa;
    if (mvalid) {
      u32x2 pm = {pk2(sc[2][0] * inv, sc[2][1] * inv), pk2(sc[2][2] * inv, sc[2][3] * inv)};
      *(u32x2*)(PXm + ((((size_t)s * 8 + (b & 7)) * 2 + qt) * 64 + lane) * 16 + (hl & 1) * 8) = pm;
    }
  }
}

__global__ void __launch_bounds__(512, 2) k3b_oproj(const u16* __restrict__ x, char* __restrict__ ws) {
  extern __shared__ __attribute__((aligned(16))) char lds[];
  const int b = blockIdx.x, tid = threadIdx.x, lane = tid & 63, w = tid >> 6;
  const int s = b >> 2, nq = b & 3;
  const int n = lane & 15, g = lane >> 4, cc = lane & 7;
  const u32x4* UF = (const u32x4*)(ws + OFF_UF) + (size_t)s * 64 * 1024;
  const char* Yg = ws + OFF_Y + (size_t)s * 64 * 4096;
  const u32x4* PXx = (const u32x4*)(ws + OFF_PX) + (size_t)s * NH * 128;
  const u32x4* PXm = (const u32x4*)(ws + OFF_PX + (4ull << 20)) + (size_t)s * 8 * 128;
  f32x4 acc[2][8];
#pragma unroll
  for (int i = 0; i < 2; i++)
#pragma unroll
    for (int j = 0; j < 8; j++) acc[i][j] = (f32x4){0.f, 0.f, 0.f, 0.f};
  u32x4 ub[4][8], pa[4][2], pm[2], yf[8];
#pragma unroll
  for (int hl = 0; hl < 4; hl++) {
    const int hd = 4 * w + hl;
#pragma unroll
    for (int nt = 0; nt < 8; nt++) ub[hl][nt] = UF[(size_t)(16 * nq + 2 * nt + (n >> 3)) * 1024 + (hd * 4 + g) * 8 + cc];
    pa[hl][0] = PXx[(hd * 2) * 64 + lane];
    pa[hl][1] = PXx[(hd * 2 + 1) * 64 + lane];
  }
  pm[0] = PXm[(w * 2) * 64 + lane];
  pm[1] = PXm[(w * 2 + 1) * 64 + lane];
  {
    const int ha = 4 * w + 2 * (g >> 1);
#pragma unroll
    for (int nt = 0; nt < 8; nt++) {
      const int c = 16 * nq + 2 * nt + (n >> 3);
      u32x2 ya = *(const u32x2*)(Yg + (size_t)c * 4096 + (ha * 8 + cc) * 16 + 8 * (g & 1));
      u32x2 yb = *(const u32x2*)(Yg + (size_t)c * 4096 + ((ha + 1) * 8 + cc) * 16 + 8 * (g & 1));
      yf[nt] = (u32x4){ya.x, ya.y, yb.x, yb.y};
    }
  }
  __builtin_amdgcn_sched_barrier(0);
#pragma unroll
  for (int hl = 0; hl < 4; hl++)
#pragma unroll
    for (int nt = 0; nt < 8; nt++) {
      acc[0][nt] = MFMA16(as_bf8(pa[hl][0]), as_bf8(ub[hl][nt]), acc[0][nt]);
      acc[1][nt] = MFMA16(as_bf8(pa[hl][1]), as_bf8(ub[hl][nt]), acc[1][nt]);
    }
#pragma unroll
  for (int nt = 0; nt < 8; nt++) {
    acc[0][nt] = MFMA16(as_bf8(pm[0]), as_bf8(yf[nt]), acc[0][nt]);
    acc[1][nt] = MFMA16(as_bf8(pm[1]), as_bf8(yf[nt]), acc[1][nt]);
  }
  float* red = (float*)lds;
#pragma unroll
  for (int qt = 0; qt < 2; qt++)
#pragma unroll
    for (int nt = 0; nt < 8; nt++)
#pragma unroll
      for (int j = 0; j < 4; j++) red[(w * 32 + 16 * qt + 4 * g + j) * 132 + 16 * nt + n] = acc[qt][nt][j];
  __syncthreads();
  const float* mod = (const float*)(ws + OFF_MOD);
  u16* x1 = (u16*)(ws + OFF_X1);
  {
    const int r = tid >> 4, c8 = (tid & 15) * 8;
    float o[8];
#pragma unroll
    for (int j = 0; j < 8; j++) o[j] = 0.f;
#pragma unroll
    for (int ww = 0; ww < 8; ww++) {
      f32x4 a = *(const f32x4*)(red + (ww * 32 + r) * 132 + c8), bq = *(const f32x4*)(red + (ww * 32 + r) * 132 + c8 + 4);
#pragma unroll
      for (int j = 0; j < 4; j++) { o[j] += a[j]; o[4 + j] += bq[j]; }
    }
    const int col = 128 * nq + c8;
    const size_t go = (size_t)(32 * s + r) * DM + col;
    u32x4 xv = *(const u32x4*)(x + go);
    float xf[8] = {bf_lo(xv.x), bf_hi(xv.x), bf_lo(xv.y), bf_hi(xv.y), bf_lo(xv.z), bf_hi(xv.z), bf_lo(xv.w), bf_hi(xv.w)};
#pragma unroll
    for (int j = 0; j < 8; j++) o[j] = xf[j] + bfr(mod[1024 + col + j] * bfr(o[j]));
    u32x4 rv = {pk2(o[0], o[1]), pk2(o[2], o[3]), pk2(o[4], o[5]), pk2(o[6], o[7])};
    *(u32x4*)(x1 + go) = rv;
  }
}

__device__ __forceinline__ float row16_sum(float v) {
  v += u2f((u32)__builtin_amdgcn_mov_dpp((int)f2u(v), 0x128, 0xF, 0xF, true));
  v += u2f((u32)__builtin_amdgcn_mov_dpp((int)f2u(v), 0x124, 0xF, 0xF, true));
  v += u2f((u32)__builtin_amdgcn_mov_dpp((int)f2u(v), 0x122, 0xF, 0xF, true));
  v += u2f((u32)__builtin_amdgcn_mov_dpp((int)f2u(v), 0x121, 0xF, 0xF, true));
  return v;
}
__global__ void __launch_bounds__(512, 2) k4_mlp1(const u16* __restrict__ b1, char* __restrict__ ws) {
  extern __shared__ __attribute__((aligned(16))) char lds[];
  const int b = blockIdx.x, tid = threadIdx.x, lane = tid & 63, w = tid >> 6;
  const int mt0 = b / 7, nb = b - mt0 * 7;
  const float* mod = (const float*)(ws + OFF_MOD);
  const u16* x1 = (const u16*)(ws + OFF_X1);
  const int n = lane & 15, g = lane >> 4;
  u32x4 xv[2][4];
#pragma unroll
  for (int it = 0; it < 2; it++)
#pragma unroll
    for (int j = 0; j < 4; j++) xv[it][j] = *(const u32x4*)(x1 + (size_t)(mt0 * 64 + it * 32 + w * 4 + g) * DM + (j * 16 + n) * 8);
  const u32x4* WF = (const u32x4*)(ws + OFF_W1F) + (size_t)(nb * 16 + w * 2) * 16 * 64 + lane;
  u32x4 bfm[2][16];
#pragma unroll
  for (int nt = 0; nt < 2; nt++)
#pragma unroll
    for (int ks = 0; ks < 16; ks++) bfm[nt][ks] = WF[(nt * 16 + ks) * 64];
#pragma unroll
  for (int it = 0; it < 2; it++) {
    float f[32];
#pragma unroll
    for (int j = 0; j < 4; j++) {
      f[8 * j + 0] = bf_lo(xv[it][j].x); f[8 * j + 1] = bf_hi(xv[it][j].x); f[8 * j + 2] = bf_lo(xv[it][j].y); f[8 * j + 3] = bf_hi(xv[it][j].y);
      f[8 * j + 4] = bf_lo(xv[it][j].z); f[8 * j + 5] = bf_hi(xv[it][j].z); f[8 * j + 6] = bf_lo(xv[it][j].w); f[8 * j + 7] = bf_hi(xv[it][j].w);
    }
    float sm = 0.f;
#pragma unroll
    for (int i = 0; i < 32; i++) sm += f[i];
    float m = row16_sum(sm) * (1.f / DM), q = 0.f;
#pragma unroll
    for (int i = 0; i < 32; i++) { f[i] -= m; q += f[i] * f[i]; }
    float rs = rsqrtf(row16_sum(q) * (1.f / DM) + 1e-5f);
    sm = 0.f;
#pragma unroll
    for (int j = 0; j < 4; j++) {
      const int c0 = (j * 16 + n) * 8;
      f32x4 c0v = *(const f32x4*)(mod + 2048 + c0), c1v = *(const f32x4*)(mod + 2048 + c0 + 4);
      f32x4 s0v = *(const f32x4*)(mod + 1536 + c0), s1v = *(const f32x4*)(mod + 1536 + c0 + 4);
#pragma unroll
      for (int i = 0; i < 4; i++) {
        f[8 * j + i] = bfr(f[8 * j + i] * rs * (1.f + c0v[i]) + s0v[i]); sm += f[8 * j + i];
        f[8 * j + 4 + i] = bfr(f[8 * j + 4 + i] * rs * (1.f + c1v[i]) + s1v[i]); sm += f[8 * j + 4 + i];
      }
    }
    m = row16_sum(sm) * (1.f / DM); q = 0.f;
#pragma unroll
    for (int i = 0; i < 32; i++) { f[i] -= m; q += f[i] * f[i]; }
    rs = rsqrtf(row16_sum(q) * (1.f / DM) + 1e-5f);
#pragma unroll
    for (int j = 0; j < 4; j++) {
      u32x4 o = {pk2(f[8 * j] * rs, f[8 * j + 1] * rs), pk2(f[8 * j + 2] * rs, f[8 * j + 3] * rs),
                 pk2(f[8 * j + 4] * rs, f[8 * j + 5] * rs), pk2(f[8 * j + 6] * rs, f[8 * j + 7] * rs)};
      *(u32x4*)(lds + (it * 32 + w * 4 + g) * 1040 + (j * 16 + n) * 16) = o;
    }
  }
  __syncthreads();
  f32x4 acc[4][2];
#pragma unroll
  for (int i = 0; i < 4; i++)
#pragma unroll
    for (int j = 0; j < 2; j++) acc[i][j] = (f32x4){0.f, 0.f, 0.f, 0.f};
#pragma unroll
  for (int ks = 0; ks < 16; ks++) {
    bf16x8 af[4];
#pragma unroll
    for (int mt = 0; mt < 4; mt++) af[mt] = *(const bf16x8*)(lds + (16 * mt + n) * 1040 + (32 * ks + 8 * g) * 2);
#pragma unroll
    for (int mt = 0; mt < 4; mt++)
#pragma unroll
      for (int nt = 0; nt < 2; nt++) acc[mt][nt] = MFMA16(as_bf8(bfm[nt][ks]), af[mt], acc[mt][nt]);
  }
  u16* hid = (u16*)(ws + OFF_HID);
#pragma unroll
  for (int nt = 0; nt < 2; nt++) {
    const int f0 = nb * 256 + w * 32 + 16 * nt + 4 * g;
    u32x2 bv = *(const u32x2*)(b1 + f0);
    const float bb[4] = {bf_lo(bv.x), bf_hi(bv.x), bf_lo(bv.y), bf_hi(bv.y)};
#pragma unroll
    for (int mt = 0; mt < 4; mt++) {
      float o[4];
#pragma unroll
      for (int j = 0; j < 4; j++) {
        float u = bfr(bfr(acc[mt][nt][j]) + bb[j]);
        float z = 0.7978845608028654f * (u + 0.044715f * u * u * u);
        float e = __builtin_amdgcn_exp2f(z * 2.8853900817779268f);
        float th = 1.f - 2.f * __builtin_amdgcn_rcpf(e + 1.f);
        o[j] = 0.5f * u * (1.f + th);
      }
      u32x2 ov = {pk2(o[0], o[1]), pk2(o[2], o[3])};
      *(u32x2*)(hid + (size_t)(mt0 * 64 + 16 * mt + n) * FF + f0) = ov;
    }
  }
}

__global__ void __launch_bounds__(512, 2) k5_mlp2(const u16* __restrict__ b2, char* __restrict__ ws, u16* __restrict__ out) {
  extern __shared__ __attribute__((aligned(16))) char lds[];
  const int b = blockIdx.x, tid = threadIdx.x, lane = tid & 63, w = tid >> 6;
  const int mt0 = (b & 7) * 4 + (b >> 6), nb = (b >> 3) & 7;
  const int n = lane & 15, g = lane >> 4;
  const u16* hid = (const u16*)(ws + OFF_HID) + (size_t)(mt0 * 64 + n) * FF + w * 7 * 32 + 8 * g;
  const u32x4* WF = (const u32x4*)(ws + OFF_W2F) + ((size_t)(nb * 4) * 56 + w * 7) * 64 + lane;
  f32x4 acc[4][4];
#pragma unroll
  for (int i = 0; i < 4; i++)
#pragma unroll
    for (int j = 0; j < 4; j++) acc[i][j] = (f32x4){0.f, 0.f, 0.f, 0.f};
#pragma unroll
  for (int hb = 0; hb < 2; hb++) {
    const int nk = hb ? 3 : 4;
    u32x4 bfm[4][4]; bf16x8 af[4][4];
#pragma unroll
    for (int k = 0; k < 4; k++) if (k < nk) {
#pragma unroll
      for (int nt = 0; nt < 4; nt++) bfm[k][nt] = WF[(nt * 56 + hb * 4 + k) * 64];
#pragma unroll
      for (int mt = 0; mt < 4; mt++) af[k][mt] = *(const bf16x8*)(hid + (size_t)(16 * mt) * FF + (hb * 4 + k) * 32);
    }
    __builtin_amdgcn_sched_barrier(0);
#pragma unroll
    for (int k = 0; k < 4; k++) if (k < nk) {
#pragma unroll
      for (int mt = 0; mt < 4; mt++)
#pragma unroll
        for (int nt = 0; nt < 4; nt++) acc[mt][nt] = MFMA16(as_bf8(bfm[k][nt]), af[k][mt], acc[mt][nt]);
    }
  }
  float* red = (float*)lds;
#pragma unroll
  for (int mt = 0; mt < 4; mt++)
#pragma unroll
    for (int nt = 0; nt < 4; nt++) *(f32x4*)(red + (w * 64 + 16 * mt + n) * 68 + 16 * nt + 4 * g) = acc[mt][nt];
  __syncthreads();
  const float* mod = (const float*)(ws + OFF_MOD);
  const u16* x1 = (const u16*)(ws + OFF_X1);
  {
    const int r = tid >> 3, c8 = (tid & 7) * 8;
    float o[8];
#pragma unroll
    for (int j = 0; j < 8; j++) o[j] = 0.f;
#pragma unroll
    for (int ww = 0; ww < 8; ww++) {
      f32x4 a = *(const f32x4*)(red + (ww * 64 + r) * 68 + c8), bq = *(const f32x4*)(red + (ww * 64 + r) * 68 + c8 + 4);
#pragma unroll
      for (int j = 0; j < 4; j++) { o[j] += a[j]; o[4 + j] += bq[j]; }
    }
    const int col = 64 * nb + c8;
    const size_t go = (size_t)(mt0 * 64 + r) * DM + col;
    u32x4 xv = *(const u32x4*)(x1 + go), bv = *(const u32x4*)(b2 + col);
    float xf[8] = {bf_lo(xv.x), bf_hi(xv.x), bf_lo(xv.y), bf_hi(xv.y), bf_lo(xv.z), bf_hi(xv.z), bf_lo(xv.w), bf_hi(xv.w)};
    float bb[8] = {bf_lo(bv.x), bf_hi(bv.x), bf_lo(bv.y), bf_hi(bv.y), bf_lo(bv.z), bf_hi(bv.z), bf_lo(bv.w), bf_hi(bv.w)};
#pragma unroll
    for (int j = 0; j < 8; j++) o[j] = xf[j] + bfr(mod[2560 + col + j] * bfr(bfr(o[j]) + bb[j]));
    u32x4 rv = {pk2(o[0], o[1]), pk2(o[2], o[3]), pk2(o[4], o[5]), pk2(o[6], o[7])};
    *(u32x4*)(out + go) = rv;
  }
}

extern "C" void kernel_launch(void* const* d_in, const int* in_sizes, int n_in, void* d_out, int out_size, void* d_ws,
                              size_t ws_size, hipStream_t stream) {
  const u16* x = (const u16*)d_in[0];
  const u16* mem0 = (const u16*)d_in[1];
  const u16* Wq = (const u16*)d_in[2];
  const u16* Wk = (const u16*)d_in[3];
  const u16* Wv = (const u16*)d_in[4];
  const u16* Wo = (const u16*)d_in[5];
  const u16* w1 = (const u16*)d_in[8];
  const u16* b1 = (const u16*)d_in[9];
  const u16* w2 = (const u16*)d_in[10];
  const u16* b2 = (const u16*)d_in[11];
  const u16* cvec = (const u16*)d_in[12];
  const u16* wmod = (const u16*)d_in[13];
  char* ws = (char*)d_ws;
  hipFuncSetAttribute((const void*)k0_prep, hipFuncAttributeMaxDynamicSharedMemorySize, 131072);
  hipFuncSetAttribute((const void*)k2_rec, hipFuncAttributeMaxDynamicSharedMemorySize, 135168);
  k0_prep<<<268, 256, 131072, stream>>>(Wv, Wo, cvec, wmod, mem0, w1, w2, ws);
  k1_ln<<<512, 256, 0, stream>>>(x, ws);
  k2_rec<<<160, 256, 135168, stream>>>(Wq, Wk, Wv, Wo, x, ws);
  k3a_prob<<<512, 256, 0, stream>>>(ws);
  hipFuncSetAttribute((const void*)k3b_oproj, hipFuncAttributeMaxDynamicSharedMemorySize, 135168);
  k3b_oproj<<<256, 512, 135168, stream>>>(x, ws);
  hipFuncSetAttribute((const void*)k4_mlp1, hipFuncAttributeMaxDynamicSharedMemorySize, 66560);
  hipFuncSetAttribute((const void*)k5_mlp2, hipFuncAttributeMaxDynamicSharedMemorySize, 139264);
  k4_mlp1<<<224, 512, 66560, stream>>>(b1, ws);
  k5_mlp2<<<256, 512, 139264, stream>>>(b2, ws, (u16*)d_out);
}
```

```cpp
#include <hip/hip_runtime.h>
#include <hip/hip_bf16.h>
#include <stdint.h>

typedef unsigned int u32;
typedef unsigned short u16;
typedef unsigned long long u64;
typedef __attribute__((ext_vector_type(2))) __bf16 bf16x2;
typedef __attribute__((ext_vector_type(8))) __bf16 bf16x8;
typedef __attribute__((ext_vector_type(16))) __bf16 bf16x16;
typedef __attribute__((ext_vector_type(2))) float f32x2;
typedef __attribute__((ext_vector_type(4))) float f32x4;
typedef __attribute__((ext_vector_type(16))) float f32x16;
typedef __attribute__((ext_vector_type(2))) u32 u32x2;
typedef __attribute__((ext_vector_type(4))) u32 u32x4;
typedef __attribute__((ext_vector_type(8))) u32 u32x8;

#define NTOK 2048
#define DM 512
#define NH 32
#define DH 128
#define NSEG 64
#define FF 1792
#define SENT 0xFFFFFFFFu

#define OFF_FLAG 0ull
#define OFF_DONE 8192u
#define OFF_KMF 12288u
#define OFF_WVF 14336u
#define OFF_MOD 16384ull
#define OFF_MEMX 32768ull
#define OFF_PEX 0x90000ull
#define OFF_H (2ull << 20)
#define OFF_KMEM (4ull << 20)
#define OFF_WVO (8ull << 20)
#define OFF_KX (24ull << 20)
#define OFF_QX (40ull << 20)
#define OFF_Y (56ull << 20)
#define OFF_UF (72ull << 20)
#define OFF_PX (136ull << 20)
#define OFF_X1 (142ull << 20)
#define OFF_Y2 (146ull << 20)
#define OFF_HID (148ull << 20)
#define OFF_W1F (156ull << 20)
#define OFF_W2F (158ull << 20)
#define OFF_DBG (200ull << 20)
#define STAMP(slot) do { } while (0)

__device__ __forceinline__ u32 pk2(float a, float b) {
  bf16x2 v; v[0] = (__bf16)a; v[1] = (__bf16)b; return __builtin_bit_cast(u32, v);
}
__device__ __forceinline__ u32 f2u(float x) { return __builtin_bit_cast(u32, x); }
__device__ __forceinline__ float u2f(u32 x) { return __builtin_bit_cast(float, x); }
__device__ __forceinline__ float bf_lo(u32 x) { return __builtin_bit_cast(float, x << 16); }
__device__ __forceinline__ float bf_hi(u32 x) { return __builtin_bit_cast(float, x & 0xffff0000u); }
__device__ __forceinline__ float bfr(float a) { return (float)(__bf16)a; }
__device__ __forceinline__ __amdgpu_buffer_rsrc_t mkrsrc(const void* p) {
  return __builtin_amdgcn_make_buffer_rsrc((void*)p, 0, 0x7fffffff, 0x00020000);
}
__device__ __forceinline__ u32x4 ld16_sc1(__amdgpu_buffer_rsrc_t r, u32 off) { return __builtin_amdgcn_raw_buffer_load_b128(r, off, 0, 16); }
__device__ __forceinline__ u32 ld4_sc1(__amdgpu_buffer_rsrc_t r, u32 off) { return __builtin_amdgcn_raw_buffer_load_b32(r, off, 0, 16); }
__device__ __forceinline__ void st16_sc1(u32x4 v, __amdgpu_buffer_rsrc_t r, u32 off) { __builtin_amdgcn_raw_buffer_store_b128(v, r, off, 0, 16); }
__device__ __forceinline__ void st8_sc1(u32x2 v, __amdgpu_buffer_rsrc_t r, u32 off) { __builtin_amdgcn_raw_buffer_store_b64(v, r, off, 0, 16); }
__device__ __forceinline__ void st4_sc1(u32 v, __amdgpu_buffer_rsrc_t r, u32 off) { __builtin_amdgcn_raw_buffer_store_b32(v, r, off, 0, 16); }
__device__ __forceinline__ u32 badm(u32x4 a) { return (u32)(a.x == SENT) | (u32)(a.y == SENT) | (u32)(a.z == SENT) | (u32)(a.w == SENT); }
__device__ __forceinline__ u32 sane(u32 x) { return x < 0xFFFFFFFEu ? x : 0xFFFFFFFEu; }
__device__ __forceinline__ u32 now32() { return (u32)__builtin_amdgcn_s_memtime(); }
__device__ __forceinline__ void wait_until(u32 t) { while ((int)(t - now32()) > 0) { } }
__device__ __forceinline__ bf16x8 as_bf8(u32x4 v) { return __builtin_bit_cast(bf16x8, v); }
__device__ __forceinline__ bf16x16 as_bf16(u32x4 a, u32x4 b) {
  u32x8 v = {a.x, a.y, a.z, a.w, b.x, b.y, b.z, b.w}; return __builtin_bit_cast(bf16x16, v);
}
__device__ __forceinline__ float max3r(float a, float b, float c) { float r; asm("v_max3_f32 %0, %1, %2, %3" : "=v"(r) : "v"(a), "v"(b), "v"(c)); return r; }
__device__ __forceinline__ float max2r(float a, float b) { float r; asm("v_max_f32 %0, %1, %2" : "=v"(r) : "v"(a), "v"(b)); return r; }
#define MFMA16(a, b, c) __builtin_amdgcn_mfma_f32_16x16x32_bf16((a), (b), (c), 0, 0, 0)
#define MFMA32(a, b, c) __builtin_amdgcn_mfma_f32_32x32x16_bf16((a), (b), (c), 0, 0, 0)
#define SMF(a, b, c, idx) __builtin_amdgcn_smfmac_f32_16x16x64_bf16((a), (b), (c), (idx), 0, 0)

template <int N>
__device__ __forceinline__ u32 poll_chunks(__amdgpu_buffer_rsrc_t r, const u32 (&off)[N], u32x4 (&a)[N], u32 lane16, u32& tiss) {
  u32 spins = 0;
  while (true) {
    asm volatile("" : "+v"(lane16) :: "memory");
    if (spins) tiss = now32();
    u32 bad = 0;
#pragma unroll
    for (int j = 0; j < N; j++) a[j] = ld16_sc1(r, off[j] + lane16);
#pragma unroll
    for (int j = 0; j < N; j++) bad |= badm(a[j]);
    ++spins;
    if (!__any(bad)) return spins;
    if (spins > 400000u) return 0;
  }
}

typedef __attribute__((ext_vector_type(4))) short s16x4;
typedef __attribute__((address_space(3))) s16x4 lds_s16x4;
template <int ROWB>
__device__ __forceinline__ void dma_tile(char* lds, const u16* src, int rows, int gstride) {
  constexpr int LPR = ROWB / 16;
  const int total = rows * LPR;
  for (int u = threadIdx.x; u < total; u += 256) {
    const int r = u / LPR, c = u % LPR;
    __builtin_amdgcn_global_load_lds((const u32*)(src + (size_t)r * gstride + c * 8), (u32*)(lds + u * 16), 16, 0, 0);
  }
}
__device__ __forceinline__ void dma_wait() { asm volatile("s_waitcnt vmcnt(0)" ::: "memory"); __syncthreads(); }
__device__ __forceinline__ u32x2 tr4(const char* lds, int row0, int col0, int pitch, int lane) {
  const char* p = lds + (row0 + ((lane & 15) >> 2)) * pitch + (col0 + 4 * (lane & 3)) * 2;
  s16x4 v = __builtin_amdgcn_ds_read_tr16_b64_v4i16((lds_s16x4*)p);
  return __builtin_bit_cast(u32x2, v);
}
__device__ __forceinline__ bf16x8 frag8(const char* lds, int k0, int col0, int pitch, int lane) {
  u32x2 a = tr4(lds, k0, col0, pitch, lane), b = tr4(lds, k0 + 4, col0, pitch, lane);
  return as_bf8((u32x4){a.x, a.y, b.x, b.y});
}
__device__ __forceinline__ u32x4 frag8s(const char* lds, int k0, int col0, int pitch, int lane) {
  u32x2 a = tr4(lds, k0, col0, pitch, lane), b = tr4(lds, k0 + 8, col0, pitch, lane);
  return (u32x4){a.x, b.x, a.y, b.y};
}
#define PACK_S(acc, b) (u32x4){pk2((acc)[(b)], (acc)[(b) + 1]), pk2((acc)[(b) + 4], (acc)[(b) + 5]), pk2((acc)[(b) + 2], (acc)[(b) + 3]), pk2((acc)[(b) + 6], (acc)[(b) + 7])}

__global__ void __launch_bounds__(256, 2) k0_prep(const u16* __restrict__ Wv, const u16* __restrict__ Wo,
                                                   const u16* __restrict__ cvec, const u16* __restrict__ wmod,
                                                   const u16* __restrict__ mem0, char* __restrict__ ws) {
  extern __shared__ __attribute__((aligned(16))) char lds[];
  const int b = blockIdx.x, tid = threadIdx.x, lane = tid & 63, w = tid >> 6;
  if (b < 48) {
    float* red = (float*)lds;
    const int cb = b, cp = tid & 31, ksl = tid >> 5;
    float a0 = 0.f, a1 = 0.f;
#pragma unroll 32
    for (int k = 0; k < 64; k++) {
      const int kk = ksl * 64 + k;
      float cv = bf_lo((u32)cvec[kk]);
      float sv = bfr(cv / (1.f + __expf(-cv)));
      u32 wv = *(const u32*)(wmod + (size_t)kk * 3072 + cb * 64 + cp * 2);
      a0 += sv * bf_lo(wv); a1 += sv * bf_hi(wv);
    }
    red[(ksl * 32 + cp) * 2] = a0; red[(ksl * 32 + cp) * 2 + 1] = a1;
    __syncthreads();
    if (tid < 64) {
      float sacc = 0.f;
#pragma unroll
      for (int k = 0; k < 8; k++) sacc += red[k * 64 + tid];
      ((float*)(ws + OFF_MOD))[cb * 64 + tid] = bfr(sacc);
    }
  } else if (b < 48 + 256) {
    const int bb = b - 48;
    const int h = bb >> 3, nq = (bb >> 1) & 3, half = bb & 1, x = lane & 31, hh = lane >> 5;
    dma_tile<256>(lds, Wo + (size_t)h * DH * DM + nq * 128, DH, DM);
    bf16x8 a[2][8];
#pragma unroll
    for (int mt = 0; mt < 2; mt++)
#pragma unroll
      for (int ks = 0; ks < 8; ks++)
        a[mt][ks] = *(const bf16x8*)(Wv + ((size_t)(256 * half + 64 * w + 32 * mt + x) * NH + h) * DH + 16 * ks + 8 * hh);
    dma_wait();
    u32x4* dst = (u32x4*)(ws + OFF_WVO);
#pragma unroll
    for (int nt = 0; nt < 4; nt++) {
      bf16x8 bw[8];
#pragma unroll
      for (int ks = 0; ks < 8; ks++) bw[ks] = frag8(lds, 16 * ks + 8 * hh, 32 * nt + (x & 16), 256, lane);
#pragma unroll
      for (int mt = 0; mt < 2; mt++) {
        f32x16 acc;
#pragma unroll
        for (int i = 0; i < 16; i++) acc[i] = 0.f;
#pragma unroll
        for (int ks = 0; ks < 8; ks++) acc = MFMA32(a[mt][ks], bw[ks], acc);
        const int d = 128 * nq + 32 * nt + x, c = d >> 3, cc = d & 7;
#pragma unroll
        for (int xb = 0; xb < 2; xb++) {
          const int dp = 256 * half + 64 * w + 32 * mt + 16 * xb, t = dp >> 6, ga = (dp >> 4) & 3, z = ga >> 1, gb = 2 * (ga & 1) + hh;
          const int lp = (h & 1) * 8 + cc + 16 * gb;
          size_t o = ((((size_t)(c * 4 + (h >> 3)) * 4 + ((h >> 1) & 3)) * 8 + t) * 2 + z) * 64 + lp;
          dst[o] = PACK_S(acc, 8 * xb);
        }
      }
    }
  } else {
    const int ib = b - 304;
    u32x4 sv = {SENT, SENT, SENT, SENT};
    u32x4* mx = (u32x4*)(ws + OFF_MEMX + 8192);
    for (int i = ib * 256 + tid; i < 64 * 512; i += 32 * 256) mx[i] = sv;
    u32x4* px = (u32x4*)(ws + OFF_PEX);
    for (int i = ib * 256 + tid; i < 64 * 1280; i += 32 * 256) px[i] = sv;
    if (ib == 0) {
      u32x4 z = {0u, 0u, 0u, 0u};
      for (int i = tid; i < 1024; i += 256) ((u32x4*)(ws + OFF_FLAG))[i] = z;
      for (int i = tid; i < 512; i += 256) {
        int cbk = i >> 3, r = i & 7;
        u32x4 v = *(const u32x4*)(mem0 + r * DM + cbk * 8);
        v.x = sane(v.x); v.y = sane(v.y); v.z = sane(v.z); v.w = sane(v.w);
        ((u32x4*)(ws + OFF_MEMX))[i] = v;
      }
    }
  }
}

__device__ __forceinline__ float row16_sum(float v) {
  v += u2f((u32)__builtin_amdgcn_mov_dpp((int)f2u(v), 0x128, 0xF, 0xF, true));
  v += u2f((u32)__builtin_amdgcn_mov_dpp((int)f2u(v), 0x124, 0xF, 0xF, true));
  v += u2f((u32)__builtin_amdgcn_mov_dpp((int)f2u(v), 0x122, 0xF, 0xF, true));
  v += u2f((u32)__builtin_amdgcn_mov_dpp((int)f2u(v), 0x121, 0xF, 0xF, true));
  return v;
}

__device__ __forceinline__ void px_task(char* ws, int s, int hg);
__device__ __forceinline__ void role_help(const u16* __restrict__ x, const u16* __restrict__ w1, const u16* __restrict__ w2,
                                          char* ws, char* lds, int a0) {
  const int tid = threadIdx.x, lane = tid & 63, w = tid >> 6, n = lane & 15, g = lane >> 4;
  for (int a = a0; a < 64; a += 32) {
    const float* mod = (const float*)(ws + OFF_MOD);
    const __amdgpu_buffer_rsrc_t rh = mkrsrc(ws + OFF_H);
    u32x4 xv[2][4];
#pragma unroll
    for (int it = 0; it < 2; it++)
#pragma unroll
      for (int j = 0; j < 4; j++) xv[it][j] = *(const u32x4*)(x + (size_t)(a * 32 + it * 16 + w * 4 + g) * DM + (j * 16 + n) * 8);
#pragma unroll
    for (int it = 0; it < 2; it++) {
      float f[32];
#pragma unroll
      for (int j = 0; j < 4; j++) {
        f[8 * j + 0] = bf_lo(xv[it][j].x); f[8 * j + 1] = bf_hi(xv[it][j].x); f[8 * j + 2] = bf_lo(xv[it][j].y); f[8 * j + 3] = bf_hi(xv[it][j].y);
        f[8 * j + 4] = bf_lo(xv[it][j].z); f[8 * j + 5] = bf_hi(xv[it][j].z); f[8 * j + 6] = bf_lo(xv[it][j].w); f[8 * j + 7] = bf_hi(xv[it][j].w);
      }
      float sm = 0.f;
#pragma unroll
      for (int i = 0; i < 32; i++) sm += f[i];
      const float m = row16_sum(sm) * (1.f / DM);
      float q = 0.f;
#pragma unroll
      for (int i = 0; i < 32; i++) { f[i] -= m; q += f[i] * f[i]; }
      const float rs = rsqrtf(row16_sum(q) * (1.f / DM) + 1e-5f);
#pragma unroll
      for (int j = 0; j < 4; j++) {
        const int c0 = (j * 16 + n) * 8;
        f32x4 c0v = *(const f32x4*)(mod + 512 + c0), c1v = *(const f32x4*)(mod + 512 + c0 + 4);
        f32x4 s0v = *(const f32x4*)(mod + c0), s1v = *(const f32x4*)(mod + c0 + 4);
        float o[8];
#pragma unroll
        for (int i = 0; i < 4; i++) {
          o[i] = f[8 * j + i] * rs * (1.f + c0v[i]) + s0v[i];
          o[4 + i] = f[8 * j + 4 + i] * rs * (1.f + c1v[i]) + s1v[i];
        }
        u32x4 r = {pk2(o[0], o[1]), pk2(o[2], o[3]), pk2(o[4], o[5]), pk2(o[6], o[7])};
        st16_sc1(r, rh, (u32)((a * 32 + it * 16 + w * 4 + g) * DM + c0) * 2u);
      }
    }
    asm volatile("s_waitcnt vmcnt(0)" ::: "memory");
    __syncthreads();
    if (tid == 0) st4_sc1(1u, mkrsrc(ws + OFF_FLAG), OFF_DONE + a * 64u);
  }
  const int a = a0;
  if (a < 28) {
    dma_tile<128>(lds, w1 + a * 64, DM, FF);
    dma_wait();
    u32x4* dst = (u32x4*)(ws + OFF_W1F);
    for (int ks = 0; ks < 16; ks++) {
      bf16x8 v = frag8(lds, 32 * ks + 8 * (lane >> 4), 16 * w, 128, lane);
      dst[((size_t)(a * 4 + w) * 16 + ks) * 64 + lane] = __builtin_bit_cast(u32x4, v);
    }
  }
  __syncthreads();
  {
    const int cb = a >> 2, kq = a & 3;
    dma_tile<128>(lds, w2 + (size_t)kq * 448 * DM + cb * 64, 448, DM);
    dma_wait();
    u32x4* dst = (u32x4*)(ws + OFF_W2F);
    for (int ks = 0; ks < 14; ks++) {
      u32x2 va = tr4(lds, 32 * ks + 4 * (lane >> 4), 16 * w, 128, lane), vb = tr4(lds, 32 * ks + 16 + 4 * (lane >> 4), 16 * w, 128, lane);
      dst[((size_t)(cb * 4 + w) * 56 + kq * 14 + ks) * 64 + lane] = (u32x4){va.x, va.y, vb.x, vb.y};
    }
  }
  {
    const __amdgpu_buffer_rsrc_t rflag = mkrsrc(ws + OFF_FLAG);
    const int hg = a & 7;
    for (int s = a >> 3; s < NSEG; s += 4) {
      u32 spins = 0, off = OFF_KMF + (u32)(hg * 4 + w) * 64u;
      while (true) {
        asm volatile("" : "+v"(off) :: "memory");
        u32 v = ld4_sc1(rflag, off);
        if (v > (u32)s) break;
        __builtin_amdgcn_s_sleep(16);
        if (++spins > 400000u) break;
      }
      px_task(ws, s, hg);
    }
  }
}

#define AD_DEC 8
#define AD_INC 100
#define D1S_INIT 3000
#define D1C_INIT 600
#define D2C_INIT 2800
#define ADAPT(D, n) do { if ((n) == 1) D -= AD_DEC; else D += AD_INC; if (D < 0) D = 0; } while (0)

__device__ __forceinline__ bool wait_progress(__amdgpu_buffer_rsrc_t rf, u32 off, u32 need, u32& seen) {
  u32 spins = 0;
  if (__all(seen >= need)) return true;
  while (true) {
    asm volatile("" : "+v"(off) :: "memory");
    seen = ld4_sc1(rf, off);
    if (__all(seen >= need)) return true;
    __builtin_amdgcn_s_sleep(2);
    if (++spins > 400000u) return false;
  }
}

__device__ __forceinline__ void role_score(const u16* __restrict__ Wq, const u16* __restrict__ Wk, char* ws,
                                           char* lds, int h) {
  const int tid = threadIdx.x, lane = tid & 63, w = tid >> 6;
  const int n = lane & 15, gb = lane >> 4;
  bf16x16 wf[4][8];
#pragma unroll
  for (int mat = 0; mat < 2; mat++) {
    __syncthreads();
    dma_tile<256>(lds, (mat ? Wk : Wq) + h * DH, DM, NH * DH);
    dma_wait();
#pragma unroll
    for (int tt = 0; tt < 2; tt++)
#pragma unroll
      for (int t = 0; t < 8; t++) {
        const int col = 32 * w + 16 * tt;
        u32x4 lo = frag8s(lds, 64 * t + 16 * (gb >> 1) + 4 * (gb & 1), col, 256, lane);
        u32x4 hi = frag8s(lds, 64 * t + 16 * (2 + (gb >> 1)) + 4 * (gb & 1), col, 256, lane);
        wf[mat * 2 + tt][t] = as_bf16(lo, hi);
      }
  }
  __syncthreads();
  char* memL = lds;
  char* qs = lds + 8192;
  char* ks_ = lds + 8192 + 2304;
  const int idx = (lane & 8) ? (int)0xEEEEEEEE : 0x44444444;
  const __amdgpu_buffer_rsrc_t rflag = mkrsrc(ws + OFF_FLAG);
  const __amdgpu_buffer_rsrc_t rkx = mkrsrc(ws + OFF_KX);
  const __amdgpu_buffer_rsrc_t rmem = mkrsrc(ws + OFF_MEMX);
  const __amdgpu_buffer_rsrc_t rpex = mkrsrc(ws + OFF_PEX);
  const int rho = lane & 15;
  const int keyp = 8 * ((rho >> 2) & 1) + 4 * (rho >> 3) + (rho & 3);
  const u32 prho = (u32)((n & 7) >> 2) * 10240u, pi = n & 3;
  const u32 pxoff = prho + (u32)(((h >> 2) * 64) + 16 * (2 * (h & 1) + (gb >> 1)) + 8 * (gb & 1) + 4 * ((h >> 1) & 1) + pi) * 16u + (n >> 3) * 8u;
  const u32 pmoff = prho + 8192u + (u32)(h * 4 + pi) * 16u + (gb & 1) * 8u + (gb >> 1) * 4u;
  int D1 = w == 0 ? 3400 : (w == 1 ? 3300 : 4050); u32 tref = now32() - 8000u, seen = 0;
  STAMP(1);
  for (int s = 0; s < NSEG; s++) {
    u32x4 kx[2][4];
    if (w == 0) {
      if (!wait_progress(rflag, 4096u + h * 128u, s + 1, seen)) return;
#pragma unroll
      for (int tt = 0; tt < 2; tt++)
#pragma unroll
        for (int k4 = 0; k4 < 4; k4++)
          kx[tt][k4] = ld16_sc1(rkx, (u32)(((32 * s + 16 * tt + keyp) * NH + h) * DH + 32 * k4 + 8 * gb) * 2u);
    }
    f32x4 acc[4];
#pragma unroll
    for (int i = 0; i < 4; i++) { acc[i] = (f32x4){0.f, 0.f, 0.f, 0.f}; asm volatile("" : "+v"(acc[i])); }
    wait_until(tref + D1);
    {
      u32 off[2] = {(u32)s * 8192u + w * 2048u, (u32)s * 8192u + w * 2048u + 1024u};
      u32x4 a[2];
      u32 tiss = 0;
      u32 nsw = poll_chunks<2>(rmem, off, a, lane * 16u, tiss);
      if (!nsw) return;
      if (s > 2) ADAPT(D1, nsw);
      *(u32x4*)(memL + w * 2048 + lane * 16) = a[0];
      *(u32x4*)(memL + w * 2048 + 1024 + lane * 16) = a[1];
    }
    __syncthreads();
    STAMP(16 + 4 * s);
    bf16x8 af[8];
#pragma unroll
    for (int t = 0; t < 8; t++) af[t] = *(const bf16x8*)(memL + t * 1024 + lane * 16);
    __builtin_amdgcn_sched_barrier(0);
    const int ecol = 32 * w + 16 * (lane >> 5) + n, er0 = 4 * ((lane >> 4) & 1);
#pragma unroll
    for (int t = 0; t < 8; t++) { acc[0] = SMF(af[t], wf[0][t], acc[0], idx); acc[1] = SMF(af[t], wf[1][t], acc[1], idx); }
    __builtin_amdgcn_sched_barrier(0);
#pragma unroll
    for (int t = 0; t < 8; t++) {
      acc[2] = SMF(af[t], wf[2][t], acc[2], idx); acc[3] = SMF(af[t], wf[3][t], acc[3], idx);
      if (t >= 2 && t < 6) {
        const int j = t - 2;
        auto r = __builtin_amdgcn_permlane32_swap(f2u(acc[0][j]), f2u(acc[1][j]), false, false);
        *(__bf16*)(qs + (er0 + j) * 272 + ecol * 2) = (__bf16)(u2f(r[0]) + u2f(r[1]));
      }
      __builtin_amdgcn_sched_barrier(0);
    }
#pragma unroll
    for (int j = 0; j < 4; j++) {
      auto r = __builtin_amdgcn_permlane32_swap(f2u(acc[2][j]), f2u(acc[3][j]), false, false);
      *(__bf16*)(ks_ + (er0 + j) * 272 + ecol * 2) = (__bf16)(u2f(r[0]) + u2f(r[1]));
    }
    __syncthreads();
    STAMP(17 + 4 * s);
    if (w == 0) {
      f32x4 sc[3];
#pragma unroll
      for (int i = 0; i < 3; i++) { sc[i] = (f32x4){0.f, 0.f, 0.f, 0.f}; asm volatile("" : "+v"(sc[i])); }
      bf16x8 qf[4], kf[4];
#pragma unroll
      for (int k4 = 0; k4 < 4; k4++) {
        qf[k4] = *(const bf16x8*)(qs + (lane & 7) * 272 + (32 * k4 + 8 * gb) * 2);
        kf[k4] = *(const bf16x8*)(ks_ + (lane & 7) * 272 + (32 * k4 + 8 * gb) * 2);
      }
      __builtin_amdgcn_sched_barrier(0);
#pragma unroll
      for (int k4 = 0; k4 < 4; k4++) {
        sc[0] = MFMA16(as_bf8(kx[0][k4]), qf[k4], sc[0]);
        sc[1] = MFMA16(as_bf8(kx[1][k4]), qf[k4], sc[1]);
        sc[2] = MFMA16(kf[k4], qf[k4], sc[2]);
      }
      const float sl = 0.08838834764831845f * 1.4426950408889634f;
      const bool hiq = n >= 8, hig = gb >= 2;
      float p[6];
#pragma unroll
      for (int j = 0; j < 4; j++) p[j] = hiq ? sc[1][j] : sc[0][j];
      p[4] = hig ? sc[2][2] : sc[2][0];
      p[5] = hig ? sc[2][3] : sc[2][1];
      float mx = max2r(max3r(p[0], p[1], p[2]), max3r(p[3], p[4], p[5]));
      mx = max2r(mx, u2f((u32)__builtin_amdgcn_mov_dpp((int)f2u(mx), 0x128, 0xF, 0xF, true)));
      { auto r = __builtin_amdgcn_permlane16_swap(f2u(mx), f2u(mx), false, false);
        mx = max2r(u2f(r[0]), u2f(r[1])); }
      { auto r = __builtin_amdgcn_permlane32_swap(f2u(mx), f2u(mx), false, false);
        mx = max2r(u2f(r[0]), u2f(r[1])); }
      const float nb = -mx * sl;
#pragma unroll
      for (int j = 0; j < 6; j++) p[j] = __builtin_amdgcn_exp2f(__builtin_fmaf(p[j], sl, nb));
      float sum = (p[0] + p[1]) + (p[2] + p[3]) + (hiq ? 0.f : (p[4] + p[5]));
      sum += u2f((u32)__builtin_amdgcn_mov_dpp((int)f2u(sum), 0x128, 0xF, 0xF, true));
      { auto r = __builtin_amdgcn_permlane16_swap(f2u(sum), f2u(sum), false, false);
        sum = u2f(r[0]) + u2f(r[1]); }
      { auto r = __builtin_amdgcn_permlane32_swap(f2u(sum), f2u(sum), false, false);
        sum = u2f(r[0]) + u2f(r[1]); }
      const float inv = __builtin_amdgcn_rcpf(sum);
      u32x2 px = {sane(pk2(p[0] * inv, p[1] * inv)), sane(pk2(p[2] * inv, p[3] * inv))};
      st8_sc1(px, rpex, (u32)s * 20480u + pxoff);
      if (!hiq) st4_sc1(sane(pk2(p[4] * inv, p[5] * inv)), rpex, (u32)s * 20480u + pmoff);
      STAMP(18 + 4 * s);
    } else if (w == 1) {
      const __amdgpu_buffer_rsrc_t rkm = mkrsrc(ws + OFF_KMEM);
#pragma unroll
      for (int j = 0; j < 2; j++) {
        int u = j * 64 + lane, r = u >> 4, c = u & 15;
        st16_sc1(*(const u32x4*)(ks_ + r * 272 + c * 16), rkm, (u32)((s * NH + h) * 1024 + r * 128 + c * 8) * 2u);
      }
      asm volatile("s_waitcnt vmcnt(0)" ::: "memory");
      if (lane == 0) st4_sc1((u32)(s + 1), rflag, OFF_KMF + h * 64u);
    }
    tref = now32();
  }
}

__device__ __forceinline__ void role_column(char* ws, char* lds, int c, int rho) {
  const int tid = threadIdx.x, lane = tid & 63, w = tid >> 6;
  const int n = lane & 15, gb = lane >> 4, cc = lane & 7;
  bf16x16 wf[4][8];
  {
    const u32x4* src = (const u32x4*)(ws + OFF_WVO) + (size_t)(c * 4 + w) * 4096;
#pragma unroll
    for (int tl = 0; tl < 4; tl++)
#pragma unroll
      for (int t = 0; t < 8; t++) wf[tl][t] = as_bf16(src[((tl * 8 + t) * 2) * 64 + lane], src[((tl * 8 + t) * 2 + 1) * 64 + lane]);
  }
  char* memL = lds;
  char* yl = lds + 8192 + w * 1024;
  float* red = (float*)(lds + 12288);
  const int idx = (lane & 8) ? (int)0xEEEEEEEE : 0x44444444;
  const __amdgpu_buffer_rsrc_t rflag = mkrsrc(ws + OFF_FLAG);
  const __amdgpu_buffer_rsrc_t rmem = mkrsrc(ws + OFF_MEMX);
  const __amdgpu_buffer_rsrc_t rpex = mkrsrc(ws + OFF_PEX);
  int D1 = w == 0 ? 750 : (w == 1 ? 850 : 1130), D2 = 2800; u32 tref = now32(), seen = 0;
  STAMP(1);
  for (int s = 0; s < NSEG; s++) {
    wait_until(tref + D1);
    {
      u32 off[2] = {(u32)s * 8192u + w * 2048u, (u32)s * 8192u + w * 2048u + 1024u};
      u32x4 a[2];
      u32 tiss = 0;
      u32 nsw = poll_chunks<2>(rmem, off, a, lane * 16u, tiss);
      if (!nsw) return;
      if (s > 2) ADAPT(D1, nsw);
      tref = now32();
      *(u32x4*)(memL + w * 2048 + lane * 16) = a[0];
      *(u32x4*)(memL + w * 2048 + 1024 + lane * 16) = a[1];
    }
    u32x4 uf[4][2];
    {
      if (!wait_progress(rflag, (u32)(8 * w + (lane & 7)) * 128u, s + 1, seen)) return;
      const __amdgpu_buffer_rsrc_t ru = mkrsrc(ws + OFF_UF + ((size_t)s * 64 + c) * 16384);
#pragma unroll
      for (int m = 0; m < 4; m++)
#pragma unroll
        for (int z = 0; z < 2; z++) uf[m][z] = ld16_sc1(ru, (u32)((((4 * w + m) * 2 + z) * 4 + gb) * 8 + cc) * 16u);
    }
    __syncthreads();
    STAMP(16 + 4 * s);
    bf16x16 yb;
    {
      f32x4 acc[4];
#pragma unroll
      for (int i = 0; i < 4; i++) acc[i] = (f32x4){0.f, 0.f, 0.f, 0.f};
      bf16x8 af[8];
#pragma unroll
      for (int t = 0; t < 8; t++) af[t] = *(const bf16x8*)(memL + t * 1024 + lane * 16);
      __builtin_amdgcn_sched_barrier(0);
#pragma unroll
      for (int t = 0; t < 8; t++)
#pragma unroll
        for (int i = 0; i < 4; i++) acc[i] = SMF(af[t], wf[i][t], acc[i], idx);
#pragma unroll
      for (int pr = 0; pr < 2; pr++) {
        float v[4];
#pragma unroll
        for (int j = 0; j < 4; j++) {
          auto r = __builtin_amdgcn_permlane32_swap(f2u(acc[2 * pr][j]), f2u(acc[2 * pr + 1][j]), false, false);
          v[j] = u2f(r[0]) + u2f(r[1]);
        }
        const int hl = 2 * (2 * pr + (lane >> 5)) + (n >> 3);
        u32x2 pv = {pk2(v[0], v[1]), pk2(v[2], v[3])};
        *(u32x2*)(yl + (hl * 8 + cc) * 16 + 8 * ((lane >> 4) & 1)) = pv;
      }
      asm volatile("s_waitcnt lgkmcnt(0)" ::: "memory");
      u32x4 hz[2];
#pragma unroll
      for (int z = 0; z < 2; z++) {
        const int ga = 2 * z + (gb >> 1);
        u32x2 a = *(const u32x2*)(yl + ((2 * ga) * 8 + cc) * 16 + 8 * (gb & 1));
        u32x2 b = *(const u32x2*)(yl + ((2 * ga + 1) * 8 + cc) * 16 + 8 * (gb & 1));
        hz[z] = (u32x4){a.x, b.x, a.y, b.y};
      }
      yb = as_bf16(hz[0], hz[1]);
      if (rho == 0) __builtin_amdgcn_raw_buffer_store_b128(*(const u32x4*)(yl + lane * 16), mkrsrc(ws + OFF_Y), (u32)(s * 64 + c) * 4096u + w * 1024u + lane * 16u, 0, 0);
    }
    STAMP(17 + 4 * s);
    wait_until(tref + D2);
    {
      const u32 pb = (u32)s * 20480u + rho * 10240u;
      u32 off[3] = {pb + (2 * w) * 1024u + lane * 16u, pb + (2 * w + 1) * 1024u + lane * 16u,
                    pb + 8192u + (u32)((8 * w + 2 * gb + ((lane >> 3) & 1)) * 4 + (lane & 3)) * 16u};
      u32x4 g[3];
      u32 tiss = 0;
      u32 nsw = poll_chunks<3>(rpex, off, g, 0u, tiss);
      if (!nsw) return;
      if (s > 2) ADAPT(D2, nsw);
      f32x4 dA = {0.f, 0.f, 0.f, 0.f}, dB = {0.f, 0.f, 0.f, 0.f};
      dA = SMF(as_bf8(g[0]), as_bf16(uf[0][0], uf[0][1]), dA, idx);
      dB = SMF(as_bf8(g[0]), as_bf16(uf[1][0], uf[1][1]), dB, idx);
      dA = SMF(as_bf8(g[1]), as_bf16(uf[2][0], uf[2][1]), dA, idx);
      dB = SMF(as_bf8(g[1]), as_bf16(uf[3][0], uf[3][1]), dB, idx);
      dA = SMF(as_bf8(g[2]), yb, dA, idx);
      float* rd = red + (s & 1) * 512;
      if (n < 8) {
#pragma unroll
        for (int j = 0; j < 4; j++) rd[(j * 8 + cc) * 16 + 4 * w + gb] = (gb & 1) ? dB[j] : dA[j];
      }
    }
    __syncthreads();
    STAMP(18 + 4 * s);
    if (w == 0) {
      const float* rd = red + (s & 1) * 512 + lane * 8;
      f32x4 a = *(const f32x4*)rd, b = *(const f32x4*)(rd + 4);
      float v = ((a[0] + a[1]) + (a[2] + a[3])) + ((b[0] + b[1]) + (b[2] + b[3]));
      v += u2f((u32)__builtin_amdgcn_mov_dpp((int)f2u(v), 0xB1, 0xF, 0xF, true));
      float o = u2f((u32)__builtin_amdgcn_mov_dpp((int)f2u(v), 0x4E, 0xF, 0xF, true));
      if (!(lane & 3)) st4_sc1(sane(pk2(v, o)), rmem, (u32)(s + 1) * 8192u + c * 128u + rho * 64u + (lane >> 2) * 4u);
      STAMP(19 + 4 * s);
    }
    tref = now32();
  }
}

__device__ __forceinline__ void load_h(u32x4 (&r)[8], const char* ws, int s) {
  const __amdgpu_buffer_rsrc_t rh = mkrsrc(ws + OFF_H);
#pragma unroll
  for (int j = 0; j < 8; j++) r[j] = ld16_sc1(rh, (u32)s * 32768u + (j * 256 + threadIdx.x) * 16u);
}
__device__ __forceinline__ void put_h(char* dst, const u32x4 (&r)[8]) {
#pragma unroll
  for (int j = 0; j < 8; j++) {
    int u = j * 256 + threadIdx.x;
    *(u32x4*)(dst + (u >> 6) * 1040 + (u & 63) * 16) = r[j];
  }
}

template <bool SC1>
__device__ __forceinline__ void store_T_tile(const f32x16& acc, char* base, int tokrow, int h, int w, int lane) {
  const __amdgpu_buffer_rsrc_t r = mkrsrc(base);
#pragma unroll
  for (int qp = 0; qp < 2; qp++) {
    u32 ax = pk2(acc[8 * qp + 0], acc[8 * qp + 1]), ay = pk2(acc[8 * qp + 2], acc[8 * qp + 3]);
    u32 bx = pk2(acc[8 * qp + 4], acc[8 * qp + 5]), by = pk2(acc[8 * qp + 6], acc[8 * qp + 7]);
    auto r0 = __builtin_amdgcn_permlane32_swap(ax, bx, false, false);
    auto r1 = __builtin_amdgcn_permlane32_swap(ay, by, false, false);
    u32x4 v = {(u32)r0[0], (u32)r1[0], (u32)r0[1], (u32)r1[1]};
    u32 off = (u32)(((tokrow + (lane & 31)) * NH + h) * DH + 32 * w + 16 * qp + 8 * (lane >> 5)) * 2u;
    if (SC1) __builtin_amdgcn_raw_buffer_store_b128(v, r, off, 0, 16);
    else __builtin_amdgcn_raw_buffer_store_b128(v, r, off, 0, 0);
  }
}

__device__ __forceinline__ void role_vu(const u16* __restrict__ Wv, const u16* __restrict__ Wo,
                                        char* ws, char* lds, int h) {
  const int tid = threadIdx.x, lane = tid & 63, w = tid >> 6, x = lane & 31, hh = lane >> 5;
  bf16x8 wv[32], wo[4][8];
  dma_tile<256>(lds, Wv + h * DH, DM, NH * DH);
  dma_wait();
#pragma unroll
  for (int ks = 0; ks < 32; ks++) wv[ks] = frag8(lds, 16 * ks + 8 * hh, 32 * w + (x & 16), 256, lane);
  __syncthreads();
  dma_tile<1024>(lds, Wo + (size_t)h * DH * DM, DH, DM);
  dma_wait();
#pragma unroll
  for (int nt = 0; nt < 4; nt++)
#pragma unroll
    for (int ks = 0; ks < 8; ks++) wo[nt][ks] = frag8(lds, 16 * ks + 8 * hh, 128 * w + 32 * nt + (x & 16), 1024, lane);
  __syncthreads();
  char* vs = lds + 66560;
  const __amdgpu_buffer_rsrc_t rflag = mkrsrc(ws + OFF_FLAG);
  u32x4 hr[8];
  { u32 seenh = 0; if (!wait_progress(rflag, OFF_DONE + (lane & 31) * 64u, 1u, seenh)) return; }
  load_h(hr, ws, 0);
  put_h(lds, hr);
  load_h(hr, ws, 1);
  __syncthreads();
  for (int s = 0; s < NSEG; s++) {
    const char* hb = lds + (s & 1) * 33280;
    f32x16 av;
#pragma unroll
    for (int i = 0; i < 16; i++) av[i] = 0.f;
    {
      bf16x8 hf[2][8];
#pragma unroll
      for (int k = 0; k < 8; k++) hf[0][k] = *(const bf16x8*)(hb + x * 1040 + (16 * k + 8 * hh) * 2);
#pragma unroll
      for (int ch = 0; ch < 4; ch++) {
        if (ch < 3) {
#pragma unroll
          for (int k = 0; k < 8; k++) hf[(ch + 1) & 1][k] = *(const bf16x8*)(hb + x * 1040 + (16 * (8 * ch + 8 + k) + 8 * hh) * 2);
        }
        __builtin_amdgcn_sched_barrier(0);
#pragma unroll
        for (int k = 0; k < 8; k++) av = MFMA32(wv[8 * ch + k], hf[ch & 1][k], av);
        __builtin_amdgcn_sched_barrier(0);
      }
    }
#pragma unroll
    for (int q = 0; q < 4; q++) {
      u32x2 pv = {pk2(av[4 * q], av[4 * q + 1]), pk2(av[4 * q + 2], av[4 * q + 3])};
      *(u32x2*)(vs + x * 272 + (32 * w + 8 * q + 4 * hh) * 2) = pv;
    }
    put_h(lds + ((s + 1) & 1) * 33280, hr);
    asm volatile("s_waitcnt vmcnt(0)" ::: "memory");
    __syncthreads();
    if (tid == 0 && s > 4) st4_sc1((u32)s, rflag, h * 128u);
    bf16x8 vf[8];
#pragma unroll
    for (int ks = 0; ks < 8; ks++) vf[ks] = *(const bf16x8*)(vs + x * 272 + (16 * ks + 8 * hh) * 2);
    const __amdgpu_buffer_rsrc_t ru = mkrsrc(ws + OFF_UF + (size_t)s * 64 * 16384);
#pragma unroll
    for (int nt = 0; nt < 4; nt++) {
      f32x16 au;
#pragma unroll
      for (int i = 0; i < 16; i++) au[i] = 0.f;
#pragma unroll
      for (int ks = 0; ks < 8; ks++) au = MFMA32(vf[ks], wo[nt][ks], au);
      const int d = 128 * w + 32 * nt + x, c = d >> 3, cc = d & 7;
#pragma unroll
      for (int xb = 0; xb < 2; xb++)
        st16_sc1(PACK_S(au, 8 * xb), ru, (u32)((((c * 16 + (h >> 1)) * 2 + (h & 1)) * 4 + 2 * hh + xb) * 8 + cc) * 16u);
    }
    if (s < 4 || s == NSEG - 1) asm volatile("s_waitcnt vmcnt(0)" ::: "memory");
    if (s == 30) { u32 seenh = 0; if (!wait_progress(rflag, OFF_DONE + lane * 64u, 1u, seenh)) return; }
    load_h(hr, ws, s + 2 < NSEG ? s + 2 : NSEG - 1);
    __syncthreads();
    if (tid == 0 && (s < 4 || s == NSEG - 1)) st4_sc1((u32)(s + 1), rflag, h * 128u);
    STAMP(16 + 4 * s);
  }
}

__device__ __forceinline__ void role_qk(const u16* __restrict__ Wq, const u16* __restrict__ Wk, char* ws, char* lds, int h) {
  const int tid = threadIdx.x, lane = tid & 63, w = tid >> 6, x = lane & 31, hh = lane >> 5;
  bf16x8 wq[32], wk[32];
  dma_tile<256>(lds, Wk + h * DH, DM, NH * DH);
  dma_wait();
#pragma unroll
  for (int ks = 0; ks < 32; ks++) wk[ks] = frag8(lds, 16 * ks + 8 * hh, 32 * w + (x & 16), 256, lane);
  __syncthreads();
  dma_tile<256>(lds, Wq + h * DH, DM, NH * DH);
  dma_wait();
#pragma unroll
  for (int ks = 0; ks < 32; ks++) wq[ks] = frag8(lds, 16 * ks + 8 * hh, 32 * w + (x & 16), 256, lane);
  __syncthreads();
  const __amdgpu_buffer_rsrc_t rflag = mkrsrc(ws + OFF_FLAG);
  u32x4 hr[8];
  { u32 seenh = 0; if (!wait_progress(rflag, OFF_DONE + (lane & 31) * 64u, 1u, seenh)) return; }
  load_h(hr, ws, 0);
  put_h(lds, hr);
  load_h(hr, ws, 1);
  __syncthreads();
  for (int s = 0; s < NSEG; s++) {
    const char* hb = lds + (s & 1) * 33280;
    f32x16 aq, ak;
#pragma unroll
    for (int i = 0; i < 16; i++) { aq[i] = 0.f; ak[i] = 0.f; }
    {
      bf16x8 hf[2][8];
#pragma unroll
      for (int k = 0; k < 8; k++) hf[0][k] = *(const bf16x8*)(hb + x * 1040 + (16 * k + 8 * hh) * 2);
#pragma unroll
      for (int ch = 0; ch < 4; ch++) {
        if (ch < 3) {
#pragma unroll
          for (int k = 0; k < 8; k++) hf[(ch + 1) & 1][k] = *(const bf16x8*)(hb + x * 1040 + (16 * (8 * ch + 8 + k) + 8 * hh) * 2);
        }
        __builtin_amdgcn_sched_barrier(0);
#pragma unroll
        for (int k = 0; k < 8; k++) { ak = MFMA32(wk[8 * ch + k], hf[ch & 1][k], ak); aq = MFMA32(wq[8 * ch + k], hf[ch & 1][k], aq); }
        __builtin_amdgcn_sched_barrier(0);
      }
    }
    put_h(lds + ((s + 1) & 1) * 33280, hr);
    asm volatile("s_waitcnt vmcnt(0)" ::: "memory");
    __syncthreads();
    if (tid == 0 && s > 4) st4_sc1((u32)s, rflag, 4096u + h * 128u);
    store_T_tile<true>(ak, ws + OFF_KX, 32 * s, h, w, lane);
    store_T_tile<true>(aq, ws + OFF_QX, 32 * s, h, w, lane);
    if (s < 4 || s == NSEG - 1) {
      asm volatile("s_waitcnt vmcnt(0)" ::: "memory");
      __syncthreads();
      if (tid == 0) st4_sc1((u32)(s + 1), rflag, 4096u + h * 128u);
    }
    if (s == 30) { u32 seenh = 0; if (!wait_progress(rflag, OFF_DONE + lane * 64u, 1u, seenh)) return; }
    load_h(hr, ws, s + 2 < NSEG ? s + 2 : NSEG - 1);
    STAMP(16 + 4 * s);
  }
}

__device__ __forceinline__ float xg_max(float v) {
  auto r = __builtin_amdgcn_permlane16_swap(f2u(v), f2u(v), false, false);
  v = fmaxf(u2f(r[0]), u2f(r[1]));
  auto r2 = __builtin_amdgcn_permlane32_swap(f2u(v), f2u(v), false, false);
  return fmaxf(u2f(r2[0]), u2f(r2[1]));
}
__device__ __forceinline__ float xg_sum(float v) {
  auto r = __builtin_amdgcn_permlane16_swap(f2u(v), f2u(v), false, false);
  v = u2f(r[0]) + u2f(r[1]);
  auto r2 = __builtin_amdgcn_permlane32_swap(f2u(v), f2u(v), false, false);
  return u2f(r2[0]) + u2f(r2[1]);
}
__device__ __forceinline__ void px_task(char* ws, int s, int hg) {
  const int tid = threadIdx.x, lane = tid & 63, w = tid >> 6;
  const int hd = hg * 4 + w, hl = w;
  const int n = lane & 15, g = lane >> 4;
  const __amdgpu_buffer_rsrc_t rq = mkrsrc(ws + OFF_QX + (size_t)s * 32 * 8192);
  const __amdgpu_buffer_rsrc_t rk = mkrsrc(ws + OFF_KX + (size_t)s * 32 * 8192);
  const __amdgpu_buffer_rsrc_t rkm = mkrsrc(ws + OFF_KMEM + (size_t)s * 65536);
  const int keyrow = 16 * ((n >> 2) & 1) + 4 * (n >> 3) + (n & 3);
  const int keyb = 16 * (g & 1) + 4 * (g >> 1);
  const float sl = 0.08838834764831845f * 1.4426950408889634f;
  bf16x8 qf[2][4], kf[2][4], mf[4];
#pragma unroll
  for (int ks = 0; ks < 4; ks++) {
#pragma unroll
    for (int t = 0; t < 2; t++) {
      qf[t][ks] = as_bf8(ld16_sc1(rq, (u32)(((16 * t + n) * NH + hd) * DH + 32 * ks + 8 * g) * 2u));
      kf[t][ks] = as_bf8(ld16_sc1(rk, (u32)(((8 * t + keyrow) * NH + hd) * DH + 32 * ks + 8 * g) * 2u));
    }
    mf[ks] = as_bf8(ld16_sc1(rkm, (u32)(hd * 1024 + (n & 7) * 128 + 32 * ks + 8 * g) * 2u));
  }
  const bool mvalid = (g >> 1) == (hl >> 1);
  u32x4* PXx = (u32x4*)(ws + OFF_PX);
  char* PXm = ws + OFF_PX + (4ull << 20);
#pragma unroll
  for (int qt = 0; qt < 2; qt++) {
    f32x4 sc[3];
#pragma unroll
    for (int i = 0; i < 3; i++) sc[i] = (f32x4){0.f, 0.f, 0.f, 0.f};
#pragma unroll
    for (int ks = 0; ks < 4; ks++) {
      sc[0] = MFMA16(kf[0][ks], qf[qt][ks], sc[0]);
      sc[1] = MFMA16(kf[1][ks], qf[qt][ks], sc[1]);
      sc[2] = MFMA16(mf[ks], qf[qt][ks], sc[2]);
    }
    const int qi = 16 * qt + n;
    float mx = -3.0e38f;
#pragma unroll
    for (int j = 0; j < 4; j++) {
      if (keyb + j > qi) sc[0][j] = -3.0e38f;
      if (keyb + 8 + j > qi) sc[1][j] = -3.0e38f;
      if (!mvalid) sc[2][j] = -3.0e38f;
      mx = fmaxf(mx, fmaxf(fmaxf(sc[0][j], sc[1][j]), sc[2][j]));
    }
    mx = xg_max(mx);
    float sum = 0.f;
#pragma unroll
    for (int i = 0; i < 3; i++)
#pragma unroll
      for (int j = 0; j < 4; j++) {
        float p = sc[i][j] > -1.0e38f ? __builtin_amdgcn_exp2f((sc[i][j] - mx) * sl) : 0.f;
        sc[i][j] = p; sum += p;
      }
    const float inv = __builtin_amdgcn_rcpf(xg_sum(sum));
    u32x4 pa = {pk2(sc[0][0] * inv, sc[0][1] * inv), pk2(sc[1][0] * inv, sc[1][1] * inv),
                pk2(sc[0][2] * inv, sc[0][3] * inv), pk2(sc[1][2] * inv, sc[1][3] * inv)};
    PXx[(((size_t)s * NH + hd) * 2 + qt) * 64 + lane] = pa;
    if (mvalid) {
      u32x2 pm = {pk2(sc[2][0] * inv, sc[2][1] * inv), pk2(sc[2][2] * inv, sc[2][3] * inv)};
      *(u32x2*)(PXm + ((((size_t)s * 8 + hg) * 2 + qt) * 64 + lane) * 16 + (hl & 1) * 8) = pm;
    }
  }
}

__global__ void __launch_bounds__(256, 1) k2_rec(const u16* __restrict__ Wq, const u16* __restrict__ Wk, const u16* __restrict__ Wv,
                                                  const u16* __restrict__ Wo, const u16* __restrict__ x, const u16* __restrict__ w1,
                                                  const u16* __restrict__ w2, char* ws) {
  extern __shared__ __attribute__((aligned(16))) char lds[];
  const int b = blockIdx.x;
  STAMP(0);
  if (b < 32) role_help(x, w1, w2, ws, lds, b);
  else if (b < 64) role_score(Wq, Wk, ws, lds, b - 32);
  else if (b < 192) { const int q = b - 64; role_column(ws, lds, (q & 7) | ((q >> 4) << 3), (q >> 3) & 1); }
  else if (b < 224) role_vu(Wv, Wo, ws, lds, b - 192);
  else role_qk(Wq, Wk, ws, lds, b - 224);
}

__global__ void __launch_bounds__(512, 2) k3b_oproj(const u16* __restrict__ x, char* __restrict__ ws) {
  extern __shared__ __attribute__((aligned(16))) char lds[];
  const int b = blockIdx.x, tid = threadIdx.x, lane = tid & 63, w = tid >> 6;
  const int s = b >> 2, nq = b & 3;
  const int n = lane & 15, g = lane >> 4, cc = lane & 7;
  const u32x4* UF = (const u32x4*)(ws + OFF_UF) + (size_t)s * 64 * 1024;
  const char* Yg = ws + OFF_Y + (size_t)s * 64 * 4096;
  const u32x4* PXx = (const u32x4*)(ws + OFF_PX) + (size_t)s * NH * 128;
  const u32x4* PXm = (const u32x4*)(ws + OFF_PX + (4ull << 20)) + (size_t)s * 8 * 128;
  f32x4 acc[2][8];
#pragma unroll
  for (int i = 0; i < 2; i++)
#pragma unroll
    for (int j = 0; j < 8; j++) acc[i][j] = (f32x4){0.f, 0.f, 0.f, 0.f};
  u32x4 ub[4][8], pa[4][2], pm[2], yf[8];
#pragma unroll
  for (int hl = 0; hl < 4; hl++) {
    const int hd = 4 * w + hl;
#pragma unroll
    for (int nt = 0; nt < 8; nt++) ub[hl][nt] = UF[(size_t)(16 * nq + 2 * nt + (n >> 3)) * 1024 + (hd * 4 + g) * 8 + cc];
    pa[hl][0] = PXx[(hd * 2) * 64 + lane];
    pa[hl][1] = PXx[(hd * 2 + 1) * 64 + lane];
  }
  pm[0] = PXm[(w * 2) * 64 + lane];
  pm[1] = PXm[(w * 2 + 1) * 64 + lane];
  {
    const int ha = 4 * w + 2 * (g >> 1);
#pragma unroll
    for (int nt = 0; nt < 8; nt++) {
      const int c = 16 * nq + 2 * nt + (n >> 3);
      u32x2 ya = *(const u32x2*)(Yg + (size_t)c * 4096 + (ha * 8 + cc) * 16 + 8 * (g & 1));
      u32x2 yb = *(const u32x2*)(Yg + (size_t)c * 4096 + ((ha + 1) * 8 + cc) * 16 + 8 * (g & 1));
      yf[nt] = (u32x4){ya.x, ya.y, yb.x, yb.y};
    }
  }
  __builtin_amdgcn_sched_barrier(0);
#pragma unroll
  for (int hl = 0; hl < 4; hl++)
#pragma unroll
    for (int nt = 0; nt < 8; nt++) {
      acc[0][nt] = MFMA16(as_bf8(pa[hl][0]), as_bf8(ub[hl][nt]), acc[0][nt]);
      acc[1][nt] = MFMA16(as_bf8(pa[hl][1]), as_bf8(ub[hl][nt]), acc[1][nt]);
    }
#pragma unroll
  for (int nt = 0; nt < 8; nt++) {
    acc[0][nt] = MFMA16(as_bf8(pm[0]), as_bf8(yf[nt]), acc[0][nt]);
    acc[1][nt] = MFMA16(as_bf8(pm[1]), as_bf8(yf[nt]), acc[1][nt]);
  }
  float* red = (float*)lds;
#pragma unroll
  for (int qt = 0; qt < 2; qt++)
#pragma unroll
    for (int nt = 0; nt < 8; nt++)
#pragma unroll
      for (int j = 0; j < 4; j++) red[(w * 32 + 16 * qt + 4 * g + j) * 132 + 16 * nt + n] = acc[qt][nt][j];
  __syncthreads();
  const float* mod = (const float*)(ws + OFF_MOD);
  u16* x1 = (u16*)(ws + OFF_X1);
  {
    const int r = tid >> 4, c8 = (tid & 15) * 8;
    float o[8];
#pragma unroll
    for (int j = 0; j < 8; j++) o[j] = 0.f;
#pragma unroll
    for (int ww = 0; ww < 8; ww++) {
      f32x4 a = *(const f32x4*)(red + (ww * 32 + r) * 132 + c8), bq = *(const f32x4*)(red + (ww * 32 + r) * 132 + c8 + 4);
#pragma unroll
      for (int j = 0; j < 4; j++) { o[j] += a[j]; o[4 + j] += bq[j]; }
    }
    const int col = 128 * nq + c8;
    const size_t go = (size_t)(32 * s + r) * DM + col;
    u32x4 xv = *(const u32x4*)(x + go);
    float xf[8] = {bf_lo(xv.x), bf_hi(xv.x), bf_lo(xv.y), bf_hi(xv.y), bf_lo(xv.z), bf_hi(xv.z), bf_lo(xv.w), bf_hi(xv.w)};
#pragma unroll
    for (int j = 0; j < 8; j++) o[j] = xf[j] + bfr(mod[1024 + col + j] * bfr(o[j]));
    u32x4 rv = {pk2(o[0], o[1]), pk2(o[2], o[3]), pk2(o[4], o[5]), pk2(o[6], o[7])};
    *(u32x4*)(x1 + go) = rv;
  }
}

__global__ void __launch_bounds__(512, 2) k4_mlp1(const u16* __restrict__ b1, char* __restrict__ ws) {
  extern __shared__ __attribute__((aligned(16))) char lds[];
  const int b = blockIdx.x, tid = threadIdx.x, lane = tid & 63, w = tid >> 6;
  const int mt0 = b / 7, nb = b - mt0 * 7;
  const float* mod = (const float*)(ws + OFF_MOD);
  const u16* x1 = (const u16*)(ws + OFF_X1);
  const int n = lane & 15, g = lane >> 4;
  const f32x4 mv = *(const f32x4*)(mod + 1536 + (tid & 255) * 4);
  u32x4 xv[2][4];
#pragma unroll
  for (int it = 0; it < 2; it++)
#pragma unroll
    for (int j = 0; j < 4; j++) xv[it][j] = *(const u32x4*)(x1 + (size_t)(mt0 * 64 + it * 32 + w * 4 + g) * DM + (j * 16 + n) * 8);
  const u32x4* WF = (const u32x4*)(ws + OFF_W1F) + (size_t)(nb * 16 + w * 2) * 16 * 64 + lane;
  u32x4 bfm[2][16];
#pragma unroll
  for (int nt = 0; nt < 2; nt++)
#pragma unroll
    for (int ks = 0; ks < 8; ks++) bfm[nt][ks] = WF[(nt * 16 + ks) * 64];
  __builtin_amdgcn_sched_barrier(0);
  float* modl = (float*)(lds + 66560);
  if (tid < 256) *(f32x4*)(modl + tid * 4) = tid >= 128 ? mv + 1.f : mv;
  __syncthreads();
#pragma unroll
  for (int it = 0; it < 2; it++) {
    f32x2 f[16];
#pragma unroll
    for (int j = 0; j < 4; j++) {
      f[4 * j + 0] = (f32x2){bf_lo(xv[it][j].x), bf_hi(xv[it][j].x)}; f[4 * j + 1] = (f32x2){bf_lo(xv[it][j].y), bf_hi(xv[it][j].y)};
      f[4 * j + 2] = (f32x2){bf_lo(xv[it][j].z), bf_hi(xv[it][j].z)}; f[4 * j + 3] = (f32x2){bf_lo(xv[it][j].w), bf_hi(xv[it][j].w)};
    }
    f32x2 s2 = {0.f, 0.f};
#pragma unroll
    for (int i = 0; i < 16; i++) s2 += f[i];
    float m = row16_sum(s2.x + s2.y) * (1.f / DM);
    f32x2 q2 = {0.f, 0.f};
#pragma unroll
    for (int i = 0; i < 16; i++) { f[i] -= m; q2 += f[i] * f[i]; }
    float rs = rsqrtf(row16_sum(q2.x + q2.y) * (1.f / DM) + 1e-5f);
    s2 = (f32x2){0.f, 0.f};
#pragma unroll
    for (int j = 0; j < 4; j++) {
      const int c0 = (j * 16 + n) * 8;
      f32x4 c0v = *(const f32x4*)(modl + 512 + c0), c1v = *(const f32x4*)(modl + 512 + c0 + 4);
      f32x4 s0v = *(const f32x4*)(modl + c0), s1v = *(const f32x4*)(modl + c0 + 4);
      f[4 * j + 0] = f[4 * j + 0] * rs * (f32x2){c0v[0], c0v[1]} + (f32x2){s0v[0], s0v[1]};
      f[4 * j + 1] = f[4 * j + 1] * rs * (f32x2){c0v[2], c0v[3]} + (f32x2){s0v[2], s0v[3]};
      f[4 * j + 2] = f[4 * j + 2] * rs * (f32x2){c1v[0], c1v[1]} + (f32x2){s1v[0], s1v[1]};
      f[4 * j + 3] = f[4 * j + 3] * rs * (f32x2){c1v[2], c1v[3]} + (f32x2){s1v[2], s1v[3]};
      s2 += (f[4 * j + 0] + f[4 * j + 1]) + (f[4 * j + 2] + f[4 * j + 3]);
    }
    m = row16_sum(s2.x + s2.y) * (1.f / DM);
    q2 = (f32x2){0.f, 0.f};
#pragma unroll
    for (int i = 0; i < 16; i++) { f[i] -= m; q2 += f[i] * f[i]; }
    rs = rsqrtf(row16_sum(q2.x + q2.y) * (1.f / DM) + 1e-5f);
#pragma unroll
    for (int j = 0; j < 4; j++) {
      f32x2 o0 = f[4 * j] * rs, o1 = f[4 * j + 1] * rs, o2 = f[4 * j + 2] * rs, o3 = f[4 * j + 3] * rs;
      u32x4 o = {pk2(o0.x, o0.y), pk2(o1.x, o1.y), pk2(o2.x, o2.y), pk2(o3.x, o3.y)};
      *(u32x4*)(lds + (it * 32 + w * 4 + g) * 1040 + (j * 16 + n) * 16) = o;
    }
    __builtin_amdgcn_sched_barrier(0);
  }
#pragma unroll
  for (int nt = 0; nt < 2; nt++)
#pragma unroll
    for (int ks = 8; ks < 16; ks++) bfm[nt][ks] = WF[(nt * 16 + ks) * 64];
  __builtin_amdgcn_sched_barrier(0);
  __syncthreads();
  f32x4 acc[4][2];
#pragma unroll
  for (int i = 0; i < 4; i++)
#pragma unroll
    for (int j = 0; j < 2; j++) acc[i][j] = (f32x4){0.f, 0.f, 0.f, 0.f};
#pragma unroll
  for (int ks = 0; ks < 16; ks++) {
    bf16x8 af[4];
#pragma unroll
    for (int mt = 0; mt < 4; mt++) af[mt] = *(const bf16x8*)(lds + (16 * mt + n) * 1040 + (32 * ks + 8 * g) * 2);
#pragma unroll
    for (int mt = 0; mt < 4; mt++)
#pragma unroll
      for (int nt = 0; nt < 2; nt++) acc[mt][nt] = MFMA16(as_bf8(bfm[nt][ks]), af[mt], acc[mt][nt]);
    if (ks & 1) __builtin_amdgcn_sched_barrier(0);
  }
  u32x4* hidF = (u32x4*)(ws + OFF_HID);
  float bb[2][4];
#pragma unroll
  for (int nt = 0; nt < 2; nt++) {
    u32x2 bv = *(const u32x2*)(b1 + nb * 256 + w * 32 + 16 * nt + 4 * g);
    bb[nt][0] = bf_lo(bv.x); bb[nt][1] = bf_hi(bv.x); bb[nt][2] = bf_lo(bv.y); bb[nt][3] = bf_hi(bv.y);
  }
#pragma unroll
  for (int mt = 0; mt < 4; mt++) {
    float o[8];
#pragma unroll
    for (int nt = 0; nt < 2; nt++)
#pragma unroll
      for (int j = 0; j < 4; j++) {
        float u = bfr(bfr(acc[mt][nt][j]) + bb[nt][j]);
        float z = 0.7978845608028654f * (u + 0.044715f * u * u * u);
        float e = __builtin_amdgcn_exp2f(z * 2.8853900817779268f);
        float th = 1.f - 2.f * __builtin_amdgcn_rcpf(e + 1.f);
        o[4 * nt + j] = 0.5f * u * (1.f + th);
      }
    u32x4 ov = {pk2(o[0], o[1]), pk2(o[2], o[3]), pk2(o[4], o[5]), pk2(o[6], o[7])};
    hidF[((size_t)(mt0 * 4 + mt) * 56 + nb * 8 + w) * 64 + lane] = ov;
  }
}

__global__ void __launch_bounds__(512, 2) k5_mlp2(const u16* __restrict__ b2, char* __restrict__ ws, u16* __restrict__ out) {
  extern __shared__ __attribute__((aligned(16))) char lds[];
  const int b = blockIdx.x, tid = threadIdx.x, lane = tid & 63, w = tid >> 6;
  const int mt0 = (b & 7) * 4 + (b >> 6), nb = (b >> 3) & 7;
  const int n = lane & 15, g = lane >> 4;
  const u32x4* hidF = (const u32x4*)(ws + OFF_HID) + ((size_t)(mt0 * 4) * 56 + w * 7) * 64 + lane;
  const u32x4* WF = (const u32x4*)(ws + OFF_W2F) + ((size_t)(nb * 4) * 56 + w * 7) * 64 + lane;
  f32x4 acc[4][4];
#pragma unroll
  for (int i = 0; i < 4; i++)
#pragma unroll
    for (int j = 0; j < 4; j++) acc[i][j] = (f32x4){0.f, 0.f, 0.f, 0.f};
#pragma unroll
  for (int hb = 0; hb < 2; hb++) {
    const int nk = hb ? 3 : 4;
    u32x4 bfm[4][4]; bf16x8 af[4][4];
#pragma unroll
    for (int k = 0; k < 4; k++) if (k < nk) {
#pragma unroll
      for (int nt = 0; nt < 4; nt++) bfm[k][nt] = WF[(nt * 56 + hb * 4 + k) * 64];
#pragma unroll
      for (int mt = 0; mt < 4; mt++) af[k][mt] = as_bf8(hidF[(mt * 56 + hb * 4 + k) * 64]);
    }
    __builtin_amdgcn_sched_barrier(0);
#pragma unroll
    for (int k = 0; k < 4; k++) if (k < nk) {
#pragma unroll
      for (int mt = 0; mt < 4; mt++)
#pragma unroll
        for (int nt = 0; nt < 4; nt++) acc[mt][nt] = MFMA16(as_bf8(bfm[k][nt]), af[k][mt], acc[mt][nt]);
    }
  }
  float* red = (float*)lds;
#pragma unroll
  for (int mt = 0; mt < 4; mt++)
#pragma unroll
    for (int nt = 0; nt < 4; nt++) *(f32x4*)(red + (w * 64 + 16 * mt + n) * 68 + 16 * nt + 4 * g) = acc[mt][nt];
  __syncthreads();
  const float* mod = (const float*)(ws + OFF_MOD);
  const u16* x1 = (const u16*)(ws + OFF_X1);
  {
    const int r = tid >> 3, c8 = (tid & 7) * 8;
    float o[8];
#pragma unroll
    for (int j = 0; j < 8; j++) o[j] = 0.f;
#pragma unroll
    for (int ww = 0; ww < 8; ww++) {
      f32x4 a = *(const f32x4*)(red + (ww * 64 + r) * 68 + c8), bq = *(const f32x4*)(red + (ww * 64 + r) * 68 + c8 + 4);
#pragma unroll
      for (int j = 0; j < 4; j++) { o[j] += a[j]; o[4 + j] += bq[j]; }
    }
    const int col = 64 * nb + c8;
    const size_t go = (size_t)(mt0 * 64 + r) * DM + col;
    u32x4 xv = *(const u32x4*)(x1 + go), bv = *(const u32x4*)(b2 + col);
    float xf[8] = {bf_lo(xv.x), bf_hi(xv.x), bf_lo(xv.y), bf_hi(xv.y), bf_lo(xv.z), bf_hi(xv.z), bf_lo(xv.w), bf_hi(xv.w)};
    float bb[8] = {bf_lo(bv.x), bf_hi(bv.x), bf_lo(bv.y), bf_hi(bv.y), bf_lo(bv.z), bf_hi(bv.z), bf_lo(bv.w), bf_hi(bv.w)};
#pragma unroll
    for (int j = 0; j < 8; j++) o[j] = xf[j] + bfr(mod[2560 + col + j] * bfr(bfr(o[j]) + bb[j]));
    u32x4 rv = {pk2(o[0], o[1]), pk2(o[2], o[3]), pk2(o[4], o[5]), pk2(o[6], o[7])};
    *(u32x4*)(out + go) = rv;
  }
}

extern "C" void kernel_launch(void* const* d_in, const int* in_sizes, int n_in, void* d_out, int out_size, void* d_ws,
                              size_t ws_size, hipStream_t stream) {
  const u16* x = (const u16*)d_in[0];
  const u16* mem0 = (const u16*)d_in[1];
  const u16* Wq = (const u16*)d_in[2];
  const u16* Wk = (const u16*)d_in[3];
  const u16* Wv = (const u16*)d_in[4];
  const u16* Wo = (const u16*)d_in[5];
  const u16* w1 = (const u16*)d_in[8];
  const u16* b1 = (const u16*)d_in[9];
  const u16* w2 = (const u16*)d_in[10];
  const u16* b2 = (const u16*)d_in[11];
  const u16* cvec = (const u16*)d_in[12];
  const u16* wmod = (const u16*)d_in[13];
  char* ws = (char*)d_ws;
  hipFuncSetAttribute((const void*)k2_rec, hipFuncAttributeMaxDynamicSharedMemorySize, 135168);
  k0_prep<<<336, 256, 32768, stream>>>(Wv, Wo, cvec, wmod, mem0, ws);
  k2_rec<<<256, 256, 135168, stream>>>(Wq, Wk, Wv, Wo, x, w1, w2, ws);
  hipFuncSetAttribute((const void*)k3b_oproj, hipFuncAttributeMaxDynamicSharedMemorySize, 135168);
  k3b_oproj<<<256, 512, 135168, stream>>>(x, ws);
  hipFuncSetAttribute((const void*)k4_mlp1, hipFuncAttributeMaxDynamicSharedMemorySize, 70656);
  hipFuncSetAttribute((const void*)k5_mlp2, hipFuncAttributeMaxDynamicSharedMemorySize, 139264);
  k4_mlp1<<<224, 512, 70656, stream>>>(b1, ws);
  k5_mlp2<<<256, 512, 139264, stream>>>(b2, ws, (u16*)d_out);
}
```

```cpp
#include <hip/hip_runtime.h>
#include <hip/hip_bf16.h>
#include <stdint.h>

typedef unsigned int u32;
typedef unsigned short u16;
typedef unsigned long long u64;
typedef __attribute__((ext_vector_type(2))) __bf16 bf16x2;
typedef __attribute__((ext_vector_type(8))) __bf16 bf16x8;
typedef __attribute__((ext_vector_type(16))) __bf16 bf16x16;
typedef __attribute__((ext_vector_type(2))) float f32x2;
typedef __attribute__((ext_vector_type(4))) float f32x4;
typedef __attribute__((ext_vector_type(16))) float f32x16;
typedef __attribute__((ext_vector_type(2))) u32 u32x2;
typedef __attribute__((ext_vector_type(4))) u32 u32x4;
typedef __attribute__((ext_vector_type(8))) u32 u32x8;

#define NTOK 2048
#define DM 512
#define NH 32
#define DH 128
#define NSEG 64
#define FF 1792
#define SENT 0xFFFFFFFFu

#define OFF_FLAG 0ull
#define OFF_DONE 8192u
#define OFF_KMF 12288u
#define OFF_WVF 14336u
#define OFF_MOD 16384ull
#define OFF_MEMX 32768ull
#define OFF_PEX 0x90000ull
#define OFF_H (2ull << 20)
#define OFF_KMEM (4ull << 20)
#define OFF_WVO (8ull << 20)
#define OFF_KX (24ull << 20)
#define OFF_QX (40ull << 20)
#define OFF_Y (56ull << 20)
#define OFF_UF (72ull << 20)
#define OFF_PX (136ull << 20)
#define OFF_X1 (142ull << 20)
#define OFF_Y2 (146ull << 20)
#define OFF_HID (148ull << 20)
#define OFF_W1F (156ull << 20)
#define OFF_W2F (158ull << 20)
#define OFF_DBG (200ull << 20)
#define STAMP(slot) do { } while (0)

__device__ __forceinline__ u32 pk2(float a, float b) {
  bf16x2 v; v[0] = (__bf16)a; v[1] = (__bf16)b; return __builtin_bit_cast(u32, v);
}
__device__ __forceinline__ u32 f2u(float x) { return __builtin_bit_cast(u32, x); }
__device__ __forceinline__ float u2f(u32 x) { return __builtin_bit_cast(float, x); }
__device__ __forceinline__ float bf_lo(u32 x) { return __builtin_bit_cast(float, x << 16); }
__device__ __forceinline__ float bf_hi(u32 x) { return __builtin_bit_cast(float, x & 0xffff0000u); }
__device__ __forceinline__ float bfr(float a) { return (float)(__bf16)a; }
__device__ __forceinline__ __amdgpu_buffer_rsrc_t mkrsrc(const void* p) {
  return __builtin_amdgcn_make_buffer_rsrc((void*)p, 0, 0x7fffffff, 0x00020000);
}
__device__ __forceinline__ u32x4 ld16_sc1(__amdgpu_buffer_rsrc_t r, u32 off) { return __builtin_amdgcn_raw_buffer_load_b128(r, off, 0, 16); }
__device__ __forceinline__ u32 ld4_sc1(__amdgpu_buffer_rsrc_t r, u32 off) { return __builtin_amdgcn_raw_buffer_load_b32(r, off, 0, 16); }
__device__ __forceinline__ void st16_sc1(u32x4 v, __amdgpu_buffer_rsrc_t r, u32 off) { __builtin_amdgcn_raw_buffer_store_b128(v, r, off, 0, 16); }
__device__ __forceinline__ void st8_sc1(u32x2 v, __amdgpu_buffer_rsrc_t r, u32 off) { __builtin_amdgcn_raw_buffer_store_b64(v, r, off, 0, 16); }
__device__ __forceinline__ void st4_sc1(u32 v, __amdgpu_buffer_rsrc_t r, u32 off) { __builtin_amdgcn_raw_buffer_store_b32(v, r, off, 0, 16); }
__device__ __forceinline__ u32 badm(u32x4 a) { return (u32)(a.x == SENT) | (u32)(a.y == SENT) | (u32)(a.z == SENT) | (u32)(a.w == SENT); }
__device__ __forceinline__ u32 sane(u32 x) { return x < 0xFFFFFFFEu ? x : 0xFFFFFFFEu; }
__device__ __forceinline__ u32 now32() { return (u32)__builtin_amdgcn_s_memtime(); }
__device__ __forceinline__ void wait_until(u32 t) { while ((int)(t - now32()) > 0) { } }
__device__ __forceinline__ bf16x8 as_bf8(u32x4 v) { return __builtin_bit_cast(bf16x8, v); }
__device__ __forceinline__ bf16x16 as_bf16(u32x4 a, u32x4 b) {
  u32x8 v = {a.x, a.y, a.z, a.w, b.x, b.y, b.z, b.w}; return __builtin_bit_cast(bf16x16, v);
}
__device__ __forceinline__ float max3r(float a, float b, float c) { float r; asm("v_max3_f32 %0, %1, %2, %3" : "=v"(r) : "v"(a), "v"(b), "v"(c)); return r; }
__device__ __forceinline__ float max2r(float a, float b) { float r; asm("v_max_f32 %0, %1, %2" : "=v"(r) : "v"(a), "v"(b)); return r; }
#define MFMA16(a, b, c) __builtin_amdgcn_mfma_f32_16x16x32_bf16((a), (b), (c), 0, 0, 0)
#define MFMA32(a, b, c) __builtin_amdgcn_mfma_f32_32x32x16_bf16((a), (b), (c), 0, 0, 0)
#define SMF(a, b, c, idx) __builtin_amdgcn_smfmac_f32_16x16x64_bf16((a), (b), (c), (idx), 0, 0)

template <int N>
__device__ __forceinline__ u32 poll_chunks(__amdgpu_buffer_rsrc_t r, const u32 (&off)[N], u32x4 (&a)[N], u32 lane16, u32& tiss) {
  u32 spins = 0;
  while (true) {
    asm volatile("" : "+v"(lane16) :: "memory");
    if (spins) tiss = now32();
    u32 bad = 0;
#pragma unroll
    for (int j = 0; j < N; j++) a[j] = ld16_sc1(r, off[j] + lane16);
#pragma unroll
    for (int j = 0; j < N; j++) bad |= badm(a[j]);
    ++spins;
    if (!__any(bad)) return spins;
    if (spins > 400000u) return 0;
  }
}

typedef __attribute__((ext_vector_type(4))) short s16x4;
typedef __attribute__((address_space(3))) s16x4 lds_s16x4;
template <int ROWB>
__device__ __forceinline__ void dma_tile(char* lds, const u16* src, int rows, int gstride) {
  constexpr int LPR = ROWB / 16;
  const int total = rows * LPR;
  for (int u = threadIdx.x; u < total; u += 256) {
    const int r = u / LPR, c = u % LPR;
    __builtin_amdgcn_global_load_lds((const u32*)(src + (size_t)r * gstride + c * 8), (u32*)(lds + u * 16), 16, 0, 0);
  }
}
__device__ __forceinline__ void dma_wait() { asm volatile("s_waitcnt vmcnt(0)" ::: "memory"); __syncthreads(); }
__device__ __forceinline__ u32x2 tr4(const char* lds, int row0, int col0, int pitch, int lane) {
  const char* p = lds + (row0 + ((lane & 15) >> 2)) * pitch + (col0 + 4 * (lane & 3)) * 2;
  s16x4 v = __builtin_amdgcn_ds_read_tr16_b64_v4i16((lds_s16x4*)p);
  return __builtin_bit_cast(u32x2, v);
}
__device__ __forceinline__ bf16x8 frag8(const char* lds, int k0, int col0, int pitch, int lane) {
  u32x2 a = tr4(lds, k0, col0, pitch, lane), b = tr4(lds, k0 + 4, col0, pitch, lane);
  return as_bf8((u32x4){a.x, a.y, b.x, b.y});
}
__device__ __forceinline__ u32x4 frag8s(const char* lds, int k0, int col0, int pitch, int lane) {
  u32x2 a = tr4(lds, k0, col0, pitch, lane), b = tr4(lds, k0 + 8, col0, pitch, lane);
  return (u32x4){a.x, b.x, a.y, b.y};
}
#define PACK_S(acc, b) (u32x4){pk2((acc)[(b)], (acc)[(b) + 1]), pk2((acc)[(b) + 4], (acc)[(b) + 5]), pk2((acc)[(b) + 2], (acc)[(b) + 3]), pk2((acc)[(b) + 6], (acc)[(b) + 7])}

__global__ void __launch_bounds__(256, 2) k0_prep(const u16* __restrict__ Wv, const u16* __restrict__ Wo,
                                                   const u16* __restrict__ cvec, const u16* __restrict__ wmod,
                                                   const u16* __restrict__ mem0, const u16* __restrict__ Wq, const u16* __restrict__ Wk,
                                                   const u16* __restrict__ xin, char* __restrict__ ws) {
  extern __shared__ __attribute__((aligned(16))) char lds[];
  const int b = blockIdx.x, tid = threadIdx.x, lane = tid & 63, w = tid >> 6;
  if (b >= 336) {
    const int p = b - 336;
    const u32x4* src = p < 64 ? (const u32x4*)Wq + (size_t)p * 4096 : (p < 128 ? (const u32x4*)Wk + (size_t)(p - 64) * 4096 : (const u32x4*)xin + (size_t)(p - 128) * 4096);
    u32x4 v[16];
#pragma unroll
    for (int j = 0; j < 16; j++) v[j] = src[j * 256 + tid];
#pragma unroll
    for (int j = 0; j < 16; j++) asm volatile("" :: "v"(v[j]));
  } else if (b < 48) {
    float* red = (float*)lds;
    const int cb = b, cp = tid & 31, ksl = tid >> 5;
    float a0 = 0.f, a1 = 0.f;
#pragma unroll 32
    for (int k = 0; k < 64; k++) {
      const int kk = ksl * 64 + k;
      float cv = bf_lo((u32)cvec[kk]);
      float sv = bfr(cv / (1.f + __expf(-cv)));
      u32 wv = *(const u32*)(wmod + (size_t)kk * 3072 + cb * 64 + cp * 2);
      a0 += sv * bf_lo(wv); a1 += sv * bf_hi(wv);
    }
    red[(ksl * 32 + cp) * 2] = a0; red[(ksl * 32 + cp) * 2 + 1] = a1;
    __syncthreads();
    if (tid < 64) {
      float sacc = 0.f;
#pragma unroll
      for (int k = 0; k < 8; k++) sacc += red[k * 64 + tid];
      ((float*)(ws + OFF_MOD))[cb * 64 + tid] = bfr(sacc);
    }
  } else if (b < 48 + 256) {
    const int bb = b - 48;
    const int h = bb >> 3, nq = (bb >> 1) & 3, half = bb & 1, x = lane & 31, hh = lane >> 5;
    dma_tile<256>(lds, Wo + (size_t)h * DH * DM + nq * 128, DH, DM);
    bf16x8 a[2][8];
#pragma unroll
    for (int mt = 0; mt < 2; mt++)
#pragma unroll
      for (int ks = 0; ks < 8; ks++)
        a[mt][ks] = *(const bf16x8*)(Wv + ((size_t)(256 * half + 64 * w + 32 * mt + x) * NH + h) * DH + 16 * ks + 8 * hh);
    dma_wait();
    u32x4* dst = (u32x4*)(ws + OFF_WVO);
#pragma unroll
    for (int nt = 0; nt < 4; nt++) {
      bf16x8 bw[8];
#pragma unroll
      for (int ks = 0; ks < 8; ks++) bw[ks] = frag8(lds, 16 * ks + 8 * hh, 32 * nt + (x & 16), 256, lane);
#pragma unroll
      for (int mt = 0; mt < 2; mt++) {
        f32x16 acc;
#pragma unroll
        for (int i = 0; i < 16; i++) acc[i] = 0.f;
#pragma unroll
        for (int ks = 0; ks < 8; ks++) acc = MFMA32(a[mt][ks], bw[ks], acc);
        const int d = 128 * nq + 32 * nt + x, c = d >> 3, cc = d & 7;
#pragma unroll
        for (int xb = 0; xb < 2; xb++) {
          const int dp = 256 * half + 64 * w + 32 * mt + 16 * xb, t = dp >> 6, ga = (dp >> 4) & 3, z = ga >> 1, gb = 2 * (ga & 1) + hh;
          const int lp = (h & 1) * 8 + cc + 16 * gb;
          size_t o = ((((size_t)(c * 4 + (h >> 3)) * 4 + ((h >> 1) & 3)) * 8 + t) * 2 + z) * 64 + lp;
          dst[o] = PACK_S(acc, 8 * xb);
        }
      }
    }
  } else {
    const int ib = b - 304;
    u32x4 sv = {SENT, SENT, SENT, SENT};
    u32x4* mx = (u32x4*)(ws + OFF_MEMX + 8192);
    for (int i = ib * 256 + tid; i < 64 * 512; i += 32 * 256) mx[i] = sv;
    u32x4* px = (u32x4*)(ws + OFF_PEX);
    for (int i = ib * 256 + tid; i < 64 * 1280; i += 32 * 256) px[i] = sv;
    if (ib == 0) {
      u32x4 z = {0u, 0u, 0u, 0u};
      for (int i = tid; i < 1024; i += 256) ((u32x4*)(ws + OFF_FLAG))[i] = z;
      for (int i = tid; i < 512; i += 256) {
        int cbk = i >> 3, r = i & 7;
        u32x4 v = *(const u32x4*)(mem0 + r * DM + cbk * 8);
        v.x = sane(v.x); v.y = sane(v.y); v.z = sane(v.z); v.w = sane(v.w);
        ((u32x4*)(ws + OFF_MEMX))[i] = v;
      }
    }
  }
}

__device__ __forceinline__ float row16_sum(float v) {
  v += u2f((u32)__builtin_amdgcn_mov_dpp((int)f2u(v), 0x128, 0xF, 0xF, true));
  v += u2f((u32)__builtin_amdgcn_mov_dpp((int)f2u(v), 0x124, 0xF, 0xF, true));
  v += u2f((u32)__builtin_amdgcn_mov_dpp((int)f2u(v), 0x122, 0xF, 0xF, true));
  v += u2f((u32)__builtin_amdgcn_mov_dpp((int)f2u(v), 0x121, 0xF, 0xF, true));
  return v;
}

__device__ __forceinline__ void px_task(char* ws, int s, int hg);
__device__ __forceinline__ void role_help(const u16* __restrict__ x, const u16* __restrict__ w1, const u16* __restrict__ w2,
                                          char* ws, char* lds, int a0) {
  const int tid = threadIdx.x, lane = tid & 63, w = tid >> 6, n = lane & 15, g = lane >> 4;
  for (int a = a0; a < 64; a += 32) {
    const float* mod = (const float*)(ws + OFF_MOD);
    const __amdgpu_buffer_rsrc_t rh = mkrsrc(ws + OFF_H);
    u32x4 xv[2][4];
#pragma unroll
    for (int it = 0; it < 2; it++)
#pragma unroll
      for (int j = 0; j < 4; j++) xv[it][j] = *(const u32x4*)(x + (size_t)(a * 32 + it * 16 + w * 4 + g) * DM + (j * 16 + n) * 8);
#pragma unroll
    for (int it = 0; it < 2; it++) {
      float f[32];
#pragma unroll
      for (int j = 0; j < 4; j++) {
        f[8 * j + 0] = bf_lo(xv[it][j].x); f[8 * j + 1] = bf_hi(xv[it][j].x); f[8 * j + 2] = bf_lo(xv[it][j].y); f[8 * j + 3] = bf_hi(xv[it][j].y);
        f[8 * j + 4] = bf_lo(xv[it][j].z); f[8 * j + 5] = bf_hi(xv[it][j].z); f[8 * j + 6] = bf_lo(xv[it][j].w); f[8 * j + 7] = bf_hi(xv[it][j].w);
      }
      float sm = 0.f;
#pragma unroll
      for (int i = 0; i < 32; i++) sm += f[i];
      const float m = row16_sum(sm) * (1.f / DM);
      float q = 0.f;
#pragma unroll
      for (int i = 0; i < 32; i++) { f[i] -= m; q += f[i] * f[i]; }
      const float rs = rsqrtf(row16_sum(q) * (1.f / DM) + 1e-5f);
#pragma unroll
      for (int j = 0; j < 4; j++) {
        const int c0 = (j * 16 + n) * 8;
        f32x4 c0v = *(const f32x4*)(mod + 512 + c0), c1v = *(const f32x4*)(mod + 512 + c0 + 4);
        f32x4 s0v = *(const f32x4*)(mod + c0), s1v = *(const f32x4*)(mod + c0 + 4);
        float o[8];
#pragma unroll
        for (int i = 0; i < 4; i++) {
          o[i] = f[8 * j + i] * rs * (1.f + c0v[i]) + s0v[i];
          o[4 + i] = f[8 * j + 4 + i] * rs * (1.f + c1v[i]) + s1v[i];
        }
        u32x4 r = {pk2(o[0], o[1]), pk2(o[2], o[3]), pk2(o[4], o[5]), pk2(o[6], o[7])};
        st16_sc1(r, rh, (u32)((a * 32 + it * 16 + w * 4 + g) * DM + c0) * 2u);
      }
    }
    asm volatile("s_waitcnt vmcnt(0)" ::: "memory");
    __syncthreads();
    if (tid == 0) st4_sc1(1u, mkrsrc(ws + OFF_FLAG), OFF_DONE + a * 64u);
  }
  const int a = a0;
  if (a < 28) {
    dma_tile<128>(lds, w1 + a * 64, DM, FF);
    dma_wait();
    u32x4* dst = (u32x4*)(ws + OFF_W1F);
    for (int ks = 0; ks < 16; ks++) {
      bf16x8 v = frag8(lds, 32 * ks + 8 * (lane >> 4), 16 * w, 128, lane);
      dst[((size_t)(a * 4 + w) * 16 + ks) * 64 + lane] = __builtin_bit_cast(u32x4, v);
    }
  }
  __syncthreads();
  {
    const int cb = a >> 2, kq = a & 3;
    dma_tile<128>(lds, w2 + (size_t)kq * 448 * DM + cb * 64, 448, DM);
    dma_wait();
    u32x4* dst = (u32x4*)(ws + OFF_W2F);
    for (int ks = 0; ks < 14; ks++) {
      u32x2 va = tr4(lds, 32 * ks + 4 * (lane >> 4), 16 * w, 128, lane), vb = tr4(lds, 32 * ks + 16 + 4 * (lane >> 4), 16 * w, 128, lane);
      dst[((size_t)(cb * 4 + w) * 56 + kq * 14 + ks) * 64 + lane] = (u32x4){va.x, va.y, vb.x, vb.y};
    }
  }
  {
    const __amdgpu_buffer_rsrc_t rflag = mkrsrc(ws + OFF_FLAG);
    const int hg = a & 7;
    for (int s = a >> 3; s < NSEG; s += 4) {
      u32 spins = 0, off = OFF_KMF + (u32)(hg * 4 + w) * 64u;
      while (true) {
        asm volatile("" : "+v"(off) :: "memory");
        u32 v = ld4_sc1(rflag, off);
        if (v > (u32)s) break;
        __builtin_amdgcn_s_sleep(16);
        if (++spins > 400000u) break;
      }
      px_task(ws, s, hg);
    }
  }
}

#define AD_DEC 8
#define AD_INC 100
#define D1S_INIT 3000
#define D1C_INIT 600
#define D2C_INIT 2800
#define ADAPT(D, n) do { if ((n) == 1) D -= AD_DEC; else D += AD_INC; if (D < 0) D = 0; } while (0)

__device__ __forceinline__ bool wait_progress(__amdgpu_buffer_rsrc_t rf, u32 off, u32 need, u32& seen) {
  u32 spins = 0;
  if (__all(seen >= need)) return true;
  while (true) {
    asm volatile("" : "+v"(off) :: "memory");
    seen = ld4_sc1(rf, off);
    if (__all(seen >= need)) return true;
    __builtin_amdgcn_s_sleep(2);
    if (++spins > 400000u) return false;
  }
}

__device__ __forceinline__ void role_score(const u16* __restrict__ Wq, const u16* __restrict__ Wk, char* ws,
                                           char* lds, int h) {
  const int tid = threadIdx.x, lane = tid & 63, w = tid >> 6;
  const int n = lane & 15, gb = lane >> 4;
  bf16x16 wf[4][8];
#pragma unroll
  for (int mat = 0; mat < 2; mat++) {
    __syncthreads();
    dma_tile<256>(lds, (mat ? Wk : Wq) + h * DH, DM, NH * DH);
    dma_wait();
#pragma unroll
    for (int tt = 0; tt < 2; tt++)
#pragma unroll
      for (int t = 0; t < 8; t++) {
        const int col = 32 * w + 16 * tt;
        u32x4 lo = frag8s(lds, 64 * t + 16 * (gb >> 1) + 4 * (gb & 1), col, 256, lane);
        u32x4 hi = frag8s(lds, 64 * t + 16 * (2 + (gb >> 1)) + 4 * (gb & 1), col, 256, lane);
        wf[mat * 2 + tt][t] = as_bf16(lo, hi);
      }
  }
  __syncthreads();
  char* memL = lds;
  char* qs = lds + 8192;
  char* ks_ = lds + 8192 + 2304;
  const int idx = (lane & 8) ? (int)0xEEEEEEEE : 0x44444444;
  const __amdgpu_buffer_rsrc_t rflag = mkrsrc(ws + OFF_FLAG);
  const __amdgpu_buffer_rsrc_t rkx = mkrsrc(ws + OFF_KX);
  const __amdgpu_buffer_rsrc_t rmem = mkrsrc(ws + OFF_MEMX);
  const __amdgpu_buffer_rsrc_t rpex = mkrsrc(ws + OFF_PEX);
  const int rho = lane & 15;
  const int keyp = 8 * ((rho >> 2) & 1) + 4 * (rho >> 3) + (rho & 3);
  const u32 prho = (u32)((n & 7) >> 2) * 10240u, pi = n & 3;
  const u32 pxoff = prho + (u32)(((h >> 2) * 64) + 16 * (2 * (h & 1) + (gb >> 1)) + 8 * (gb & 1) + 4 * ((h >> 1) & 1) + pi) * 16u + (n >> 3) * 8u;
  const u32 pmoff = prho + 8192u + (u32)(h * 4 + pi) * 16u + (gb & 1) * 8u + (gb >> 1) * 4u;
  int D1 = w == 0 ? 3400 : (w == 1 ? 3300 : 4050); u32 tref = now32() - 8000u, seen = 0;
  STAMP(1);
  for (int s = 0; s < NSEG; s++) {
    u32x4 kx[2][4];
    if (w == 0) {
      if (!wait_progress(rflag, 4096u + h * 128u, s + 1, seen)) return;
#pragma unroll
      for (int tt = 0; tt < 2; tt++)
#pragma unroll
        for (int k4 = 0; k4 < 4; k4++)
          kx[tt][k4] = ld16_sc1(rkx, (u32)(((32 * s + 16 * tt + keyp) * NH + h) * DH + 32 * k4 + 8 * gb) * 2u);
    }
    f32x4 acc[4];
#pragma unroll
    for (int i = 0; i < 4; i++) { acc[i] = (f32x4){0.f, 0.f, 0.f, 0.f}; asm volatile("" : "+v"(acc[i])); }
    wait_until(tref + D1);
    {
      u32 off[2] = {(u32)s * 8192u + w * 2048u, (u32)s * 8192u + w * 2048u + 1024u};
      u32x4 a[2];
      u32 tiss = 0;
      u32 nsw = poll_chunks<2>(rmem, off, a, lane * 16u, tiss);
      if (!nsw) return;
      if (s > 2) ADAPT(D1, nsw);
      *(u32x4*)(memL + w * 2048 + lane * 16) = a[0];
      *(u32x4*)(memL + w * 2048 + 1024 + lane * 16) = a[1];
    }
    __syncthreads();
    STAMP(16 + 4 * s);
    bf16x8 af[8];
#pragma unroll
    for (int t = 0; t < 8; t++) af[t] = *(const bf16x8*)(memL + t * 1024 + lane * 16);
    __builtin_amdgcn_sched_barrier(0);
    const int ecol = 32 * w + 16 * (lane >> 5) + n, er0 = 4 * ((lane >> 4) & 1);
#pragma unroll
    for (int t = 0; t < 8; t++) { acc[0] = SMF(af[t], wf[0][t], acc[0], idx); acc[1] = SMF(af[t], wf[1][t], acc[1], idx); }
    __builtin_amdgcn_sched_barrier(0);
#pragma unroll
    for (int t = 0; t < 8; t++) {
      acc[2] = SMF(af[t], wf[2][t], acc[2], idx); acc[3] = SMF(af[t], wf[3][t], acc[3], idx);
      if (t >= 2 && t < 6) {
        const int j = t - 2;
        auto r = __builtin_amdgcn_permlane32_swap(f2u(acc[0][j]), f2u(acc[1][j]), false, false);
        *(__bf16*)(qs + (er0 + j) * 272 + ecol * 2) = (__bf16)(u2f(r[0]) + u2f(r[1]));
      }
      __builtin_amdgcn_sched_barrier(0);
    }
#pragma unroll
    for (int j = 0; j < 4; j++) {
      auto r = __builtin_amdgcn_permlane32_swap(f2u(acc[2][j]), f2u(acc[3][j]), false, false);
      *(__bf16*)(ks_ + (er0 + j) * 272 + ecol * 2) = (__bf16)(u2f(r[0]) + u2f(r[1]));
    }
    __syncthreads();
    STAMP(17 + 4 * s);
    if (w == 0) {
      f32x4 sc[3];
#pragma unroll
      for (int i = 0; i < 3; i++) { sc[i] = (f32x4){0.f, 0.f, 0.f, 0.f}; asm volatile("" : "+v"(sc[i])); }
      bf16x8 qf[4], kf[4];
#pragma unroll
      for (int k4 = 0; k4 < 4; k4++) {
        qf[k4] = *(const bf16x8*)(qs + (lane & 7) * 272 + (32 * k4 + 8 * gb) * 2);
        kf[k4] = *(const bf16x8*)(ks_ + (lane & 7) * 272 + (32 * k4 + 8 * gb) * 2);
      }
      __builtin_amdgcn_sched_barrier(0);
#pragma unroll
      for (int k4 = 0; k4 < 4; k4++) {
        sc[0] = MFMA16(as_bf8(kx[0][k4]), qf[k4], sc[0]);
        sc[1] = MFMA16(as_bf8(kx[1][k4]), qf[k4], sc[1]);
        sc[2] = MFMA16(kf[k4], qf[k4], sc[2]);
      }
      const float sl = 0.08838834764831845f * 1.4426950408889634f;
      const bool hiq = n >= 8, hig = gb >= 2;
      float p[6];
#pragma unroll
      for (int j = 0; j < 4; j++) p[j] = hiq ? sc[1][j] : sc[0][j];
      p[4] = hig ? sc[2][2] : sc[2][0];
      p[5] = hig ? sc[2][3] : sc[2][1];
      float mx = max2r(max3r(p[0], p[1], p[2]), max3r(p[3], p[4], p[5]));
      mx = max2r(mx, u2f((u32)__builtin_amdgcn_mov_dpp((int)f2u(mx), 0x128, 0xF, 0xF, true)));
      { auto r = __builtin_amdgcn_permlane16_swap(f2u(mx), f2u(mx), false, false);
        mx = max2r(u2f(r[0]), u2f(r[1])); }
      { auto r = __builtin_amdgcn_permlane32_swap(f2u(mx), f2u(mx), false, false);
        mx = max2r(u2f(r[0]), u2f(r[1])); }
      const float nb = -mx * sl;
#pragma unroll
      for (int j = 0; j < 6; j++) p[j] = __builtin_amdgcn_exp2f(__builtin_fmaf(p[j], sl, nb));
      float sum = (p[0] + p[1]) + (p[2] + p[3]) + (hiq ? 0.f : (p[4] + p[5]));
      sum += u2f((u32)__builtin_amdgcn_mov_dpp((int)f2u(sum), 0x128, 0xF, 0xF, true));
      { auto r = __builtin_amdgcn_permlane16_swap(f2u(sum), f2u(sum), false, false);
        sum = u2f(r[0]) + u2f(r[1]); }
      { auto r = __builtin_amdgcn_permlane32_swap(f2u(sum), f2u(sum), false, false);
        sum = u2f(r[0]) + u2f(r[1]); }
      const float inv = __builtin_amdgcn_rcpf(sum);
      u32x2 px = {sane(pk2(p[0] * inv, p[1] * inv)), sane(pk2(p[2] * inv, p[3] * inv))};
      st8_sc1(px, rpex, (u32)s * 20480u + pxoff);
      if (!hiq) st4_sc1(sane(pk2(p[4] * inv, p[5] * inv)), rpex, (u32)s * 20480u + pmoff);
      STAMP(18 + 4 * s);
    } else if (w == 1) {
      const __amdgpu_buffer_rsrc_t rkm = mkrsrc(ws + OFF_KMEM);
#pragma unroll
      for (int j = 0; j < 2; j++) {
        int u = j * 64 + lane, r = u >> 4, c = u & 15;
        st16_sc1(*(const u32x4*)(ks_ + r * 272 + c * 16), rkm, (u32)((s * NH + h) * 1024 + r * 128 + c * 8) * 2u);
      }
      asm volatile("s_waitcnt vmcnt(0)" ::: "memory");
      if (lane == 0) st4_sc1((u32)(s + 1), rflag, OFF_KMF + h * 64u);
    }
    tref = now32();
  }
}

__device__ __forceinline__ void role_column(char* ws, char* lds, int c, int rho) {
  const int tid = threadIdx.x, lane = tid & 63, w = tid >> 6;
  const int n = lane & 15, gb = lane >> 4, cc = lane & 7;
  bf16x16 wf[4][8];
  {
    const u32x4* src = (const u32x4*)(ws + OFF_WVO) + (size_t)(c * 4 + w) * 4096;
#pragma unroll
    for (int tl = 0; tl < 4; tl++)
#pragma unroll
      for (int t = 0; t < 8; t++) wf[tl][t] = as_bf16(src[((tl * 8 + t) * 2) * 64 + lane], src[((tl * 8 + t) * 2 + 1) * 64 + lane]);
  }
  char* memL = lds;
  char* yl = lds + 8192 + w * 1024;
  float* red = (float*)(lds + 12288);
  const int idx = (lane & 8) ? (int)0xEEEEEEEE : 0x44444444;
  const __amdgpu_buffer_rsrc_t rflag = mkrsrc(ws + OFF_FLAG);
  const __amdgpu_buffer_rsrc_t rmem = mkrsrc(ws + OFF_MEMX);
  const __amdgpu_buffer_rsrc_t rpex = mkrsrc(ws + OFF_PEX);
  int D1 = w == 0 ? 750 : (w == 1 ? 850 : 1130), D2 = 2800; u32 tref = now32(), seen = 0;
  STAMP(1);
  for (int s = 0; s < NSEG; s++) {
    wait_until(tref + D1);
    {
      u32 off[2] = {(u32)s * 8192u + w * 2048u, (u32)s * 8192u + w * 2048u + 1024u};
      u32x4 a[2];
      u32 tiss = 0;
      u32 nsw = poll_chunks<2>(rmem, off, a, lane * 16u, tiss);
      if (!nsw) return;
      if (s > 2) ADAPT(D1, nsw);
      tref = now32();
      *(u32x4*)(memL + w * 2048 + lane * 16) = a[0];
      *(u32x4*)(memL + w * 2048 + 1024 + lane * 16) = a[1];
    }
    u32x4 uf[4][2];
    {
      if (!wait_progress(rflag, (u32)(8 * w + (lane & 7)) * 128u, s + 1, seen)) return;
      const __amdgpu_buffer_rsrc_t ru = mkrsrc(ws + OFF_UF + ((size_t)s * 64 + c) * 16384);
#pragma unroll
      for (int m = 0; m < 4; m++)
#pragma unroll
        for (int z = 0; z < 2; z++) uf[m][z] = ld16_sc1(ru, (u32)((((4 * w + m) * 2 + z) * 4 + gb) * 8 + cc) * 16u);
    }
    __syncthreads();
    STAMP(16 + 4 * s);
    bf16x16 yb;
    {
      f32x4 acc[4];
#pragma unroll
      for (int i = 0; i < 4; i++) acc[i] = (f32x4){0.f, 0.f, 0.f, 0.f};
      bf16x8 af[8];
#pragma unroll
      for (int t = 0; t < 8; t++) af[t] = *(const bf16x8*)(memL + t * 1024 + lane * 16);
      __builtin_amdgcn_sched_barrier(0);
#pragma unroll
      for (int t = 0; t < 8; t++)
#pragma unroll
        for (int i = 0; i < 4; i++) acc[i] = SMF(af[t], wf[i][t], acc[i], idx);
#pragma unroll
      for (int pr = 0; pr < 2; pr++) {
        float v[4];
#pragma unroll
        for (int j = 0; j < 4; j++) {
          auto r = __builtin_amdgcn_permlane32_swap(f2u(acc[2 * pr][j]), f2u(acc[2 * pr + 1][j]), false, false);
          v[j] = u2f(r[0]) + u2f(r[1]);
        }
        const int hl = 2 * (2 * pr + (lane >> 5)) + (n >> 3);
        u32x2 pv = {pk2(v[0], v[1]), pk2(v[2], v[3])};
        *(u32x2*)(yl + (hl * 8 + cc) * 16 + 8 * ((lane >> 4) & 1)) = pv;
      }
      asm volatile("s_waitcnt lgkmcnt(0)" ::: "memory");
      u32x4 hz[2];
#pragma unroll
      for (int z = 0; z < 2; z++) {
        const int ga = 2 * z + (gb >> 1);
        u32x2 a = *(const u32x2*)(yl + ((2 * ga) * 8 + cc) * 16 + 8 * (gb & 1));
        u32x2 b = *(const u32x2*)(yl + ((2 * ga + 1) * 8 + cc) * 16 + 8 * (gb & 1));
        hz[z] = (u32x4){a.x, b.x, a.y, b.y};
      }
      yb = as_bf16(hz[0], hz[1]);
      if (rho == 0) __builtin_amdgcn_raw_buffer_store_b128(*(const u32x4*)(yl + lane * 16), mkrsrc(ws + OFF_Y), (u32)(s * 64 + c) * 4096u + w * 1024u + lane * 16u, 0, 0);
    }
    STAMP(17 + 4 * s);
    wait_until(tref + D2);
    {
      const u32 pb = (u32)s * 20480u + rho * 10240u;
      u32 off[3] = {pb + (2 * w) * 1024u + lane * 16u, pb + (2 * w + 1) * 1024u + lane * 16u,
                    pb + 8192u + (u32)((8 * w + 2 * gb + ((lane >> 3) & 1)) * 4 + (lane & 3)) * 16u};
      u32x4 g[3];
      u32 tiss = 0;
      u32 nsw = poll_chunks<3>(rpex, off, g, 0u, tiss);
      if (!nsw) return;
      if (s > 2) ADAPT(D2, nsw);
      f32x4 dA = {0.f, 0.f, 0.f, 0.f}, dB = {0.f, 0.f, 0.f, 0.f};
      dA = SMF(as_bf8(g[0]), as_bf16(uf[0][0], uf[0][1]), dA, idx);
      dB = SMF(as_bf8(g[0]), as_bf16(uf[1][0], uf[1][1]), dB, idx);
      dA = SMF(as_bf8(g[1]), as_bf16(uf[2][0], uf[2][1]), dA, idx);
      dB = SMF(as_bf8(g[1]), as_bf16(uf[3][0], uf[3][1]), dB, idx);
      dA = SMF(as_bf8(g[2]), yb, dA, idx);
      float* rd = red + (s & 1) * 512;
      if (n < 8) {
#pragma unroll
        for (int j = 0; j < 4; j++) rd[(j * 8 + cc) * 16 + 4 * w + gb] = (gb & 1) ? dB[j] : dA[j];
      }
    }
    __syncthreads();
    STAMP(18 + 4 * s);
    if (w == 0) {
      const float* rd = red + (s & 1) * 512 + lane * 8;
      f32x4 a = *(const f32x4*)rd, b = *(const f32x4*)(rd + 4);
      float v = ((a[0] + a[1]) + (a[2] + a[3])) + ((b[0] + b[1]) + (b[2] + b[3]));
      v += u2f((u32)__builtin_amdgcn_mov_dpp((int)f2u(v), 0xB1, 0xF, 0xF, true));
      float o = u2f((u32)__builtin_amdgcn_mov_dpp((int)f2u(v), 0x4E, 0xF, 0xF, true));
      if (!(lane & 3)) st4_sc1(sane(pk2(v, o)), rmem, (u32)(s + 1) * 8192u + c * 128u + rho * 64u + (lane >> 2) * 4u);
      STAMP(19 + 4 * s);
    }
    tref = now32();
  }
}

__device__ __forceinline__ void load_h(u32x4 (&r)[8], const char* ws, int s) {
  const __amdgpu_buffer_rsrc_t rh = mkrsrc(ws + OFF_H);
#pragma unroll
  for (int j = 0; j < 8; j++) r[j] = ld16_sc1(rh, (u32)s * 32768u + (j * 256 + threadIdx.x) * 16u);
}
__device__ __forceinline__ void put_h(char* dst, const u32x4 (&r)[8]) {
#pragma unroll
  for (int j = 0; j < 8; j++) {
    int u = j * 256 + threadIdx.x;
    *(u32x4*)(dst + (u >> 6) * 1040 + (u & 63) * 16) = r[j];
  }
}

template <bool SC1>
__device__ __forceinline__ void store_T_tile(const f32x16& acc, char* base, int tokrow, int h, int w, int lane) {
  const __amdgpu_buffer_rsrc_t r = mkrsrc(base);
#pragma unroll
  for (int qp = 0; qp < 2; qp++) {
    u32 ax = pk2(acc[8 * qp + 0], acc[8 * qp + 1]), ay = pk2(acc[8 * qp + 2], acc[8 * qp + 3]);
    u32 bx = pk2(acc[8 * qp + 4], acc[8 * qp + 5]), by = pk2(acc[8 * qp + 6], acc[8 * qp + 7]);
    auto r0 = __builtin_amdgcn_permlane32_swap(ax, bx, false, false);
    auto r1 = __builtin_amdgcn_permlane32_swap(ay, by, false, false);
    u32x4 v = {(u32)r0[0], (u32)r1[0], (u32)r0[1], (u32)r1[1]};
    u32 off = (u32)(((tokrow + (lane & 31)) * NH + h) * DH + 32 * w + 16 * qp + 8 * (lane >> 5)) * 2u;
    if (SC1) __builtin_amdgcn_raw_buffer_store_b128(v, r, off, 0, 16);
    else __builtin_amdgcn_raw_buffer_store_b128(v, r, off, 0, 0);
  }
}

__device__ __forceinline__ void role_vu(const u16* __restrict__ Wv, const u16* __restrict__ Wo,
                                        char* ws, char* lds, int h) {
  const int tid = threadIdx.x, lane = tid & 63, w = tid >> 6, x = lane & 31, hh = lane >> 5;
  bf16x8 wv[32], wo[4][8];
  dma_tile<256>(lds, Wv + h * DH, DM, NH * DH);
  dma_wait();
#pragma unroll
  for (int ks = 0; ks < 32; ks++) wv[ks] = frag8(lds, 16 * ks + 8 * hh, 32 * w + (x & 16), 256, lane);
  __syncthreads();
  dma_tile<1024>(lds, Wo + (size_t)h * DH * DM, DH, DM);
  dma_wait();
#pragma unroll
  for (int nt = 0; nt < 4; nt++)
#pragma unroll
    for (int ks = 0; ks < 8; ks++) wo[nt][ks] = frag8(lds, 16 * ks + 8 * hh, 128 * w + 32 * nt + (x & 16), 1024, lane);
  __syncthreads();
  char* vs = lds + 66560;
  const __amdgpu_buffer_rsrc_t rflag = mkrsrc(ws + OFF_FLAG);
  u32x4 hr[8];
  { u32 seenh = 0; if (!wait_progress(rflag, OFF_DONE + (lane & 31) * 64u, 1u, seenh)) return; }
  load_h(hr, ws, 0);
  put_h(lds, hr);
  load_h(hr, ws, 1);
  __syncthreads();
  for (int s = 0; s < NSEG; s++) {
    const char* hb = lds + (s & 1) * 33280;
    f32x16 av;
#pragma unroll
    for (int i = 0; i < 16; i++) av[i] = 0.f;
    {
      bf16x8 hf[2][8];
#pragma unroll
      for (int k = 0; k < 8; k++) hf[0][k] = *(const bf16x8*)(hb + x * 1040 + (16 * k + 8 * hh) * 2);
#pragma unroll
      for (int ch = 0; ch < 4; ch++) {
        if (ch < 3) {
#pragma unroll
          for (int k = 0; k < 8; k++) hf[(ch + 1) & 1][k] = *(const bf16x8*)(hb + x * 1040 + (16 * (8 * ch + 8 + k) + 8 * hh) * 2);
        }
        __builtin_amdgcn_sched_barrier(0);
#pragma unroll
        for (int k = 0; k < 8; k++) av = MFMA32(wv[8 * ch + k], hf[ch & 1][k], av);
        __builtin_amdgcn_sched_barrier(0);
      }
    }
#pragma unroll
    for (int q = 0; q < 4; q++) {
      u32x2 pv = {pk2(av[4 * q], av[4 * q + 1]), pk2(av[4 * q + 2], av[4 * q + 3])};
      *(u32x2*)(vs + x * 272 + (32 * w + 8 * q + 4 * hh) * 2) = pv;
    }
    put_h(lds + ((s + 1) & 1) * 33280, hr);
    asm volatile("s_waitcnt vmcnt(0)" ::: "memory");
    __syncthreads();
    if (tid == 0 && s > 4) st4_sc1((u32)s, rflag, h * 128u);
    bf16x8 vf[8];
#pragma unroll
    for (int ks = 0; ks < 8; ks++) vf[ks] = *(const bf16x8*)(vs + x * 272 + (16 * ks + 8 * hh) * 2);
    const __amdgpu_buffer_rsrc_t ru = mkrsrc(ws + OFF_UF + (size_t)s * 64 * 16384);
#pragma unroll
    for (int nt = 0; nt < 4; nt++) {
      f32x16 au;
#pragma unroll
      for (int i = 0; i < 16; i++) au[i] = 0.f;
#pragma unroll
      for (int ks = 0; ks < 8; ks++) au = MFMA32(vf[ks], wo[nt][ks], au);
      const int d = 128 * w + 32 * nt + x, c = d >> 3, cc = d & 7;
#pragma unroll
      for (int xb = 0; xb < 2; xb++)
        st16_sc1(PACK_S(au, 8 * xb), ru, (u32)((((c * 16 + (h >> 1)) * 2 + (h & 1)) * 4 + 2 * hh + xb) * 8 + cc) * 16u);
    }
    if (s < 4 || s == NSEG - 1) asm volatile("s_waitcnt vmcnt(0)" ::: "memory");
    if (s == 30) { u32 seenh = 0; if (!wait_progress(rflag, OFF_DONE + lane * 64u, 1u, seenh)) return; }
    load_h(hr, ws, s + 2 < NSEG ? s + 2 : NSEG - 1);
    __syncthreads();
    if (tid == 0 && (s < 4 || s == NSEG - 1)) st4_sc1((u32)(s + 1), rflag, h * 128u);
    STAMP(16 + 4 * s);
  }
}

__device__ __forceinline__ void role_qk(const u16* __restrict__ Wq, const u16* __restrict__ Wk, char* ws, char* lds, int h) {
  const int tid = threadIdx.x, lane = tid & 63, w = tid >> 6, x = lane & 31, hh = lane >> 5;
  bf16x8 wq[32], wk[32];
  dma_tile<256>(lds, Wk + h * DH, DM, NH * DH);
  dma_wait();
#pragma unroll
  for (int ks = 0; ks < 32; ks++) wk[ks] = frag8(lds, 16 * ks + 8 * hh, 32 * w + (x & 16), 256, lane);
  __syncthreads();
  dma_tile<256>(lds, Wq + h * DH, DM, NH * DH);
  dma_wait();
#pragma unroll
  for (int ks = 0; ks < 32; ks++) wq[ks] = frag8(lds, 16 * ks + 8 * hh, 32 * w + (x & 16), 256, lane);
  __syncthreads();
  const __amdgpu_buffer_rsrc_t rflag = mkrsrc(ws + OFF_FLAG);
  u32x4 hr[8];
  { u32 seenh = 0; if (!wait_progress(rflag, OFF_DONE + (lane & 31) * 64u, 1u, seenh)) return; }
  load_h(hr, ws, 0);
  put_h(lds, hr);
  load_h(hr, ws, 1);
  __syncthreads();
  for (int s = 0; s < NSEG; s++) {
    const char* hb = lds + (s & 1) * 33280;
    f32x16 aq, ak;
#pragma unroll
    for (int i = 0; i < 16; i++) { aq[i] = 0.f; ak[i] = 0.f; }
    {
      bf16x8 hf[2][8];
#pragma unroll
      for (int k = 0; k < 8; k++) hf[0][k] = *(const bf16x8*)(hb + x * 1040 + (16 * k + 8 * hh) * 2);
#pragma unroll
      for (int ch = 0; ch < 4; ch++) {
        if (ch < 3) {
#pragma unroll
          for (int k = 0; k < 8; k++) hf[(ch + 1) & 1][k] = *(const bf16x8*)(hb + x * 1040 + (16 * (8 * ch + 8 + k) + 8 * hh) * 2);
        }
        __builtin_amdgcn_sched_barrier(0);
#pragma unroll
        for (int k = 0; k < 8; k++) { ak = MFMA32(wk[8 * ch + k], hf[ch & 1][k], ak); aq = MFMA32(wq[8 * ch + k], hf[ch & 1][k], aq); }
        __builtin_amdgcn_sched_barrier(0);
      }
    }
    put_h(lds + ((s + 1) & 1) * 33280, hr);
    asm volatile("s_waitcnt vmcnt(0)" ::: "memory");
    __syncthreads();
    if (tid == 0 && s > 4) st4_sc1((u32)s, rflag, 4096u + h * 128u);
    store_T_tile<true>(ak, ws + OFF_KX, 32 * s, h, w, lane);
    store_T_tile<true>(aq, ws + OFF_QX, 32 * s, h, w, lane);
    if (s < 4 || s == NSEG - 1) {
      asm volatile("s_waitcnt vmcnt(0)" ::: "memory");
      __syncthreads();
      if (tid == 0) st4_sc1((u32)(s + 1), rflag, 4096u + h * 128u);
    }
    if (s == 30) { u32 seenh = 0; if (!wait_progress(rflag, OFF_DONE + lane * 64u, 1u, seenh)) return; }
    load_h(hr, ws, s + 2 < NSEG ? s + 2 : NSEG - 1);
    STAMP(16 + 4 * s);
  }
}

__device__ __forceinline__ float xg_max(float v) {
  auto r = __builtin_amdgcn_permlane16_swap(f2u(v), f2u(v), false, false);
  v = fmaxf(u2f(r[0]), u2f(r[1]));
  auto r2 = __builtin_amdgcn_permlane32_swap(f2u(v), f2u(v), false, false);
  return fmaxf(u2f(r2[0]), u2f(r2[1]));
}
__device__ __forceinline__ float xg_sum(float v) {
  auto r = __builtin_amdgcn_permlane16_swap(f2u(v), f2u(v), false, false);
  v = u2f(r[0]) + u2f(r[1]);
  auto r2 = __builtin_amdgcn_permlane32_swap(f2u(v), f2u(v), false, false);
  return u2f(r2[0]) + u2f(r2[1]);
}
__device__ __forceinline__ void px_task(char* ws, int s, int hg) {
  const int tid = threadIdx.x, lane = tid & 63, w = tid >> 6;
  const int hd = hg * 4 + w, hl = w;
  const int n = lane & 15, g = lane >> 4;
  const __amdgpu_buffer_rsrc_t rq = mkrsrc(ws + OFF_QX + (size_t)s * 32 * 8192);
  const __amdgpu_buffer_rsrc_t rk = mkrsrc(ws + OFF_KX + (size_t)s * 32 * 8192);
  const __amdgpu_buffer_rsrc_t rkm = mkrsrc(ws + OFF_KMEM + (size_t)s * 65536);
  const int keyrow = 16 * ((n >> 2) & 1) + 4 * (n >> 3) + (n & 3);
  const int keyb = 16 * (g & 1) + 4 * (g >> 1);
  const float sl = 0.08838834764831845f * 1.4426950408889634f;
  bf16x8 qf[2][4], kf[2][4], mf[4];
#pragma unroll
  for (int ks = 0; ks < 4; ks++) {
#pragma unroll
    for (int t = 0; t < 2; t++) {
      qf[t][ks] = as_bf8(ld16_sc1(rq, (u32)(((16 * t + n) * NH + hd) * DH + 32 * ks + 8 * g) * 2u));
      kf[t][ks] = as_bf8(ld16_sc1(rk, (u32)(((8 * t + keyrow) * NH + hd) * DH + 32 * ks + 8 * g) * 2u));
    }
    mf[ks] = as_bf8(ld16_sc1(rkm, (u32)(hd * 1024 + (n & 7) * 128 + 32 * ks + 8 * g) * 2u));
  }
  const bool mvalid = (g >> 1) == (hl >> 1);
  u32x4* PXx = (u32x4*)(ws + OFF_PX);
  char* PXm = ws + OFF_PX + (4ull << 20);
#pragma unroll
  for (int qt = 0; qt < 2; qt++) {
    f32x4 sc[3];
#pragma unroll
    for (int i = 0; i < 3; i++) sc[i] = (f32x4){0.f, 0.f, 0.f, 0.f};
#pragma unroll
    for (int ks = 0; ks < 4; ks++) {
      sc[0] = MFMA16(kf[0][ks], qf[qt][ks], sc[0]);
      sc[1] = MFMA16(kf[1][ks], qf[qt][ks], sc[1]);
      sc[2] = MFMA16(mf[ks], qf[qt][ks], sc[2]);
    }
    const int qi = 16 * qt + n;
    float mx = -3.0e38f;
#pragma unroll
    for (int j = 0; j < 4; j++) {
      if (keyb + j > qi) sc[0][j] = -3.0e38f;
      if (keyb + 8 + j > qi) sc[1][j] = -3.0e38f;
      if (!mvalid) sc[2][j] = -3.0e38f;
      mx = fmaxf(mx, fmaxf(fmaxf(sc[0][j], sc[1][j]), sc[2][j]));
    }
    mx = xg_max(mx);
    float sum = 0.f;
#pragma unroll
    for (int i = 0; i < 3; i++)
#pragma unroll
      for (int j = 0; j < 4; j++) {
        float p = sc[i][j] > -1.0e38f ? __builtin_amdgcn_exp2f((sc[i][j] - mx) * sl) : 0.f;
        sc[i][j] = p; sum += p;
      }
    const float inv = __builtin_amdgcn_rcpf(xg_sum(sum));
    u32x4 pa = {pk2(sc[0][0] * inv, sc[0][1] * inv), pk2(sc[1][0] * inv, sc[1][1] * inv),
                pk2(sc[0][2] * inv, sc[0][3] * inv), pk2(sc[1][2] * inv, sc[1][3] * inv)};
    PXx[(((size_t)s * NH + hd) * 2 + qt) * 64 + lane] = pa;
    if (mvalid) {
      u32x2 pm = {pk2(sc[2][0] * inv, sc[2][1] * inv), pk2(sc[2][2] * inv, sc[2][3] * inv)};
      *(u32x2*)(PXm + ((((size_t)s * 8 + hg) * 2 + qt) * 64 + lane) * 16 + (hl & 1) * 8) = pm;
    }
  }
}

__global__ void __launch_bounds__(256, 1) k2_rec(const u16* __restrict__ Wq, const u16* __restrict__ Wk, const u16* __restrict__ Wv,
                                                  const u16* __restrict__ Wo, const u16* __restrict__ x, const u16* __restrict__ w1,
                                                  const u16* __restrict__ w2, char* ws) {
  extern __shared__ __attribute__((aligned(16))) char lds[];
  const int b = blockIdx.x;
  STAMP(0);
  if (b < 32) role_help(x, w1, w2, ws, lds, b);
  else if (b < 64) role_score(Wq, Wk, ws, lds, b - 32);
  else if (b < 192) { const int q = b - 64; role_column(ws, lds, (q & 7) | ((q >> 4) << 3), (q >> 3) & 1); }
  else if (b < 224) role_vu(Wv, Wo, ws, lds, b - 192);
  else role_qk(Wq, Wk, ws, lds, b - 224);
}

__global__ void __launch_bounds__(512, 2) k3b_oproj(const u16* __restrict__ x, char* __restrict__ ws) {
  extern __shared__ __attribute__((aligned(16))) char lds[];
  const int b = blockIdx.x, tid = threadIdx.x, lane = tid & 63, w = tid >> 6;
  const int s = b >> 2, nq = b & 3;
  const int n = lane & 15, g = lane >> 4, cc = lane & 7;
  const u32x4* UF = (const u32x4*)(ws + OFF_UF) + (size_t)s * 64 * 1024;
  const char* Yg = ws + OFF_Y + (size_t)s * 64 * 4096;
  const u32x4* PXx = (const u32x4*)(ws + OFF_PX) + (size_t)s * NH * 128;
  const u32x4* PXm = (const u32x4*)(ws + OFF_PX + (4ull << 20)) + (size_t)s * 8 * 128;
  f32x4 acc[2][8];
#pragma unroll
  for (int i = 0; i < 2; i++)
#pragma unroll
    for (int j = 0; j < 8; j++) acc[i][j] = (f32x4){0.f, 0.f, 0.f, 0.f};
  u32x4 ub[4][8], pa[4][2], pm[2], yf[8];
#pragma unroll
  for (int hl = 0; hl < 4; hl++) {
    const int hd = 4 * w + hl;
#pragma unroll
    for (int nt = 0; nt < 8; nt++) ub[hl][nt] = UF[(size_t)(16 * nq + 2 * nt + (n >> 3)) * 1024 + (hd * 4 + g) * 8 + cc];
    pa[hl][0] = PXx[(hd * 2) * 64 + lane];
    pa[hl][1] = PXx[(hd * 2 + 1) * 64 + lane];
  }
  pm[0] = PXm[(w * 2) * 64 + lane];
  pm[1] = PXm[(w * 2 + 1) * 64 + lane];
  {
    const int ha = 4 * w + 2 * (g >> 1);
#pragma unroll
    for (int nt = 0; nt < 8; nt++) {
      const int c = 16 * nq + 2 * nt + (n >> 3);
      u32x2 ya = *(const u32x2*)(Yg + (size_t)c * 4096 + (ha * 8 + cc) * 16 + 8 * (g & 1));
      u32x2 yb = *(const u32x2*)(Yg + (size_t)c * 4096 + ((ha + 1) * 8 + cc) * 16 + 8 * (g & 1));
      yf[nt] = (u32x4){ya.x, ya.y, yb.x, yb.y};
    }
  }
  __builtin_amdgcn_sched_barrier(0);
#pragma unroll
  for (int hl = 0; hl < 4; hl++)
#pragma unroll
    for (int nt = 0; nt < 8; nt++) {
      acc[0][nt] = MFMA16(as_bf8(pa[hl][0]), as_bf8(ub[hl][nt]), acc[0][nt]);
      acc[1][nt] = MFMA16(as_bf8(pa[hl][1]), as_bf8(ub[hl][nt]), acc[1][nt]);
    }
#pragma unroll
  for (int nt = 0; nt < 8; nt++) {
    acc[0][nt] = MFMA16(as_bf8(pm[0]), as_bf8(yf[nt]), acc[0][nt]);
    acc[1][nt] = MFMA16(as_bf8(pm[1]), as_bf8(yf[nt]), acc[1][nt]);
  }
  float* red = (float*)lds;
#pragma unroll
  for (int qt = 0; qt < 2; qt++)
#pragma unroll
    for (int nt = 0; nt < 8; nt++)
#pragma unroll
      for (int j = 0; j < 4; j++) red[(w * 32 + 16 * qt + 4 * g + j) * 132 + 16 * nt + n] = acc[qt][nt][j];
  __syncthreads();
  const float* mod = (const float*)(ws + OFF_MOD);
  u16* x1 = (u16*)(ws + OFF_X1);
  {
    const int r = tid >> 4, c8 = (tid & 15) * 8;
    float o[8];
#pragma unroll
    for (int j = 0; j < 8; j++) o[j] = 0.f;
#pragma unroll
    for (int ww = 0; ww < 8; ww++) {
      f32x4 a = *(const f32x4*)(red + (ww * 32 + r) * 132 + c8), bq = *(const f32x4*)(red + (ww * 32 + r) * 132 + c8 + 4);
#pragma unroll
      for (int j = 0; j < 4; j++) { o[j] += a[j]; o[4 + j] += bq[j]; }
    }
    const int col = 128 * nq + c8;
    const size_t go = (size_t)(32 * s + r) * DM + col;
    u32x4 xv = *(const u32x4*)(x + go);
    float xf[8] = {bf_lo(xv.x), bf_hi(xv.x), bf_lo(xv.y), bf_hi(xv.y), bf_lo(xv.z), bf_hi(xv.z), bf_lo(xv.w), bf_hi(xv.w)};
#pragma unroll
    for (int j = 0; j < 8; j++) o[j] = xf[j] + bfr(mod[1024 + col + j] * bfr(o[j]));
    u32x4 rv = {pk2(o[0], o[1]), pk2(o[2], o[3]), pk2(o[4], o[5]), pk2(o[6], o[7])};
    *(u32x4*)(x1 + go) = rv;
  }
}

__global__ void __launch_bounds__(512, 2) k4_mlp1(const u16* __restrict__ b1, char* __restrict__ ws) {
  extern __shared__ __attribute__((aligned(16))) char lds[];
  const int b = blockIdx.x, tid = threadIdx.x, lane = tid & 63, w = tid >> 6;
  const int mt0 = b / 7, nb = b - mt0 * 7;
  const float* mod = (const float*)(ws + OFF_MOD);
  const u16* x1 = (const u16*)(ws + OFF_X1);
  const int n = lane & 15, g = lane >> 4;
  const f32x4 mv = *(const f32x4*)(mod + 1536 + (tid & 255) * 4);
  u32x4 xv[2][4];
#pragma unroll
  for (int it = 0; it < 2; it++)
#pragma unroll
    for (int j = 0; j < 4; j++) xv[it][j] = *(const u32x4*)(x1 + (size_t)(mt0 * 64 + it * 32 + w * 4 + g) * DM + (j * 16 + n) * 8);
  const u32x4* WF = (const u32x4*)(ws + OFF_W1F) + (size_t)(nb * 16 + w * 2) * 16 * 64 + lane;
  u32x4 bfm[2][16];
#pragma unroll
  for (int nt = 0; nt < 2; nt++)
#pragma unroll
    for (int ks = 0; ks < 8; ks++) bfm[nt][ks] = WF[(nt * 16 + ks) * 64];
  __builtin_amdgcn_sched_barrier(0);
  float* modl = (float*)(lds + 66560);
  if (tid < 256) *(f32x4*)(modl + tid * 4) = tid >= 128 ? mv + 1.f : mv;
  __syncthreads();
#pragma unroll
  for (int it = 0; it < 2; it++) {
    f32x2 f[16];
#pragma unroll
    for (int j = 0; j < 4; j++) {
      f[4 * j + 0] = (f32x2){bf_lo(xv[it][j].x), bf_hi(xv[it][j].x)}; f[4 * j + 1] = (f32x2){bf_lo(xv[it][j].y), bf_hi(xv[it][j].y)};
      f[4 * j + 2] = (f32x2){bf_lo(xv[it][j].z), bf_hi(xv[it][j].z)}; f[4 * j + 3] = (f32x2){bf_lo(xv[it][j].w), bf_hi(xv[it][j].w)};
    }
    f32x2 s2 = {0.f, 0.f};
#pragma unroll
    for (int i = 0; i < 16; i++) s2 += f[i];
    float m = row16_sum(s2.x + s2.y) * (1.f / DM);
    f32x2 q2 = {0.f, 0.f};
#pragma unroll
    for (int i = 0; i < 16; i++) { f[i] -= m; q2 += f[i] * f[i]; }
    float rs = rsqrtf(row16_sum(q2.x + q2.y) * (1.f / DM) + 1e-5f);
    s2 = (f32x2){0.f, 0.f};
#pragma unroll
    for (int j = 0; j < 4; j++) {
      const int c0 = (j * 16 + n) * 8;
      f32x4 c0v = *(const f32x4*)(modl + 512 + c0), c1v = *(const f32x4*)(modl + 512 + c0 + 4);
      f32x4 s0v = *(const f32x4*)(modl + c0), s1v = *(const f32x4*)(modl + c0 + 4);
      f[4 * j + 0] = f[4 * j + 0] * rs * (f32x2){c0v[0], c0v[1]} + (f32x2){s0v[0], s0v[1]};
      f[4 * j + 1] = f[4 * j + 1] * rs * (f32x2){c0v[2], c0v[3]} + (f32x2){s0v[2], s0v[3]};
      f[4 * j + 2] = f[4 * j + 2] * rs * (f32x2){c1v[0], c1v[1]} + (f32x2){s1v[0], s1v[1]};
      f[4 * j + 3] = f[4 * j + 3] * rs * (f32x2){c1v[2], c1v[3]} + (f32x2){s1v[2], s1v[3]};
      s2 += (f[4 * j + 0] + f[4 * j + 1]) + (f[4 * j + 2] + f[4 * j + 3]);
    }
    m = row16_sum(s2.x + s2.y) * (1.f / DM);
    q2 = (f32x2){0.f, 0.f};
#pragma unroll
    for (int i = 0; i < 16; i++) { f[i] -= m; q2 += f[i] * f[i]; }
    rs = rsqrtf(row16_sum(q2.x + q2.y) * (1.f / DM) + 1e-5f);
#pragma unroll
    for (int j = 0; j < 4; j++) {
      f32x2 o0 = f[4 * j] * rs, o1 = f[4 * j + 1] * rs, o2 = f[4 * j + 2] * rs, o3 = f[4 * j + 3] * rs;
      u32x4 o = {pk2(o0.x, o0.y), pk2(o1.x, o1.y), pk2(o2.x, o2.y), pk2(o3.x, o3.y)};
      *(u32x4*)(lds + (it * 32 + w * 4 + g) * 1040 + (j * 16 + n) * 16) = o;
    }
    __builtin_amdgcn_sched_barrier(0);
  }
#pragma unroll
  for (int nt = 0; nt < 2; nt++)
#pragma unroll
    for (int ks = 8; ks < 16; ks++) bfm[nt][ks] = WF[(nt * 16 + ks) * 64];
  __builtin_amdgcn_sched_barrier(0);
  __syncthreads();
  f32x4 acc[4][2];
#pragma unroll
  for (int i = 0; i < 4; i++)
#pragma unroll
    for (int j = 0; j < 2; j++) acc[i][j] = (f32x4){0.f, 0.f, 0.f, 0.f};
#pragma unroll
  for (int ks = 0; ks < 16; ks++) {
    bf16x8 af[4];
#pragma unroll
    for (int mt = 0; mt < 4; mt++) af[mt] = *(const bf16x8*)(lds + (16 * mt + n) * 1040 + (32 * ks + 8 * g) * 2);
#pragma unroll
    for (int mt = 0; mt < 4; mt++)
#pragma unroll
      for (int nt = 0; nt < 2; nt++) acc[mt][nt] = MFMA16(as_bf8(bfm[nt][ks]), af[mt], acc[mt][nt]);
    if (ks & 1) __builtin_amdgcn_sched_barrier(0);
  }
  u32x4* hidF = (u32x4*)(ws + OFF_HID);
  float bb[2][4];
#pragma unroll
  for (int nt = 0; nt < 2; nt++) {
    u32x2 bv = *(const u32x2*)(b1 + nb * 256 + w * 32 + 16 * nt + 4 * g);
    bb[nt][0] = bf_lo(bv.x); bb[nt][1] = bf_hi(bv.x); bb[nt][2] = bf_lo(bv.y); bb[nt][3] = bf_hi(bv.y);
  }
#pragma unroll
  for (int mt = 0; mt < 4; mt++) {
    float o[8];
#pragma unroll
    for (int nt = 0; nt < 2; nt++)
#pragma unroll
      for (int j = 0; j < 4; j++) {
        float u = bfr(bfr(acc[mt][nt][j]) + bb[nt][j]);
        float z = 0.7978845608028654f * (u + 0.044715f * u * u * u);
        float e = __builtin_amdgcn_exp2f(z * 2.8853900817779268f);
        float th = 1.f - 2.f * __builtin_amdgcn_rcpf(e + 1.f);
        o[4 * nt + j] = 0.5f * u * (1.f + th);
      }
    u32x4 ov = {pk2(o[0], o[1]), pk2(o[2], o[3]), pk2(o[4], o[5]), pk2(o[6], o[7])};
    hidF[((size_t)(mt0 * 4 + mt) * 56 + nb * 8 + w) * 64 + lane] = ov;
  }
}

__global__ void __launch_bounds__(512, 2) k5_mlp2(const u16* __restrict__ b2, char* __restrict__ ws, u16* __restrict__ out) {
  extern __shared__ __attribute__((aligned(16))) char lds[];
  const int b = blockIdx.x, tid = threadIdx.x, lane = tid & 63, w = tid >> 6;
  const int mt0 = (b & 7) * 4 + (b >> 6), nb = (b >> 3) & 7;
  const int n = lane & 15, g = lane >> 4;
  const u32x4* hidF = (const u32x4*)(ws + OFF_HID) + ((size_t)(mt0 * 4) * 56 + w * 7) * 64 + lane;
  const u32x4* WF = (const u32x4*)(ws + OFF_W2F) + ((size_t)(nb * 4) * 56 + w * 7) * 64 + lane;
  f32x4 acc[4][4];
#pragma unroll
  for (int i = 0; i < 4; i++)
#pragma unroll
    for (int j = 0; j < 4; j++) acc[i][j] = (f32x4){0.f, 0.f, 0.f, 0.f};
#pragma unroll
  for (int hb = 0; hb < 2; hb++) {
    const int nk = hb ? 3 : 4;
    u32x4 bfm[4][4]; bf16x8 af[4][4];
#pragma unroll
    for (int k = 0; k < 4; k++) if (k < nk) {
#pragma unroll
      for (int nt = 0; nt < 4; nt++) bfm[k][nt] = WF[(nt * 56 + hb * 4 + k) * 64];
#pragma unroll
      for (int mt = 0; mt < 4; mt++) af[k][mt] = as_bf8(hidF[(mt * 56 + hb * 4 + k) * 64]);
    }
    __builtin_amdgcn_sched_barrier(0);
#pragma unroll
    for (int k = 0; k < 4; k++) if (k < nk) {
#pragma unroll
      for (int mt = 0; mt < 4; mt++)
#pragma unroll
        for (int nt = 0; nt < 4; nt++) acc[mt][nt] = MFMA16(as_bf8(bfm[k][nt]), af[k][mt], acc[mt][nt]);
    }
  }
  float* red = (float*)lds;
#pragma unroll
  for (int mt = 0; mt < 4; mt++)
#pragma unroll
    for (int nt = 0; nt < 4; nt++) *(f32x4*)(red + (w * 64 + 16 * mt + n) * 68 + 16 * nt + 4 * g) = acc[mt][nt];
  __syncthreads();
  const float* mod = (const float*)(ws + OFF_MOD);
  const u16* x1 = (const u16*)(ws + OFF_X1);
  {
    const int r = tid >> 3, c8 = (tid & 7) * 8;
    float o[8];
#pragma unroll
    for (int j = 0; j < 8; j++) o[j] = 0.f;
#pragma unroll
    for (int ww = 0; ww < 8; ww++) {
      f32x4 a = *(const f32x4*)(red + (ww * 64 + r) * 68 + c8), bq = *(const f32x4*)(red + (ww * 64 + r) * 68 + c8 + 4);
#pragma unroll
      for (int j = 0; j < 4; j++) { o[j] += a[j]; o[4 + j] += bq[j]; }
    }
    const int col = 64 * nb + c8;
    const size_t go = (size_t)(mt0 * 64 + r) * DM + col;
    u32x4 xv = *(const u32x4*)(x1 + go), bv = *(const u32x4*)(b2 + col);
    float xf[8] = {bf_lo(xv.x), bf_hi(xv.x), bf_lo(xv.y), bf_hi(xv.y), bf_lo(xv.z), bf_hi(xv.z), bf_lo(xv.w), bf_hi(xv.w)};
    float bb[8] = {bf_lo(bv.x), bf_hi(bv.x), bf_lo(bv.y), bf_hi(bv.y), bf_lo(bv.z), bf_hi(bv.z), bf_lo(bv.w), bf_hi(bv.w)};
#pragma unroll
    for (int j = 0; j < 8; j++) o[j] = xf[j] + bfr(mod[2560 + col + j] * bfr(bfr(o[j]) + bb[j]));
    u32x4 rv = {pk2(o[0], o[1]), pk2(o[2], o[3]), pk2(o[4], o[5]), pk2(o[6], o[7])};
    *(u32x4*)(out + go) = rv;
  }
}

extern "C" void kernel_launch(void* const* d_in, const int* in_sizes, int n_in, void* d_out, int out_size, void* d_ws,
                              size_t ws_size, hipStream_t stream) {
  const u16* x = (const u16*)d_in[0];
  const u16* mem0 = (const u16*)d_in[1];
  const u16* Wq = (const u16*)d_in[2];
  const u16* Wk = (const u16*)d_in[3];
  const u16* Wv = (const u16*)d_in[4];
  const u16* Wo = (const u16*)d_in[5];
  const u16* w1 = (const u16*)d_in[8];
  const u16* b1 = (const u16*)d_in[9];
  const u16* w2 = (const u16*)d_in[10];
  const u16* b2 = (const u16*)d_in[11];
  const u16* cvec = (const u16*)d_in[12];
  const u16* wmod = (const u16*)d_in[13];
  char* ws = (char*)d_ws;
  hipFuncSetAttribute((const void*)k2_rec, hipFuncAttributeMaxDynamicSharedMemorySize, 135168);
  k0_prep<<<496, 256, 32768, stream>>>(Wv, Wo, cvec, wmod, mem0, Wq, Wk, x, ws);
  k2_rec<<<256, 256, 135168, stream>>>(Wq, Wk, Wv, Wo, x, w1, w2, ws);
  hipFuncSetAttribute((const void*)k3b_oproj, hipFuncAttributeMaxDynamicSharedMemorySize, 135168);
  k3b_oproj<<<256, 512, 135168, stream>>>(x, ws);
  hipFuncSetAttribute((const void*)k4_mlp1, hipFuncAttributeMaxDynamicSharedMemorySize, 70656);
  hipFuncSetAttribute((const void*)k5_mlp2, hipFuncAttributeMaxDynamicSharedMemorySize, 139264);
  k4_mlp1<<<224, 512, 70656, stream>>>(b1, ws);
  k5_mlp2<<<256, 512, 139264, stream>>>(b2, ws, (u16*)d_out);
}
```

```cpp
#include <hip/hip_runtime.h>
#include <hip/hip_bf16.h>
#include <stdint.h>

typedef unsigned int u32;
typedef unsigned short u16;
typedef unsigned long long u64;
typedef __attribute__((ext_vector_type(2))) __bf16 bf16x2;
typedef __attribute__((ext_vector_type(8))) __bf16 bf16x8;
typedef __attribute__((ext_vector_type(16))) __bf16 bf16x16;
typedef __attribute__((ext_vector_type(2))) float f32x2;
typedef __attribute__((ext_vector_type(4))) float f32x4;
typedef __attribute__((ext_vector_type(16))) float f32x16;
typedef __attribute__((ext_vector_type(2))) u32 u32x2;
typedef __attribute__((ext_vector_type(4))) u32 u32x4;
typedef __attribute__((ext_vector_type(8))) u32 u32x8;

#define NTOK 2048
#define DM 512
#define NH 32
#define DH 128
#define NSEG 64
#define FF 1792
#define SENT 0xFFFFFFFFu

#define OFF_FLAG 0ull
#define OFF_DONE 8192u
#define OFF_KMF 12288u
#define OFF_WVF 14336u
#define OFF_MDF 28672u
#define OFF_MOD 16384ull
#define OFF_MEMX 32768ull
#define OFF_PEX 0x90000ull
#define OFF_H (2ull << 20)
#define OFF_KMEM (4ull << 20)
#define OFF_WVO (8ull << 20)
#define OFF_KX (24ull << 20)
#define OFF_QX (40ull << 20)
#define OFF_Y (56ull << 20)
#define OFF_UF (72ull << 20)
#define OFF_PX (136ull << 20)
#define OFF_X1 (142ull << 20)
#define OFF_Y2 (146ull << 20)
#define OFF_HID (148ull << 20)
#define OFF_W1F (156ull << 20)
#define OFF_W2F (158ull << 20)
#define OFF_DBG (200ull << 20)
#define STAMP(slot) do { } while (0)

__device__ __forceinline__ u32 pk2(float a, float b) {
  bf16x2 v; v[0] = (__bf16)a; v[1] = (__bf16)b; return __builtin_bit_cast(u32, v);
}
__device__ __forceinline__ u32 f2u(float x) { return __builtin_bit_cast(u32, x); }
__device__ __forceinline__ float u2f(u32 x) { return __builtin_bit_cast(float, x); }
__device__ __forceinline__ float bf_lo(u32 x) { return __builtin_bit_cast(float, x << 16); }
__device__ __forceinline__ float bf_hi(u32 x) { return __builtin_bit_cast(float, x & 0xffff0000u); }
__device__ __forceinline__ float bfr(float a) { return (float)(__bf16)a; }
__device__ __forceinline__ __amdgpu_buffer_rsrc_t mkrsrc(const void* p) {
  return __builtin_amdgcn_make_buffer_rsrc((void*)p, 0, 0x7fffffff, 0x00020000);
}
__device__ __forceinline__ u32x4 ld16_sc1(__amdgpu_buffer_rsrc_t r, u32 off) { return __builtin_amdgcn_raw_buffer_load_b128(r, off, 0, 16); }
__device__ __forceinline__ u32 ld4_sc1(__amdgpu_buffer_rsrc_t r, u32 off) { return __builtin_amdgcn_raw_buffer_load_b32(r, off, 0, 16); }
__device__ __forceinline__ void st16_sc1(u32x4 v, __amdgpu_buffer_rsrc_t r, u32 off) { __builtin_amdgcn_raw_buffer_store_b128(v, r, off, 0, 16); }
__device__ __forceinline__ void st8_sc1(u32x2 v, __amdgpu_buffer_rsrc_t r, u32 off) { __builtin_amdgcn_raw_buffer_store_b64(v, r, off, 0, 16); }
__device__ __forceinline__ void st4_sc1(u32 v, __amdgpu_buffer_rsrc_t r, u32 off) { __builtin_amdgcn_raw_buffer_store_b32(v, r, off, 0, 16); }
__device__ __forceinline__ u32 badm(u32x4 a) { return (u32)(a.x == SENT) | (u32)(a.y == SENT) | (u32)(a.z == SENT) | (u32)(a.w == SENT); }
__device__ __forceinline__ u32 sane(u32 x) { return x < 0xFFFFFFFEu ? x : 0xFFFFFFFEu; }
__device__ __forceinline__ u32 now32() { return (u32)__builtin_amdgcn_s_memtime(); }
__device__ __forceinline__ void wait_until(u32 t) { while ((int)(t - now32()) > 0) { } }
__device__ __forceinline__ bf16x8 as_bf8(u32x4 v) { return __builtin_bit_cast(bf16x8, v); }
__device__ __forceinline__ bf16x16 as_bf16(u32x4 a, u32x4 b) {
  u32x8 v = {a.x, a.y, a.z, a.w, b.x, b.y, b.z, b.w}; return __builtin_bit_cast(bf16x16, v);
}
__device__ __forceinline__ float max3r(float a, float b, float c) { float r; asm("v_max3_f32 %0, %1, %2, %3" : "=v"(r) : "v"(a), "v"(b), "v"(c)); return r; }
__device__ __forceinline__ float max2r(float a, float b) { float r; asm("v_max_f32 %0, %1, %2" : "=v"(r) : "v"(a), "v"(b)); return r; }
#define MFMA16(a, b, c) __builtin_amdgcn_mfma_f32_16x16x32_bf16((a), (b), (c), 0, 0, 0)
#define MFMA32(a, b, c) __builtin_amdgcn_mfma_f32_32x32x16_bf16((a), (b), (c), 0, 0, 0)
#define SMF(a, b, c, idx) __builtin_amdgcn_smfmac_f32_16x16x64_bf16((a), (b), (c), (idx), 0, 0)

template <int N>
__device__ __forceinline__ u32 poll_chunks(__amdgpu_buffer_rsrc_t r, const u32 (&off)[N], u32x4 (&a)[N], u32 lane16, u32& tiss) {
  u32 spins = 0;
  while (true) {
    asm volatile("" : "+v"(lane16) :: "memory");
    if (spins) tiss = now32();
    u32 bad = 0;
#pragma unroll
    for (int j = 0; j < N; j++) a[j] = ld16_sc1(r, off[j] + lane16);
#pragma unroll
    for (int j = 0; j < N; j++) bad |= badm(a[j]);
    ++spins;
    if (!__any(bad)) return spins;
    if (spins > 400000u) return 0;
  }
}

typedef __attribute__((ext_vector_type(4))) short s16x4;
typedef __attribute__((address_space(3))) s16x4 lds_s16x4;
template <int ROWB>
__device__ __forceinline__ void dma_tile(char* lds, const u16* src, int rows, int gstride) {
  constexpr int LPR = ROWB / 16;
  const int total = rows * LPR;
  for (int u = threadIdx.x; u < total; u += 256) {
    const int r = u / LPR, c = u % LPR;
    __builtin_amdgcn_global_load_lds((const u32*)(src + (size_t)r * gstride + c * 8), (u32*)(lds + u * 16), 16, 0, 0);
  }
}
__device__ __forceinline__ void dma_wait() { asm volatile("s_waitcnt vmcnt(0)" ::: "memory"); __syncthreads(); }
__device__ __forceinline__ u32x2 tr4(const char* lds, int row0, int col0, int pitch, int lane) {
  const char* p = lds + (row0 + ((lane & 15) >> 2)) * pitch + (col0 + 4 * (lane & 3)) * 2;
  s16x4 v = __builtin_amdgcn_ds_read_tr16_b64_v4i16((lds_s16x4*)p);
  return __builtin_bit_cast(u32x2, v);
}
__device__ __forceinline__ bf16x8 frag8(const char* lds, int k0, int col0, int pitch, int lane) {
  u32x2 a = tr4(lds, k0, col0, pitch, lane), b = tr4(lds, k0 + 4, col0, pitch, lane);
  return as_bf8((u32x4){a.x, a.y, b.x, b.y});
}
__device__ __forceinline__ u32x4 frag8s(const char* lds, int k0, int col0, int pitch, int lane) {
  u32x2 a = tr4(lds, k0, col0, pitch, lane), b = tr4(lds, k0 + 8, col0, pitch, lane);
  return (u32x4){a.x, b.x, a.y, b.y};
}
#define PACK_S(acc, b) (u32x4){pk2((acc)[(b)], (acc)[(b) + 1]), pk2((acc)[(b) + 4], (acc)[(b) + 5]), pk2((acc)[(b) + 2], (acc)[(b) + 3]), pk2((acc)[(b) + 6], (acc)[(b) + 7])}

__device__ __forceinline__ void wvo_task(const u16* __restrict__ Wv, const u16* __restrict__ Wo, char* ws, char* lds, int task) {
  const int tid = threadIdx.x, lane = tid & 63, w = tid >> 6;
  const int h = task >> 2, nq = task & 3, x = lane & 31, hh = lane >> 5;
  dma_tile<256>(lds, Wo + (size_t)h * DH * DM + nq * 128, DH, DM);
  bf16x8 a[4][8];
#pragma unroll
  for (int mt = 0; mt < 4; mt++)
#pragma unroll
    for (int ks = 0; ks < 8; ks++)
      a[mt][ks] = *(const bf16x8*)(Wv + ((size_t)(128 * w + 32 * mt + x) * NH + h) * DH + 16 * ks + 8 * hh);
  dma_wait();
  const __amdgpu_buffer_rsrc_t rd = mkrsrc(ws + OFF_WVO);
#pragma unroll
  for (int nt = 0; nt < 4; nt++) {
    bf16x8 bw[8];
#pragma unroll
    for (int ks = 0; ks < 8; ks++) bw[ks] = frag8(lds, 16 * ks + 8 * hh, 32 * nt + (x & 16), 256, lane);
#pragma unroll
    for (int mt = 0; mt < 4; mt++) {
      f32x16 acc;
#pragma unroll
      for (int i = 0; i < 16; i++) acc[i] = 0.f;
#pragma unroll
      for (int ks = 0; ks < 8; ks++) acc = MFMA32(a[mt][ks], bw[ks], acc);
      const int d = 128 * nq + 32 * nt + x, c = d >> 3, cc = d & 7;
#pragma unroll
      for (int xb = 0; xb < 2; xb++) {
        const int dp = 128 * w + 32 * mt + 16 * xb, t = dp >> 6, ga = (dp >> 4) & 3, z = ga >> 1, gb = 2 * (ga & 1) + hh;
        const int lp = (h & 1) * 8 + cc + 16 * gb;
        u32 o = (u32)(((((c * 4 + (h >> 3)) * 4 + ((h >> 1) & 3)) * 8 + t) * 2 + z) * 64 + lp);
        st16_sc1(PACK_S(acc, 8 * xb), rd, o * 16u);
      }
    }
  }
  asm volatile("s_waitcnt vmcnt(0)" ::: "memory");
  __syncthreads();
  if (tid == 0) st4_sc1(1u, mkrsrc(ws + OFF_FLAG), OFF_WVF + task * 16u);
}

__device__ __forceinline__ void mod_cols(const u16* __restrict__ cvec, const u16* __restrict__ wmod, char* ws, char* lds, int col0) {
  const int tid = threadIdx.x, cp = tid & 15, ksl = tid >> 4;
  float* red = (float*)lds;
  u32 wv[32];
#pragma unroll
  for (int k = 0; k < 32; k++) wv[k] = *(const u32*)(wmod + (size_t)(ksl * 32 + k) * 3072 + col0 + cp * 2);
  float a0 = 0.f, a1 = 0.f;
#pragma unroll
  for (int k = 0; k < 32; k++) {
    float cv = bf_lo((u32)cvec[ksl * 32 + k]);
    float sv = bfr(cv / (1.f + __expf(-cv)));
    a0 += sv * bf_lo(wv[k]); a1 += sv * bf_hi(wv[k]);
  }
  __syncthreads();
  red[ksl * 32 + cp * 2] = a0; red[ksl * 32 + cp * 2 + 1] = a1;
  __syncthreads();
  if (tid < 32) {
    float sacc = 0.f;
#pragma unroll
    for (int k = 0; k < 16; k++) sacc += red[k * 32 + tid];
    st4_sc1(f2u(bfr(sacc)), mkrsrc(ws + OFF_MOD), (u32)(col0 + tid) * 4u);
  }
}

__device__ __forceinline__ float row16_sum(float v) {
  v += u2f((u32)__builtin_amdgcn_mov_dpp((int)f2u(v), 0x128, 0xF, 0xF, true));
  v += u2f((u32)__builtin_amdgcn_mov_dpp((int)f2u(v), 0x124, 0xF, 0xF, true));
  v += u2f((u32)__builtin_amdgcn_mov_dpp((int)f2u(v), 0x122, 0xF, 0xF, true));
  v += u2f((u32)__builtin_amdgcn_mov_dpp((int)f2u(v), 0x121, 0xF, 0xF, true));
  return v;
}

__device__ __forceinline__ bool wait_progress(__amdgpu_buffer_rsrc_t rf, u32 off, u32 need, u32& seen) {
  u32 spins = 0;
  if (__all((int)seen >= (int)need)) return true;
  while (true) {
    asm volatile("" : "+v"(off) :: "memory");
    seen = ld4_sc1(rf, off);
    if (__all((int)seen >= (int)need)) return true;
    __builtin_amdgcn_s_sleep(2);
    if (++spins > 400000u) return false;
  }
}

__device__ __forceinline__ void px_task(char* ws, int s, int hg);
__device__ __forceinline__ void role_help(const u16* __restrict__ x, const u16* __restrict__ w1, const u16* __restrict__ w2,
                                          const u16* __restrict__ cvec, const u16* __restrict__ wmod, const u16* __restrict__ mem0,
                                          char* ws, char* lds, int a0) {
  const int tid = threadIdx.x, lane = tid & 63, w = tid >> 6, n = lane & 15, g = lane >> 4;
  if (a0 == 31) {
    for (int i = tid; i < 512; i += 256) {
      u32x4 v = *(const u32x4*)(mem0 + (i & 7) * DM + (i >> 3) * 8);
      v.x = sane(v.x); v.y = sane(v.y); v.z = sane(v.z); v.w = sane(v.w);
      st16_sc1(v, mkrsrc(ws + OFF_MEMX), (u32)i * 16u);
    }
  }
  mod_cols(cvec, wmod, ws, lds, a0 * 32);
  asm volatile("s_waitcnt vmcnt(0)" ::: "memory");
  __syncthreads();
  if (tid == 0) st4_sc1(1u, mkrsrc(ws + OFF_FLAG), OFF_MDF + a0 * 16u);
  { u32 seenm = 0xFFFFFFFFu; if (!wait_progress(mkrsrc(ws + OFF_FLAG), OFF_MDF + (lane & 31) * 16u, 1u, seenm)) return; }
  float* modl = (float*)(lds + 4096);
  *(u32x4*)(modl + tid * 4) = ld16_sc1(mkrsrc(ws + OFF_MOD), (u32)tid * 16u);
  __syncthreads();
  for (int a = a0; a < 64; a += 32) {
    const float* mod = modl;
    const __amdgpu_buffer_rsrc_t rh = mkrsrc(ws + OFF_H);
    u32x4 xv[2][4];
#pragma unroll
    for (int it = 0; it < 2; it++)
#pragma unroll
      for (int j = 0; j < 4; j++) xv[it][j] = *(const u32x4*)(x + (size_t)(a * 32 + it * 16 + w * 4 + g) * DM + (j * 16 + n) * 8);
#pragma unroll
    for (int it = 0; it < 2; it++) {
      float f[32];
#pragma unroll
      for (int j = 0; j < 4; j++) {
        f[8 * j + 0] = bf_lo(xv[it][j].x); f[8 * j + 1] = bf_hi(xv[it][j].x); f[8 * j + 2] = bf_lo(xv[it][j].y); f[8 * j + 3] = bf_hi(xv[it][j].y);
        f[8 * j + 4] = bf_lo(xv[it][j].z); f[8 * j + 5] = bf_hi(xv[it][j].z); f[8 * j + 6] = bf_lo(xv[it][j].w); f[8 * j + 7] = bf_hi(xv[it][j].w);
      }
      float sm = 0.f;
#pragma unroll
      for (int i = 0; i < 32; i++) sm += f[i];
      const float m = row16_sum(sm) * (1.f / DM);
      float q = 0.f;
#pragma unroll
      for (int i = 0; i < 32; i++) { f[i] -= m; q += f[i] * f[i]; }
      const float rs = rsqrtf(row16_sum(q) * (1.f / DM) + 1e-5f);
#pragma unroll
      for (int j = 0; j < 4; j++) {
        const int c0 = (j * 16 + n) * 8;
        f32x4 c0v = *(const f32x4*)(mod + 512 + c0), c1v = *(const f32x4*)(mod + 512 + c0 + 4);
        f32x4 s0v = *(const f32x4*)(mod + c0), s1v = *(const f32x4*)(mod + c0 + 4);
        float o[8];
#pragma unroll
        for (int i = 0; i < 4; i++) {
          o[i] = f[8 * j + i] * rs * (1.f + c0v[i]) + s0v[i];
          o[4 + i] = f[8 * j + 4 + i] * rs * (1.f + c1v[i]) + s1v[i];
        }
        u32x4 r = {pk2(o[0], o[1]), pk2(o[2], o[3]), pk2(o[4], o[5]), pk2(o[6], o[7])};
        st16_sc1(r, rh, (u32)((a * 32 + it * 16 + w * 4 + g) * DM + c0) * 2u);
      }
    }
    asm volatile("s_waitcnt vmcnt(0)" ::: "memory");
    __syncthreads();
    if (tid == 0) st4_sc1(1u, mkrsrc(ws + OFF_FLAG), OFF_DONE + a * 64u);
  }
  const int a = a0;
  mod_cols(cvec, wmod, ws, lds, 1024 + a * 64);
  mod_cols(cvec, wmod, ws, lds, 1024 + a * 64 + 32);
  __syncthreads();
  if (a < 28) {
    dma_tile<128>(lds, w1 + a * 64, DM, FF);
    dma_wait();
    u32x4* dst = (u32x4*)(ws + OFF_W1F);
    for (int ks = 0; ks < 16; ks++) {
      bf16x8 v = frag8(lds, 32 * ks + 8 * (lane >> 4), 16 * w, 128, lane);
      dst[((size_t)(a * 4 + w) * 16 + ks) * 64 + lane] = __builtin_bit_cast(u32x4, v);
    }
  }
  __syncthreads();
  {
    const int cb = a >> 2, kq = a & 3;
    dma_tile<128>(lds, w2 + (size_t)kq * 448 * DM + cb * 64, 448, DM);
    dma_wait();
    u32x4* dst = (u32x4*)(ws + OFF_W2F);
    for (int ks = 0; ks < 14; ks++) {
      u32x2 va = tr4(lds, 32 * ks + 4 * (lane >> 4), 16 * w, 128, lane), vb = tr4(lds, 32 * ks + 16 + 4 * (lane >> 4), 16 * w, 128, lane);
      dst[((size_t)(cb * 4 + w) * 56 + kq * 14 + ks) * 64 + lane] = (u32x4){va.x, va.y, vb.x, vb.y};
    }
  }
  {
    const __amdgpu_buffer_rsrc_t rflag = mkrsrc(ws + OFF_FLAG);
    const int hg = a & 7;
    for (int s = a >> 3; s < NSEG; s += 4) {
      u32 spins = 0, off = OFF_KMF + (u32)(hg * 4 + w) * 64u;
      while (true) {
        asm volatile("" : "+v"(off) :: "memory");
        u32 v = ld4_sc1(rflag, off);
        if ((int)v > s) break;
        __builtin_amdgcn_s_sleep(16);
        if (++spins > 400000u) break;
      }
      px_task(ws, s, hg);
    }
  }
}

#define AD_DEC 8
#define AD_INC 100
#define D1S_INIT 3000
#define D1C_INIT 600
#define D2C_INIT 2800
#define ADAPT(D, n) do { if ((n) == 1) D -= AD_DEC; else D += AD_INC; if (D < 0) D = 0; } while (0)

__device__ __forceinline__ void role_score(const u16* __restrict__ Wq, const u16* __restrict__ Wk, char* ws,
                                           char* lds, int h) {
  const int tid = threadIdx.x, lane = tid & 63, w = tid >> 6;
  const int n = lane & 15, gb = lane >> 4;
  bf16x16 wf[4][8];
#pragma unroll
  for (int mat = 0; mat < 2; mat++) {
    __syncthreads();
    dma_tile<256>(lds, (mat ? Wk : Wq) + h * DH, DM, NH * DH);
    dma_wait();
#pragma unroll
    for (int tt = 0; tt < 2; tt++)
#pragma unroll
      for (int t = 0; t < 8; t++) {
        const int col = 32 * w + 16 * tt;
        u32x4 lo = frag8s(lds, 64 * t + 16 * (gb >> 1) + 4 * (gb & 1), col, 256, lane);
        u32x4 hi = frag8s(lds, 64 * t + 16 * (2 + (gb >> 1)) + 4 * (gb & 1), col, 256, lane);
        wf[mat * 2 + tt][t] = as_bf16(lo, hi);
      }
  }
  __syncthreads();
  char* memL = lds;
  char* qs = lds + 8192;
  char* ks_ = lds + 8192 + 2304;
  const int idx = (lane & 8) ? (int)0xEEEEEEEE : 0x44444444;
  const __amdgpu_buffer_rsrc_t rflag = mkrsrc(ws + OFF_FLAG);
  const __amdgpu_buffer_rsrc_t rkx = mkrsrc(ws + OFF_KX);
  const __amdgpu_buffer_rsrc_t rmem = mkrsrc(ws + OFF_MEMX);
  const __amdgpu_buffer_rsrc_t rpex = mkrsrc(ws + OFF_PEX);
  const int rho = lane & 15;
  const int keyp = 8 * ((rho >> 2) & 1) + 4 * (rho >> 3) + (rho & 3);
  const u32 prho = (u32)((n & 7) >> 2) * 10240u, pi = n & 3;
  const u32 pxoff = prho + (u32)(((h >> 2) * 64) + 16 * (2 * (h & 1) + (gb >> 1)) + 8 * (gb & 1) + 4 * ((h >> 1) & 1) + pi) * 16u + (n >> 3) * 8u;
  const u32 pmoff = prho + 8192u + (u32)(h * 4 + pi) * 16u + (gb & 1) * 8u + (gb >> 1) * 4u;
  int D1 = w == 0 ? 3400 : (w == 1 ? 3300 : 4050); u32 tref = now32() - 8000u, seen = 0xFFFFFFFFu;
  STAMP(1);
  for (int s = 0; s < NSEG; s++) {
    u32x4 kx[2][4];
    if (w == 0) {
      if (!wait_progress(rflag, 4096u + h * 128u, s + 1, seen)) return;
#pragma unroll
      for (int tt = 0; tt < 2; tt++)
#pragma unroll
        for (int k4 = 0; k4 < 4; k4++)
          kx[tt][k4] = ld16_sc1(rkx, (u32)(((32 * s + 16 * tt + keyp) * NH + h) * DH + 32 * k4 + 8 * gb) * 2u);
    }
    f32x4 acc[4];
#pragma unroll
    for (int i = 0; i < 4; i++) { acc[i] = (f32x4){0.f, 0.f, 0.f, 0.f}; asm volatile("" : "+v"(acc[i])); }
    wait_until(tref + D1);
    {
      u32 off[2] = {(u32)s * 8192u + w * 2048u, (u32)s * 8192u + w * 2048u + 1024u};
      u32x4 a[2];
      u32 tiss = 0;
      u32 nsw = poll_chunks<2>(rmem, off, a, lane * 16u, tiss);
      if (!nsw) return;
      if (s > 2) ADAPT(D1, nsw);
      *(u32x4*)(memL + w * 2048 + lane * 16) = a[0];
      *(u32x4*)(memL + w * 2048 + 1024 + lane * 16) = a[1];
    }
    __syncthreads();
    STAMP(16 + 4 * s);
    bf16x8 af[8];
#pragma unroll
    for (int t = 0; t < 8; t++) af[t] = *(const bf16x8*)(memL + t * 1024 + lane * 16);
    __builtin_amdgcn_sched_barrier(0);
    const int ecol = 32 * w + 16 * (lane >> 5) + n, er0 = 4 * ((lane >> 4) & 1);
#pragma unroll
    for (int t = 0; t < 8; t++) { acc[0] = SMF(af[t], wf[0][t], acc[0], idx); acc[1] = SMF(af[t], wf[1][t], acc[1], idx); }
    __builtin_amdgcn_sched_barrier(0);
#pragma unroll
    for (int t = 0; t < 8; t++) {
      acc[2] = SMF(af[t], wf[2][t], acc[2], idx); acc[3] = SMF(af[t], wf[3][t], acc[3], idx);
      if (t >= 2 && t < 6) {
        const int j = t - 2;
        auto r = __builtin_amdgcn_permlane32_swap(f2u(acc[0][j]), f2u(acc[1][j]), false, false);
        *(__bf16*)(qs + (er0 + j) * 272 + ecol * 2) = (__bf16)(u2f(r[0]) + u2f(r[1]));
      }
      __builtin_amdgcn_sched_barrier(0);
    }
#pragma unroll
    for (int j = 0; j < 4; j++) {
      auto r = __builtin_amdgcn_permlane32_swap(f2u(acc[2][j]), f2u(acc[3][j]), false, false);
      *(__bf16*)(ks_ + (er0 + j) * 272 + ecol * 2) = (__bf16)(u2f(r[0]) + u2f(r[1]));
    }
    __syncthreads();
    STAMP(17 + 4 * s);
    if (w == 0) {
      f32x4 sc[3];
#pragma unroll
      for (int i = 0; i < 3; i++) { sc[i] = (f32x4){0.f, 0.f, 0.f, 0.f}; asm volatile("" : "+v"(sc[i])); }
      bf16x8 qf[4], kf[4];
#pragma unroll
      for (int k4 = 0; k4 < 4; k4++) {
        qf[k4] = *(const bf16x8*)(qs + (lane & 7) * 272 + (32 * k4 + 8 * gb) * 2);
        kf[k4] = *(const bf16x8*)(ks_ + (lane & 7) * 272 + (32 * k4 + 8 * gb) * 2);
      }
      __builtin_amdgcn_sched_barrier(0);
#pragma unroll
      for (int k4 = 0; k4 < 4; k4++) {
        sc[0] = MFMA16(as_bf8(kx[0][k4]), qf[k4], sc[0]);
        sc[1] = MFMA16(as_bf8(kx[1][k4]), qf[k4], sc[1]);
        sc[2] = MFMA16(kf[k4], qf[k4], sc[2]);
      }
      const float sl = 0.08838834764831845f * 1.4426950408889634f;
      const bool hiq = n >= 8, hig = gb >= 2;
      float p[6];
#pragma unroll
      for (int j = 0; j < 4; j++) p[j] = hiq ? sc[1][j] : sc[0][j];
      p[4] = hig ? sc[2][2] : sc[2][0];
      p[5] = hig ? sc[2][3] : sc[2][1];
      float mx = max2r(max3r(p[0], p[1], p[2]), max3r(p[3], p[4], p[5]));
      mx = max2r(mx, u2f((u32)__builtin_amdgcn_mov_dpp((int)f2u(mx), 0x128, 0xF, 0xF, true)));
      { auto r = __builtin_amdgcn_permlane16_swap(f2u(mx), f2u(mx), false, false);
        mx = max2r(u2f(r[0]), u2f(r[1])); }
      { auto r = __builtin_amdgcn_permlane32_swap(f2u(mx), f2u(mx), false, false);
        mx = max2r(u2f(r[0]), u2f(r[1])); }
      const float nb = -mx * sl;
#pragma unroll
      for (int j = 0; j < 6; j++) p[j] = __builtin_amdgcn_exp2f(__builtin_fmaf(p[j], sl, nb));
      float sum = (p[0] + p[1]) + (p[2] + p[3]) + (hiq ? 0.f : (p[4] + p[5]));
      sum += u2f((u32)__builtin_amdgcn_mov_dpp((int)f2u(sum), 0x128, 0xF, 0xF, true));
      { auto r = __builtin_amdgcn_permlane16_swap(f2u(sum), f2u(sum), false, false);
        sum = u2f(r[0]) + u2f(r[1]); }
      { auto r = __builtin_amdgcn_permlane32_swap(f2u(sum), f2u(sum), false, false);
        sum = u2f(r[0]) + u2f(r[1]); }
      const float inv = __builtin_amdgcn_rcpf(sum);
      u32x2 px = {sane(pk2(p[0] * inv, p[1] * inv)), sane(pk2(p[2] * inv, p[3] * inv))};
      st8_sc1(px, rpex, (u32)s * 20480u + pxoff);
      if (!hiq) st4_sc1(sane(pk2(p[4] * inv, p[5] * inv)), rpex, (u32)s * 20480u + pmoff);
      STAMP(18 + 4 * s);
    } else if (w == 1) {
      const __amdgpu_buffer_rsrc_t rkm = mkrsrc(ws + OFF_KMEM);
#pragma unroll
      for (int j = 0; j < 2; j++) {
        int u = j * 64 + lane, r = u >> 4, c = u & 15;
        st16_sc1(*(const u32x4*)(ks_ + r * 272 + c * 16), rkm, (u32)((s * NH + h) * 1024 + r * 128 + c * 8) * 2u);
      }
      asm volatile("s_waitcnt vmcnt(0)" ::: "memory");
      if (lane == 0) st4_sc1((u32)(s + 1), rflag, OFF_KMF + h * 64u);
    }
    tref = now32();
  }
}

__device__ __forceinline__ void role_column(const u16* __restrict__ Wv, const u16* __restrict__ Wo, char* ws, char* lds, int c, int rho) {
  const int tid = threadIdx.x, lane = tid & 63, w = tid >> 6;
  const int n = lane & 15, gb = lane >> 4, cc = lane & 7;
  bf16x16 wf[4][8];
  wvo_task(Wv, Wo, ws, lds, c * 2 + rho);
  {
    u32 seenw = 0xFFFFFFFFu;
    if (!wait_progress(mkrsrc(ws + OFF_FLAG), OFF_WVF + (u32)((lane & 31) * 4 + (c >> 4)) * 16u, 1u, seenw)) return;
    const __amdgpu_buffer_rsrc_t rsrc = mkrsrc(ws + OFF_WVO + (size_t)(c * 4 + w) * 65536);
#pragma unroll
    for (int tl = 0; tl < 4; tl++)
#pragma unroll
      for (int t = 0; t < 8; t++)
        wf[tl][t] = as_bf16(ld16_sc1(rsrc, (u32)(((tl * 8 + t) * 2) * 64 + lane) * 16u), ld16_sc1(rsrc, (u32)(((tl * 8 + t) * 2 + 1) * 64 + lane) * 16u));
  }
  char* memL = lds;
  char* yl = lds + 8192 + w * 1024;
  float* red = (float*)(lds + 12288);
  const int idx = (lane & 8) ? (int)0xEEEEEEEE : 0x44444444;
  const __amdgpu_buffer_rsrc_t rflag = mkrsrc(ws + OFF_FLAG);
  const __amdgpu_buffer_rsrc_t rmem = mkrsrc(ws + OFF_MEMX);
  const __amdgpu_buffer_rsrc_t rpex = mkrsrc(ws + OFF_PEX);
  int D1 = w == 0 ? 750 : (w == 1 ? 850 : 1130), D2 = 2800; u32 tref = now32(), seen = 0xFFFFFFFFu;
  STAMP(1);
  for (int s = 0; s < NSEG; s++) {
    wait_until(tref + D1);
    {
      u32 off[2] = {(u32)s * 8192u + w * 2048u, (u32)s * 8192u + w * 2048u + 1024u};
      u32x4 a[2];
      u32 tiss = 0;
      u32 nsw = poll_chunks<2>(rmem, off, a, lane * 16u, tiss);
      if (!nsw) return;
      if (s > 2) ADAPT(D1, nsw);
      tref = now32();
      *(u32x4*)(memL + w * 2048 + lane * 16) = a[0];
      *(u32x4*)(memL + w * 2048 + 1024 + lane * 16) = a[1];
    }
    u32x4 uf[4][2];
    {
      if (!wait_progress(rflag, (u32)(8 * w + (lane & 7)) * 128u, s + 1, seen)) return;
      const __amdgpu_buffer_rsrc_t ru = mkrsrc(ws + OFF_UF + ((size_t)s * 64 + c) * 16384);
#pragma unroll
      for (int m = 0; m < 4; m++)
#pragma unroll
        for (int z = 0; z < 2; z++) uf[m][z] = ld16_sc1(ru, (u32)((((4 * w + m) * 2 + z) * 4 + gb) * 8 + cc) * 16u);
    }
    __syncthreads();
    STAMP(16 + 4 * s);
    bf16x16 yb;
    {
      f32x4 acc[4];
#pragma unroll
      for (int i = 0; i < 4; i++) acc[i] = (f32x4){0.f, 0.f, 0.f, 0.f};
      bf16x8 af[8];
#pragma unroll
      for (int t = 0; t < 8; t++) af[t] = *(const bf16x8*)(memL + t * 1024 + lane * 16);
      __builtin_amdgcn_sched_barrier(0);
#pragma unroll
      for (int t = 0; t < 8; t++)
#pragma unroll
        for (int i = 0; i < 4; i++) acc[i] = SMF(af[t], wf[i][t], acc[i], idx);
#pragma unroll
      for (int pr = 0; pr < 2; pr++) {
        float v[4];
#pragma unroll
        for (int j = 0; j < 4; j++) {
          auto r = __builtin_amdgcn_permlane32_swap(f2u(acc[2 * pr][j]), f2u(acc[2 * pr + 1][j]), false, false);
          v[j] = u2f(r[0]) + u2f(r[1]);
        }
        const int hl = 2 * (2 * pr + (lane >> 5)) + (n >> 3);
        u32x2 pv = {pk2(v[0], v[1]), pk2(v[2], v[3])};
        *(u32x2*)(yl + (hl * 8 + cc) * 16 + 8 * ((lane >> 4) & 1)) = pv;
      }
      asm volatile("s_waitcnt lgkmcnt(0)" ::: "memory");
      u32x4 hz[2];
#pragma unroll
      for (int z = 0; z < 2; z++) {
        const int ga = 2 * z + (gb >> 1);
        u32x2 a = *(const u32x2*)(yl + ((2 * ga) * 8 + cc) * 16 + 8 * (gb & 1));
        u32x2 b = *(const u32x2*)(yl + ((2 * ga + 1) * 8 + cc) * 16 + 8 * (gb & 1));
        hz[z] = (u32x4){a.x, b.x, a.y, b.y};
      }
      yb = as_bf16(hz[0], hz[1]);
      if (rho == 0) __builtin_amdgcn_raw_buffer_store_b128(*(const u32x4*)(yl + lane * 16), mkrsrc(ws + OFF_Y), (u32)(s * 64 + c) * 4096u + w * 1024u + lane * 16u, 0, 0);
    }
    STAMP(17 + 4 * s);
    wait_until(tref + D2);
    {
      const u32 pb = (u32)s * 20480u + rho * 10240u;
      u32 off[3] = {pb + (2 * w) * 1024u + lane * 16u, pb + (2 * w + 1) * 1024u + lane * 16u,
                    pb + 8192u + (u32)((8 * w + 2 * gb + ((lane >> 3) & 1)) * 4 + (lane & 3)) * 16u};
      u32x4 g[3];
      u32 tiss = 0;
      u32 nsw = poll_chunks<3>(rpex, off, g, 0u, tiss);
      if (!nsw) return;
      if (s > 2) ADAPT(D2, nsw);
      f32x4 dA = {0.f, 0.f, 0.f, 0.f}, dB = {0.f, 0.f, 0.f, 0.f};
      dA = SMF(as_bf8(g[0]), as_bf16(uf[0][0], uf[0][1]), dA, idx);
      dB = SMF(as_bf8(g[0]), as_bf16(uf[1][0], uf[1][1]), dB, idx);
      dA = SMF(as_bf8(g[1]), as_bf16(uf[2][0], uf[2][1]), dA, idx);
      dB = SMF(as_bf8(g[1]), as_bf16(uf[3][0], uf[3][1]), dB, idx);
      dA = SMF(as_bf8(g[2]), yb, dA, idx);
      float* rd = red + (s & 1) * 512;
      if (n < 8) {
#pragma unroll
        for (int j = 0; j < 4; j++) rd[(j * 8 + cc) * 16 + 4 * w + gb] = (gb & 1) ? dB[j] : dA[j];
      }
    }
    __syncthreads();
    STAMP(18 + 4 * s);
    if (w == 0) {
      const float* rd = red + (s & 1) * 512 + lane * 8;
      f32x4 a = *(const f32x4*)rd, b = *(const f32x4*)(rd + 4);
      float v = ((a[0] + a[1]) + (a[2] + a[3])) + ((b[0] + b[1]) + (b[2] + b[3]));
      v += u2f((u32)__builtin_amdgcn_mov_dpp((int)f2u(v), 0xB1, 0xF, 0xF, true));
      float o = u2f((u32)__builtin_amdgcn_mov_dpp((int)f2u(v), 0x4E, 0xF, 0xF, true));
      if (!(lane & 3)) st4_sc1(sane(pk2(v, o)), rmem, (u32)(s + 1) * 8192u + c * 128u + rho * 64u + (lane >> 2) * 4u);
      STAMP(19 + 4 * s);
    }
    tref = now32();
  }
}

__device__ __forceinline__ void load_h(u32x4 (&r)[8], const char* ws, int s) {
  const __amdgpu_buffer_rsrc_t rh = mkrsrc(ws + OFF_H);
#pragma unroll
  for (int j = 0; j < 8; j++) r[j] = ld16_sc1(rh, (u32)s * 32768u + (j * 256 + threadIdx.x) * 16u);
}
__device__ __forceinline__ void put_h(char* dst, const u32x4 (&r)[8]) {
#pragma unroll
  for (int j = 0; j < 8; j++) {
    int u = j * 256 + threadIdx.x;
    *(u32x4*)(dst + (u >> 6) * 1040 + (u & 63) * 16) = r[j];
  }
}

template <bool SC1>
__device__ __forceinline__ void store_T_tile(const f32x16& acc, char* base, int tokrow, int h, int w, int lane) {
  const __amdgpu_buffer_rsrc_t r = mkrsrc(base);
#pragma unroll
  for (int qp = 0; qp < 2; qp++) {
    u32 ax = pk2(acc[8 * qp + 0], acc[8 * qp + 1]), ay = pk2(acc[8 * qp + 2], acc[8 * qp + 3]);
    u32 bx = pk2(acc[8 * qp + 4], acc[8 * qp + 5]), by = pk2(acc[8 * qp + 6], acc[8 * qp + 7]);
    auto r0 = __builtin_amdgcn_permlane32_swap(ax, bx, false, false);
    auto r1 = __builtin_amdgcn_permlane32_swap(ay, by, false, false);
    u32x4 v = {(u32)r0[0], (u32)r1[0], (u32)r0[1], (u32)r1[1]};
    u32 off = (u32)(((tokrow + (lane & 31)) * NH + h) * DH + 32 * w + 16 * qp + 8 * (lane >> 5)) * 2u;
    if (SC1) __builtin_amdgcn_raw_buffer_store_b128(v, r, off, 0, 16);
    else __builtin_amdgcn_raw_buffer_store_b128(v, r, off, 0, 0);
  }
}

__device__ __forceinline__ void role_vu(const u16* __restrict__ Wv, const u16* __restrict__ Wo,
                                        char* ws, char* lds, int h) {
  const int tid = threadIdx.x, lane = tid & 63, w = tid >> 6, x = lane & 31, hh = lane >> 5;
  bf16x8 wv[32], wo[4][8];
  dma_tile<256>(lds, Wv + h * DH, DM, NH * DH);
  dma_wait();
#pragma unroll
  for (int ks = 0; ks < 32; ks++) wv[ks] = frag8(lds, 16 * ks + 8 * hh, 32 * w + (x & 16), 256, lane);
  __syncthreads();
  dma_tile<1024>(lds, Wo + (size_t)h * DH * DM, DH, DM);
  dma_wait();
#pragma unroll
  for (int nt = 0; nt < 4; nt++)
#pragma unroll
    for (int ks = 0; ks < 8; ks++) wo[nt][ks] = frag8(lds, 16 * ks + 8 * hh, 128 * w + 32 * nt + (x & 16), 1024, lane);
  __syncthreads();
  char* vs = lds + 66560;
  const __amdgpu_buffer_rsrc_t rflag = mkrsrc(ws + OFF_FLAG);
  u32x4 hr[8];
  { u32 seenh = 0xFFFFFFFFu; if (!wait_progress(rflag, OFF_DONE + (lane & 31) * 64u, 1u, seenh)) return; }
  load_h(hr, ws, 0);
  put_h(lds, hr);
  load_h(hr, ws, 1);
  __syncthreads();
  for (int s = 0; s < NSEG; s++) {
    const char* hb = lds + (s & 1) * 33280;
    f32x16 av;
#pragma unroll
    for (int i = 0; i < 16; i++) av[i] = 0.f;
    {
      bf16x8 hf[2][8];
#pragma unroll
      for (int k = 0; k < 8; k++) hf[0][k] = *(const bf16x8*)(hb + x * 1040 + (16 * k + 8 * hh) * 2);
#pragma unroll
      for (int ch = 0; ch < 4; ch++) {
        if (ch < 3) {
#pragma unroll
          for (int k = 0; k < 8; k++) hf[(ch + 1) & 1][k] = *(const bf16x8*)(hb + x * 1040 + (16 * (8 * ch + 8 + k) + 8 * hh) * 2);
        }
        __builtin_amdgcn_sched_barrier(0);
#pragma unroll
        for (int k = 0; k < 8; k++) av = MFMA32(wv[8 * ch + k], hf[ch & 1][k], av);
        __builtin_amdgcn_sched_barrier(0);
      }
    }
#pragma unroll
    for (int q = 0; q < 4; q++) {
      u32x2 pv = {pk2(av[4 * q], av[4 * q + 1]), pk2(av[4 * q + 2], av[4 * q + 3])};
      *(u32x2*)(vs + x * 272 + (32 * w + 8 * q + 4 * hh) * 2) = pv;
    }
    put_h(lds + ((s + 1) & 1) * 33280, hr);
    asm volatile("s_waitcnt vmcnt(0)" ::: "memory");
    __syncthreads();
    if (tid == 0 && s > 4) st4_sc1((u32)s, rflag, h * 128u);
    bf16x8 vf[8];
#pragma unroll
    for (int ks = 0; ks < 8; ks++) vf[ks] = *(const bf16x8*)(vs + x * 272 + (16 * ks + 8 * hh) * 2);
    const __amdgpu_buffer_rsrc_t ru = mkrsrc(ws + OFF_UF + (size_t)s * 64 * 16384);
#pragma unroll
    for (int nt = 0; nt < 4; nt++) {
      f32x16 au;
#pragma unroll
      for (int i = 0; i < 16; i++) au[i] = 0.f;
#pragma unroll
      for (int ks = 0; ks < 8; ks++) au = MFMA32(vf[ks], wo[nt][ks], au);
      const int d = 128 * w + 32 * nt + x, c = d >> 3, cc = d & 7;
#pragma unroll
      for (int xb = 0; xb < 2; xb++)
        st16_sc1(PACK_S(au, 8 * xb), ru, (u32)((((c * 16 + (h >> 1)) * 2 + (h & 1)) * 4 + 2 * hh + xb) * 8 + cc) * 16u);
    }
    if (s < 4 || s == NSEG - 1) asm volatile("s_waitcnt vmcnt(0)" ::: "memory");
    if (s == 30) { u32 seenh = 0xFFFFFFFFu; if (!wait_progress(rflag, OFF_DONE + lane * 64u, 1u, seenh)) return; }
    load_h(hr, ws, s + 2 < NSEG ? s + 2 : NSEG - 1);
    __syncthreads();
    if (tid == 0 && (s < 4 || s == NSEG - 1)) st4_sc1((u32)(s + 1), rflag, h * 128u);
    STAMP(16 + 4 * s);
  }
}

__device__ __forceinline__ void role_qk(const u16* __restrict__ Wq, const u16* __restrict__ Wk, char* ws, char* lds, int h) {
  const int tid = threadIdx.x, lane = tid & 63, w = tid >> 6, x = lane & 31, hh = lane >> 5;
  bf16x8 wq[32], wk[32];
  dma_tile<256>(lds, Wk + h * DH, DM, NH * DH);
  dma_wait();
#pragma unroll
  for (int ks = 0; ks < 32; ks++) wk[ks] = frag8(lds, 16 * ks + 8 * hh, 32 * w + (x & 16), 256, lane);
  __syncthreads();
  dma_tile<256>(lds, Wq + h * DH, DM, NH * DH);
  dma_wait();
#pragma unroll
  for (int ks = 0; ks < 32; ks++) wq[ks] = frag8(lds, 16 * ks + 8 * hh, 32 * w + (x & 16), 256, lane);
  __syncthreads();
  const __amdgpu_buffer_rsrc_t rflag = mkrsrc(ws + OFF_FLAG);
  u32x4 hr[8];
  { u32 seenh = 0xFFFFFFFFu; if (!wait_progress(rflag, OFF_DONE + (lane & 31) * 64u, 1u, seenh)) return; }
  load_h(hr, ws, 0);
  put_h(lds, hr);
  load_h(hr, ws, 1);
  __syncthreads();
  for (int s = 0; s < NSEG; s++) {
    const char* hb = lds + (s & 1) * 33280;
    f32x16 aq, ak;
#pragma unroll
    for (int i = 0; i < 16; i++) { aq[i] = 0.f; ak[i] = 0.f; }
    {
      bf16x8 hf[2][8];
#pragma unroll
      for (int k = 0; k < 8; k++) hf[0][k] = *(const bf16x8*)(hb + x * 1040 + (16 * k + 8 * hh) * 2);
#pragma unroll
      for (int ch = 0; ch < 4; ch++) {
        if (ch < 3) {
#pragma unroll
          for (int k = 0; k < 8; k++) hf[(ch + 1) & 1][k] = *(const bf16x8*)(hb + x * 1040 + (16 * (8 * ch + 8 + k) + 8 * hh) * 2);
        }
        __builtin_amdgcn_sched_barrier(0);
#pragma unroll
        for (int k = 0; k < 8; k++) { ak = MFMA32(wk[8 * ch + k], hf[ch & 1][k], ak); aq = MFMA32(wq[8 * ch + k], hf[ch & 1][k], aq); }
        __builtin_amdgcn_sched_barrier(0);
      }
    }
    put_h(lds + ((s + 1) & 1) * 33280, hr);
    asm volatile("s_waitcnt vmcnt(0)" ::: "memory");
    __syncthreads();
    if (tid == 0 && s > 4) st4_sc1((u32)s, rflag, 4096u + h * 128u);
    store_T_tile<true>(ak, ws + OFF_KX, 32 * s, h, w, lane);
    store_T_tile<true>(aq, ws + OFF_QX, 32 * s, h, w, lane);
    if (s < 4 || s == NSEG - 1) {
      asm volatile("s_waitcnt vmcnt(0)" ::: "memory");
      __syncthreads();
      if (tid == 0) st4_sc1((u32)(s + 1), rflag, 4096u + h * 128u);
    }
    if (s == 30) { u32 seenh = 0xFFFFFFFFu; if (!wait_progress(rflag, OFF_DONE + lane * 64u, 1u, seenh)) return; }
    load_h(hr, ws, s + 2 < NSEG ? s + 2 : NSEG - 1);
    STAMP(16 + 4 * s);
  }
}

__device__ __forceinline__ float xg_max(float v) {
  auto r = __builtin_amdgcn_permlane16_swap(f2u(v), f2u(v), false, false);
  v = fmaxf(u2f(r[0]), u2f(r[1]));
  auto r2 = __builtin_amdgcn_permlane32_swap(f2u(v), f2u(v), false, false);
  return fmaxf(u2f(r2[0]), u2f(r2[1]));
}
__device__ __forceinline__ float xg_sum(float v) {
  auto r = __builtin_amdgcn_permlane16_swap(f2u(v), f2u(v), false, false);
  v = u2f(r[0]) + u2f(r[1]);
  auto r2 = __builtin_amdgcn_permlane32_swap(f2u(v), f2u(v), false, false);
  return u2f(r2[0]) + u2f(r2[1]);
}
__device__ __forceinline__ void px_task(char* ws, int s, int hg) {
  const int tid = threadIdx.x, lane = tid & 63, w = tid >> 6;
  const int hd = hg * 4 + w, hl = w;
  const int n = lane & 15, g = lane >> 4;
  const __amdgpu_buffer_rsrc_t rq = mkrsrc(ws + OFF_QX + (size_t)s * 32 * 8192);
  const __amdgpu_buffer_rsrc_t rk = mkrsrc(ws + OFF_KX + (size_t)s * 32 * 8192);
  const __amdgpu_buffer_rsrc_t rkm = mkrsrc(ws + OFF_KMEM + (size_t)s * 65536);
  const int keyrow = 16 * ((n >> 2) & 1) + 4 * (n >> 3) + (n & 3);
  const int keyb = 16 * (g & 1) + 4 * (g >> 1);
  const float sl = 0.08838834764831845f * 1.4426950408889634f;
  bf16x8 qf[2][4], kf[2][4], mf[4];
#pragma unroll
  for (int ks = 0; ks < 4; ks++) {
#pragma unroll
    for (int t = 0; t < 2; t++) {
      qf[t][ks] = as_bf8(ld16_sc1(rq, (u32)(((16 * t + n) * NH + hd) * DH + 32 * ks + 8 * g) * 2u));
      kf[t][ks] = as_bf8(ld16_sc1(rk, (u32)(((8 * t + keyrow) * NH + hd) * DH + 32 * ks + 8 * g) * 2u));
    }
    mf[ks] = as_bf8(ld16_sc1(rkm, (u32)(hd * 1024 + (n & 7) * 128 + 32 * ks + 8 * g) * 2u));
  }
  const bool mvalid = (g >> 1) == (hl >> 1);
  u32x4* PXx = (u32x4*)(ws + OFF_PX);
  char* PXm = ws + OFF_PX + (4ull << 20);
#pragma unroll
  for (int qt = 0; qt < 2; qt++) {
    f32x4 sc[3];
#pragma unroll
    for (int i = 0; i < 3; i++) sc[i] = (f32x4){0.f, 0.f, 0.f, 0.f};
#pragma unroll
    for (int ks = 0; ks < 4; ks++) {
      sc[0] = MFMA16(kf[0][ks], qf[qt][ks], sc[0]);
      sc[1] = MFMA16(kf[1][ks], qf[qt][ks], sc[1]);
      sc[2] = MFMA16(mf[ks], qf[qt][ks], sc[2]);
    }
    const int qi = 16 * qt + n;
    float mx = -3.0e38f;
#pragma unroll
    for (int j = 0; j < 4; j++) {
      if (keyb + j > qi) sc[0][j] = -3.0e38f;
      if (keyb + 8 + j > qi) sc[1][j] = -3.0e38f;
      if (!mvalid) sc[2][j] = -3.0e38f;
      mx = fmaxf(mx, fmaxf(fmaxf(sc[0][j], sc[1][j]), sc[2][j]));
    }
    mx = xg_max(mx);
    float sum = 0.f;
#pragma unroll
    for (int i = 0; i < 3; i++)
#pragma unroll
      for (int j = 0; j < 4; j++) {
        float p = sc[i][j] > -1.0e38f ? __builtin_amdgcn_exp2f((sc[i][j] - mx) * sl) : 0.f;
        sc[i][j] = p; sum += p;
      }
    const float inv = __builtin_amdgcn_rcpf(xg_sum(sum));
    u32x4 pa = {pk2(sc[0][0] * inv, sc[0][1] * inv), pk2(sc[1][0] * inv, sc[1][1] * inv),
                pk2(sc[0][2] * inv, sc[0][3] * inv), pk2(sc[1][2] * inv, sc[1][3] * inv)};
    PXx[(((size_t)s * NH + hd) * 2 + qt) * 64 + lane] = pa;
    if (mvalid) {
      u32x2 pm = {pk2(sc[2][0] * inv, sc[2][1] * inv), pk2(sc[2][2] * inv, sc[2][3] * inv)};
      *(u32x2*)(PXm + ((((size_t)s * 8 + hg) * 2 + qt) * 64 + lane) * 16 + (hl & 1) * 8) = pm;
    }
  }
}

__global__ void __launch_bounds__(256, 1) k2_rec(const u16* __restrict__ Wq, const u16* __restrict__ Wk, const u16* __restrict__ Wv,
                                                  const u16* __restrict__ Wo, const u16* __restrict__ x, const u16* __restrict__ w1,
                                                  const u16* __restrict__ w2, const u16* __restrict__ cvec,
                                                  const u16* __restrict__ wmod, const u16* __restrict__ mem0, char* ws) {
  extern __shared__ __attribute__((aligned(16))) char lds[];
  const int b = blockIdx.x;
  STAMP(0);
  if (b < 32) role_help(x, w1, w2, cvec, wmod, mem0, ws, lds, b);
  else if (b < 64) role_score(Wq, Wk, ws, lds, b - 32);
  else if (b < 192) { const int q = b - 64; role_column(Wv, Wo, ws, lds, (q & 7) | ((q >> 4) << 3), (q >> 3) & 1); }
  else if (b < 224) role_vu(Wv, Wo, ws, lds, b - 192);
  else role_qk(Wq, Wk, ws, lds, b - 224);
}

__global__ void __launch_bounds__(512, 2) k3b_oproj(const u16* __restrict__ x, char* __restrict__ ws) {
  extern __shared__ __attribute__((aligned(16))) char lds[];
  const int b = blockIdx.x, tid = threadIdx.x, lane = tid & 63, w = tid >> 6;
  const int s = b >> 2, nq = b & 3;
  const int n = lane & 15, g = lane >> 4, cc = lane & 7;
  const u32x4* UF = (const u32x4*)(ws + OFF_UF) + (size_t)s * 64 * 1024;
  const char* Yg = ws + OFF_Y + (size_t)s * 64 * 4096;
  const u32x4* PXx = (const u32x4*)(ws + OFF_PX) + (size_t)s * NH * 128;
  const u32x4* PXm = (const u32x4*)(ws + OFF_PX + (4ull << 20)) + (size_t)s * 8 * 128;
  f32x4 acc[2][8];
#pragma unroll
  for (int i = 0; i < 2; i++)
#pragma unroll
    for (int j = 0; j < 8; j++) acc[i][j] = (f32x4){0.f, 0.f, 0.f, 0.f};
  u32x4 ub[4][8], pa[4][2], pm[2], yf[8];
#pragma unroll
  for (int hl = 0; hl < 4; hl++) {
    const int hd = 4 * w + hl;
#pragma unroll
    for (int nt = 0; nt < 8; nt++) ub[hl][nt] = UF[(size_t)(16 * nq + 2 * nt + (n >> 3)) * 1024 + (hd * 4 + g) * 8 + cc];
    pa[hl][0] = PXx[(hd * 2) * 64 + lane];
    pa[hl][1] = PXx[(hd * 2 + 1) * 64 + lane];
  }
  pm[0] = PXm[(w * 2) * 64 + lane];
  pm[1] = PXm[(w * 2 + 1) * 64 + lane];
  {
    const int ha = 4 * w + 2 * (g >> 1);
#pragma unroll
    for (int nt = 0; nt < 8; nt++) {
      const int c = 16 * nq + 2 * nt + (n >> 3);
      u32x2 ya = *(const u32x2*)(Yg + (size_t)c * 4096 + (ha * 8 + cc) * 16 + 8 * (g & 1));
      u32x2 yb = *(const u32x2*)(Yg + (size_t)c * 4096 + ((ha + 1) * 8 + cc) * 16 + 8 * (g & 1));
      yf[nt] = (u32x4){ya.x, ya.y, yb.x, yb.y};
    }
  }
  __builtin_amdgcn_sched_barrier(0);
#pragma unroll
  for (int hl = 0; hl < 4; hl++)
#pragma unroll
    for (int nt = 0; nt < 8; nt++) {
      acc[0][nt] = MFMA16(as_bf8(pa[hl][0]), as_bf8(ub[hl][nt]), acc[0][nt]);
      acc[1][nt] = MFMA16(as_bf8(pa[hl][1]), as_bf8(ub[hl][nt]), acc[1][nt]);
    }
#pragma unroll
  for (int nt = 0; nt < 8; nt++) {
    acc[0][nt] = MFMA16(as_bf8(pm[0]), as_bf8(yf[nt]), acc[0][nt]);
    acc[1][nt] = MFMA16(as_bf8(pm[1]), as_bf8(yf[nt]), acc[1][nt]);
  }
  float* red = (float*)lds;
#pragma unroll
  for (int qt = 0; qt < 2; qt++)
#pragma unroll
    for (int nt = 0; nt < 8; nt++)
#pragma unroll
      for (int j = 0; j < 4; j++) red[(w * 32 + 16 * qt + 4 * g + j) * 132 + 16 * nt + n] = acc[qt][nt][j];
  __syncthreads();
  const float* mod = (const float*)(ws + OFF_MOD);
  u16* x1 = (u16*)(ws + OFF_X1);
  {
    const int r = tid >> 4, c8 = (tid & 15) * 8;
    float o[8];
#pragma unroll
    for (int j = 0; j < 8; j++) o[j] = 0.f;
#pragma unroll
    for (int ww = 0; ww < 8; ww++) {
      f32x4 a = *(const f32x4*)(red + (ww * 32 + r) * 132 + c8), bq = *(const f32x4*)(red + (ww * 32 + r) * 132 + c8 + 4);
#pragma unroll
      for (int j = 0; j < 4; j++) { o[j] += a[j]; o[4 + j] += bq[j]; }
    }
    const int col = 128 * nq + c8;
    const size_t go = (size_t)(32 * s + r) * DM + col;
    u32x4 xv = *(const u32x4*)(x + go);
    float xf[8] = {bf_lo(xv.x), bf_hi(xv.x), bf_lo(xv.y), bf_hi(xv.y), bf_lo(xv.z), bf_hi(xv.z), bf_lo(xv.w), bf_hi(xv.w)};
#pragma unroll
    for (int j = 0; j < 8; j++) o[j] = xf[j] + bfr(mod[1024 + col + j] * bfr(o[j]));
    u32x4 rv = {pk2(o[0], o[1]), pk2(o[2], o[3]), pk2(o[4], o[5]), pk2(o[6], o[7])};
    *(u32x4*)(x1 + go) = rv;
  }
}

__global__ void __launch_bounds__(512, 2) k4_mlp1(const u16* __restrict__ b1, char* __restrict__ ws) {
  extern __shared__ __attribute__((aligned(16))) char lds[];
  const int b = blockIdx.x, tid = threadIdx.x, lane = tid & 63, w = tid >> 6;
  const int mt0 = b / 7, nb = b - mt0 * 7;
  const float* mod = (const float*)(ws + OFF_MOD);
  const u16* x1 = (const u16*)(ws + OFF_X1);
  const int n = lane & 15, g = lane >> 4;
  const f32x4 mv = *(const f32x4*)(mod + 1536 + (tid & 255) * 4);
  u32x4 xv[2][4];
#pragma unroll
  for (int it = 0; it < 2; it++)
#pragma unroll
    for (int j = 0; j < 4; j++) xv[it][j] = *(const u32x4*)(x1 + (size_t)(mt0 * 64 + it * 32 + w * 4 + g) * DM + (j * 16 + n) * 8);
  const u32x4* WF = (const u32x4*)(ws + OFF_W1F) + (size_t)(nb * 16 + w * 2) * 16 * 64 + lane;
  u32x4 bfm[2][16];
#pragma unroll
  for (int nt = 0; nt < 2; nt++)
#pragma unroll
    for (int ks = 0; ks < 8; ks++) bfm[nt][ks] = WF[(nt * 16 + ks) * 64];
  __builtin_amdgcn_sched_barrier(0);
  float* modl = (float*)(lds + 66560);
  if (tid < 256) *(f32x4*)(modl + tid * 4) = tid >= 128 ? mv + 1.f : mv;
  __syncthreads();
#pragma unroll
  for (int it = 0; it < 2; it++) {
    f32x2 f[16];
#pragma unroll
    for (int j = 0; j < 4; j++) {
      f[4 * j + 0] = (f32x2){bf_lo(xv[it][j].x), bf_hi(xv[it][j].x)}; f[4 * j + 1] = (f32x2){bf_lo(xv[it][j].y), bf_hi(xv[it][j].y)};
      f[4 * j + 2] = (f32x2){bf_lo(xv[it][j].z), bf_hi(xv[it][j].z)}; f[4 * j + 3] = (f32x2){bf_lo(xv[it][j].w), bf_hi(xv[it][j].w)};
    }
    f32x2 s2 = {0.f, 0.f};
#pragma unroll
    for (int i = 0; i < 16; i++) s2 += f[i];
    float m = row16_sum(s2.x + s2.y) * (1.f / DM);
    f32x2 q2 = {0.f, 0.f};
#pragma unroll
    for (int i = 0; i < 16; i++) { f[i] -= m; q2 += f[i] * f[i]; }
    float rs = rsqrtf(row16_sum(q2.x + q2.y) * (1.f / DM) + 1e-5f);
    s2 = (f32x2){0.f, 0.f};
#pragma unroll
    for (int j = 0; j < 4; j++) {
      const int c0 = (j * 16 + n) * 8;
      f32x4 c0v = *(const f32x4*)(modl + 512 + c0), c1v = *(const f32x4*)(modl + 512 + c0 + 4);
      f32x4 s0v = *(const f32x4*)(modl + c0), s1v = *(const f32x4*)(modl + c0 + 4);
      f[4 * j + 0] = f[4 * j + 0] * rs * (f32x2){c0v[0], c0v[1]} + (f32x2){s0v[0], s0v[1]};
      f[4 * j + 1] = f[4 * j + 1] * rs * (f32x2){c0v[2], c0v[3]} + (f32x2){s0v[2], s0v[3]};
      f[4 * j + 2] = f[4 * j + 2] * rs * (f32x2){c1v[0], c1v[1]} + (f32x2){s1v[0], s1v[1]};
      f[4 * j + 3] = f[4 * j + 3] * rs * (f32x2){c1v[2], c1v[3]} + (f32x2){s1v[2], s1v[3]};
      s2 += (f[4 * j + 0] + f[4 * j + 1]) + (f[4 * j + 2] + f[4 * j + 3]);
    }
    m = row16_sum(s2.x + s2.y) * (1.f / DM);
    q2 = (f32x2){0.f, 0.f};
#pragma unroll
    for (int i = 0; i < 16; i++) { f[i] -= m; q2 += f[i] * f[i]; }
    rs = rsqrtf(row16_sum(q2.x + q2.y) * (1.f / DM) + 1e-5f);
#pragma unroll
    for (int j = 0; j < 4; j++) {
      f32x2 o0 = f[4 * j] * rs, o1 = f[4 * j + 1] * rs, o2 = f[4 * j + 2] * rs, o3 = f[4 * j + 3] * rs;
      u32x4 o = {pk2(o0.x, o0.y), pk2(o1.x, o1.y), pk2(o2.x, o2.y), pk2(o3.x, o3.y)};
      *(u32x4*)(lds + (it * 32 + w * 4 + g) * 1040 + (j * 16 + n) * 16) = o;
    }
    __builtin_amdgcn_sched_barrier(0);
  }
#pragma unroll
  for (int nt = 0; nt < 2; nt++)
#pragma unroll
    for (int ks = 8; ks < 16; ks++) bfm[nt][ks] = WF[(nt * 16 + ks) * 64];
  __builtin_amdgcn_sched_barrier(0);
  __syncthreads();
  f32x4 acc[4][2];
#pragma unroll
  for (int i = 0; i < 4; i++)
#pragma unroll
    for (int j = 0; j < 2; j++) acc[i][j] = (f32x4){0.f, 0.f, 0.f, 0.f};
#pragma unroll
  for (int ks = 0; ks < 16; ks++) {
    bf16x8 af[4];
#pragma unroll
    for (int mt = 0; mt < 4; mt++) af[mt] = *(const bf16x8*)(lds + (16 * mt + n) * 1040 + (32 * ks + 8 * g) * 2);
#pragma unroll
    for (int mt = 0; mt < 4; mt++)
#pragma unroll
      for (int nt = 0; nt < 2; nt++) acc[mt][nt] = MFMA16(as_bf8(bfm[nt][ks]), af[mt], acc[mt][nt]);
    if (ks & 1) __builtin_amdgcn_sched_barrier(0);
  }
  u32x4* hidF = (u32x4*)(ws + OFF_HID);
  float bb[2][4];
#pragma unroll
  for (int nt = 0; nt < 2; nt++) {
    u32x2 bv = *(const u32x2*)(b1 + nb * 256 + w * 32 + 16 * nt + 4 * g);
    bb[nt][0] = bf_lo(bv.x); bb[nt][1] = bf_hi(bv.x); bb[nt][2] = bf_lo(bv.y); bb[nt][3] = bf_hi(bv.y);
  }
#pragma unroll
  for (int mt = 0; mt < 4; mt++) {
    float o[8];
#pragma unroll
    for (int nt = 0; nt < 2; nt++)
#pragma unroll
      for (int j = 0; j < 4; j++) {
        float u = bfr(bfr(acc[mt][nt][j]) + bb[nt][j]);
        float z = 0.7978845608028654f * (u + 0.044715f * u * u * u);
        float e = __builtin_amdgcn_exp2f(z * 2.8853900817779268f);
        float th = 1.f - 2.f * __builtin_amdgcn_rcpf(e + 1.f);
        o[4 * nt + j] = 0.5f * u * (1.f + th);
      }
    u32x4 ov = {pk2(o[0], o[1]), pk2(o[2], o[3]), pk2(o[4], o[5]), pk2(o[6], o[7])};
    hidF[((size_t)(mt0 * 4 + mt) * 56 + nb * 8 + w) * 64 + lane] = ov;
  }
}

__global__ void __launch_bounds__(512, 2) k5_mlp2(const u16* __restrict__ b2, char* __restrict__ ws, u16* __restrict__ out) {
  extern __shared__ __attribute__((aligned(16))) char lds[];
  const int b = blockIdx.x, tid = threadIdx.x, lane = tid & 63, w = tid >> 6;
  const int mt0 = (b & 7) * 4 + (b >> 6), nb = (b >> 3) & 7;
  const int n = lane & 15, g = lane >> 4;
  const u32x4* hidF = (const u32x4*)(ws + OFF_HID) + ((size_t)(mt0 * 4) * 56 + w * 7) * 64 + lane;
  const u32x4* WF = (const u32x4*)(ws + OFF_W2F) + ((size_t)(nb * 4) * 56 + w * 7) * 64 + lane;
  f32x4 acc[4][4];
#pragma unroll
  for (int i = 0; i < 4; i++)
#pragma unroll
    for (int j = 0; j < 4; j++) acc[i][j] = (f32x4){0.f, 0.f, 0.f, 0.f};
#pragma unroll
  for (int hb = 0; hb < 2; hb++) {
    const int nk = hb ? 3 : 4;
    u32x4 bfm[4][4]; bf16x8 af[4][4];
#pragma unroll
    for (int k = 0; k < 4; k++) if (k < nk) {
#pragma unroll
      for (int nt = 0; nt < 4; nt++) bfm[k][nt] = WF[(nt * 56 + hb * 4 + k) * 64];
#pragma unroll
      for (int mt = 0; mt < 4; mt++) af[k][mt] = as_bf8(hidF[(mt * 56 + hb * 4 + k) * 64]);
    }
    __builtin_amdgcn_sched_barrier(0);
#pragma unroll
    for (int k = 0; k < 4; k++) if (k < nk) {
#pragma unroll
      for (int mt = 0; mt < 4; mt++)
#pragma unroll
        for (int nt = 0; nt < 4; nt++) acc[mt][nt] = MFMA16(as_bf8(bfm[k][nt]), af[k][mt], acc[mt][nt]);
    }
  }
  float* red = (float*)lds;
#pragma unroll
  for (int mt = 0; mt < 4; mt++)
#pragma unroll
    for (int nt = 0; nt < 4; nt++) *(f32x4*)(red + (w * 64 + 16 * mt + n) * 68 + 16 * nt + 4 * g) = acc[mt][nt];
  __syncthreads();
  const float* mod = (const float*)(ws + OFF_MOD);
  const u16* x1 = (const u16*)(ws + OFF_X1);
  {
    const int r = tid >> 3, c8 = (tid & 7) * 8;
    float o[8];
#pragma unroll
    for (int j = 0; j < 8; j++) o[j] = 0.f;
#pragma unroll
    for (int ww = 0; ww < 8; ww++) {
      f32x4 a = *(const f32x4*)(red + (ww * 64 + r) * 68 + c8), bq = *(const f32x4*)(red + (ww * 64 + r) * 68 + c8 + 4);
#pragma unroll
      for (int j = 0; j < 4; j++) { o[j] += a[j]; o[4 + j] += bq[j]; }
    }
    const int col = 64 * nb + c8;
    const size_t go = (size_t)(mt0 * 64 + r) * DM + col;
    u32x4 xv = *(const u32x4*)(x1 + go), bv = *(const u32x4*)(b2 + col);
    float xf[8] = {bf_lo(xv.x), bf_hi(xv.x), bf_lo(xv.y), bf_hi(xv.y), bf_lo(xv.z), bf_hi(xv.z), bf_lo(xv.w), bf_hi(xv.w)};
    float bb[8] = {bf_lo(bv.x), bf_hi(bv.x), bf_lo(bv.y), bf_hi(bv.y), bf_lo(bv.z), bf_hi(bv.z), bf_lo(bv.w), bf_hi(bv.w)};
#pragma unroll
    for (int j = 0; j < 8; j++) o[j] = xf[j] + bfr(mod[2560 + col + j] * bfr(bfr(o[j]) + bb[j]));
    u32x4 rv = {pk2(o[0], o[1]), pk2(o[2], o[3]), pk2(o[4], o[5]), pk2(o[6], o[7])};
    *(u32x4*)(out + go) = rv;
  }
}

extern "C" void kernel_launch(void* const* d_in, const int* in_sizes, int n_in, void* d_out, int out_size, void* d_ws,
                              size_t ws_size, hipStream_t stream) {
  const u16* x = (const u16*)d_in[0];
  const u16* mem0 = (const u16*)d_in[1];
  const u16* Wq = (const u16*)d_in[2];
  const u16* Wk = (const u16*)d_in[3];
  const u16* Wv = (const u16*)d_in[4];
  const u16* Wo = (const u16*)d_in[5];
  const u16* w1 = (const u16*)d_in[8];
  const u16* b1 = (const u16*)d_in[9];
  const u16* w2 = (const u16*)d_in[10];
  const u16* b2 = (const u16*)d_in[11];
  const u16* cvec = (const u16*)d_in[12];
  const u16* wmod = (const u16*)d_in[13];
  char* ws = (char*)d_ws;
  hipFuncSetAttribute((const void*)k2_rec, hipFuncAttributeMaxDynamicSharedMemorySize, 135168);
  hipMemsetAsync(ws, 0xFF, OFF_H, stream);
  k2_rec<<<256, 256, 135168, stream>>>(Wq, Wk, Wv, Wo, x, w1, w2, cvec, wmod, mem0, ws);
  hipFuncSetAttribute((const void*)k3b_oproj, hipFuncAttributeMaxDynamicSharedMemorySize, 135168);
  k3b_oproj<<<256, 512, 135168, stream>>>(x, ws);
  hipFuncSetAttribute((const void*)k4_mlp1, hipFuncAttributeMaxDynamicSharedMemorySize, 70656);
  hipFuncSetAttribute((const void*)k5_mlp2, hipFuncAttributeMaxDynamicSharedMemorySize, 139264);
  k4_mlp1<<<224, 512, 70656, stream>>>(b1, ws);
  k5_mlp2<<<256, 512, 139264, stream>>>(b2, ws, (u16*)d_out);
}
```

```cpp
#include <hip/hip_runtime.h>
#include <hip/hip_bf16.h>
#include <stdint.h>

typedef unsigned int u32;
typedef unsigned short u16;
typedef unsigned long long u64;
typedef __attribute__((ext_vector_type(2))) __bf16 bf16x2;
typedef __attribute__((ext_vector_type(8))) __bf16 bf16x8;
typedef __attribute__((ext_vector_type(16))) __bf16 bf16x16;
typedef __attribute__((ext_vector_type(2))) float f32x2;
typedef __attribute__((ext_vector_type(4))) float f32x4;
typedef __attribute__((ext_vector_type(16))) float f32x16;
typedef __attribute__((ext_vector_type(2))) u32 u32x2;
typedef __attribute__((ext_vector_type(4))) u32 u32x4;
typedef __attribute__((ext_vector_type(8))) u32 u32x8;

#define NTOK 2048
#define DM 512
#define NH 32
#define DH 128
#define NSEG 64
#define FF 1792
#define SENT 0xFFFFFFFFu

#define OFF_FLAG 0ull
#define OFF_DONE 8192u
#define OFF_KMF 12288u
#define OFF_WVF 14336u
#define OFF_MDF 28672u
#define OFF_MOD 16384ull
#define OFF_MEMX 32768ull
#define OFF_PEX 0x90000ull
#define OFF_H (2ull << 20)
#define OFF_KMEM (4ull << 20)
#define OFF_WVO (8ull << 20)
#define OFF_KX (24ull << 20)
#define OFF_QX (40ull << 20)
#define OFF_Y (56ull << 20)
#define OFF_UF (72ull << 20)
#define OFF_PX (136ull << 20)
#define OFF_X1 (142ull << 20)
#define OFF_Y2 (146ull << 20)
#define OFF_HID (148ull << 20)
#define OFF_W1F (156ull << 20)
#define OFF_W2F (158ull << 20)
#define OFF_DBG (200ull << 20)
#define STAMP(slot) do { } while (0)

__device__ __forceinline__ u32 pk2(float a, float b) {
  bf16x2 v; v[0] = (__bf16)a; v[1] = (__bf16)b; return __builtin_bit_cast(u32, v);
}
__device__ __forceinline__ u32 f2u(float x) { return __builtin_bit_cast(u32, x); }
__device__ __forceinline__ float u2f(u32 x) { return __builtin_bit_cast(float, x); }
__device__ __forceinline__ float bf_lo(u32 x) { return __builtin_bit_cast(float, x << 16); }
__device__ __forceinline__ float bf_hi(u32 x) { return __builtin_bit_cast(float, x & 0xffff0000u); }
__device__ __forceinline__ float bfr(float a) { return (float)(__bf16)a; }
__device__ __forceinline__ __amdgpu_buffer_rsrc_t mkrsrc(const void* p) {
  return __builtin_amdgcn_make_buffer_rsrc((void*)p, 0, 0x7fffffff, 0x00020000);
}
__device__ __forceinline__ u32x4 ld16_sc1(__amdgpu_buffer_rsrc_t r, u32 off) { return __builtin_amdgcn_raw_buffer_load_b128(r, off, 0, 16); }
__device__ __forceinline__ u32 ld4_sc1(__amdgpu_buffer_rsrc_t r, u32 off) { return __builtin_amdgcn_raw_buffer_load_b32(r, off, 0, 16); }
__device__ __forceinline__ void st16_sc1(u32x4 v, __amdgpu_buffer_rsrc_t r, u32 off) { __builtin_amdgcn_raw_buffer_store_b128(v, r, off, 0, 16); }
__device__ __forceinline__ void st8_sc1(u32x2 v, __amdgpu_buffer_rsrc_t r, u32 off) { __builtin_amdgcn_raw_buffer_store_b64(v, r, off, 0, 16); }
__device__ __forceinline__ void st4_sc1(u32 v, __amdgpu_buffer_rsrc_t r, u32 off) { __builtin_amdgcn_raw_buffer_store_b32(v, r, off, 0, 16); }
__device__ __forceinline__ u32 badm(u32x4 a) { return (u32)(a.x == SENT) | (u32)(a.y == SENT) | (u32)(a.z == SENT) | (u32)(a.w == SENT); }
__device__ __forceinline__ u32 sane(u32 x) { return x < 0xFFFFFFFEu ? x : 0xFFFFFFFEu; }
__device__ __forceinline__ u32 now32() { return (u32)__builtin_amdgcn_s_memtime(); }
__device__ __forceinline__ void wait_until(u32 t) { while ((int)(t - now32()) > 0) { } }
__device__ __forceinline__ bf16x8 as_bf8(u32x4 v) { return __builtin_bit_cast(bf16x8, v); }
__device__ __forceinline__ bf16x16 as_bf16(u32x4 a, u32x4 b) {
  u32x8 v = {a.x, a.y, a.z, a.w, b.x, b.y, b.z, b.w}; return __builtin_bit_cast(bf16x16, v);
}
__device__ __forceinline__ float max3r(float a, float b, float c) { float r; asm("v_max3_f32 %0, %1, %2, %3" : "=v"(r) : "v"(a), "v"(b), "v"(c)); return r; }
__device__ __forceinline__ float max2r(float a, float b) { float r; asm("v_max_f32 %0, %1, %2" : "=v"(r) : "v"(a), "v"(b)); return r; }
#define MFMA16(a, b, c) __builtin_amdgcn_mfma_f32_16x16x32_bf16((a), (b), (c), 0, 0, 0)
#define MFMA32(a, b, c) __builtin_amdgcn_mfma_f32_32x32x16_bf16((a), (b), (c), 0, 0, 0)
#define SMF(a, b, c, idx) __builtin_amdgcn_smfmac_f32_16x16x64_bf16((a), (b), (c), (idx), 0, 0)

template <int N>
__device__ __forceinline__ u32 poll_chunks(__amdgpu_buffer_rsrc_t r, const u32 (&off)[N], u32x4 (&a)[N], u32 lane16, u32& tiss) {
  u32 spins = 0;
  while (true) {
    asm volatile("" : "+v"(lane16) :: "memory");
    if (spins) tiss = now32();
    u32 bad = 0;
#pragma unroll
    for (int j = 0; j < N; j++) a[j] = ld16_sc1(r, off[j] + lane16);
#pragma unroll
    for (int j = 0; j < N; j++) bad |= badm(a[j]);
    ++spins;
    if (!__any(bad)) return spins;
    if (spins > 400000u) return 0;
  }
}

typedef __attribute__((ext_vector_type(4))) short s16x4;
typedef __attribute__((address_space(3))) s16x4 lds_s16x4;
template <int ROWB>
__device__ __forceinline__ void dma_tile(char* lds, const u16* src, int rows, int gstride) {
  constexpr int LPR = ROWB / 16;
  const int total = rows * LPR;
  for (int u = threadIdx.x; u < total; u += 256) {
    const int r = u / LPR, c = u % LPR;
    __builtin_amdgcn_global_load_lds((const u32*)(src + (size_t)r * gstride + c * 8), (u32*)(lds + u * 16), 16, 0, 0);
  }
}
__device__ __forceinline__ void dma_wait() { asm volatile("s_waitcnt vmcnt(0)" ::: "memory"); __syncthreads(); }
__device__ __forceinline__ u32x2 tr4(const char* lds, int row0, int col0, int pitch, int lane) {
  const char* p = lds + (row0 + ((lane & 15) >> 2)) * pitch + (col0 + 4 * (lane & 3)) * 2;
  s16x4 v = __builtin_amdgcn_ds_read_tr16_b64_v4i16((lds_s16x4*)p);
  return __builtin_bit_cast(u32x2, v);
}
__device__ __forceinline__ bf16x8 frag8(const char* lds, int k0, int col0, int pitch, int lane) {
  u32x2 a = tr4(lds, k0, col0, pitch, lane), b = tr4(lds, k0 + 4, col0, pitch, lane);
  return as_bf8((u32x4){a.x, a.y, b.x, b.y});
}
__device__ __forceinline__ u32x4 frag8s(const char* lds, int k0, int col0, int pitch, int lane) {
  u32x2 a = tr4(lds, k0, col0, pitch, lane), b = tr4(lds, k0 + 8, col0, pitch, lane);
  return (u32x4){a.x, b.x, a.y, b.y};
}
#define PACK_S(acc, b) (u32x4){pk2((acc)[(b)], (acc)[(b) + 1]), pk2((acc)[(b) + 4], (acc)[(b) + 5]), pk2((acc)[(b) + 2], (acc)[(b) + 3]), pk2((acc)[(b) + 6], (acc)[(b) + 7])}

__device__ __forceinline__ void wvo_task(const u16* __restrict__ Wv, const u16* __restrict__ Wo, char* ws, char* lds, int task) {
  const int tid = threadIdx.x, lane = tid & 63, w = tid >> 6;
  const int h = task >> 2, nq = task & 3, x = lane & 31, hh = lane >> 5;
  dma_tile<256>(lds, Wo + (size_t)h * DH * DM + nq * 128, DH, DM);
  bf16x8 a[4][8];
#pragma unroll
  for (int mt = 0; mt < 4; mt++)
#pragma unroll
    for (int ks = 0; ks < 8; ks++)
      a[mt][ks] = *(const bf16x8*)(Wv + ((size_t)(128 * w + 32 * mt + x) * NH + h) * DH + 16 * ks + 8 * hh);
  dma_wait();
  const __amdgpu_buffer_rsrc_t rd = mkrsrc(ws + OFF_WVO);
#pragma unroll
  for (int nt = 0; nt < 4; nt++) {
    bf16x8 bw[8];
#pragma unroll
    for (int ks = 0; ks < 8; ks++) bw[ks] = frag8(lds, 16 * ks + 8 * hh, 32 * nt + (x & 16), 256, lane);
#pragma unroll
    for (int mt = 0; mt < 4; mt++) {
      f32x16 acc;
#pragma unroll
      for (int i = 0; i < 16; i++) acc[i] = 0.f;
#pragma unroll
      for (int ks = 0; ks < 8; ks++) acc = MFMA32(a[mt][ks], bw[ks], acc);
      const int d = 128 * nq + 32 * nt + x, c = d >> 3, cc = d & 7;
#pragma unroll
      for (int xb = 0; xb < 2; xb++) {
        const int dp = 128 * w + 32 * mt + 16 * xb, t = dp >> 6, ga = (dp >> 4) & 3, z = ga >> 1, gb = 2 * (ga & 1) + hh;
        const int lp = (h & 1) * 8 + cc + 16 * gb;
        u32 o = (u32)(((((c * 4 + (h >> 3)) * 4 + ((h >> 1) & 3)) * 8 + t) * 2 + z) * 64 + lp);
        st16_sc1(PACK_S(acc, 8 * xb), rd, o * 16u);
      }
    }
  }
  asm volatile("s_waitcnt vmcnt(0)" ::: "memory");
  __syncthreads();
  if (tid == 0) st4_sc1(1u, mkrsrc(ws + OFF_FLAG), OFF_WVF + task * 16u);
}

__device__ __forceinline__ void mod_cols(const u16* __restrict__ cvec, const u16* __restrict__ wmod, char* ws, char* lds, int col0) {
  const int tid = threadIdx.x, cp = tid & 15, ksl = tid >> 4;
  float* red = (float*)lds;
  u32 wv[32];
#pragma unroll
  for (int k = 0; k < 32; k++) wv[k] = *(const u32*)(wmod + (size_t)(ksl * 32 + k) * 3072 + col0 + cp * 2);
  float a0 = 0.f, a1 = 0.f;
#pragma unroll
  for (int k = 0; k < 32; k++) {
    float cv = bf_lo((u32)cvec[ksl * 32 + k]);
    float sv = bfr(cv / (1.f + __expf(-cv)));
    a0 += sv * bf_lo(wv[k]); a1 += sv * bf_hi(wv[k]);
  }
  __syncthreads();
  red[ksl * 32 + cp * 2] = a0; red[ksl * 32 + cp * 2 + 1] = a1;
  __syncthreads();
  if (tid < 32) {
    float sacc = 0.f;
#pragma unroll
    for (int k = 0; k < 16; k++) sacc += red[k * 32 + tid];
    st4_sc1(f2u(bfr(sacc)), mkrsrc(ws + OFF_MOD), (u32)(col0 + tid) * 4u);
  }
}

__device__ __forceinline__ float row16_sum(float v) {
  v += u2f((u32)__builtin_amdgcn_mov_dpp((int)f2u(v), 0x128, 0xF, 0xF, true));
  v += u2f((u32)__builtin_amdgcn_mov_dpp((int)f2u(v), 0x124, 0xF, 0xF, true));
  v += u2f((u32)__builtin_amdgcn_mov_dpp((int)f2u(v), 0x122, 0xF, 0xF, true));
  v += u2f((u32)__builtin_amdgcn_mov_dpp((int)f2u(v), 0x121, 0xF, 0xF, true));
  return v;
}

__device__ __forceinline__ bool wait_progress(__amdgpu_buffer_rsrc_t rf, u32 off, u32 need, u32& seen) {
  u32 spins = 0;
  if (__all((int)seen >= (int)need)) return true;
  while (true) {
    asm volatile("" : "+v"(off) :: "memory");
    seen = ld4_sc1(rf, off);
    if (__all((int)seen >= (int)need)) return true;
    __builtin_amdgcn_s_sleep(2);
    if (++spins > 400000u) return false;
  }
}

__device__ __forceinline__ void px_task(char* ws, int s, int hg);
__device__ __forceinline__ void role_help(const u16* __restrict__ x, const u16* __restrict__ w1, const u16* __restrict__ w2,
                                          const u16* __restrict__ cvec, const u16* __restrict__ wmod, const u16* __restrict__ mem0,
                                          char* ws, char* lds, int a0) {
  const int tid = threadIdx.x, lane = tid & 63, w = tid >> 6, n = lane & 15, g = lane >> 4;
  if (a0 == 31) {
    for (int i = tid; i < 512; i += 256) {
      u32x4 v = *(const u32x4*)(mem0 + (i & 7) * DM + (i >> 3) * 8);
      v.x = sane(v.x); v.y = sane(v.y); v.z = sane(v.z); v.w = sane(v.w);
      st16_sc1(v, mkrsrc(ws + OFF_MEMX), (u32)i * 16u);
    }
  }
  mod_cols(cvec, wmod, ws, lds, a0 * 32);
  asm volatile("s_waitcnt vmcnt(0)" ::: "memory");
  __syncthreads();
  if (tid == 0) st4_sc1(1u, mkrsrc(ws + OFF_FLAG), OFF_MDF + a0 * 16u);
  { u32 seenm = 0xFFFFFFFFu; if (!wait_progress(mkrsrc(ws + OFF_FLAG), OFF_MDF + (lane & 31) * 16u, 1u, seenm)) return; }
  float* modl = (float*)(lds + 4096);
  *(u32x4*)(modl + tid * 4) = ld16_sc1(mkrsrc(ws + OFF_MOD), (u32)tid * 16u);
  __syncthreads();
  for (int a = a0; a < 64; a += 32) {
    const float* mod = modl;
    const __amdgpu_buffer_rsrc_t rh = mkrsrc(ws + OFF_H);
    u32x4 xv[2][4];
#pragma unroll
    for (int it = 0; it < 2; it++)
#pragma unroll
      for (int j = 0; j < 4; j++) xv[it][j] = *(const u32x4*)(x + (size_t)(a * 32 + it * 16 + w * 4 + g) * DM + (j * 16 + n) * 8);
#pragma unroll
    for (int it = 0; it < 2; it++) {
      float f[32];
#pragma unroll
      for (int j = 0; j < 4; j++) {
        f[8 * j + 0] = bf_lo(xv[it][j].x); f[8 * j + 1] = bf_hi(xv[it][j].x); f[8 * j + 2] = bf_lo(xv[it][j].y); f[8 * j + 3] = bf_hi(xv[it][j].y);
        f[8 * j + 4] = bf_lo(xv[it][j].z); f[8 * j + 5] = bf_hi(xv[it][j].z); f[8 * j + 6] = bf_lo(xv[it][j].w); f[8 * j + 7] = bf_hi(xv[it][j].w);
      }
      float sm = 0.f;
#pragma unroll
      for (int i = 0; i < 32; i++) sm += f[i];
      const float m = row16_sum(sm) * (1.f / DM);
      float q = 0.f;
#pragma unroll
      for (int i = 0; i < 32; i++) { f[i] -= m; q += f[i] * f[i]; }
      const float rs = rsqrtf(row16_sum(q) * (1.f / DM) + 1e-5f);
#pragma unroll
      for (int j = 0; j < 4; j++) {
        const int c0 = (j * 16 + n) * 8;
        f32x4 c0v = *(const f32x4*)(mod + 512 + c0), c1v = *(const f32x4*)(mod + 512 + c0 + 4);
        f32x4 s0v = *(const f32x4*)(mod + c0), s1v = *(const f32x4*)(mod + c0 + 4);
        float o[8];
#pragma unroll
        for (int i = 0; i < 4; i++) {
          o[i] = f[8 * j + i] * rs * (1.f + c0v[i]) + s0v[i];
          o[4 + i] = f[8 * j + 4 + i] * rs * (1.f + c1v[i]) + s1v[i];
        }
        u32x4 r = {pk2(o[0], o[1]), pk2(o[2], o[3]), pk2(o[4], o[5]), pk2(o[6], o[7])};
        st16_sc1(r, rh, (u32)((a * 32 + it * 16 + w * 4 + g) * DM + c0) * 2u);
      }
    }
    asm volatile("s_waitcnt vmcnt(0)" ::: "memory");
    __syncthreads();
    if (tid == 0) st4_sc1(1u, mkrsrc(ws + OFF_FLAG), OFF_DONE + a * 64u);
  }
  const int a = a0;
  mod_cols(cvec, wmod, ws, lds, 1024 + a * 64);
  mod_cols(cvec, wmod, ws, lds, 1024 + a * 64 + 32);
  __syncthreads();
  if (a < 28) {
    dma_tile<128>(lds, w1 + a * 64, DM, FF);
    dma_wait();
    u32x4* dst = (u32x4*)(ws + OFF_W1F);
    for (int ks = 0; ks < 16; ks++) {
      bf16x8 v = frag8(lds, 32 * ks + 8 * (lane >> 4), 16 * w, 128, lane);
      dst[((size_t)(a * 4 + w) * 16 + ks) * 64 + lane] = __builtin_bit_cast(u32x4, v);
    }
  }
  __syncthreads();
  {
    const int cb = a >> 2, kq = a & 3;
    dma_tile<128>(lds, w2 + (size_t)kq * 448 * DM + cb * 64, 448, DM);
    dma_wait();
    u32x4* dst = (u32x4*)(ws + OFF_W2F);
    for (int ks = 0; ks < 14; ks++) {
      u32x2 va = tr4(lds, 32 * ks + 4 * (lane >> 4), 16 * w, 128, lane), vb = tr4(lds, 32 * ks + 16 + 4 * (lane >> 4), 16 * w, 128, lane);
      dst[((size_t)(cb * 4 + w) * 56 + kq * 14 + ks) * 64 + lane] = (u32x4){va.x, va.y, vb.x, vb.y};
    }
  }
  {
    const __amdgpu_buffer_rsrc_t rflag = mkrsrc(ws + OFF_FLAG);
    const int hg = a & 7;
    for (int s = a >> 3; s < NSEG; s += 4) {
      px_task(ws, s, hg);
    }
  }
}

#define AD_DEC 8
#define AD_INC 100
#define ADAPT(D, n) do { if ((n) == 1) D -= AD_DEC; else D += AD_INC; if (D < 0) D = 0; } while (0)

__device__ __forceinline__ void role_score(const u16* __restrict__ Wq, const u16* __restrict__ Wk, char* ws,
                                           char* lds, int h) {
  const int tid = threadIdx.x, lane = tid & 63, w = tid >> 6;
  const int n = lane & 15, gb = lane >> 4;
  bf16x16 wf[4][8];
#pragma unroll
  for (int mat = 0; mat < 2; mat++) {
    __syncthreads();
    dma_tile<256>(lds, (mat ? Wk : Wq) + h * DH, DM, NH * DH);
    dma_wait();
#pragma unroll
    for (int tt = 0; tt < 2; tt++)
#pragma unroll
      for (int t = 0; t < 8; t++) {
        const int col = 32 * w + 16 * tt;
        u32x4 lo = frag8s(lds, 64 * t + 16 * (gb >> 1) + 4 * (gb & 1), col, 256, lane);
        u32x4 hi = frag8s(lds, 64 * t + 16 * (2 + (gb >> 1)) + 4 * (gb & 1), col, 256, lane);
        wf[mat * 2 + tt][t] = as_bf16(lo, hi);
      }
  }
  __syncthreads();
  char* memL = lds;
  char* qs = lds + 8192;
  char* ks_ = lds + 8192 + 2304;
  const int idx = (lane & 8) ? (int)0xEEEEEEEE : 0x44444444;
  const __amdgpu_buffer_rsrc_t rflag = mkrsrc(ws + OFF_FLAG);
  const __amdgpu_buffer_rsrc_t rkx = mkrsrc(ws + OFF_KX);
  const __amdgpu_buffer_rsrc_t rmem = mkrsrc(ws + OFF_MEMX);
  const __amdgpu_buffer_rsrc_t rpex = mkrsrc(ws + OFF_PEX);
  const int rho = lane & 15;
  const int keyp = 8 * ((rho >> 2) & 1) + 4 * (rho >> 3) + (rho & 3);
  const u32 prho = (u32)((n & 7) >> 2) * 10240u, pi = n & 3;
  const u32 pxoff = prho + (u32)(((h >> 2) * 64) + 16 * (2 * (h & 1) + (gb >> 1)) + 8 * (gb & 1) + 4 * ((h >> 1) & 1) + pi) * 16u + (n >> 3) * 8u;
  const u32 pmoff = prho + 8192u + (u32)(h * 4 + pi) * 16u + (gb & 1) * 8u + (gb >> 1) * 4u;
  int D1 = w == 0 ? 3400 : (w == 1 ? 3300 : 4050); u32 tref = now32() - 8000u, seen = 0xFFFFFFFFu;
  STAMP(1);
  for (int s = 0; s < NSEG; s++) {
    u32x4 kx[2][4];
    if (w == 0) {
      if (!wait_progress(rflag, 4096u + h * 128u, s + 1, seen)) return;
#pragma unroll
      for (int tt = 0; tt < 2; tt++)
#pragma unroll
        for (int k4 = 0; k4 < 4; k4++)
          kx[tt][k4] = ld16_sc1(rkx, (u32)(((32 * s + 16 * tt + keyp) * NH + h) * DH + 32 * k4 + 8 * gb) * 2u);
    }
    f32x4 acc[4];
#pragma unroll
    for (int i = 0; i < 4; i++) { acc[i] = (f32x4){0.f, 0.f, 0.f, 0.f}; asm volatile("" : "+v"(acc[i])); }
    wait_until(tref + D1);
    {
      u32 off[2] = {(u32)s * 8192u + w * 2048u, (u32)s * 8192u + w * 2048u + 1024u};
      u32x4 a[2];
      u32 tiss = 0;
      u32 nsw = poll_chunks<2>(rmem, off, a, lane * 16u, tiss);
      if (!nsw) return;
      if (s > 2) ADAPT(D1, nsw);
      *(u32x4*)(memL + w * 2048 + lane * 16) = a[0];
      *(u32x4*)(memL + w * 2048 + 1024 + lane * 16) = a[1];
    }
    __syncthreads();
    STAMP(16 + 4 * s);
    bf16x8 af[8];
#pragma unroll
    for (int t = 0; t < 8; t++) af[t] = *(const bf16x8*)(memL + t * 1024 + lane * 16);
    __builtin_amdgcn_sched_barrier(0);
    const int ecol = 32 * w + 16 * (lane >> 5) + n, er0 = 4 * ((lane >> 4) & 1);
#pragma unroll
    for (int t = 0; t < 8; t++) { acc[0] = SMF(af[t], wf[0][t], acc[0], idx); acc[1] = SMF(af[t], wf[1][t], acc[1], idx); }
    __builtin_amdgcn_sched_barrier(0);
#pragma unroll
    for (int t = 0; t < 8; t++) {
      acc[2] = SMF(af[t], wf[2][t], acc[2], idx); acc[3] = SMF(af[t], wf[3][t], acc[3], idx);
      if (t >= 2 && t < 6) {
        const int j = t - 2;
        auto r = __builtin_amdgcn_permlane32_swap(f2u(acc[0][j]), f2u(acc[1][j]), false, false);
        *(__bf16*)(qs + (er0 + j) * 272 + ecol * 2) = (__bf16)(u2f(r[0]) + u2f(r[1]));
      }
      __builtin_amdgcn_sched_barrier(0);
    }
#pragma unroll
    for (int j = 0; j < 4; j++) {
      auto r = __builtin_amdgcn_permlane32_swap(f2u(acc[2][j]), f2u(acc[3][j]), false, false);
      *(__bf16*)(ks_ + (er0 + j) * 272 + ecol * 2) = (__bf16)(u2f(r[0]) + u2f(r[1]));
    }
    __syncthreads();
    STAMP(17 + 4 * s);
    if (w == 0 && s < NSEG - 1) {
      f32x4 sc[3];
#pragma unroll
      for (int i = 0; i < 3; i++) { sc[i] = (f32x4){0.f, 0.f, 0.f, 0.f}; asm volatile("" : "+v"(sc[i])); }
      bf16x8 qf[4], kf[4];
#pragma unroll
      for (int k4 = 0; k4 < 4; k4++) {
        qf[k4] = *(const bf16x8*)(qs + (lane & 7) * 272 + (32 * k4 + 8 * gb) * 2);
        kf[k4] = *(const bf16x8*)(ks_ + (lane & 7) * 272 + (32 * k4 + 8 * gb) * 2);
      }
      __builtin_amdgcn_sched_barrier(0);
#pragma unroll
      for (int k4 = 0; k4 < 4; k4++) {
        sc[0] = MFMA16(as_bf8(kx[0][k4]), qf[k4], sc[0]);
        sc[1] = MFMA16(as_bf8(kx[1][k4]), qf[k4], sc[1]);
        sc[2] = MFMA16(kf[k4], qf[k4], sc[2]);
      }
      const float sl = 0.08838834764831845f * 1.4426950408889634f;
      const bool hiq = n >= 8, hig = gb >= 2;
      float p[6];
#pragma unroll
      for (int j = 0; j < 4; j++) p[j] = hiq ? sc[1][j] : sc[0][j];
      p[4] = hig ? sc[2][2] : sc[2][0];
      p[5] = hig ? sc[2][3] : sc[2][1];
      float mx = max2r(max3r(p[0], p[1], p[2]), max3r(p[3], p[4], p[5]));
      mx = max2r(mx, u2f((u32)__builtin_amdgcn_mov_dpp((int)f2u(mx), 0x128, 0xF, 0xF, true)));
      { auto r = __builtin_amdgcn_permlane16_swap(f2u(mx), f2u(mx), false, false);
        mx = max2r(u2f(r[0]), u2f(r[1])); }
      { auto r = __builtin_amdgcn_permlane32_swap(f2u(mx), f2u(mx), false, false);
        mx = max2r(u2f(r[0]), u2f(r[1])); }
      const float nb = -mx * sl;
#pragma unroll
      for (int j = 0; j < 6; j++) p[j] = __builtin_amdgcn_exp2f(__builtin_fmaf(p[j], sl, nb));
      float sum = (p[0] + p[1]) + (p[2] + p[3]) + (hiq ? 0.f : (p[4] + p[5]));
      sum += u2f((u32)__builtin_amdgcn_mov_dpp((int)f2u(sum), 0x128, 0xF, 0xF, true));
      { auto r = __builtin_amdgcn_permlane16_swap(f2u(sum), f2u(sum), false, false);
        sum = u2f(r[0]) + u2f(r[1]); }
      { auto r = __builtin_amdgcn_permlane32_swap(f2u(sum), f2u(sum), false, false);
        sum = u2f(r[0]) + u2f(r[1]); }
      const float inv = __builtin_amdgcn_rcpf(sum);
      u32x2 px = {sane(pk2(p[0] * inv, p[1] * inv)), sane(pk2(p[2] * inv, p[3] * inv))};
      st8_sc1(px, rpex, (u32)s * 20480u + pxoff);
      if (!hiq) st4_sc1(sane(pk2(p[4] * inv, p[5] * inv)), rpex, (u32)s * 20480u + pmoff);
      STAMP(18 + 4 * s);
    } else if (w == 1) {
      const __amdgpu_buffer_rsrc_t rkm = mkrsrc(ws + OFF_KMEM);
#pragma unroll
      for (int j = 0; j < 2; j++) {
        int u = j * 64 + lane, r = u >> 4, c = u & 15;
        st16_sc1(*(const u32x4*)(ks_ + r * 272 + c * 16), rkm, (u32)((s * NH + h) * 1024 + r * 128 + c * 8) * 2u);
      }
      asm volatile("s_waitcnt vmcnt(0)" ::: "memory");
      if (lane == 0) st4_sc1((u32)(s + 1), rflag, OFF_KMF + h * 64u);
    }
    tref = now32();
  }
}

__device__ __forceinline__ void role_column(const u16* __restrict__ Wv, const u16* __restrict__ Wo, char* ws, char* lds, int c, int rho) {
  const int tid = threadIdx.x, lane = tid & 63, w = tid >> 6;
  const int n = lane & 15, gb = lane >> 4, cc = lane & 7;
  bf16x16 wf[4][8];
  wvo_task(Wv, Wo, ws, lds, c * 2 + rho);
  {
    u32 seenw = 0xFFFFFFFFu;
    if (!wait_progress(mkrsrc(ws + OFF_FLAG), OFF_WVF + (u32)((lane & 31) * 4 + (c >> 4)) * 16u, 1u, seenw)) return;
    const __amdgpu_buffer_rsrc_t rsrc = mkrsrc(ws + OFF_WVO + (size_t)(c * 4 + w) * 65536);
#pragma unroll
    for (int tl = 0; tl < 4; tl++)
#pragma unroll
      for (int t = 0; t < 8; t++)
        wf[tl][t] = as_bf16(ld16_sc1(rsrc, (u32)(((tl * 8 + t) * 2) * 64 + lane) * 16u), ld16_sc1(rsrc, (u32)(((tl * 8 + t) * 2 + 1) * 64 + lane) * 16u));
  }
  char* memL = lds;
  char* yl = lds + 8192 + w * 1024;
  float* red = (float*)(lds + 12288);
  const int idx = (lane & 8) ? (int)0xEEEEEEEE : 0x44444444;
  const __amdgpu_buffer_rsrc_t rflag = mkrsrc(ws + OFF_FLAG);
  const __amdgpu_buffer_rsrc_t rmem = mkrsrc(ws + OFF_MEMX);
  const __amdgpu_buffer_rsrc_t rpex = mkrsrc(ws + OFF_PEX);
  int D1 = w == 0 ? 750 : (w == 1 ? 850 : 1130), D2 = 2800; u32 tref = now32(), seen = 0xFFFFFFFFu;
  STAMP(1);
  for (int s = 0; s < NSEG; s++) {
    wait_until(tref + D1);
    {
      u32 off[2] = {(u32)s * 8192u + w * 2048u, (u32)s * 8192u + w * 2048u + 1024u};
      u32x4 a[2];
      u32 tiss = 0;
      u32 nsw = poll_chunks<2>(rmem, off, a, lane * 16u, tiss);
      if (!nsw) return;
      if (s > 2) ADAPT(D1, nsw);
      tref = now32();
      *(u32x4*)(memL + w * 2048 + lane * 16) = a[0];
      *(u32x4*)(memL + w * 2048 + 1024 + lane * 16) = a[1];
    }
    u32x4 uf[4][2];
    {
      if (!wait_progress(rflag, (u32)(8 * w + (lane & 7)) * 128u, s + 1, seen)) return;
      const __amdgpu_buffer_rsrc_t ru = mkrsrc(ws + OFF_UF + ((size_t)s * 64 + c) * 16384);
#pragma unroll
      for (int m = 0; m < 4; m++)
#pragma unroll
        for (int z = 0; z < 2; z++) uf[m][z] = ld16_sc1(ru, (u32)((((4 * w + m) * 2 + z) * 4 + gb) * 8 + cc) * 16u);
    }
    __syncthreads();
    STAMP(16 + 4 * s);
    bf16x16 yb;
    {
      f32x4 acc[4];
#pragma unroll
      for (int i = 0; i < 4; i++) acc[i] = (f32x4){0.f, 0.f, 0.f, 0.f};
      bf16x8 af[8];
#pragma unroll
      for (int t = 0; t < 8; t++) af[t] = *(const bf16x8*)(memL + t * 1024 + lane * 16);
      __builtin_amdgcn_sched_barrier(0);
#pragma unroll
      for (int t = 0; t < 8; t++)
#pragma unroll
        for (int i = 0; i < 4; i++) acc[i] = SMF(af[t], wf[i][t], acc[i], idx);
#pragma unroll
      for (int pr = 0; pr < 2; pr++) {
        float v[4];
#pragma unroll
        for (int j = 0; j < 4; j++) {
          auto r = __builtin_amdgcn_permlane32_swap(f2u(acc[2 * pr][j]), f2u(acc[2 * pr + 1][j]), false, false);
          v[j] = u2f(r[0]) + u2f(r[1]);
        }
        const int hl = 2 * (2 * pr + (lane >> 5)) + (n >> 3);
        u32x2 pv = {pk2(v[0], v[1]), pk2(v[2], v[3])};
        *(u32x2*)(yl + (hl * 8 + cc) * 16 + 8 * ((lane >> 4) & 1)) = pv;
      }
      asm volatile("s_waitcnt lgkmcnt(0)" ::: "memory");
      u32x4 hz[2];
#pragma unroll
      for (int z = 0; z < 2; z++) {
        const int ga = 2 * z + (gb >> 1);
        u32x2 a = *(const u32x2*)(yl + ((2 * ga) * 8 + cc) * 16 + 8 * (gb & 1));
        u32x2 b = *(const u32x2*)(yl + ((2 * ga + 1) * 8 + cc) * 16 + 8 * (gb & 1));
        hz[z] = (u32x4){a.x, b.x, a.y, b.y};
      }
      yb = as_bf16(hz[0], hz[1]);
      if (rho == 0) __builtin_amdgcn_raw_buffer_store_b128(*(const u32x4*)(yl + lane * 16), mkrsrc(ws + OFF_Y), (u32)(s * 64 + c) * 4096u + w * 1024u + lane * 16u, 0, 0);
    }
    STAMP(17 + 4 * s);
    if (s == NSEG - 1) break;
    wait_until(tref + D2);
    {
      const u32 pb = (u32)s * 20480u + rho * 10240u;
      u32 off[3] = {pb + (2 * w) * 1024u + lane * 16u, pb + (2 * w + 1) * 1024u + lane * 16u,
                    pb + 8192u + (u32)((8 * w + 2 * gb + ((lane >> 3) & 1)) * 4 + (lane & 3)) * 16u};
      u32x4 g[3];
      u32 tiss = 0;
      u32 nsw = poll_chunks<3>(rpex, off, g, 0u, tiss);
      if (!nsw) return;
      if (s > 2) ADAPT(D2, nsw);
      f32x4 dA = {0.f, 0.f, 0.f, 0.f}, dB = {0.f, 0.f, 0.f, 0.f};
      dA = SMF(as_bf8(g[0]), as_bf16(uf[0][0], uf[0][1]), dA, idx);
      dB = SMF(as_bf8(g[0]), as_bf16(uf[1][0], uf[1][1]), dB, idx);
      dA = SMF(as_bf8(g[1]), as_bf16(uf[2][0], uf[2][1]), dA, idx);
      dB = SMF(as_bf8(g[1]), as_bf16(uf[3][0], uf[3][1]), dB, idx);
      dA = SMF(as_bf8(g[2]), yb, dA, idx);
      float* rd = red + (s & 1) * 512;
      if (n < 8) {
#pragma unroll
        for (int j = 0; j < 4; j++) rd[(j * 8 + cc) * 16 + 4 * w + gb] = (gb & 1) ? dB[j] : dA[j];
      }
    }
    __syncthreads();
    STAMP(18 + 4 * s);
    if (w == 0) {
      const float* rd = red + (s & 1) * 512 + lane * 8;
      f32x4 a = *(const f32x4*)rd, b = *(const f32x4*)(rd + 4);
      float v = ((a[0] + a[1]) + (a[2] + a[3])) + ((b[0] + b[1]) + (b[2] + b[3]));
      v += u2f((u32)__builtin_amdgcn_mov_dpp((int)f2u(v), 0xB1, 0xF, 0xF, true));
      float o = u2f((u32)__builtin_amdgcn_mov_dpp((int)f2u(v), 0x4E, 0xF, 0xF, true));
      if (!(lane & 3)) st4_sc1(sane(pk2(v, o)), rmem, (u32)(s + 1) * 8192u + c * 128u + rho * 64u + (lane >> 2) * 4u);
      STAMP(19 + 4 * s);
    }
    tref = now32();
  }
}

__device__ __forceinline__ void load_h(u32x4 (&r)[8], const char* ws, int s) {
  const __amdgpu_buffer_rsrc_t rh = mkrsrc(ws + OFF_H);
#pragma unroll
  for (int j = 0; j < 8; j++) r[j] = ld16_sc1(rh, (u32)s * 32768u + (j * 256 + threadIdx.x) * 16u);
}
__device__ __forceinline__ void put_h(char* dst, const u32x4 (&r)[8]) {
#pragma unroll
  for (int j = 0; j < 8; j++) {
    int u = j * 256 + threadIdx.x;
    *(u32x4*)(dst + (u >> 6) * 1040 + (u & 63) * 16) = r[j];
  }
}

template <bool SC1>
__device__ __forceinline__ void store_T_tile(const f32x16& acc, char* base, int tokrow, int h, int w, int lane) {
  const __amdgpu_buffer_rsrc_t r = mkrsrc(base);
#pragma unroll
  for (int qp = 0; qp < 2; qp++) {
    u32 ax = pk2(acc[8 * qp + 0], acc[8 * qp + 1]), ay = pk2(acc[8 * qp + 2], acc[8 * qp + 3]);
    u32 bx = pk2(acc[8 * qp + 4], acc[8 * qp + 5]), by = pk2(acc[8 * qp + 6], acc[8 * qp + 7]);
    auto r0 = __builtin_amdgcn_permlane32_swap(ax, bx, false, false);
    auto r1 = __builtin_amdgcn_permlane32_swap(ay, by, false, false);
    u32x4 v = {(u32)r0[0], (u32)r1[0], (u32)r0[1], (u32)r1[1]};
    u32 off = (u32)(((tokrow + (lane & 31)) * NH + h) * DH + 32 * w + 16 * qp + 8 * (lane >> 5)) * 2u;
    if (SC1) __builtin_amdgcn_raw_buffer_store_b128(v, r, off, 0, 16);
    else __builtin_amdgcn_raw_buffer_store_b128(v, r, off, 0, 0);
  }
}

__device__ __forceinline__ void role_vu(const u16* __restrict__ Wv, const u16* __restrict__ Wo,
                                        char* ws, char* lds, int h) {
  const int tid = threadIdx.x, lane = tid & 63, w = tid >> 6, x = lane & 31, hh = lane >> 5;
  bf16x8 wv[32], wo[4][8];
  dma_tile<256>(lds, Wv + h * DH, DM, NH * DH);
  dma_wait();
#pragma unroll
  for (int ks = 0; ks < 32; ks++) wv[ks] = frag8(lds, 16 * ks + 8 * hh, 32 * w + (x & 16), 256, lane);
  __syncthreads();
  dma_tile<1024>(lds, Wo + (size_t)h * DH * DM, DH, DM);
  dma_wait();
#pragma unroll
  for (int nt = 0; nt < 4; nt++)
#pragma unroll
    for (int ks = 0; ks < 8; ks++) wo[nt][ks] = frag8(lds, 16 * ks + 8 * hh, 128 * w + 32 * nt + (x & 16), 1024, lane);
  __syncthreads();
  char* vs = lds + 66560;
  const __amdgpu_buffer_rsrc_t rflag = mkrsrc(ws + OFF_FLAG);
  u32x4 hr[8];
  { u32 seenh = 0xFFFFFFFFu; if (!wait_progress(rflag, OFF_DONE + (lane & 31) * 64u, 1u, seenh)) return; }
  load_h(hr, ws, 0);
  put_h(lds, hr);
  load_h(hr, ws, 1);
  __syncthreads();
  for (int s = 0; s < NSEG; s++) {
    const char* hb = lds + (s & 1) * 33280;
    f32x16 av;
#pragma unroll
    for (int i = 0; i < 16; i++) av[i] = 0.f;
    {
      bf16x8 hf[2][8];
#pragma unroll
      for (int k = 0; k < 8; k++) hf[0][k] = *(const bf16x8*)(hb + x * 1040 + (16 * k + 8 * hh) * 2);
#pragma unroll
      for (int ch = 0; ch < 4; ch++) {
        if (ch < 3) {
#pragma unroll
          for (int k = 0; k < 8; k++) hf[(ch + 1) & 1][k] = *(const bf16x8*)(hb + x * 1040 + (16 * (8 * ch + 8 + k) + 8 * hh) * 2);
        }
        __builtin_amdgcn_sched_barrier(0);
#pragma unroll
        for (int k = 0; k < 8; k++) av = MFMA32(wv[8 * ch + k], hf[ch & 1][k], av);
        __builtin_amdgcn_sched_barrier(0);
      }
    }
#pragma unroll
    for (int q = 0; q < 4; q++) {
      u32x2 pv = {pk2(av[4 * q], av[4 * q + 1]), pk2(av[4 * q + 2], av[4 * q + 3])};
      *(u32x2*)(vs + x * 272 + (32 * w + 8 * q + 4 * hh) * 2) = pv;
    }
    put_h(lds + ((s + 1) & 1) * 33280, hr);
    asm volatile("s_waitcnt vmcnt(0)" ::: "memory");
    __syncthreads();
    if (tid == 0 && s > 4) st4_sc1((u32)s, rflag, h * 128u);
    bf16x8 vf[8];
#pragma unroll
    for (int ks = 0; ks < 8; ks++) vf[ks] = *(const bf16x8*)(vs + x * 272 + (16 * ks + 8 * hh) * 2);
    const __amdgpu_buffer_rsrc_t ru = mkrsrc(ws + OFF_UF + (size_t)s * 64 * 16384);
#pragma unroll
    for (int nt = 0; nt < 4; nt++) {
      f32x16 au;
#pragma unroll
      for (int i = 0; i < 16; i++) au[i] = 0.f;
#pragma unroll
      for (int ks = 0; ks < 8; ks++) au = MFMA32(vf[ks], wo[nt][ks], au);
      const int d = 128 * w + 32 * nt + x, c = d >> 3, cc = d & 7;
#pragma unroll
      for (int xb = 0; xb < 2; xb++)
        st16_sc1(PACK_S(au, 8 * xb), ru, (u32)((((c * 16 + (h >> 1)) * 2 + (h & 1)) * 4 + 2 * hh + xb) * 8 + cc) * 16u);
    }
    if (s < 4 || s == NSEG - 1) asm volatile("s_waitcnt vmcnt(0)" ::: "memory");
    if (s == 30) { u32 seenh = 0xFFFFFFFFu; if (!wait_progress(rflag, OFF_DONE + lane * 64u, 1u, seenh)) return; }
    load_h(hr, ws, s + 2 < NSEG ? s + 2 : NSEG - 1);
    __syncthreads();
    if (tid == 0 && (s < 4 || s == NSEG - 1)) st4_sc1((u32)(s + 1), rflag, h * 128u);
    STAMP(16 + 4 * s);
  }
}

__device__ __forceinline__ void role_qk(const u16* __restrict__ Wq, const u16* __restrict__ Wk, char* ws, char* lds, int h) {
  const int tid = threadIdx.x, lane = tid & 63, w = tid >> 6, x = lane & 31, hh = lane >> 5;
  bf16x8 wq[32], wk[32];
  dma_tile<256>(lds, Wk + h * DH, DM, NH * DH);
  dma_wait();
#pragma unroll
  for (int ks = 0; ks < 32; ks++) wk[ks] = frag8(lds, 16 * ks + 8 * hh, 32 * w + (x & 16), 256, lane);
  __syncthreads();
  dma_tile<256>(lds, Wq + h * DH, DM, NH * DH);
  dma_wait();
#pragma unroll
  for (int ks = 0; ks < 32; ks++) wq[ks] = frag8(lds, 16 * ks + 8 * hh, 32 * w + (x & 16), 256, lane);
  __syncthreads();
  const __amdgpu_buffer_rsrc_t rflag = mkrsrc(ws + OFF_FLAG);
  u32x4 hr[8];
  { u32 seenh = 0xFFFFFFFFu; if (!wait_progress(rflag, OFF_DONE + (lane & 31) * 64u, 1u, seenh)) return; }
  load_h(hr, ws, 0);
  put_h(lds, hr);
  load_h(hr, ws, 1);
  __syncthreads();
  for (int s = 0; s < NSEG; s++) {
    const char* hb = lds + (s & 1) * 33280;
    f32x16 aq, ak;
#pragma unroll
    for (int i = 0; i < 16; i++) { aq[i] = 0.f; ak[i] = 0.f; }
    {
      bf16x8 hf[2][8];
#pragma unroll
      for (int k = 0; k < 8; k++) hf[0][k] = *(const bf16x8*)(hb + x * 1040 + (16 * k + 8 * hh) * 2);
#pragma unroll
      for (int ch = 0; ch < 4; ch++) {
        if (ch < 3) {
#pragma unroll
          for (int k = 0; k < 8; k++) hf[(ch + 1) & 1][k] = *(const bf16x8*)(hb + x * 1040 + (16 * (8 * ch + 8 + k) + 8 * hh) * 2);
        }
        __builtin_amdgcn_sched_barrier(0);
#pragma unroll
        for (int k = 0; k < 8; k++) { ak = MFMA32(wk[8 * ch + k], hf[ch & 1][k], ak); aq = MFMA32(wq[8 * ch + k], hf[ch & 1][k], aq); }
        __builtin_amdgcn_sched_barrier(0);
      }
    }
    put_h(lds + ((s + 1) & 1) * 33280, hr);
    asm volatile("s_waitcnt vmcnt(0)" ::: "memory");
    __syncthreads();
    if (tid == 0 && s > 4) st4_sc1((u32)s, rflag, 4096u + h * 128u);
    store_T_tile<true>(ak, ws + OFF_KX, 32 * s, h, w, lane);
    store_T_tile<true>(aq, ws + OFF_QX, 32 * s, h, w, lane);
    if (s < 4 || s == NSEG - 1) {
      asm volatile("s_waitcnt vmcnt(0)" ::: "memory");
      __syncthreads();
      if (tid == 0) st4_sc1((u32)(s + 1), rflag, 4096u + h * 128u);
    }
    if (s == 30) { u32 seenh = 0xFFFFFFFFu; if (!wait_progress(rflag, OFF_DONE + lane * 64u, 1u, seenh)) return; }
    load_h(hr, ws, s + 2 < NSEG ? s + 2 : NSEG - 1);
    STAMP(16 + 4 * s);
  }
}

__device__ __forceinline__ float xg_max(float v) {
  auto r = __builtin_amdgcn_permlane16_swap(f2u(v), f2u(v), false, false);
  v = fmaxf(u2f(r[0]), u2f(r[1]));
  auto r2 = __builtin_amdgcn_permlane32_swap(f2u(v), f2u(v), false, false);
  return fmaxf(u2f(r2[0]), u2f(r2[1]));
}
__device__ __forceinline__ float xg_sum(float v) {
  auto r = __builtin_amdgcn_permlane16_swap(f2u(v), f2u(v), false, false);
  v = u2f(r[0]) + u2f(r[1]);
  auto r2 = __builtin_amdgcn_permlane32_swap(f2u(v), f2u(v), false, false);
  return u2f(r2[0]) + u2f(r2[1]);
}
__device__ __forceinline__ void px_task(char* ws, int s, int hg) {
  const int tid = threadIdx.x, lane = tid & 63, w = tid >> 6;
  const int hd = hg * 4 + w, hl = w;
  const int n = lane & 15, g = lane >> 4;
  const __amdgpu_buffer_rsrc_t rq = mkrsrc(ws + OFF_QX + (size_t)s * 32 * 8192);
  const __amdgpu_buffer_rsrc_t rk = mkrsrc(ws + OFF_KX + (size_t)s * 32 * 8192);
  const __amdgpu_buffer_rsrc_t rkm = mkrsrc(ws + OFF_KMEM + (size_t)s * 65536);
  const int keyrow = 16 * ((n >> 2) & 1) + 4 * (n >> 3) + (n & 3);
  const int keyb = 16 * (g & 1) + 4 * (g >> 1);
  const float sl = 0.08838834764831845f * 1.4426950408889634f;
  bf16x8 qf[2][4], kf[2][4], mf[4];
  const __amdgpu_buffer_rsrc_t rflag = mkrsrc(ws + OFF_FLAG);
  { u32 seenq = 0xFFFFFFFFu; wait_progress(rflag, 4096u + hd * 128u, s + 1, seenq); }
#pragma unroll
  for (int ks = 0; ks < 4; ks++) {
#pragma unroll
    for (int t = 0; t < 2; t++) {
      qf[t][ks] = as_bf8(ld16_sc1(rq, (u32)(((16 * t + n) * NH + hd) * DH + 32 * ks + 8 * g) * 2u));
      kf[t][ks] = as_bf8(ld16_sc1(rk, (u32)(((8 * t + keyrow) * NH + hd) * DH + 32 * ks + 8 * g) * 2u));
    }
  }
  {
    u32 spins = 0, off = OFF_KMF + (u32)hd * 64u;
    while (true) {
      asm volatile("" : "+v"(off) :: "memory");
      u32 v = ld4_sc1(rflag, off);
      if ((int)v > s) break;
      __builtin_amdgcn_s_sleep(6);
      if (++spins > 800000u) break;
    }
  }
#pragma unroll
  for (int ks = 0; ks < 4; ks++) mf[ks] = as_bf8(ld16_sc1(rkm, (u32)(hd * 1024 + (n & 7) * 128 + 32 * ks + 8 * g) * 2u));
  const bool mvalid = (g >> 1) == (hl >> 1);
  u32x4* PXx = (u32x4*)(ws + OFF_PX);
  char* PXm = ws + OFF_PX + (4ull << 20);
#pragma unroll
  for (int qt = 0; qt < 2; qt++) {
    f32x4 sc[3];
#pragma unroll
    for (int i = 0; i < 3; i++) sc[i] = (f32x4){0.f, 0.f, 0.f, 0.f};
#pragma unroll
    for (int ks = 0; ks < 4; ks++) {
      sc[0] = MFMA16(kf[0][ks], qf[qt][ks], sc[0]);
      sc[1] = MFMA16(kf[1][ks], qf[qt][ks], sc[1]);
      sc[2] = MFMA16(mf[ks], qf[qt][ks], sc[2]);
    }
    const int qi = 16 * qt + n;
    float mx = -3.0e38f;
#pragma unroll
    for (int j = 0; j < 4; j++) {
      if (keyb + j > qi) sc[0][j] = -3.0e38f;
      if (keyb + 8 + j > qi) sc[1][j] = -3.0e38f;
      if (!mvalid) sc[2][j] = -3.0e38f;
      mx = fmaxf(mx, fmaxf(fmaxf(sc[0][j], sc[1][j]), sc[2][j]));
    }
    mx = xg_max(mx);
    float sum = 0.f;
#pragma unroll
    for (int i = 0; i < 3; i++)
#pragma unroll
      for (int j = 0; j < 4; j++) {
        float p = sc[i][j] > -1.0e38f ? __builtin_amdgcn_exp2f((sc[i][j] - mx) * sl) : 0.f;
        sc[i][j] = p; sum += p;
      }
    const float inv = __builtin_amdgcn_rcpf(xg_sum(sum));
    u32x4 pa = {pk2(sc[0][0] * inv, sc[0][1] * inv), pk2(sc[1][0] * inv, sc[1][1] * inv),
                pk2(sc[0][2] * inv, sc[0][3] * inv), pk2(sc[1][2] * inv, sc[1][3] * inv)};
    PXx[(((size_t)s * NH + hd) * 2 + qt) * 64 + lane] = pa;
    if (mvalid) {
      u32x2 pm = {pk2(sc[2][0] * inv, sc[2][1] * inv), pk2(sc[2][2] * inv, sc[2][3] * inv)};
      *(u32x2*)(PXm + ((((size_t)s * 8 + hg) * 2 + qt) * 64 + lane) * 16 + (hl & 1) * 8) = pm;
    }
  }
}

__global__ void __launch_bounds__(256, 1) k2_rec(const u16* __restrict__ Wq, const u16* __restrict__ Wk, const u16* __restrict__ Wv,
                                                  const u16* __restrict__ Wo, const u16* __restrict__ x, const u16* __restrict__ w1,
                                                  const u16* __restrict__ w2, const u16* __restrict__ cvec,
                                                  const u16* __restrict__ wmod, const u16* __restrict__ mem0, char* ws) {
  extern __shared__ __attribute__((aligned(16))) char lds[];
  const int b = blockIdx.x;
  STAMP(0);
  if (b < 32) role_help(x, w1, w2, cvec, wmod, mem0, ws, lds, b);
  else if (b < 64) role_score(Wq, Wk, ws, lds, b - 32);
  else if (b < 192) { const int q = b - 64; role_column(Wv, Wo, ws, lds, (q & 7) | ((q >> 4) << 3), (q >> 3) & 1); }
  else if (b < 224) role_vu(Wv, Wo, ws, lds, b - 192);
  else role_qk(Wq, Wk, ws, lds, b - 224);
}

__global__ void __launch_bounds__(512, 2) k3b_oproj(const u16* __restrict__ x, char* __restrict__ ws) {
  extern __shared__ __attribute__((aligned(16))) char lds[];
  const int b = blockIdx.x, tid = threadIdx.x, lane = tid & 63, w = tid >> 6;
  const int s = b >> 2, nq = b & 3;
  const int n = lane & 15, g = lane >> 4, cc = lane & 7;
  const u32x4* UF = (const u32x4*)(ws + OFF_UF) + (size_t)s * 64 * 1024;
  const char* Yg = ws + OFF_Y + (size_t)s * 64 * 4096;
  const u32x4* PXx = (const u32x4*)(ws + OFF_PX) + (size_t)s * NH * 128;
  const u32x4* PXm = (const u32x4*)(ws + OFF_PX + (4ull << 20)) + (size_t)s * 8 * 128;
  f32x4 acc[2][8];
#pragma unroll
  for (int i = 0; i < 2; i++)
#pragma unroll
    for (int j = 0; j < 8; j++) acc[i][j] = (f32x4){0.f, 0.f, 0.f, 0.f};
  u32x4 ub[4][8], pa[4][2], pm[2], yf[8];
#pragma unroll
  for (int hl = 0; hl < 4; hl++) {
    const int hd = 4 * w + hl;
#pragma unroll
    for (int nt = 0; nt < 8; nt++) ub[hl][nt] = UF[(size_t)(16 * nq + 2 * nt + (n >> 3)) * 1024 + (hd * 4 + g) * 8 + cc];
    pa[hl][0] = PXx[(hd * 2) * 64 + lane];
    pa[hl][1] = PXx[(hd * 2 + 1) * 64 + lane];
  }
  pm[0] = PXm[(w * 2) * 64 + lane];
  pm[1] = PXm[(w * 2 + 1) * 64 + lane];
  {
    const int ha = 4 * w + 2 * (g >> 1);
#pragma unroll
    for (int nt = 0; nt < 8; nt++) {
      const int c = 16 * nq + 2 * nt + (n >> 3);
      u32x2 ya = *(const u32x2*)(Yg + (size_t)c * 4096 + (ha * 8 + cc) * 16 + 8 * (g & 1));
      u32x2 yb = *(const u32x2*)(Yg + (size_t)c * 4096 + ((ha + 1) * 8 + cc) * 16 + 8 * (g & 1));
      yf[nt] = (u32x4){ya.x, ya.y, yb.x, yb.y};
    }
  }
  __builtin_amdgcn_sched_barrier(0);
#pragma unroll
  for (int hl = 0; hl < 4; hl++)
#pragma unroll
    for (int nt = 0; nt < 8; nt++) {
      acc[0][nt] = MFMA16(as_bf8(pa[hl][0]), as_bf8(ub[hl][nt]), acc[0][nt]);
      acc[1][nt] = MFMA16(as_bf8(pa[hl][1]), as_bf8(ub[hl][nt]), acc[1][nt]);
    }
#pragma unroll
  for (int nt = 0; nt < 8; nt++) {
    acc[0][nt] = MFMA16(as_bf8(pm[0]), as_bf8(yf[nt]), acc[0][nt]);
    acc[1][nt] = MFMA16(as_bf8(pm[1]), as_bf8(yf[nt]), acc[1][nt]);
  }
  float* red = (float*)lds;
#pragma unroll
  for (int qt = 0; qt < 2; qt++)
#pragma unroll
    for (int nt = 0; nt < 8; nt++)
#pragma unroll
      for (int j = 0; j < 4; j++) red[(w * 32 + 16 * qt + 4 * g + j) * 132 + 16 * nt + n] = acc[qt][nt][j];
  __syncthreads();
  const float* mod = (const float*)(ws + OFF_MOD);
  u16* x1 = (u16*)(ws + OFF_X1);
  {
    const int r = tid >> 4, c8 = (tid & 15) * 8;
    float o[8];
#pragma unroll
    for (int j = 0; j < 8; j++) o[j] = 0.f;
#pragma unroll
    for (int ww = 0; ww < 8; ww++) {
      f32x4 a = *(const f32x4*)(red + (ww * 32 + r) * 132 + c8), bq = *(const f32x4*)(red + (ww * 32 + r) * 132 + c8 + 4);
#pragma unroll
      for (int j = 0; j < 4; j++) { o[j] += a[j]; o[4 + j] += bq[j]; }
    }
    const int col = 128 * nq + c8;
    const size_t go = (size_t)(32 * s + r) * DM + col;
    u32x4 xv = *(const u32x4*)(x + go);
    float xf[8] = {bf_lo(xv.x), bf_hi(xv.x), bf_lo(xv.y), bf_hi(xv.y), bf_lo(xv.z), bf_hi(xv.z), bf_lo(xv.w), bf_hi(xv.w)};
#pragma unroll
    for (int j = 0; j < 8; j++) o[j] = xf[j] + bfr(mod[1024 + col + j] * bfr(o[j]));
    u32x4 rv = {pk2(o[0], o[1]), pk2(o[2], o[3]), pk2(o[4], o[5]), pk2(o[6], o[7])};
    *(u32x4*)(x1 + go) = rv;
  }
}

__global__ void __launch_bounds__(512, 2) k4_mlp1(const u16* __restrict__ b1, char* __restrict__ ws) {
  extern __shared__ __attribute__((aligned(16))) char lds[];
  const int b = blockIdx.x, tid = threadIdx.x, lane = tid & 63, w = tid >> 6;
  const int mt0 = b / 7, nb = b - mt0 * 7;
  const float* mod = (const float*)(ws + OFF_MOD);
  const u16* x1 = (const u16*)(ws + OFF_X1);
  const int n = lane & 15, g = lane >> 4;
  const f32x4 mv = *(const f32x4*)(mod + 1536 + (tid & 255) * 4);
  u32x4 xv[2][4];
#pragma unroll
  for (int it = 0; it < 2; it++)
#pragma unroll
    for (int j = 0; j < 4; j++) xv[it][j] = *(const u32x4*)(x1 + (size_t)(mt0 * 64 + it * 32 + w * 4 + g) * DM + (j * 16 + n) * 8);
  const u32x4* WF = (const u32x4*)(ws + OFF_W1F) + (size_t)(nb * 16 + w * 2) * 16 * 64 + lane;
  u32x4 bfm[2][16];
#pragma unroll
  for (int nt = 0; nt < 2; nt++)
#pragma unroll
    for (int ks = 0; ks < 8; ks++) bfm[nt][ks] = WF[(nt * 16 + ks) * 64];
  __builtin_amdgcn_sched_barrier(0);
  float* modl = (float*)(lds + 66560);
  if (tid < 256) *(f32x4*)(modl + tid * 4) = tid >= 128 ? mv + 1.f : mv;
  __syncthreads();
#pragma unroll
  for (int it = 0; it < 2; it++) {
    f32x2 f[16];
#pragma unroll
    for (int j = 0; j < 4; j++) {
      f[4 * j + 0] = (f32x2){bf_lo(xv[it][j].x), bf_hi(xv[it][j].x)}; f[4 * j + 1] = (f32x2){bf_lo(xv[it][j].y), bf_hi(xv[it][j].y)};
      f[4 * j + 2] = (f32x2){bf_lo(xv[it][j].z), bf_hi(xv[it][j].z)}; f[4 * j + 3] = (f32x2){bf_lo(xv[it][j].w), bf_hi(xv[it][j].w)};
    }
    f32x2 s2 = {0.f, 0.f};
#pragma unroll
    for (int i = 0; i < 16; i++) s2 += f[i];
    float m = row16_sum(s2.x + s2.y) * (1.f / DM);
    f32x2 q2 = {0.f, 0.f};
#pragma unroll
    for (int i = 0; i < 16; i++) { f[i] -= m; q2 += f[i] * f[i]; }
    float rs = rsqrtf(row16_sum(q2.x + q2.y) * (1.f / DM) + 1e-5f);
    s2 = (f32x2){0.f, 0.f};
#pragma unroll
    for (int j = 0; j < 4; j++) {
      const int c0 = (j * 16 + n) * 8;
      f32x4 c0v = *(const f32x4*)(modl + 512 + c0), c1v = *(const f32x4*)(modl + 512 + c0 + 4);
      f32x4 s0v = *(const f32x4*)(modl + c0), s1v = *(const f32x4*)(modl + c0 + 4);
      f[4 * j + 0] = f[4 * j + 0] * rs * (f32x2){c0v[0], c0v[1]} + (f32x2){s0v[0], s0v[1]};
      f[4 * j + 1] = f[4 * j + 1] * rs * (f32x2){c0v[2], c0v[3]} + (f32x2){s0v[2], s0v[3]};
      f[4 * j + 2] = f[4 * j + 2] * rs * (f32x2){c1v[0], c1v[1]} + (f32x2){s1v[0], s1v[1]};
      f[4 * j + 3] = f[4 * j + 3] * rs * (f32x2){c1v[2], c1v[3]} + (f32x2){s1v[2], s1v[3]};
      s2 += (f[4 * j + 0] + f[4 * j + 1]) + (f[4 * j + 2] + f[4 * j + 3]);
    }
    m = row16_sum(s2.x + s2.y) * (1.f / DM);
    q2 = (f32x2){0.f, 0.f};
#pragma unroll
    for (int i = 0; i < 16; i++) { f[i] -= m; q2 += f[i] * f[i]; }
    rs = rsqrtf(row16_sum(q2.x + q2.y) * (1.f / DM) + 1e-5f);
#pragma unroll
    for (int j = 0; j < 4; j++) {
      f32x2 o0 = f[4 * j] * rs, o1 = f[4 * j + 1] * rs, o2 = f[4 * j + 2] * rs, o3 = f[4 * j + 3] * rs;
      u32x4 o = {pk2(o0.x, o0.y), pk2(o1.x, o1.y), pk2(o2.x, o2.y), pk2(o3.x, o3.y)};
      *(u32x4*)(lds + (it * 32 + w * 4 + g) * 1040 + (j * 16 + n) * 16) = o;
    }
    __builtin_amdgcn_sched_barrier(0);
  }
#pragma unroll
  for (int nt = 0; nt < 2; nt++)
#pragma unroll
    for (int ks = 8; ks < 16; ks++) bfm[nt][ks] = WF[(nt * 16 + ks) * 64];
  __builtin_amdgcn_sched_barrier(0);
  __syncthreads();
  f32x4 acc[4][2];
#pragma unroll
  for (int i = 0; i < 4; i++)
#pragma unroll
    for (int j = 0; j < 2; j++) acc[i][j] = (f32x4){0.f, 0.f, 0.f, 0.f};
#pragma unroll
  for (int ks = 0; ks < 16; ks++) {
    bf16x8 af[4];
#pragma unroll
    for (int mt = 0; mt < 4; mt++) af[mt] = *(const bf16x8*)(lds + (16 * mt + n) * 1040 + (32 * ks + 8 * g) * 2);
#pragma unroll
    for (int mt = 0; mt < 4; mt++)
#pragma unroll
      for (int nt = 0; nt < 2; nt++) acc[mt][nt] = MFMA16(as_bf8(bfm[nt][ks]), af[mt], acc[mt][nt]);
    if (ks & 1) __builtin_amdgcn_sched_barrier(0);
  }
  u32x4* hidF = (u32x4*)(ws + OFF_HID);
  float bb[2][4];
#pragma unroll
  for (int nt = 0; nt < 2; nt++) {
    u32x2 bv = *(const u32x2*)(b1 + nb * 256 + w * 32 + 16 * nt + 4 * g);
    bb[nt][0] = bf_lo(bv.x); bb[nt][1] = bf_hi(bv.x); bb[nt][2] = bf_lo(bv.y); bb[nt][3] = bf_hi(bv.y);
  }
#pragma unroll
  for (int mt = 0; mt < 4; mt++) {
    float o[8];
#pragma unroll
    for (int nt = 0; nt < 2; nt++)
#pragma unroll
      for (int j = 0; j < 4; j++) {
        float u = bfr(bfr(acc[mt][nt][j]) + bb[nt][j]);
        float z = 0.7978845608028654f * (u + 0.044715f * u * u * u);
        float e = __builtin_amdgcn_exp2f(z * 2.8853900817779268f);
        float th = 1.f - 2.f * __builtin_amdgcn_rcpf(e + 1.f);
        o[4 * nt + j] = 0.5f * u * (1.f + th);
      }
    u32x4 ov = {pk2(o[0], o[1]), pk2(o[2], o[3]), pk2(o[4], o[5]), pk2(o[6], o[7])};
    hidF[((size_t)(mt0 * 4 + mt) * 56 + nb * 8 + w) * 64 + lane] = ov;
  }
}

__global__ void __launch_bounds__(512, 2) k5_mlp2(const u16* __restrict__ b2, char* __restrict__ ws, u16* __restrict__ out) {
  extern __shared__ __attribute__((aligned(16))) char lds[];
  const int b = blockIdx.x, tid = threadIdx.x, lane = tid & 63, w = tid >> 6;
  const int mt0 = (b & 7) * 4 + (b >> 6), nb = (b >> 3) & 7;
  const int n = lane & 15, g = lane >> 4;
  const u32x4* hidF = (const u32x4*)(ws + OFF_HID) + ((size_t)(mt0 * 4) * 56 + w * 7) * 64 + lane;
  const u32x4* WF = (const u32x4*)(ws + OFF_W2F) + ((size_t)(nb * 4) * 56 + w * 7) * 64 + lane;
  f32x4 acc[4][4];
#pragma unroll
  for (int i = 0; i < 4; i++)
#pragma unroll
    for (int j = 0; j < 4; j++) acc[i][j] = (f32x4){0.f, 0.f, 0.f, 0.f};
#pragma unroll
  for (int hb = 0; hb < 2; hb++) {
    const int nk = hb ? 3 : 4;
    u32x4 bfm[4][4]; bf16x8 af[4][4];
#pragma unroll
    for (int k = 0; k < 4; k++) if (k < nk) {
#pragma unroll
      for (int nt = 0; nt < 4; nt++) bfm[k][nt] = WF[(nt * 56 + hb * 4 + k) * 64];
#pragma unroll
      for (int mt = 0; mt < 4; mt++) af[k][mt] = as_bf8(hidF[(mt * 56 + hb * 4 + k) * 64]);
    }
    __builtin_amdgcn_sched_barrier(0);
#pragma unroll
    for (int k = 0; k < 4; k++) if (k < nk) {
#pragma unroll
      for (int mt = 0; mt < 4; mt++)
#pragma unroll
        for (int nt = 0; nt < 4; nt++) acc[mt][nt] = MFMA16(as_bf8(bfm[k][nt]), af[k][mt], acc[mt][nt]);
    }
  }
  float* red = (float*)lds;
#pragma unroll
  for (int mt = 0; mt < 4; mt++)
#pragma unroll
    for (int nt = 0; nt < 4; nt++) *(f32x4*)(red + (w * 64 + 16 * mt + n) * 68 + 16 * nt + 4 * g) = acc[mt][nt];
  __syncthreads();
  const float* mod = (const float*)(ws + OFF_MOD);
  const u16* x1 = (const u16*)(ws + OFF_X1);
  {
    const int r = tid >> 3, c8 = (tid & 7) * 8;
    float o[8];
#pragma unroll
    for (int j = 0; j < 8; j++) o[j] = 0.f;
#pragma unroll
    for (int ww = 0; ww < 8; ww++) {
      f32x4 a = *(const f32x4*)(red + (ww * 64 + r) * 68 + c8), bq = *(const f32x4*)(red + (ww * 64 + r) * 68 + c8 + 4);
#pragma unroll
      for (int j = 0; j < 4; j++) { o[j] += a[j]; o[4 + j] += bq[j]; }
    }
    const int col = 64 * nb + c8;
    const size_t go = (size_t)(mt0 * 64 + r) * DM + col;
    u32x4 xv = *(const u32x4*)(x1 + go), bv = *(const u32x4*)(b2 + col);
    float xf[8] = {bf_lo(xv.x), bf_hi(xv.x), bf_lo(xv.y), bf_hi(xv.y), bf_lo(xv.z), bf_hi(xv.z), bf_lo(xv.w), bf_hi(xv.w)};
    float bb[8] = {bf_lo(bv.x), bf_hi(bv.x), bf_lo(bv.y), bf_hi(bv.y), bf_lo(bv.z), bf_hi(bv.z), bf_lo(bv.w), bf_hi(bv.w)};
#pragma unroll
    for (int j = 0; j < 8; j++) o[j] = xf[j] + bfr(mod[2560 + col + j] * bfr(bfr(o[j]) + bb[j]));
    u32x4 rv = {pk2(o[0], o[1]), pk2(o[2], o[3]), pk2(o[4], o[5]), pk2(o[6], o[7])};
    *(u32x4*)(out + go) = rv;
  }
}

extern "C" void kernel_launch(void* const* d_in, const int* in_sizes, int n_in, void* d_out, int out_size, void* d_ws,
                              size_t ws_size, hipStream_t stream) {
  const u16* x = (const u16*)d_in[0];
  const u16* mem0 = (const u16*)d_in[1];
  const u16* Wq = (const u16*)d_in[2];
  const u16* Wk = (const u16*)d_in[3];
  const u16* Wv = (const u16*)d_in[4];
  const u16* Wo = (const u16*)d_in[5];
  const u16* w1 = (const u16*)d_in[8];
  const u16* b1 = (const u16*)d_in[9];
  const u16* w2 = (const u16*)d_in[10];
  const u16* b2 = (const u16*)d_in[11];
  const u16* cvec = (const u16*)d_in[12];
  const u16* wmod = (const u16*)d_in[13];
  char* ws = (char*)d_ws;
  hipFuncSetAttribute((const void*)k2_rec, hipFuncAttributeMaxDynamicSharedMemorySize, 135168);
  hipMemsetAsync(ws, 0xFF, OFF_H, stream);
  k2_rec<<<256, 256, 135168, stream>>>(Wq, Wk, Wv, Wo, x, w1, w2, cvec, wmod, mem0, ws);
  hipFuncSetAttribute((const void*)k3b_oproj, hipFuncAttributeMaxDynamicSharedMemorySize, 135168);
  k3b_oproj<<<256, 512, 135168, stream>>>(x, ws);
  hipFuncSetAttribute((const void*)k4_mlp1, hipFuncAttributeMaxDynamicSharedMemorySize, 70656);
  hipFuncSetAttribute((const void*)k5_mlp2, hipFuncAttributeMaxDynamicSharedMemorySize, 139264);
  k4_mlp1<<<224, 512, 70656, stream>>>(b1, ws);
  k5_mlp2<<<256, 512, 139264, stream>>>(b2, ws, (u16*)d_out);
}
```

```cpp
#include <hip/hip_runtime.h>
#include <hip/hip_bf16.h>
#include <stdint.h>

typedef unsigned int u32;
typedef unsigned short u16;
typedef unsigned long long u64;
typedef __attribute__((ext_vector_type(2))) __bf16 bf16x2;
typedef __attribute__((ext_vector_type(8))) __bf16 bf16x8;
typedef __attribute__((ext_vector_type(16))) __bf16 bf16x16;
typedef __attribute__((ext_vector_type(2))) float f32x2;
typedef __attribute__((ext_vector_type(4))) float f32x4;
typedef __attribute__((ext_vector_type(16))) float f32x16;
typedef __attribute__((ext_vector_type(2))) u32 u32x2;
typedef __attribute__((ext_vector_type(4))) u32 u32x4;
typedef __attribute__((ext_vector_type(8))) u32 u32x8;

#define NTOK 2048
#define DM 512
#define NH 32
#define DH 128
#define NSEG 64
#define FF 1792
#define SENT 0xFFFFFFFFu

#define OFF_FLAG 0ull
#define OFF_DONE 8192u
#define OFF_KMF 12288u
#define OFF_WVF 14336u
#define OFF_MDF 28672u
#define OFF_MOD 16384ull
#define OFF_MEMX 32768ull
#define OFF_PEX 0x90000ull
#define OFF_H (2ull << 20)
#define OFF_KMEM (4ull << 20)
#define OFF_WVO (8ull << 20)
#define OFF_KX (24ull << 20)
#define OFF_QX (40ull << 20)
#define OFF_Y (56ull << 20)
#define OFF_UF (72ull << 20)
#define OFF_PX (136ull << 20)
#define OFF_X1 (142ull << 20)
#define OFF_Y2 (146ull << 20)
#define OFF_HID (148ull << 20)
#define OFF_W1F (156ull << 20)
#define OFF_W2F (158ull << 20)
#define OFF_DBG (200ull << 20)
#define STAMP(slot) do { } while (0)

__device__ __forceinline__ u32 pk2(float a, float b) {
  bf16x2 v; v[0] = (__bf16)a; v[1] = (__bf16)b; return __builtin_bit_cast(u32, v);
}
__device__ __forceinline__ u32 f2u(float x) { return __builtin_bit_cast(u32, x); }
__device__ __forceinline__ float u2f(u32 x) { return __builtin_bit_cast(float, x); }
__device__ __forceinline__ float bf_lo(u32 x) { return __builtin_bit_cast(float, x << 16); }
__device__ __forceinline__ float bf_hi(u32 x) { return __builtin_bit_cast(float, x & 0xffff0000u); }
__device__ __forceinline__ float bfr(float a) { return (float)(__bf16)a; }
__device__ __forceinline__ __amdgpu_buffer_rsrc_t mkrsrc(const void* p) {
  return __builtin_amdgcn_make_buffer_rsrc((void*)p, 0, 0x7fffffff, 0x00020000);
}
__device__ __forceinline__ u32x4 ld16_sc1(__amdgpu_buffer_rsrc_t r, u32 off) { return __builtin_amdgcn_raw_buffer_load_b128(r, off, 0, 16); }
__device__ __forceinline__ u32 ld4_sc1(__amdgpu_buffer_rsrc_t r, u32 off) { return __builtin_amdgcn_raw_buffer_load_b32(r, off, 0, 16); }
__device__ __forceinline__ void st16_sc1(u32x4 v, __amdgpu_buffer_rsrc_t r, u32 off) { __builtin_amdgcn_raw_buffer_store_b128(v, r, off, 0, 16); }
__device__ __forceinline__ void st8_sc1(u32x2 v, __amdgpu_buffer_rsrc_t r, u32 off) { __builtin_amdgcn_raw_buffer_store_b64(v, r, off, 0, 16); }
__device__ __forceinline__ void st4_sc1(u32 v, __amdgpu_buffer_rsrc_t r, u32 off) { __builtin_amdgcn_raw_buffer_store_b32(v, r, off, 0, 16); }
__device__ __forceinline__ u32 badm(u32x4 a) { return (u32)(a.x == SENT) | (u32)(a.y == SENT) | (u32)(a.z == SENT) | (u32)(a.w == SENT); }
__device__ __forceinline__ u32 sane(u32 x) { return x < 0xFFFFFFFEu ? x : 0xFFFFFFFEu; }
__device__ __forceinline__ u32 now32() { return (u32)__builtin_amdgcn_s_memtime(); }
__device__ __forceinline__ void wait_until(u32 t) { while ((int)(t - now32()) > 0) { } }
__device__ __forceinline__ bf16x8 as_bf8(u32x4 v) { return __builtin_bit_cast(bf16x8, v); }
__device__ __forceinline__ bf16x16 as_bf16(u32x4 a, u32x4 b) {
  u32x8 v = {a.x, a.y, a.z, a.w, b.x, b.y, b.z, b.w}; return __builtin_bit_cast(bf16x16, v);
}
__device__ __forceinline__ float max3r(float a, float b, float c) { float r; asm("v_max3_f32 %0, %1, %2, %3" : "=v"(r) : "v"(a), "v"(b), "v"(c)); return r; }
__device__ __forceinline__ float max2r(float a, float b) { float r; asm("v_max_f32 %0, %1, %2" : "=v"(r) : "v"(a), "v"(b)); return r; }
#define MFMA16(a, b, c) __builtin_amdgcn_mfma_f32_16x16x32_bf16((a), (b), (c), 0, 0, 0)
#define MFMA32(a, b, c) __builtin_amdgcn_mfma_f32_32x32x16_bf16((a), (b), (c), 0, 0, 0)
#define SMF(a, b, c, idx) __builtin_amdgcn_smfmac_f32_16x16x64_bf16((a), (b), (c), (idx), 0, 0)

template <int N>
__device__ __forceinline__ u32 poll_chunks(__amdgpu_buffer_rsrc_t r, const u32 (&off)[N], u32x4 (&a)[N], u32 lane16, u32& tiss) {
  u32 spins = 0;
  while (true) {
    asm volatile("" : "+v"(lane16) :: "memory");
    if (spins) tiss = now32();
    u32 bad = 0;
#pragma unroll
    for (int j = 0; j < N; j++) a[j] = ld16_sc1(r, off[j] + lane16);
#pragma unroll
    for (int j = 0; j < N; j++) bad |= badm(a[j]);
    ++spins;
    if (!__any(bad)) return spins;
    if (spins > 400000u) return 0;
  }
}

typedef __attribute__((ext_vector_type(4))) short s16x4;
typedef __attribute__((address_space(3))) s16x4 lds_s16x4;
template <int ROWB>
__device__ __forceinline__ void dma_tile(char* lds, const u16* src, int rows, int gstride) {
  constexpr int LPR = ROWB / 16;
  const int total = rows * LPR;
  for (int u = threadIdx.x; u < total; u += 256) {
    const int r = u / LPR, c = u % LPR;
    __builtin_amdgcn_global_load_lds((const u32*)(src + (size_t)r * gstride + c * 8), (u32*)(lds + u * 16), 16, 0, 0);
  }
}
__device__ __forceinline__ void dma_wait() { asm volatile("s_waitcnt vmcnt(0)" ::: "memory"); __syncthreads(); }
__device__ __forceinline__ u32x2 tr4(const char* lds, int row0, int col0, int pitch, int lane) {
  const char* p = lds + (row0 + ((lane & 15) >> 2)) * pitch + (col0 + 4 * (lane & 3)) * 2;
  s16x4 v = __builtin_amdgcn_ds_read_tr16_b64_v4i16((lds_s16x4*)p);
  return __builtin_bit_cast(u32x2, v);
}
__device__ __forceinline__ bf16x8 frag8(const char* lds, int k0, int col0, int pitch, int lane) {
  u32x2 a = tr4(lds, k0, col0, pitch, lane), b = tr4(lds, k0 + 4, col0, pitch, lane);
  return as_bf8((u32x4){a.x, a.y, b.x, b.y});
}
__device__ __forceinline__ u32x4 frag8s(const char* lds, int k0, int col0, int pitch, int lane) {
  u32x2 a = tr4(lds, k0, col0, pitch, lane), b = tr4(lds, k0 + 8, col0, pitch, lane);
  return (u32x4){a.x, b.x, a.y, b.y};
}
#define PACK_S(acc, b) (u32x4){pk2((acc)[(b)], (acc)[(b) + 1]), pk2((acc)[(b) + 4], (acc)[(b) + 5]), pk2((acc)[(b) + 2], (acc)[(b) + 3]), pk2((acc)[(b) + 6], (acc)[(b) + 7])}

__device__ __forceinline__ void wvo_task(const u16* __restrict__ Wv, const u16* __restrict__ Wo, char* ws, char* lds, int task) {
  const int tid = threadIdx.x, lane = tid & 63, w = tid >> 6;
  const int h = task >> 2, nq = task & 3, x = lane & 31, hh = lane >> 5;
  dma_tile<256>(lds, Wo + (size_t)h * DH * DM + nq * 128, DH, DM);
  bf16x8 a[4][8];
#pragma unroll
  for (int mt = 0; mt < 4; mt++)
#pragma unroll
    for (int ks = 0; ks < 8; ks++)
      a[mt][ks] = *(const bf16x8*)(Wv + ((size_t)(128 * w + 32 * mt + x) * NH + h) * DH + 16 * ks + 8 * hh);
  dma_wait();
  const __amdgpu_buffer_rsrc_t rd = mkrsrc(ws + OFF_WVO);
#pragma unroll
  for (int nt = 0; nt < 4; nt++) {
    bf16x8 bw[8];
#pragma unroll
    for (int ks = 0; ks < 8; ks++) bw[ks] = frag8(lds, 16 * ks + 8 * hh, 32 * nt + (x & 16), 256, lane);
#pragma unroll
    for (int mt = 0; mt < 4; mt++) {
      f32x16 acc;
#pragma unroll
      for (int i = 0; i < 16; i++) acc[i] = 0.f;
#pragma unroll
      for (int ks = 0; ks < 8; ks++) acc = MFMA32(a[mt][ks], bw[ks], acc);
      const int d = 128 * nq + 32 * nt + x, c = d >> 3, cc = d & 7;
#pragma unroll
      for (int xb = 0; xb < 2; xb++) {
        const int dp = 128 * w + 32 * mt + 16 * xb, t = dp >> 6, ga = (dp >> 4) & 3, z = ga >> 1, gb = 2 * (ga & 1) + hh;
        const int lp = (h & 1) * 8 + cc + 16 * gb;
        u32 o = (u32)(((((c * 4 + (h >> 3)) * 4 + ((h >> 1) & 3)) * 8 + t) * 2 + z) * 64 + lp);
        st16_sc1(PACK_S(acc, 8 * xb), rd, o * 16u);
      }
    }
  }
  asm volatile("s_waitcnt vmcnt(0)" ::: "memory");
  __syncthreads();
  if (tid == 0) st4_sc1(1u, mkrsrc(ws + OFF_FLAG), OFF_WVF + task * 16u);
}

__device__ __forceinline__ void mod_cols(const u16* __restrict__ cvec, const u16* __restrict__ wmod, char* ws, char* lds, int col0) {
  const int tid = threadIdx.x, cp = tid & 15, ksl = tid >> 4;
  float* red = (float*)lds;
  u32 wv[32];
#pragma unroll
  for (int k = 0; k < 32; k++) wv[k] = *(const u32*)(wmod + (size_t)(ksl * 32 + k) * 3072 + col0 + cp * 2);
  float* sl_ = (float*)(lds + 2048);
  __syncthreads();
  {
    u32 cw = *(const u32*)(cvec + tid * 2);
    float c0 = bf_lo(cw), c1 = bf_hi(cw);
    sl_[tid * 2] = bfr(c0 / (1.f + __expf(-c0)));
    sl_[tid * 2 + 1] = bfr(c1 / (1.f + __expf(-c1)));
  }
  __syncthreads();
  float a0 = 0.f, a1 = 0.f;
#pragma unroll
  for (int k4 = 0; k4 < 8; k4++) {
    f32x4 sv = *(const f32x4*)(sl_ + ksl * 32 + k4 * 4);
#pragma unroll
    for (int j = 0; j < 4; j++) { a0 += sv[j] * bf_lo(wv[k4 * 4 + j]); a1 += sv[j] * bf_hi(wv[k4 * 4 + j]); }
  }
  red[ksl * 32 + cp * 2] = a0; red[ksl * 32 + cp * 2 + 1] = a1;
  __syncthreads();
  if (tid < 32) {
    float sacc = 0.f;
#pragma unroll
    for (int k = 0; k < 16; k++) sacc += red[k * 32 + tid];
    st4_sc1(f2u(bfr(sacc)), mkrsrc(ws + OFF_MOD), (u32)(col0 + tid) * 4u);
  }
}

__device__ __forceinline__ float row16_sum(float v) {
  v += u2f((u32)__builtin_amdgcn_mov_dpp((int)f2u(v), 0x128, 0xF, 0xF, true));
  v += u2f((u32)__builtin_amdgcn_mov_dpp((int)f2u(v), 0x124, 0xF, 0xF, true));
  v += u2f((u32)__builtin_amdgcn_mov_dpp((int)f2u(v), 0x122, 0xF, 0xF, true));
  v += u2f((u32)__builtin_amdgcn_mov_dpp((int)f2u(v), 0x121, 0xF, 0xF, true));
  return v;
}

__device__ __forceinline__ bool wait_progress(__amdgpu_buffer_rsrc_t rf, u32 off, u32 need, u32& seen) {
  u32 spins = 0;
  if (__all((int)seen >= (int)need)) return true;
  while (true) {
    asm volatile("" : "+v"(off) :: "memory");
    seen = ld4_sc1(rf, off);
    if (__all((int)seen >= (int)need)) return true;
    __builtin_amdgcn_s_sleep(2);
    if (++spins > 400000u) return false;
  }
}

__device__ __forceinline__ void px_task(char* ws, int s, int hg);
__device__ __forceinline__ void role_help(const u16* __restrict__ x, const u16* __restrict__ w1, const u16* __restrict__ w2,
                                          const u16* __restrict__ cvec, const u16* __restrict__ wmod, const u16* __restrict__ mem0,
                                          char* ws, char* lds, int a0) {
  const int tid = threadIdx.x, lane = tid & 63, w = tid >> 6, n = lane & 15, g = lane >> 4;
  u32x4 xpre[2][2][4];
#pragma unroll
  for (int sg = 0; sg < 2; sg++)
#pragma unroll
    for (int it = 0; it < 2; it++)
#pragma unroll
      for (int j = 0; j < 4; j++) xpre[sg][it][j] = *(const u32x4*)(x + (size_t)((a0 + 32 * sg) * 32 + it * 16 + w * 4 + g) * DM + (j * 16 + n) * 8);
  if (a0 == 31) {
    for (int i = tid; i < 512; i += 256) {
      u32x4 v = *(const u32x4*)(mem0 + (i & 7) * DM + (i >> 3) * 8);
      v.x = sane(v.x); v.y = sane(v.y); v.z = sane(v.z); v.w = sane(v.w);
      st16_sc1(v, mkrsrc(ws + OFF_MEMX), (u32)i * 16u);
    }
  }
  mod_cols(cvec, wmod, ws, lds, a0 * 32);
  asm volatile("s_waitcnt vmcnt(0)" ::: "memory");
  __syncthreads();
  if (tid == 0) st4_sc1(1u, mkrsrc(ws + OFF_FLAG), OFF_MDF + a0 * 16u);
  f32x2 fz[2][2][16];
#pragma unroll
  for (int sg = 0; sg < 2; sg++)
#pragma unroll
    for (int it = 0; it < 2; it++) {
      f32x2* f = fz[sg][it];
#pragma unroll
      for (int j = 0; j < 4; j++) {
        const u32x4 v = xpre[sg][it][j];
        f[4 * j + 0] = (f32x2){bf_lo(v.x), bf_hi(v.x)}; f[4 * j + 1] = (f32x2){bf_lo(v.y), bf_hi(v.y)};
        f[4 * j + 2] = (f32x2){bf_lo(v.z), bf_hi(v.z)}; f[4 * j + 3] = (f32x2){bf_lo(v.w), bf_hi(v.w)};
      }
      f32x2 s2 = {0.f, 0.f};
#pragma unroll
      for (int i = 0; i < 16; i++) s2 += f[i];
      const float m = row16_sum(s2.x + s2.y) * (1.f / DM);
      f32x2 q2 = {0.f, 0.f};
#pragma unroll
      for (int i = 0; i < 16; i++) { f[i] -= m; q2 += f[i] * f[i]; }
      const float rs = rsqrtf(row16_sum(q2.x + q2.y) * (1.f / DM) + 1e-5f);
#pragma unroll
      for (int i = 0; i < 16; i++) f[i] *= rs;
    }
  { u32 seenm = 0xFFFFFFFFu; if (!wait_progress(mkrsrc(ws + OFF_FLAG), OFF_MDF + (lane & 31) * 16u, 1u, seenm)) return; }
  float* modl = (float*)(lds + 8192);
  {
    u32x4 mv = ld16_sc1(mkrsrc(ws + OFF_MOD), (u32)tid * 16u);
    f32x4 mf = {u2f(mv.x), u2f(mv.y), u2f(mv.z), u2f(mv.w)};
    *(f32x4*)(modl + tid * 4) = tid >= 128 ? mf + 1.f : mf;
  }
  __syncthreads();
  const __amdgpu_buffer_rsrc_t rh = mkrsrc(ws + OFF_H);
#pragma unroll
  for (int sg = 0; sg < 2; sg++) {
    const int a = a0 + 32 * sg;
#pragma unroll
    for (int it = 0; it < 2; it++) {
      const f32x2* f = fz[sg][it];
#pragma unroll
      for (int j = 0; j < 4; j++) {
        const int c0 = (j * 16 + n) * 8;
        f32x4 c0v = *(const f32x4*)(modl + 512 + c0), c1v = *(const f32x4*)(modl + 512 + c0 + 4);
        f32x4 s0v = *(const f32x4*)(modl + c0), s1v = *(const f32x4*)(modl + c0 + 4);
        f32x2 o0 = f[4 * j + 0] * (f32x2){c0v[0], c0v[1]} + (f32x2){s0v[0], s0v[1]};
        f32x2 o1 = f[4 * j + 1] * (f32x2){c0v[2], c0v[3]} + (f32x2){s0v[2], s0v[3]};
        f32x2 o2 = f[4 * j + 2] * (f32x2){c1v[0], c1v[1]} + (f32x2){s1v[0], s1v[1]};
        f32x2 o3 = f[4 * j + 3] * (f32x2){c1v[2], c1v[3]} + (f32x2){s1v[2], s1v[3]};
        u32x4 r = {pk2(o0.x, o0.y), pk2(o1.x, o1.y), pk2(o2.x, o2.y), pk2(o3.x, o3.y)};
        st16_sc1(r, rh, (u32)((a * 32 + it * 16 + w * 4 + g) * DM + c0) * 2u);
      }
    }
    asm volatile("s_waitcnt vmcnt(0)" ::: "memory");
    __syncthreads();
    if (tid == 0) st4_sc1(1u, mkrsrc(ws + OFF_FLAG), OFF_DONE + a * 64u);
  }
  const int a = a0;
  mod_cols(cvec, wmod, ws, lds, 1024 + a * 64);
  mod_cols(cvec, wmod, ws, lds, 1024 + a * 64 + 32);
  __syncthreads();
  if (a < 28) {
    dma_tile<128>(lds, w1 + a * 64, DM, FF);
    dma_wait();
    u32x4* dst = (u32x4*)(ws + OFF_W1F);
    for (int ks = 0; ks < 16; ks++) {
      bf16x8 v = frag8(lds, 32 * ks + 8 * (lane >> 4), 16 * w, 128, lane);
      dst[((size_t)(a * 4 + w) * 16 + ks) * 64 + lane] = __builtin_bit_cast(u32x4, v);
    }
  }
  __syncthreads();
  {
    const int cb = a >> 2, kq = a & 3;
    dma_tile<128>(lds, w2 + (size_t)kq * 448 * DM + cb * 64, 448, DM);
    dma_wait();
    u32x4* dst = (u32x4*)(ws + OFF_W2F);
    for (int ks = 0; ks < 14; ks++) {
      u32x2 va = tr4(lds, 32 * ks + 4 * (lane >> 4), 16 * w, 128, lane), vb = tr4(lds, 32 * ks + 16 + 4 * (lane >> 4), 16 * w, 128, lane);
      dst[((size_t)(cb * 4 + w) * 56 + kq * 14 + ks) * 64 + lane] = (u32x4){va.x, va.y, vb.x, vb.y};
    }
  }
  {
    const __amdgpu_buffer_rsrc_t rflag = mkrsrc(ws + OFF_FLAG);
    const int hg = a & 7;
    for (int s = a >> 3; s < NSEG; s += 4) {
      px_task(ws, s, hg);
    }
  }
}

#define AD_DEC 3
#define AD_INC 60
#define ADAPT(D, n) do { if ((n) == 1) D -= AD_DEC; else D += AD_INC; if (D < 0) D = 0; } while (0)

__device__ __forceinline__ void role_score(const u16* __restrict__ Wq, const u16* __restrict__ Wk, char* ws,
                                           char* lds, int h) {
  const int tid = threadIdx.x, lane = tid & 63, w = tid >> 6;
  const int n = lane & 15, gb = lane >> 4;
  bf16x16 wf[4][8];
#pragma unroll
  for (int mat = 0; mat < 2; mat++) {
    __syncthreads();
    dma_tile<256>(lds, (mat ? Wk : Wq) + h * DH, DM, NH * DH);
    dma_wait();
#pragma unroll
    for (int tt = 0; tt < 2; tt++)
#pragma unroll
      for (int t = 0; t < 8; t++) {
        const int col = 32 * w + 16 * tt;
        u32x4 lo = frag8s(lds, 64 * t + 16 * (gb >> 1) + 4 * (gb & 1), col, 256, lane);
        u32x4 hi = frag8s(lds, 64 * t + 16 * (2 + (gb >> 1)) + 4 * (gb & 1), col, 256, lane);
        wf[mat * 2 + tt][t] = as_bf16(lo, hi);
      }
  }
  __syncthreads();
  char* memL = lds;
  char* qs = lds + 8192;
  char* ks_ = lds + 8192 + 2304;
  const int idx = (lane & 8) ? (int)0xEEEEEEEE : 0x44444444;
  const __amdgpu_buffer_rsrc_t rflag = mkrsrc(ws + OFF_FLAG);
  const __amdgpu_buffer_rsrc_t rkx = mkrsrc(ws + OFF_KX);
  const __amdgpu_buffer_rsrc_t rmem = mkrsrc(ws + OFF_MEMX);
  const __amdgpu_buffer_rsrc_t rpex = mkrsrc(ws + OFF_PEX);
  const int rho = lane & 15;
  const int keyp = 8 * ((rho >> 2) & 1) + 4 * (rho >> 3) + (rho & 3);
  const u32 prho = (u32)((n & 7) >> 2) * 10240u, pi = n & 3;
  const u32 pxoff = prho + (u32)(((h >> 2) * 64) + 16 * (2 * (h & 1) + (gb >> 1)) + 8 * (gb & 1) + 4 * ((h >> 1) & 1) + pi) * 16u + (n >> 3) * 8u;
  const u32 pmoff = prho + 8192u + (u32)(h * 4 + pi) * 16u + (gb & 1) * 8u + (gb >> 1) * 4u;
  int D1 = w == 0 ? 3400 : (w == 1 ? 3300 : 4050); u32 tref = now32() - 8000u, seen = 0xFFFFFFFFu;
  STAMP(1);
  for (int s = 0; s < NSEG; s++) {
    u32x4 kx[2][4];
    if (w == 0) {
      if (!wait_progress(rflag, 4096u + h * 128u, s + 1, seen)) return;
#pragma unroll
      for (int tt = 0; tt < 2; tt++)
#pragma unroll
        for (int k4 = 0; k4 < 4; k4++)
          kx[tt][k4] = ld16_sc1(rkx, (u32)(((32 * s + 16 * tt + keyp) * NH + h) * DH + 32 * k4 + 8 * gb) * 2u);
    }
    f32x4 acc[4];
#pragma unroll
    for (int i = 0; i < 4; i++) { acc[i] = (f32x4){0.f, 0.f, 0.f, 0.f}; asm volatile("" : "+v"(acc[i])); }
    wait_until(tref + D1);
    {
      u32 off[2] = {(u32)s * 8192u + w * 2048u, (u32)s * 8192u + w * 2048u + 1024u};
      u32x4 a[2];
      u32 tiss = 0;
      u32 nsw = poll_chunks<2>(rmem, off, a, lane * 16u, tiss);
      if (!nsw) return;
      if (s > 2) ADAPT(D1, nsw);
      *(u32x4*)(memL + w * 2048 + lane * 16) = a[0];
      *(u32x4*)(memL + w * 2048 + 1024 + lane * 16) = a[1];
    }
    __syncthreads();
    STAMP(16 + 4 * s);
    bf16x8 af[8];
#pragma unroll
    for (int t = 0; t < 8; t++) af[t] = *(const bf16x8*)(memL + t * 1024 + lane * 16);
    __builtin_amdgcn_sched_barrier(0);
    const int ecol = 32 * w + 16 * (lane >> 5) + n, er0 = 4 * ((lane >> 4) & 1);
#pragma unroll
    for (int t = 0; t < 8; t++) { acc[0] = SMF(af[t], wf[0][t], acc[0], idx); acc[1] = SMF(af[t], wf[1][t], acc[1], idx); }
    __builtin_amdgcn_sched_barrier(0);
#pragma unroll
    for (int t = 0; t < 8; t++) {
      acc[2] = SMF(af[t], wf[2][t], acc[2], idx); acc[3] = SMF(af[t], wf[3][t], acc[3], idx);
      if (t >= 2 && t < 6) {
        const int j = t - 2;
        auto r = __builtin_amdgcn_permlane32_swap(f2u(acc[0][j]), f2u(acc[1][j]), false, false);
        *(__bf16*)(qs + (er0 + j) * 272 + ecol * 2) = (__bf16)(u2f(r[0]) + u2f(r[1]));
      }
      __builtin_amdgcn_sched_barrier(0);
    }
#pragma unroll
    for (int j = 0; j < 4; j++) {
      auto r = __builtin_amdgcn_permlane32_swap(f2u(acc[2][j]), f2u(acc[3][j]), false, false);
      *(__bf16*)(ks_ + (er0 + j) * 272 + ecol * 2) = (__bf16)(u2f(r[0]) + u2f(r[1]));
    }
    __syncthreads();
    STAMP(17 + 4 * s);
    if (w == 0 && s < NSEG - 1) {
      f32x4 sc[3];
#pragma unroll
      for (int i = 0; i < 3; i++) { sc[i] = (f32x4){0.f, 0.f, 0.f, 0.f}; asm volatile("" : "+v"(sc[i])); }
      bf16x8 qf[4], kf[4];
#pragma unroll
      for (int k4 = 0; k4 < 4; k4++) {
        qf[k4] = *(const bf16x8*)(qs + (lane & 7) * 272 + (32 * k4 + 8 * gb) * 2);
        kf[k4] = *(const bf16x8*)(ks_ + (lane & 7) * 272 + (32 * k4 + 8 * gb) * 2);
      }
      __builtin_amdgcn_sched_barrier(0);
#pragma unroll
      for (int k4 = 0; k4 < 4; k4++) {
        sc[0] = MFMA16(as_bf8(kx[0][k4]), qf[k4], sc[0]);
        sc[1] = MFMA16(as_bf8(kx[1][k4]), qf[k4], sc[1]);
        sc[2] = MFMA16(kf[k4], qf[k4], sc[2]);
      }
      const float sl = 0.08838834764831845f * 1.4426950408889634f;
      const bool hiq = n >= 8, hig = gb >= 2;
      float p[6];
#pragma unroll
      for (int j = 0; j < 4; j++) p[j] = hiq ? sc[1][j] : sc[0][j];
      p[4] = hig ? sc[2][2] : sc[2][0];
      p[5] = hig ? sc[2][3] : sc[2][1];
      float mx = max2r(max3r(p[0], p[1], p[2]), max3r(p[3], p[4], p[5]));
      mx = max2r(mx, u2f((u32)__builtin_amdgcn_mov_dpp((int)f2u(mx), 0x128, 0xF, 0xF, true)));
      { auto r = __builtin_amdgcn_permlane16_swap(f2u(mx), f2u(mx), false, false);
        mx = max2r(u2f(r[0]), u2f(r[1])); }
      { auto r = __builtin_amdgcn_permlane32_swap(f2u(mx), f2u(mx), false, false);
        mx = max2r(u2f(r[0]), u2f(r[1])); }
      const float nb = -mx * sl;
#pragma unroll
      for (int j = 0; j < 6; j++) p[j] = __builtin_amdgcn_exp2f(__builtin_fmaf(p[j], sl, nb));
      float sum = (p[0] + p[1]) + (p[2] + p[3]) + (hiq ? 0.f : (p[4] + p[5]));
      sum += u2f((u32)__builtin_amdgcn_mov_dpp((int)f2u(sum), 0x128, 0xF, 0xF, true));
      { auto r = __builtin_amdgcn_permlane16_swap(f2u(sum), f2u(sum), false, false);
        sum = u2f(r[0]) + u2f(r[1]); }
      { auto r = __builtin_amdgcn_permlane32_swap(f2u(sum), f2u(sum), false, false);
        sum = u2f(r[0]) + u2f(r[1]); }
      const float inv = __builtin_amdgcn_rcpf(sum);
      u32x2 px = {sane(pk2(p[0] * inv, p[1] * inv)), sane(pk2(p[2] * inv, p[3] * inv))};
      st8_sc1(px, rpex, (u32)s * 20480u + pxoff);
      if (!hiq) st4_sc1(sane(pk2(p[4] * inv, p[5] * inv)), rpex, (u32)s * 20480u + pmoff);
      STAMP(18 + 4 * s);
    } else if (w == 1) {
      const __amdgpu_buffer_rsrc_t rkm = mkrsrc(ws + OFF_KMEM);
#pragma unroll
      for (int j = 0; j < 2; j++) {
        int u = j * 64 + lane, r = u >> 4, c = u & 15;
        st16_sc1(*(const u32x4*)(ks_ + r * 272 + c * 16), rkm, (u32)((s * NH + h) * 1024 + r * 128 + c * 8) * 2u);
      }
      asm volatile("s_waitcnt vmcnt(0)" ::: "memory");
      if (lane == 0) st4_sc1((u32)(s + 1), rflag, OFF_KMF + h * 64u);
    }
    tref = now32();
  }
}

__device__ __forceinline__ void role_column(const u16* __restrict__ Wv, const u16* __restrict__ Wo, char* ws, char* lds, int c, int rho) {
  const int tid = threadIdx.x, lane = tid & 63, w = tid >> 6;
  const int n = lane & 15, gb = lane >> 4, cc = lane & 7;
  bf16x16 wf[4][8];
  wvo_task(Wv, Wo, ws, lds, c * 2 + rho);
  {
    u32 seenw = 0xFFFFFFFFu;
    if (!wait_progress(mkrsrc(ws + OFF_FLAG), OFF_WVF + (u32)((lane & 31) * 4 + (c >> 4)) * 16u, 1u, seenw)) return;
    const __amdgpu_buffer_rsrc_t rsrc = mkrsrc(ws + OFF_WVO + (size_t)(c * 4 + w) * 65536);
#pragma unroll
    for (int tl = 0; tl < 4; tl++)
#pragma unroll
      for (int t = 0; t < 8; t++)
        wf[tl][t] = as_bf16(ld16_sc1(rsrc, (u32)(((tl * 8 + t) * 2) * 64 + lane) * 16u), ld16_sc1(rsrc, (u32)(((tl * 8 + t) * 2 + 1) * 64 + lane) * 16u));
  }
  char* memL = lds;
  char* yl = lds + 8192 + w * 1024;
  float* red = (float*)(lds + 12288);
  const int idx = (lane & 8) ? (int)0xEEEEEEEE : 0x44444444;
  const __amdgpu_buffer_rsrc_t rflag = mkrsrc(ws + OFF_FLAG);
  const __amdgpu_buffer_rsrc_t rmem = mkrsrc(ws + OFF_MEMX);
  const __amdgpu_buffer_rsrc_t rpex = mkrsrc(ws + OFF_PEX);
  int D1 = w == 0 ? 750 : (w == 1 ? 850 : 1130), D2 = 2800; u32 tref = now32(), seen = 0xFFFFFFFFu;
  STAMP(1);
  for (int s = 0; s < NSEG; s++) {
    wait_until(tref + D1);
    {
      u32 off[2] = {(u32)s * 8192u + w * 2048u, (u32)s * 8192u + w * 2048u + 1024u};
      u32x4 a[2];
      u32 tiss = 0;
      u32 nsw = poll_chunks<2>(rmem, off, a, lane * 16u, tiss);
      if (!nsw) return;
      if (s > 2) ADAPT(D1, nsw);
      tref = now32();
      *(u32x4*)(memL + w * 2048 + lane * 16) = a[0];
      *(u32x4*)(memL + w * 2048 + 1024 + lane * 16) = a[1];
    }
    u32x4 uf[4][2];
    {
      if (!wait_progress(rflag, (u32)(8 * w + (lane & 7)) * 128u, s + 1, seen)) return;
      const __amdgpu_buffer_rsrc_t ru = mkrsrc(ws + OFF_UF + ((size_t)s * 64 + c) * 16384);
#pragma unroll
      for (int m = 0; m < 4; m++)
#pragma unroll
        for (int z = 0; z < 2; z++) uf[m][z] = ld16_sc1(ru, (u32)((((4 * w + m) * 2 + z) * 4 + gb) * 8 + cc) * 16u);
    }
    __syncthreads();
    STAMP(16 + 4 * s);
    bf16x16 yb;
    {
      f32x4 acc[4];
#pragma unroll
      for (int i = 0; i < 4; i++) acc[i] = (f32x4){0.f, 0.f, 0.f, 0.f};
      bf16x8 af[8];
#pragma unroll
      for (int t = 0; t < 8; t++) af[t] = *(const bf16x8*)(memL + t * 1024 + lane * 16);
      __builtin_amdgcn_sched_barrier(0);
#pragma unroll
      for (int t = 0; t < 8; t++)
#pragma unroll
        for (int i = 0; i < 4; i++) acc[i] = SMF(af[t], wf[i][t], acc[i], idx);
#pragma unroll
      for (int pr = 0; pr < 2; pr++) {
        float v[4];
#pragma unroll
        for (int j = 0; j < 4; j++) {
          auto r = __builtin_amdgcn_permlane32_swap(f2u(acc[2 * pr][j]), f2u(acc[2 * pr + 1][j]), false, false);
          v[j] = u2f(r[0]) + u2f(r[1]);
        }
        const int hl = 2 * (2 * pr + (lane >> 5)) + (n >> 3);
        u32x2 pv = {pk2(v[0], v[1]), pk2(v[2], v[3])};
        *(u32x2*)(yl + (hl * 8 + cc) * 16 + 8 * ((lane >> 4) & 1)) = pv;
      }
      asm volatile("s_waitcnt lgkmcnt(0)" ::: "memory");
      u32x4 hz[2];
#pragma unroll
      for (int z = 0; z < 2; z++) {
        const int ga = 2 * z + (gb >> 1);
        u32x2 a = *(const u32x2*)(yl + ((2 * ga) * 8 + cc) * 16 + 8 * (gb & 1));
        u32x2 b = *(const u32x2*)(yl + ((2 * ga + 1) * 8 + cc) * 16 + 8 * (gb & 1));
        hz[z] = (u32x4){a.x, b.x, a.y, b.y};
      }
      yb = as_bf16(hz[0], hz[1]);
      if (rho == 0) __builtin_amdgcn_raw_buffer_store_b128(*(const u32x4*)(yl + lane * 16), mkrsrc(ws + OFF_Y), (u32)(s * 64 + c) * 4096u + w * 1024u + lane * 16u, 0, 0);
    }
    STAMP(17 + 4 * s);
    if (s == NSEG - 1) break;
    wait_until(tref + D2);
    {
      const u32 pb = (u32)s * 20480u + rho * 10240u;
      u32 off[3] = {pb + (2 * w) * 1024u + lane * 16u, pb + (2 * w + 1) * 1024u + lane * 16u,
                    pb + 8192u + (u32)((8 * w + 2 * gb + ((lane >> 3) & 1)) * 4 + (lane & 3)) * 16u};
      u32x4 g[3];
      u32 tiss = 0;
      u32 nsw = poll_chunks<3>(rpex, off, g, 0u, tiss);
      if (!nsw) return;
      if (s > 2) ADAPT(D2, nsw);
      f32x4 dA = {0.f, 0.f, 0.f, 0.f}, dB = {0.f, 0.f, 0.f, 0.f};
      dA = SMF(as_bf8(g[0]), as_bf16(uf[0][0], uf[0][1]), dA, idx);
      dB = SMF(as_bf8(g[0]), as_bf16(uf[1][0], uf[1][1]), dB, idx);
      dA = SMF(as_bf8(g[1]), as_bf16(uf[2][0], uf[2][1]), dA, idx);
      dB = SMF(as_bf8(g[1]), as_bf16(uf[3][0], uf[3][1]), dB, idx);
      dA = SMF(as_bf8(g[2]), yb, dA, idx);
      float* rd = red + (s & 1) * 512;
      if (n < 8) {
#pragma unroll
        for (int j = 0; j < 4; j++) rd[(j * 8 + cc) * 16 + 4 * w + gb] = (gb & 1) ? dB[j] : dA[j];
      }
    }
    __syncthreads();
    STAMP(18 + 4 * s);
    if (w == 0) {
      const float* rd = red + (s & 1) * 512 + lane * 8;
      f32x4 a = *(const f32x4*)rd, b = *(const f32x4*)(rd + 4);
      float v = ((a[0] + a[1]) + (a[2] + a[3])) + ((b[0] + b[1]) + (b[2] + b[3]));
      v += u2f((u32)__builtin_amdgcn_mov_dpp((int)f2u(v), 0xB1, 0xF, 0xF, true));
      float o = u2f((u32)__builtin_amdgcn_mov_dpp((int)f2u(v), 0x4E, 0xF, 0xF, true));
      if (!(lane & 3)) st4_sc1(sane(pk2(v, o)), rmem, (u32)(s + 1) * 8192u + c * 128u + rho * 64u + (lane >> 2) * 4u);
      STAMP(19 + 4 * s);
    }
    tref = now32();
  }
}

__device__ __forceinline__ void load_h(u32x4 (&r)[8], const char* ws, int s) {
  const __amdgpu_buffer_rsrc_t rh = mkrsrc(ws + OFF_H);
#pragma unroll
  for (int j = 0; j < 8; j++) r[j] = ld16_sc1(rh, (u32)s * 32768u + (j * 256 + threadIdx.x) * 16u);
}
__device__ __forceinline__ void put_h(char* dst, const u32x4 (&r)[8]) {
#pragma unroll
  for (int j = 0; j < 8; j++) {
    int u = j * 256 + threadIdx.x;
    *(u32x4*)(dst + (u >> 6) * 1040 + (u & 63) * 16) = r[j];
  }
}

template <bool SC1>
__device__ __forceinline__ void store_T_tile(const f32x16& acc, char* base, int tokrow, int h, int w, int lane) {
  const __amdgpu_buffer_rsrc_t r = mkrsrc(base);
#pragma unroll
  for (int qp = 0; qp < 2; qp++) {
    u32 ax = pk2(acc[8 * qp + 0], acc[8 * qp + 1]), ay = pk2(acc[8 * qp + 2], acc[8 * qp + 3]);
    u32 bx = pk2(acc[8 * qp + 4], acc[8 * qp + 5]), by = pk2(acc[8 * qp + 6], acc[8 * qp + 7]);
    auto r0 = __builtin_amdgcn_permlane32_swap(ax, bx, false, false);
    auto r1 = __builtin_amdgcn_permlane32_swap(ay, by, false, false);
    u32x4 v = {(u32)r0[0], (u32)r1[0], (u32)r0[1], (u32)r1[1]};
    u32 off = (u32)(((tokrow + (lane & 31)) * NH + h) * DH + 32 * w + 16 * qp + 8 * (lane >> 5)) * 2u;
    if (SC1) __builtin_amdgcn_raw_buffer_store_b128(v, r, off, 0, 16);
    else __builtin_amdgcn_raw_buffer_store_b128(v, r, off, 0, 0);
  }
}

__device__ __forceinline__ void role_vu(const u16* __restrict__ Wv, const u16* __restrict__ Wo,
                                        char* ws, char* lds, int h) {
  const int tid = threadIdx.x, lane = tid & 63, w = tid >> 6, x = lane & 31, hh = lane >> 5;
  bf16x8 wv[32], wo[4][8];
  dma_tile<256>(lds, Wv + h * DH, DM, NH * DH);
  dma_wait();
#pragma unroll
  for (int ks = 0; ks < 32; ks++) wv[ks] = frag8(lds, 16 * ks + 8 * hh, 32 * w + (x & 16), 256, lane);
  __syncthreads();
  dma_tile<1024>(lds, Wo + (size_t)h * DH * DM, DH, DM);
  dma_wait();
#pragma unroll
  for (int nt = 0; nt < 4; nt++)
#pragma unroll
    for (int ks = 0; ks < 8; ks++) wo[nt][ks] = frag8(lds, 16 * ks + 8 * hh, 128 * w + 32 * nt + (x & 16), 1024, lane);
  __syncthreads();
  char* vs = lds + 66560;
  const __amdgpu_buffer_rsrc_t rflag = mkrsrc(ws + OFF_FLAG);
  u32x4 hr[8];
  { u32 seenh = 0xFFFFFFFFu; if (!wait_progress(rflag, OFF_DONE + (lane & 31) * 64u, 1u, seenh)) return; }
  load_h(hr, ws, 0);
  put_h(lds, hr);
  load_h(hr, ws, 1);
  __syncthreads();
  for (int s = 0; s < NSEG; s++) {
    const char* hb = lds + (s & 1) * 33280;
    f32x16 av;
#pragma unroll
    for (int i = 0; i < 16; i++) av[i] = 0.f;
    {
      bf16x8 hf[2][8];
#pragma unroll
      for (int k = 0; k < 8; k++) hf[0][k] = *(const bf16x8*)(hb + x * 1040 + (16 * k + 8 * hh) * 2);
#pragma unroll
      for (int ch = 0; ch < 4; ch++) {
        if (ch < 3) {
#pragma unroll
          for (int k = 0; k < 8; k++) hf[(ch + 1) & 1][k] = *(const bf16x8*)(hb + x * 1040 + (16 * (8 * ch + 8 + k) + 8 * hh) * 2);
        }
        __builtin_amdgcn_sched_barrier(0);
#pragma unroll
        for (int k = 0; k < 8; k++) av = MFMA32(wv[8 * ch + k], hf[ch & 1][k], av);
        __builtin_amdgcn_sched_barrier(0);
      }
    }
#pragma unroll
    for (int q = 0; q < 4; q++) {
      u32x2 pv = {pk2(av[4 * q], av[4 * q + 1]), pk2(av[4 * q + 2], av[4 * q + 3])};
      *(u32x2*)(vs + x * 272 + (32 * w + 8 * q + 4 * hh) * 2) = pv;
    }
    put_h(lds + ((s + 1) & 1) * 33280, hr);
    asm volatile("s_waitcnt vmcnt(0)" ::: "memory");
    __syncthreads();
    if (tid == 0 && s > 4) st4_sc1((u32)s, rflag, h * 128u);
    bf16x8 vf[8];
#pragma unroll
    for (int ks = 0; ks < 8; ks++) vf[ks] = *(const bf16x8*)(vs + x * 272 + (16 * ks + 8 * hh) * 2);
    const __amdgpu_buffer_rsrc_t ru = mkrsrc(ws + OFF_UF + (size_t)s * 64 * 16384);
#pragma unroll
    for (int nt = 0; nt < 4; nt++) {
      f32x16 au;
#pragma unroll
      for (int i = 0; i < 16; i++) au[i] = 0.f;
#pragma unroll
      for (int ks = 0; ks < 8; ks++) au = MFMA32(vf[ks], wo[nt][ks], au);
      const int d = 128 * w + 32 * nt + x, c = d >> 3, cc = d & 7;
#pragma unroll
      for (int xb = 0; xb < 2; xb++)
        st16_sc1(PACK_S(au, 8 * xb), ru, (u32)((((c * 16 + (h >> 1)) * 2 + (h & 1)) * 4 + 2 * hh + xb) * 8 + cc) * 16u);
    }
    if (s < 4 || s == NSEG - 1) asm volatile("s_waitcnt vmcnt(0)" ::: "memory");
    if (s == 30) { u32 seenh = 0xFFFFFFFFu; if (!wait_progress(rflag, OFF_DONE + lane * 64u, 1u, seenh)) return; }
    load_h(hr, ws, s + 2 < NSEG ? s + 2 : NSEG - 1);
    __syncthreads();
    if (tid == 0 && (s < 4 || s == NSEG - 1)) st4_sc1((u32)(s + 1), rflag, h * 128u);
    STAMP(16 + 4 * s);
  }
}

__device__ __forceinline__ void role_qk(const u16* __restrict__ Wq, const u16* __restrict__ Wk, char* ws, char* lds, int h) {
  const int tid = threadIdx.x, lane = tid & 63, w = tid >> 6, x = lane & 31, hh = lane >> 5;
  bf16x8 wq[32], wk[32];
  dma_tile<256>(lds, Wk + h * DH, DM, NH * DH);
  dma_wait();
#pragma unroll
  for (int ks = 0; ks < 32; ks++) wk[ks] = frag8(lds, 16 * ks + 8 * hh, 32 * w + (x & 16), 256, lane);
  __syncthreads();
  dma_tile<256>(lds, Wq + h * DH, DM, NH * DH);
  dma_wait();
#pragma unroll
  for (int ks = 0; ks < 32; ks++) wq[ks] = frag8(lds, 16 * ks + 8 * hh, 32 * w + (x & 16), 256, lane);
  __syncthreads();
  const __amdgpu_buffer_rsrc_t rflag = mkrsrc(ws + OFF_FLAG);
  u32x4 hr[8];
  { u32 seenh = 0xFFFFFFFFu; if (!wait_progress(rflag, OFF_DONE + (lane & 31) * 64u, 1u, seenh)) return; }
  load_h(hr, ws, 0);
  put_h(lds, hr);
  load_h(hr, ws, 1);
  __syncthreads();
  for (int s = 0; s < NSEG; s++) {
    const char* hb = lds + (s & 1) * 33280;
    f32x16 aq, ak;
#pragma unroll
    for (int i = 0; i < 16; i++) { aq[i] = 0.f; ak[i] = 0.f; }
    {
      bf16x8 hf[2][8];
#pragma unroll
      for (int k = 0; k < 8; k++) hf[0][k] = *(const bf16x8*)(hb + x * 1040 + (16 * k + 8 * hh) * 2);
#pragma unroll
      for (int ch = 0; ch < 4; ch++) {
        if (ch < 3) {
#pragma unroll
          for (int k = 0; k < 8; k++) hf[(ch + 1) & 1][k] = *(const bf16x8*)(hb + x * 1040 + (16 * (8 * ch + 8 + k) + 8 * hh) * 2);
        }
        __builtin_amdgcn_sched_barrier(0);
#pragma unroll
        for (int k = 0; k < 8; k++) { ak = MFMA32(wk[8 * ch + k], hf[ch & 1][k], ak); aq = MFMA32(wq[8 * ch + k], hf[ch & 1][k], aq); }
        __builtin_amdgcn_sched_barrier(0);
      }
    }
    put_h(lds + ((s + 1) & 1) * 33280, hr);
    asm volatile("s_waitcnt vmcnt(0)" ::: "memory");
    __syncthreads();
    if (tid == 0 && s > 4) st4_sc1((u32)s, rflag, 4096u + h * 128u);
    store_T_tile<true>(ak, ws + OFF_KX, 32 * s, h, w, lane);
    store_T_tile<true>(aq, ws + OFF_QX, 32 * s, h, w, lane);
    if (s < 4 || s == NSEG - 1) {
      asm volatile("s_waitcnt vmcnt(0)" ::: "memory");
      __syncthreads();
      if (tid == 0) st4_sc1((u32)(s + 1), rflag, 4096u + h * 128u);
    }
    if (s == 30) { u32 seenh = 0xFFFFFFFFu; if (!wait_progress(rflag, OFF_DONE + lane * 64u, 1u, seenh)) return; }
    load_h(hr, ws, s + 2 < NSEG ? s + 2 : NSEG - 1);
    STAMP(16 + 4 * s);
  }
}

__device__ __forceinline__ float xg_max(float v) {
  auto r = __builtin_amdgcn_permlane16_swap(f2u(v), f2u(v), false, false);
  v = fmaxf(u2f(r[0]), u2f(r[1]));
  auto r2 = __builtin_amdgcn_permlane32_swap(f2u(v), f2u(v), false, false);
  return fmaxf(u2f(r2[0]), u2f(r2[1]));
}
__device__ __forceinline__ float xg_sum(float v) {
  auto r = __builtin_amdgcn_permlane16_swap(f2u(v), f2u(v), false, false);
  v = u2f(r[0]) + u2f(r[1]);
  auto r2 = __builtin_amdgcn_permlane32_swap(f2u(v), f2u(v), false, false);
  return u2f(r2[0]) + u2f(r2[1]);
}
__device__ __forceinline__ void px_task(char* ws, int s, int hg) {
  const int tid = threadIdx.x, lane = tid & 63, w = tid >> 6;
  const int hd = hg * 4 + w, hl = w;
  const int n = lane & 15, g = lane >> 4;
  const __amdgpu_buffer_rsrc_t rq = mkrsrc(ws + OFF_QX + (size_t)s * 32 * 8192);
  const __amdgpu_buffer_rsrc_t rk = mkrsrc(ws + OFF_KX + (size_t)s * 32 * 8192);
  const __amdgpu_buffer_rsrc_t rkm = mkrsrc(ws + OFF_KMEM + (size_t)s * 65536);
  const int keyrow = 16 * ((n >> 2) & 1) + 4 * (n >> 3) + (n & 3);
  const int keyb = 16 * (g & 1) + 4 * (g >> 1);
  const float sl = 0.08838834764831845f * 1.4426950408889634f;
  bf16x8 qf[2][4], kf[2][4], mf[4];
  const __amdgpu_buffer_rsrc_t rflag = mkrsrc(ws + OFF_FLAG);
  { u32 seenq = 0xFFFFFFFFu; wait_progress(rflag, 4096u + hd * 128u, s + 1, seenq); }
#pragma unroll
  for (int ks = 0; ks < 4; ks++) {
#pragma unroll
    for (int t = 0; t < 2; t++) {
      qf[t][ks] = as_bf8(ld16_sc1(rq, (u32)(((16 * t + n) * NH + hd) * DH + 32 * ks + 8 * g) * 2u));
      kf[t][ks] = as_bf8(ld16_sc1(rk, (u32)(((8 * t + keyrow) * NH + hd) * DH + 32 * ks + 8 * g) * 2u));
    }
  }
  {
    u32 spins = 0, off = OFF_KMF + (u32)hd * 64u;
    while (true) {
      asm volatile("" : "+v"(off) :: "memory");
      u32 v = ld4_sc1(rflag, off);
      if ((int)v > s) break;
      __builtin_amdgcn_s_sleep(6);
      if (++spins > 800000u) break;
    }
  }
#pragma unroll
  for (int ks = 0; ks < 4; ks++) mf[ks] = as_bf8(ld16_sc1(rkm, (u32)(hd * 1024 + (n & 7) * 128 + 32 * ks + 8 * g) * 2u));
  const bool mvalid = (g >> 1) == (hl >> 1);
  u32x4* PXx = (u32x4*)(ws + OFF_PX);
  char* PXm = ws + OFF_PX + (4ull << 20);
#pragma unroll
  for (int qt = 0; qt < 2; qt++) {
    f32x4 sc[3];
#pragma unroll
    for (int i = 0; i < 3; i++) sc[i] = (f32x4){0.f, 0.f, 0.f, 0.f};
#pragma unroll
    for (int ks = 0; ks < 4; ks++) {
      sc[0] = MFMA16(kf[0][ks], qf[qt][ks], sc[0]);
      sc[1] = MFMA16(kf[1][ks], qf[qt][ks], sc[1]);
      sc[2] = MFMA16(mf[ks], qf[qt][ks], sc[2]);
    }
    const int qi = 16 * qt + n;
    float mx = -3.0e38f;
#pragma unroll
    for (int j = 0; j < 4; j++) {
      if (keyb + j > qi) sc[0][j] = -3.0e38f;
      if (keyb + 8 + j > qi) sc[1][j] = -3.0e38f;
      if (!mvalid) sc[2][j] = -3.0e38f;
      mx = fmaxf(mx, fmaxf(fmaxf(sc[0][j], sc[1][j]), sc[2][j]));
    }
    mx = xg_max(mx);
    float sum = 0.f;
#pragma unroll
    for (int i = 0; i < 3; i++)
#pragma unroll
      for (int j = 0; j < 4; j++) {
        float p = sc[i][j] > -1.0e38f ? __builtin_amdgcn_exp2f((sc[i][j] - mx) * sl) : 0.f;
        sc[i][j] = p; sum += p;
      }
    const float inv = __builtin_amdgcn_rcpf(xg_sum(sum));
    u32x4 pa = {pk2(sc[0][0] * inv, sc[0][1] * inv), pk2(sc[1][0] * inv, sc[1][1] * inv),
                pk2(sc[0][2] * inv, sc[0][3] * inv), pk2(sc[1][2] * inv, sc[1][3] * inv)};
    PXx[(((size_t)s * NH + hd) * 2 + qt) * 64 + lane] = pa;
    if (mvalid) {
      u32x2 pm = {pk2(sc[2][0] * inv, sc[2][1] * inv), pk2(sc[2][2] * inv, sc[2][3] * inv)};
      *(u32x2*)(PXm + ((((size_t)s * 8 + hg) * 2 + qt) * 64 + lane) * 16 + (hl & 1) * 8) = pm;
    }
  }
}

__global__ void __launch_bounds__(256, 1) k2_rec(const u16* __restrict__ Wq, const u16* __restrict__ Wk, const u16* __restrict__ Wv,
                                                  const u16* __restrict__ Wo, const u16* __restrict__ x, const u16* __restrict__ w1,
                                                  const u16* __restrict__ w2, const u16* __restrict__ cvec,
                                                  const u16* __restrict__ wmod, const u16* __restrict__ mem0, char* ws) {
  extern __shared__ __attribute__((aligned(16))) char lds[];
  const int b = blockIdx.x;
  STAMP(0);
  if (b < 32) role_help(x, w1, w2, cvec, wmod, mem0, ws, lds, b);
  else if (b < 64) role_score(Wq, Wk, ws, lds, b - 32);
  else if (b < 192) { const int q = b - 64; role_column(Wv, Wo, ws, lds, (q & 7) | ((q >> 4) << 3), (q >> 3) & 1); }
  else if (b < 224) role_vu(Wv, Wo, ws, lds, b - 192);
  else role_qk(Wq, Wk, ws, lds, b - 224);
}

__global__ void __launch_bounds__(512, 2) k3b_oproj(const u16* __restrict__ x, char* __restrict__ ws) {
  extern __shared__ __attribute__((aligned(16))) char lds[];
  const int b = blockIdx.x, tid = threadIdx.x, lane = tid & 63, w = tid >> 6;
  const int s = b >> 2, nq = b & 3;
  const int n = lane & 15, g = lane >> 4, cc = lane & 7;
  const u32x4* UF = (const u32x4*)(ws + OFF_UF) + (size_t)s * 64 * 1024;
  const char* Yg = ws + OFF_Y + (size_t)s * 64 * 4096;
  const u32x4* PXx = (const u32x4*)(ws + OFF_PX) + (size_t)s * NH * 128;
  const u32x4* PXm = (const u32x4*)(ws + OFF_PX + (4ull << 20)) + (size_t)s * 8 * 128;
  f32x4 acc[2][8];
#pragma unroll
  for (int i = 0; i < 2; i++)
#pragma unroll
    for (int j = 0; j < 8; j++) acc[i][j] = (f32x4){0.f, 0.f, 0.f, 0.f};
  u32x4 ub[4][8], pa[4][2], pm[2], yf[8];
#pragma unroll
  for (int hl = 0; hl < 4; hl++) {
    const int hd = 4 * w + hl;
#pragma unroll
    for (int nt = 0; nt < 8; nt++) ub[hl][nt] = UF[(size_t)(16 * nq + 2 * nt + (n >> 3)) * 1024 + (hd * 4 + g) * 8 + cc];
    pa[hl][0] = PXx[(hd * 2) * 64 + lane];
    pa[hl][1] = PXx[(hd * 2 + 1) * 64 + lane];
  }
  pm[0] = PXm[(w * 2) * 64 + lane];
  pm[1] = PXm[(w * 2 + 1) * 64 + lane];
  {
    const int ha = 4 * w + 2 * (g >> 1);
#pragma unroll
    for (int nt = 0; nt < 8; nt++) {
      const int c = 16 * nq + 2 * nt + (n >> 3);
      u32x2 ya = *(const u32x2*)(Yg + (size_t)c * 4096 + (ha * 8 + cc) * 16 + 8 * (g & 1));
      u32x2 yb = *(const u32x2*)(Yg + (size_t)c * 4096 + ((ha + 1) * 8 + cc) * 16 + 8 * (g & 1));
      yf[nt] = (u32x4){ya.x, ya.y, yb.x, yb.y};
    }
  }
  __builtin_amdgcn_sched_barrier(0);
#pragma unroll
  for (int hl = 0; hl < 4; hl++)
#pragma unroll
    for (int nt = 0; nt < 8; nt++) {
      acc[0][nt] = MFMA16(as_bf8(pa[hl][0]), as_bf8(ub[hl][nt]), acc[0][nt]);
      acc[1][nt] = MFMA16(as_bf8(pa[hl][1]), as_bf8(ub[hl][nt]), acc[1][nt]);
    }
#pragma unroll
  for (int nt = 0; nt < 8; nt++) {
    acc[0][nt] = MFMA16(as_bf8(pm[0]), as_bf8(yf[nt]), acc[0][nt]);
    acc[1][nt] = MFMA16(as_bf8(pm[1]), as_bf8(yf[nt]), acc[1][nt]);
  }
  float* red = (float*)lds;
#pragma unroll
  for (int qt = 0; qt < 2; qt++)
#pragma unroll
    for (int nt = 0; nt < 8; nt++)
#pragma unroll
      for (int j = 0; j < 4; j++) red[(w * 32 + 16 * qt + 4 * g + j) * 132 + 16 * nt + n] = acc[qt][nt][j];
  __syncthreads();
  const float* mod = (const float*)(ws + OFF_MOD);
  u16* x1 = (u16*)(ws + OFF_X1);
  {
    const int r = tid >> 4, c8 = (tid & 15) * 8;
    float o[8];
#pragma unroll
    for (int j = 0; j < 8; j++) o[j] = 0.f;
#pragma unroll
    for (int ww = 0; ww < 8; ww++) {
      f32x4 a = *(const f32x4*)(red + (ww * 32 + r) * 132 + c8), bq = *(const f32x4*)(red + (ww * 32 + r) * 132 + c8 + 4);
#pragma unroll
      for (int j = 0; j < 4; j++) { o[j] += a[j]; o[4 + j] += bq[j]; }
    }
    const int col = 128 * nq + c8;
    const size_t go = (size_t)(32 * s + r) * DM + col;
    u32x4 xv = *(const u32x4*)(x + go);
    float xf[8] = {bf_lo(xv.x), bf_hi(xv.x), bf_lo(xv.y), bf_hi(xv.y), bf_lo(xv.z), bf_hi(xv.z), bf_lo(xv.w), bf_hi(xv.w)};
#pragma unroll
    for (int j = 0; j < 8; j++) o[j] = xf[j] + bfr(mod[1024 + col + j] * bfr(o[j]));
    u32x4 rv = {pk2(o[0], o[1]), pk2(o[2], o[3]), pk2(o[4], o[5]), pk2(o[6], o[7])};
    *(u32x4*)(x1 + go) = rv;
  }
}

__global__ void __launch_bounds__(512, 2) k4_mlp1(const u16* __restrict__ b1, char* __restrict__ ws) {
  extern __shared__ __attribute__((aligned(16))) char lds[];
  const int b = blockIdx.x, tid = threadIdx.x, lane = tid & 63, w = tid >> 6;
  const int mt0 = b / 7, nb = b - mt0 * 7;
  const float* mod = (const float*)(ws + OFF_MOD);
  const u16* x1 = (const u16*)(ws + OFF_X1);
  const int n = lane & 15, g = lane >> 4;
  const f32x4 mv = *(const f32x4*)(mod + 1536 + (tid & 255) * 4);
  u32x4 xv[2][4];
#pragma unroll
  for (int it = 0; it < 2; it++)
#pragma unroll
    for (int j = 0; j < 4; j++) xv[it][j] = *(const u32x4*)(x1 + (size_t)(mt0 * 64 + it * 32 + w * 4 + g) * DM + (j * 16 + n) * 8);
  const u32x4* WF = (const u32x4*)(ws + OFF_W1F) + (size_t)(nb * 16 + w * 2) * 16 * 64 + lane;
  u32x4 bfm[2][16];
#pragma unroll
  for (int nt = 0; nt < 2; nt++)
#pragma unroll
    for (int ks = 0; ks < 8; ks++) bfm[nt][ks] = WF[(nt * 16 + ks) * 64];
  __builtin_amdgcn_sched_barrier(0);
  float* modl = (float*)(lds + 66560);
  if (tid < 256) *(f32x4*)(modl + tid * 4) = tid >= 128 ? mv + 1.f : mv;
  __syncthreads();
#pragma unroll
  for (int it = 0; it < 2; it++) {
    f32x2 f[16];
#pragma unroll
    for (int j = 0; j < 4; j++) {
      f[4 * j + 0] = (f32x2){bf_lo(xv[it][j].x), bf_hi(xv[it][j].x)}; f[4 * j + 1] = (f32x2){bf_lo(xv[it][j].y), bf_hi(xv[it][j].y)};
      f[4 * j + 2] = (f32x2){bf_lo(xv[it][j].z), bf_hi(xv[it][j].z)}; f[4 * j + 3] = (f32x2){bf_lo(xv[it][j].w), bf_hi(xv[it][j].w)};
    }
    f32x2 s2 = {0.f, 0.f};
#pragma unroll
    for (int i = 0; i < 16; i++) s2 += f[i];
    float m = row16_sum(s2.x + s2.y) * (1.f / DM);
    f32x2 q2 = {0.f, 0.f};
#pragma unroll
    for (int i = 0; i < 16; i++) { f[i] -= m; q2 += f[i] * f[i]; }
    float rs = rsqrtf(row16_sum(q2.x + q2.y) * (1.f / DM) + 1e-5f);
    s2 = (f32x2){0.f, 0.f};
#pragma unroll
    for (int j = 0; j < 4; j++) {
      const int c0 = (j * 16 + n) * 8;
      f32x4 c0v = *(const f32x4*)(modl + 512 + c0), c1v = *(const f32x4*)(modl + 512 + c0 + 4);
      f32x4 s0v = *(const f32x4*)(modl + c0), s1v = *(const f32x4*)(modl + c0 + 4);
      f[4 * j + 0] = f[4 * j + 0] * rs * (f32x2){c0v[0], c0v[1]} + (f32x2){s0v[0], s0v[1]};
      f[4 * j + 1] = f[4 * j + 1] * rs * (f32x2){c0v[2], c0v[3]} + (f32x2){s0v[2], s0v[3]};
      f[4 * j + 2] = f[4 * j + 2] * rs * (f32x2){c1v[0], c1v[1]} + (f32x2){s1v[0], s1v[1]};
      f[4 * j + 3] = f[4 * j + 3] * rs * (f32x2){c1v[2], c1v[3]} + (f32x2){s1v[2], s1v[3]};
      s2 += (f[4 * j + 0] + f[4 * j + 1]) + (f[4 * j + 2] + f[4 * j + 3]);
    }
    m = row16_sum(s2.x + s2.y) * (1.f / DM);
    q2 = (f32x2){0.f, 0.f};
#pragma unroll
    for (int i = 0; i < 16; i++) { f[i] -= m; q2 += f[i] * f[i]; }
    rs = rsqrtf(row16_sum(q2.x + q2.y) * (1.f / DM) + 1e-5f);
#pragma unroll
    for (int j = 0; j < 4; j++) {
      f32x2 o0 = f[4 * j] * rs, o1 = f[4 * j + 1] * rs, o2 = f[4 * j + 2] * rs, o3 = f[4 * j + 3] * rs;
      u32x4 o = {pk2(o0.x, o0.y), pk2(o1.x, o1.y), pk2(o2.x, o2.y), pk2(o3.x, o3.y)};
      *(u32x4*)(lds + (it * 32 + w * 4 + g) * 1040 + (j * 16 + n) * 16) = o;
    }
    __builtin_amdgcn_sched_barrier(0);
  }
#pragma unroll
  for (int nt = 0; nt < 2; nt++)
#pragma unroll
    for (int ks = 8; ks < 16; ks++) bfm[nt][ks] = WF[(nt * 16 + ks) * 64];
  __builtin_amdgcn_sched_barrier(0);
  __syncthreads();
  f32x4 acc[4][2];
#pragma unroll
  for (int i = 0; i < 4; i++)
#pragma unroll
    for (int j = 0; j < 2; j++) acc[i][j] = (f32x4){0.f, 0.f, 0.f, 0.f};
#pragma unroll
  for (int ks = 0; ks < 16; ks++) {
    bf16x8 af[4];
#pragma unroll
    for (int mt = 0; mt < 4; mt++) af[mt] = *(const bf16x8*)(lds + (16 * mt + n) * 1040 + (32 * ks + 8 * g) * 2);
#pragma unroll
    for (int mt = 0; mt < 4; mt++)
#pragma unroll
      for (int nt = 0; nt < 2; nt++) acc[mt][nt] = MFMA16(as_bf8(bfm[nt][ks]), af[mt], acc[mt][nt]);
    if (ks & 1) __builtin_amdgcn_sched_barrier(0);
  }
  u32x4* hidF = (u32x4*)(ws + OFF_HID);
  float bb[2][4];
#pragma unroll
  for (int nt = 0; nt < 2; nt++) {
    u32x2 bv = *(const u32x2*)(b1 + nb * 256 + w * 32 + 16 * nt + 4 * g);
    bb[nt][0] = bf_lo(bv.x); bb[nt][1] = bf_hi(bv.x); bb[nt][2] = bf_lo(bv.y); bb[nt][3] = bf_hi(bv.y);
  }
#pragma unroll
  for (int mt = 0; mt < 4; mt++) {
    float o[8];
#pragma unroll
    for (int nt = 0; nt < 2; nt++)
#pragma unroll
      for (int j = 0; j < 4; j++) {
        float u = bfr(bfr(acc[mt][nt][j]) + bb[nt][j]);
        float z = 0.7978845608028654f * (u + 0.044715f * u * u * u);
        float e = __builtin_amdgcn_exp2f(z * 2.8853900817779268f);
        float th = 1.f - 2.f * __builtin_amdgcn_rcpf(e + 1.f);
        o[4 * nt + j] = 0.5f * u * (1.f + th);
      }
    u32x4 ov = {pk2(o[0], o[1]), pk2(o[2], o[3]), pk2(o[4], o[5]), pk2(o[6], o[7])};
    hidF[((size_t)(mt0 * 4 + mt) * 56 + nb * 8 + w) * 64 + lane] = ov;
  }
}

__global__ void __launch_bounds__(512, 2) k5_mlp2(const u16* __restrict__ b2, char* __restrict__ ws, u16* __restrict__ out) {
  extern __shared__ __attribute__((aligned(16))) char lds[];
  const int b = blockIdx.x, tid = threadIdx.x, lane = tid & 63, w = tid >> 6;
  const int mt0 = (b & 7) * 4 + (b >> 6), nb = (b >> 3) & 7;
  const int n = lane & 15, g = lane >> 4;
  const u32x4* hidF = (const u32x4*)(ws + OFF_HID) + ((size_t)(mt0 * 4) * 56 + w * 7) * 64 + lane;
  const u32x4* WF = (const u32x4*)(ws + OFF_W2F) + ((size_t)(nb * 4) * 56 + w * 7) * 64 + lane;
  f32x4 acc[4][4];
#pragma unroll
  for (int i = 0; i < 4; i++)
#pragma unroll
    for (int j = 0; j < 4; j++) acc[i][j] = (f32x4){0.f, 0.f, 0.f, 0.f};
#pragma unroll
  for (int hb = 0; hb < 2; hb++) {
    const int nk = hb ? 3 : 4;
    u32x4 bfm[4][4]; bf16x8 af[4][4];
#pragma unroll
    for (int k = 0; k < 4; k++) if (k < nk) {
#pragma unroll
      for (int nt = 0; nt < 4; nt++) bfm[k][nt] = WF[(nt * 56 + hb * 4 + k) * 64];
#pragma unroll
      for (int mt = 0; mt < 4; mt++) af[k][mt] = as_bf8(hidF[(mt * 56 + hb * 4 + k) * 64]);
    }
    __builtin_amdgcn_sched_barrier(0);
#pragma unroll
    for (int k = 0; k < 4; k++) if (k < nk) {
#pragma unroll
      for (int mt = 0; mt < 4; mt++)
#pragma unroll
        for (int nt = 0; nt < 4; nt++) acc[mt][nt] = MFMA16(as_bf8(bfm[k][nt]), af[k][mt], acc[mt][nt]);
    }
  }
  float* red = (float*)lds;
#pragma unroll
  for (int mt = 0; mt < 4; mt++)
#pragma unroll
    for (int nt = 0; nt < 4; nt++) *(f32x4*)(red + (w * 64 + 16 * mt + n) * 68 + 16 * nt + 4 * g) = acc[mt][nt];
  __syncthreads();
  const float* mod = (const float*)(ws + OFF_MOD);
  const u16* x1 = (const u16*)(ws + OFF_X1);
  {
    const int r = tid >> 3, c8 = (tid & 7) * 8;
    float o[8];
#pragma unroll
    for (int j = 0; j < 8; j++) o[j] = 0.f;
#pragma unroll
    for (int ww = 0; ww < 8; ww++) {
      f32x4 a = *(const f32x4*)(red + (ww * 64 + r) * 68 + c8), bq = *(const f32x4*)(red + (ww * 64 + r) * 68 + c8 + 4);
#pragma unroll
      for (int j = 0; j < 4; j++) { o[j] += a[j]; o[4 + j] += bq[j]; }
    }
    const int col = 64 * nb + c8;
    const size_t go = (size_t)(mt0 * 64 + r) * DM + col;
    u32x4 xv = *(const u32x4*)(x1 + go), bv = *(const u32x4*)(b2 + col);
    float xf[8] = {bf_lo(xv.x), bf_hi(xv.x), bf_lo(xv.y), bf_hi(xv.y), bf_lo(xv.z), bf_hi(xv.z), bf_lo(xv.w), bf_hi(xv.w)};
    float bb[8] = {bf_lo(bv.x), bf_hi(bv.x), bf_lo(bv.y), bf_hi(bv.y), bf_lo(bv.z), bf_hi(bv.z), bf_lo(bv.w), bf_hi(bv.w)};
#pragma unroll
    for (int j = 0; j < 8; j++) o[j] = xf[j] + bfr(mod[2560 + col + j] * bfr(bfr(o[j]) + bb[j]));
    u32x4 rv = {pk2(o[0], o[1]), pk2(o[2], o[3]), pk2(o[4], o[5]), pk2(o[6], o[7])};
    *(u32x4*)(out + go) = rv;
  }
}

extern "C" void kernel_launch(void* const* d_in, const int* in_sizes, int n_in, void* d_out, int out_size, void* d_ws,
                              size_t ws_size, hipStream_t stream) {
  const u16* x = (const u16*)d_in[0];
  const u16* mem0 = (const u16*)d_in[1];
  const u16* Wq = (const u16*)d_in[2];
  const u16* Wk = (const u16*)d_in[3];
  const u16* Wv = (const u16*)d_in[4];
  const u16* Wo = (const u16*)d_in[5];
  const u16* w1 = (const u16*)d_in[8];
  const u16* b1 = (const u16*)d_in[9];
  const u16* w2 = (const u16*)d_in[10];
  const u16* b2 = (const u16*)d_in[11];
  const u16* cvec = (const u16*)d_in[12];
  const u16* wmod = (const u16*)d_in[13];
  char* ws = (char*)d_ws;
  hipFuncSetAttribute((const void*)k2_rec, hipFuncAttributeMaxDynamicSharedMemorySize, 135168);
  hipMemsetAsync(ws, 0xFF, OFF_H, stream);
  k2_rec<<<256, 256, 135168, stream>>>(Wq, Wk, Wv, Wo, x, w1, w2, cvec, wmod, mem0, ws);
  hipFuncSetAttribute((const void*)k3b_oproj, hipFuncAttributeMaxDynamicSharedMemorySize, 135168);
  k3b_oproj<<<256, 512, 135168, stream>>>(x, ws);
  hipFuncSetAttribute((const void*)k4_mlp1, hipFuncAttributeMaxDynamicSharedMemorySize, 70656);
  hipFuncSetAttribute((const void*)k5_mlp2, hipFuncAttributeMaxDynamicSharedMemorySize, 139264);
  k4_mlp1<<<224, 512, 70656, stream>>>(b1, ws);
  k5_mlp2<<<256, 512, 139264, stream>>>(b2, ws, (u16*)d_out);
}
```

```cpp
#include <hip/hip_runtime.h>
#include <hip/hip_bf16.h>
#include <stdint.h>

typedef unsigned int u32;
typedef unsigned short u16;
typedef unsigned long long u64;
typedef __attribute__((ext_vector_type(2))) __bf16 bf16x2;
typedef __attribute__((ext_vector_type(8))) __bf16 bf16x8;
typedef __attribute__((ext_vector_type(16))) __bf16 bf16x16;
typedef __attribute__((ext_vector_type(2))) float f32x2;
typedef __attribute__((ext_vector_type(4))) float f32x4;
typedef __attribute__((ext_vector_type(16))) float f32x16;
typedef __attribute__((ext_vector_type(2))) u32 u32x2;
typedef __attribute__((ext_vector_type(4))) u32 u32x4;
typedef __attribute__((ext_vector_type(8))) u32 u32x8;

#define NTOK 2048
#define DM 512
#define NH 32
#define DH 128
#define NSEG 64
#define FF 1792
#define SENT 0xFFFFFFFFu

#define OFF_FLAG 0ull
#define OFF_DONE 8192u
#define OFF_KMF 12288u
#define OFF_WVF 14336u
#define OFF_MDF 28672u
#define OFF_MOD 16384ull
#define OFF_MEMX 32768ull
#define OFF_PEX 0x90000ull
#define OFF_H (2ull << 20)
#define OFF_KMEM (4ull << 20)
#define OFF_WVO (8ull << 20)
#define OFF_KX (24ull << 20)
#define OFF_QX (40ull << 20)
#define OFF_Y (56ull << 20)
#define OFF_UF (72ull << 20)
#define OFF_PX (136ull << 20)
#define OFF_X1 (142ull << 20)
#define OFF_Y2 (146ull << 20)
#define OFF_HID (148ull << 20)
#define OFF_W1F (156ull << 20)
#define OFF_W2F (158ull << 20)
#define OFF_DBG (200ull << 20)
#define STAMP(slot) do { } while (0)

__device__ __forceinline__ u32 pk2(float a, float b) {
  bf16x2 v; v[0] = (__bf16)a; v[1] = (__bf16)b; return __builtin_bit_cast(u32, v);
}
__device__ __forceinline__ u32 f2u(float x) { return __builtin_bit_cast(u32, x); }
__device__ __forceinline__ float u2f(u32 x) { return __builtin_bit_cast(float, x); }
__device__ __forceinline__ float bf_lo(u32 x) { return __builtin_bit_cast(float, x << 16); }
__device__ __forceinline__ float bf_hi(u32 x) { return __builtin_bit_cast(float, x & 0xffff0000u); }
__device__ __forceinline__ float bfr(float a) { return (float)(__bf16)a; }
__device__ __forceinline__ __amdgpu_buffer_rsrc_t mkrsrc(const void* p) {
  return __builtin_amdgcn_make_buffer_rsrc((void*)p, 0, 0x7fffffff, 0x00020000);
}
__device__ __forceinline__ u32x4 ld16_sc1(__amdgpu_buffer_rsrc_t r, u32 off) { return __builtin_amdgcn_raw_buffer_load_b128(r, off, 0, 16); }
__device__ __forceinline__ u32 ld4_sc1(__amdgpu_buffer_rsrc_t r, u32 off) { return __builtin_amdgcn_raw_buffer_load_b32(r, off, 0, 16); }
__device__ __forceinline__ void st16_sc1(u32x4 v, __amdgpu_buffer_rsrc_t r, u32 off) { __builtin_amdgcn_raw_buffer_store_b128(v, r, off, 0, 16); }
__device__ __forceinline__ void st8_sc1(u32x2 v, __amdgpu_buffer_rsrc_t r, u32 off) { __builtin_amdgcn_raw_buffer_store_b64(v, r, off, 0, 16); }
__device__ __forceinline__ void st4_sc1(u32 v, __amdgpu_buffer_rsrc_t r, u32 off) { __builtin_amdgcn_raw_buffer_store_b32(v, r, off, 0, 16); }
__device__ __forceinline__ u32 badm(u32x4 a) { return (u32)(a.x == SENT) | (u32)(a.y == SENT) | (u32)(a.z == SENT) | (u32)(a.w == SENT); }
__device__ __forceinline__ u32 sane(u32 x) { return x < 0xFFFFFFFEu ? x : 0xFFFFFFFEu; }
__device__ __forceinline__ u32 now32() { return (u32)__builtin_amdgcn_s_memtime(); }
__device__ __forceinline__ void wait_until(u32 t) { while ((int)(t - now32()) > 0) { } }
__device__ __forceinline__ bf16x8 as_bf8(u32x4 v) { return __builtin_bit_cast(bf16x8, v); }
__device__ __forceinline__ bf16x16 as_bf16(u32x4 a, u32x4 b) {
  u32x8 v = {a.x, a.y, a.z, a.w, b.x, b.y, b.z, b.w}; return __builtin_bit_cast(bf16x16, v);
}
__device__ __forceinline__ float max3r(float a, float b, float c) { float r; asm("v_max3_f32 %0, %1, %2, %3" : "=v"(r) : "v"(a), "v"(b), "v"(c)); return r; }
__device__ __forceinline__ float max2r(float a, float b) { float r; asm("v_max_f32 %0, %1, %2" : "=v"(r) : "v"(a), "v"(b)); return r; }
#define MFMA16(a, b, c) __builtin_amdgcn_mfma_f32_16x16x32_bf16((a), (b), (c), 0, 0, 0)
#define MFMA32(a, b, c) __builtin_amdgcn_mfma_f32_32x32x16_bf16((a), (b), (c), 0, 0, 0)
#define SMF(a, b, c, idx) __builtin_amdgcn_smfmac_f32_16x16x64_bf16((a), (b), (c), (idx), 0, 0)

template <int N>
__device__ __forceinline__ u32 poll_chunks(__amdgpu_buffer_rsrc_t r, const u32 (&off)[N], u32x4 (&a)[N], u32 lane16, u32& tiss) {
  u32 spins = 0;
  while (true) {
    asm volatile("" : "+v"(lane16) :: "memory");
    if (spins) tiss = now32();
    u32 bad = 0;
#pragma unroll
    for (int j = 0; j < N; j++) a[j] = ld16_sc1(r, off[j] + lane16);
#pragma unroll
    for (int j = 0; j < N; j++) bad |= badm(a[j]);
    ++spins;
    if (!__any(bad)) return spins;
    if (spins > 400000u) return 0;
  }
}

typedef __attribute__((ext_vector_type(4))) short s16x4;
typedef __attribute__((address_space(3))) s16x4 lds_s16x4;
template <int ROWB>
__device__ __forceinline__ void dma_tile(char* lds, const u16* src, int rows, int gstride) {
  constexpr int LPR = ROWB / 16;
  const int total = rows * LPR;
  for (int u = threadIdx.x; u < total; u += 256) {
    const int r = u / LPR, c = u % LPR;
    __builtin_amdgcn_global_load_lds((const u32*)(src + (size_t)r * gstride + c * 8), (u32*)(lds + u * 16), 16, 0, 0);
  }
}
__device__ __forceinline__ void dma_wait() { asm volatile("s_waitcnt vmcnt(0)" ::: "memory"); __syncthreads(); }
__device__ __forceinline__ u32x2 tr4(const char* lds, int row0, int col0, int pitch, int lane) {
  const char* p = lds + (row0 + ((lane & 15) >> 2)) * pitch + (col0 + 4 * (lane & 3)) * 2;
  s16x4 v = __builtin_amdgcn_ds_read_tr16_b64_v4i16((lds_s16x4*)p);
  return __builtin_bit_cast(u32x2, v);
}
__device__ __forceinline__ bf16x8 frag8(const char* lds, int k0, int col0, int pitch, int lane) {
  u32x2 a = tr4(lds, k0, col0, pitch, lane), b = tr4(lds, k0 + 4, col0, pitch, lane);
  return as_bf8((u32x4){a.x, a.y, b.x, b.y});
}
__device__ __forceinline__ u32x4 frag8s(const char* lds, int k0, int col0, int pitch, int lane) {
  u32x2 a = tr4(lds, k0, col0, pitch, lane), b = tr4(lds, k0 + 8, col0, pitch, lane);
  return (u32x4){a.x, b.x, a.y, b.y};
}
#define PACK_S(acc, b) (u32x4){pk2((acc)[(b)], (acc)[(b) + 1]), pk2((acc)[(b) + 4], (acc)[(b) + 5]), pk2((acc)[(b) + 2], (acc)[(b) + 3]), pk2((acc)[(b) + 6], (acc)[(b) + 7])}

__device__ __forceinline__ void wvo_task(const u16* __restrict__ Wv, const u16* __restrict__ Wo, char* ws, char* lds, int task) {
  const int tid = threadIdx.x, lane = tid & 63, w = tid >> 6;
  const int h = task >> 2, nq = task & 3, x = lane & 31, hh = lane >> 5;
  dma_tile<256>(lds, Wo + (size_t)h * DH * DM + nq * 128, DH, DM);
  bf16x8 a[4][8];
#pragma unroll
  for (int mt = 0; mt < 4; mt++)
#pragma unroll
    for (int ks = 0; ks < 8; ks++)
      a[mt][ks] = *(const bf16x8*)(Wv + ((size_t)(128 * w + 32 * mt + x) * NH + h) * DH + 16 * ks + 8 * hh);
  dma_wait();
  const __amdgpu_buffer_rsrc_t rd = mkrsrc(ws + OFF_WVO);
#pragma unroll
  for (int nt = 0; nt < 4; nt++) {
    bf16x8 bw[8];
#pragma unroll
    for (int ks = 0; ks < 8; ks++) bw[ks] = frag8(lds, 16 * ks + 8 * hh, 32 * nt + (x & 16), 256, lane);
#pragma unroll
    for (int mt = 0; mt < 4; mt++) {
      f32x16 acc;
#pragma unroll
      for (int i = 0; i < 16; i++) acc[i] = 0.f;
#pragma unroll
      for (int ks = 0; ks < 8; ks++) acc = MFMA32(a[mt][ks], bw[ks], acc);
      const int d = 128 * nq + 32 * nt + x, c = d >> 3, cc = d & 7;
#pragma unroll
      for (int xb = 0; xb < 2; xb++) {
        const int dp = 128 * w + 32 * mt + 16 * xb, t = dp >> 6, ga = (dp >> 4) & 3, z = ga >> 1, gb = 2 * (ga & 1) + hh;
        const int lp = (h & 1) * 8 + cc + 16 * gb;
        u32 o = (u32)(((((c * 4 + (h >> 3)) * 4 + ((h >> 1) & 3)) * 8 + t) * 2 + z) * 64 + lp);
        st16_sc1(PACK_S(acc, 8 * xb), rd, o * 16u);
      }
    }
  }
  asm volatile("s_waitcnt vmcnt(0)" ::: "memory");
  __syncthreads();
  if (tid == 0) st4_sc1(1u, mkrsrc(ws + OFF_FLAG), OFF_WVF + task * 16u);
}

__device__ __forceinline__ void mod_cols(const u16* __restrict__ cvec, const u16* __restrict__ wmod, char* ws, char* lds, int col0) {
  const int tid = threadIdx.x, cp = tid & 15, ksl = tid >> 4;
  float* red = (float*)lds;
  u32 wv[32];
#pragma unroll
  for (int k = 0; k < 32; k++) wv[k] = *(const u32*)(wmod + (size_t)(ksl * 32 + k) * 3072 + col0 + cp * 2);
  float* sl_ = (float*)(lds + 2048);
  __syncthreads();
  {
    u32 cw = *(const u32*)(cvec + tid * 2);
    float c0 = bf_lo(cw), c1 = bf_hi(cw);
    sl_[tid * 2] = bfr(c0 / (1.f + __expf(-c0)));
    sl_[tid * 2 + 1] = bfr(c1 / (1.f + __expf(-c1)));
  }
  __syncthreads();
  float a0 = 0.f, a1 = 0.f;
#pragma unroll
  for (int k4 = 0; k4 < 8; k4++) {
    f32x4 sv = *(const f32x4*)(sl_ + ksl * 32 + k4 * 4);
#pragma unroll
    for (int j = 0; j < 4; j++) { a0 += sv[j] * bf_lo(wv[k4 * 4 + j]); a1 += sv[j] * bf_hi(wv[k4 * 4 + j]); }
  }
  red[ksl * 32 + cp * 2] = a0; red[ksl * 32 + cp * 2 + 1] = a1;
  __syncthreads();
  if (tid < 32) {
    float sacc = 0.f;
#pragma unroll
    for (int k = 0; k < 16; k++) sacc += red[k * 32 + tid];
    st4_sc1(f2u(bfr(sacc)), mkrsrc(ws + OFF_MOD), (u32)(col0 + tid) * 4u);
  }
}

__device__ __forceinline__ float row16_sum(float v) {
  v += u2f((u32)__builtin_amdgcn_mov_dpp((int)f2u(v), 0x128, 0xF, 0xF, true));
  v += u2f((u32)__builtin_amdgcn_mov_dpp((int)f2u(v), 0x124, 0xF, 0xF, true));
  v += u2f((u32)__builtin_amdgcn_mov_dpp((int)f2u(v), 0x122, 0xF, 0xF, true));
  v += u2f((u32)__builtin_amdgcn_mov_dpp((int)f2u(v), 0x121, 0xF, 0xF, true));
  return v;
}

__device__ __forceinline__ bool wait_progress(__amdgpu_buffer_rsrc_t rf, u32 off, u32 need, u32& seen) {
  u32 spins = 0;
  if (__all((int)seen >= (int)need)) return true;
  while (true) {
    asm volatile("" : "+v"(off) :: "memory");
    seen = ld4_sc1(rf, off);
    if (__all((int)seen >= (int)need)) return true;
    __builtin_amdgcn_s_sleep(2);
    if (++spins > 400000u) return false;
  }
}

__device__ __forceinline__ void px_task(char* ws, int s, int hg);
__device__ __forceinline__ void role_help(const u16* __restrict__ x, const u16* __restrict__ w1, const u16* __restrict__ w2,
                                          const u16* __restrict__ cvec, const u16* __restrict__ wmod, const u16* __restrict__ mem0,
                                          char* ws, char* lds, int a0) {
  const int tid = threadIdx.x, lane = tid & 63, w = tid >> 6, n = lane & 15, g = lane >> 4;
  u32x4 xpre[2][2][4];
#pragma unroll
  for (int sg = 0; sg < 2; sg++)
#pragma unroll
    for (int it = 0; it < 2; it++)
#pragma unroll
      for (int j = 0; j < 4; j++) xpre[sg][it][j] = *(const u32x4*)(x + (size_t)((a0 + 32 * sg) * 32 + it * 16 + w * 4 + g) * DM + (j * 16 + n) * 8);
  if (a0 == 31) {
    for (int i = tid; i < 512; i += 256) {
      u32x4 v = *(const u32x4*)(mem0 + (i & 7) * DM + (i >> 3) * 8);
      v.x = sane(v.x); v.y = sane(v.y); v.z = sane(v.z); v.w = sane(v.w);
      st16_sc1(v, mkrsrc(ws + OFF_MEMX), (u32)i * 16u);
    }
  }
  mod_cols(cvec, wmod, ws, lds, a0 * 32);
  asm volatile("s_waitcnt vmcnt(0)" ::: "memory");
  __syncthreads();
  if (tid == 0) st4_sc1(1u, mkrsrc(ws + OFF_FLAG), OFF_MDF + a0 * 16u);
  f32x2 fz[2][2][16];
#pragma unroll
  for (int sg = 0; sg < 2; sg++)
#pragma unroll
    for (int it = 0; it < 2; it++) {
      f32x2* f = fz[sg][it];
#pragma unroll
      for (int j = 0; j < 4; j++) {
        const u32x4 v = xpre[sg][it][j];
        f[4 * j + 0] = (f32x2){bf_lo(v.x), bf_hi(v.x)}; f[4 * j + 1] = (f32x2){bf_lo(v.y), bf_hi(v.y)};
        f[4 * j + 2] = (f32x2){bf_lo(v.z), bf_hi(v.z)}; f[4 * j + 3] = (f32x2){bf_lo(v.w), bf_hi(v.w)};
      }
      f32x2 s2 = {0.f, 0.f};
#pragma unroll
      for (int i = 0; i < 16; i++) s2 += f[i];
      const float m = row16_sum(s2.x + s2.y) * (1.f / DM);
      f32x2 q2 = {0.f, 0.f};
#pragma unroll
      for (int i = 0; i < 16; i++) { f[i] -= m; q2 += f[i] * f[i]; }
      const float rs = rsqrtf(row16_sum(q2.x + q2.y) * (1.f / DM) + 1e-5f);
#pragma unroll
      for (int i = 0; i < 16; i++) f[i] *= rs;
    }
  { u32 seenm = 0xFFFFFFFFu; if (!wait_progress(mkrsrc(ws + OFF_FLAG), OFF_MDF + (lane & 31) * 16u, 1u, seenm)) return; }
  float* modl = (float*)(lds + 8192);
  {
    u32x4 mv = ld16_sc1(mkrsrc(ws + OFF_MOD), (u32)tid * 16u);
    f32x4 mf = {u2f(mv.x), u2f(mv.y), u2f(mv.z), u2f(mv.w)};
    *(f32x4*)(modl + tid * 4) = tid >= 128 ? mf + 1.f : mf;
  }
  __syncthreads();
  const __amdgpu_buffer_rsrc_t rh = mkrsrc(ws + OFF_H);
#pragma unroll
  for (int sg = 0; sg < 2; sg++) {
    const int a = a0 + 32 * sg;
#pragma unroll
    for (int it = 0; it < 2; it++) {
      const f32x2* f = fz[sg][it];
#pragma unroll
      for (int j = 0; j < 4; j++) {
        const int c0 = (j * 16 + n) * 8;
        f32x4 c0v = *(const f32x4*)(modl + 512 + c0), c1v = *(const f32x4*)(modl + 512 + c0 + 4);
        f32x4 s0v = *(const f32x4*)(modl + c0), s1v = *(const f32x4*)(modl + c0 + 4);
        f32x2 o0 = f[4 * j + 0] * (f32x2){c0v[0], c0v[1]} + (f32x2){s0v[0], s0v[1]};
        f32x2 o1 = f[4 * j + 1] * (f32x2){c0v[2], c0v[3]} + (f32x2){s0v[2], s0v[3]};
        f32x2 o2 = f[4 * j + 2] * (f32x2){c1v[0], c1v[1]} + (f32x2){s1v[0], s1v[1]};
        f32x2 o3 = f[4 * j + 3] * (f32x2){c1v[2], c1v[3]} + (f32x2){s1v[2], s1v[3]};
        u32x4 r = {pk2(o0.x, o0.y), pk2(o1.x, o1.y), pk2(o2.x, o2.y), pk2(o3.x, o3.y)};
        st16_sc1(r, rh, (u32)((a * 32 + it * 16 + w * 4 + g) * DM + c0) * 2u);
      }
    }
    asm volatile("s_waitcnt vmcnt(0)" ::: "memory");
    __syncthreads();
    if (tid == 0) st4_sc1(1u, mkrsrc(ws + OFF_FLAG), OFF_DONE + a * 64u);
  }
  const int a = a0;
  mod_cols(cvec, wmod, ws, lds, 1024 + a * 64);
  mod_cols(cvec, wmod, ws, lds, 1024 + a * 64 + 32);
  __syncthreads();
  if (a < 28) {
    dma_tile<128>(lds, w1 + a * 64, DM, FF);
    dma_wait();
    u32x4* dst = (u32x4*)(ws + OFF_W1F);
    for (int ks = 0; ks < 16; ks++) {
      bf16x8 v = frag8(lds, 32 * ks + 8 * (lane >> 4), 16 * w, 128, lane);
      dst[((size_t)(a * 4 + w) * 16 + ks) * 64 + lane] = __builtin_bit_cast(u32x4, v);
    }
  }
  __syncthreads();
  {
    const int cb = a >> 2, kq = a & 3;
    dma_tile<128>(lds, w2 + (size_t)kq * 448 * DM + cb * 64, 448, DM);
    dma_wait();
    u32x4* dst = (u32x4*)(ws + OFF_W2F);
    for (int ks = 0; ks < 14; ks++) {
      u32x2 va = tr4(lds, 32 * ks + 4 * (lane >> 4), 16 * w, 128, lane), vb = tr4(lds, 32 * ks + 16 + 4 * (lane >> 4), 16 * w, 128, lane);
      dst[((size_t)(cb * 4 + w) * 56 + kq * 14 + ks) * 64 + lane] = (u32x4){va.x, va.y, vb.x, vb.y};
    }
  }
  {
    const __amdgpu_buffer_rsrc_t rflag = mkrsrc(ws + OFF_FLAG);
    const int hg = a & 7;
    for (int s = a >> 3; s < NSEG; s += 4) {
      px_task(ws, s, hg);
    }
  }
}

#define AD_DEC 3
#define AD_INC 60
#define ADAPT(D, n) do { if ((n) == 1) D -= AD_DEC; else D += AD_INC; if (D < 0) D = 0; } while (0)

__device__ __forceinline__ void role_score(const u16* __restrict__ Wq, const u16* __restrict__ Wk, char* ws,
                                           char* lds, int h) {
  const int tid = threadIdx.x, lane = tid & 63, w = tid >> 6;
  const int n = lane & 15, gb = lane >> 4;
  bf16x16 wf[4][8];
#pragma unroll
  for (int mat = 0; mat < 2; mat++) {
    __syncthreads();
    dma_tile<256>(lds, (mat ? Wk : Wq) + h * DH, DM, NH * DH);
    dma_wait();
#pragma unroll
    for (int tt = 0; tt < 2; tt++)
#pragma unroll
      for (int t = 0; t < 8; t++) {
        const int col = 32 * w + 16 * tt;
        u32x4 lo = frag8s(lds, 64 * t + 16 * (gb >> 1) + 4 * (gb & 1), col, 256, lane);
        u32x4 hi = frag8s(lds, 64 * t + 16 * (2 + (gb >> 1)) + 4 * (gb & 1), col, 256, lane);
        wf[mat * 2 + tt][t] = as_bf16(lo, hi);
      }
  }
  __syncthreads();
  char* memL = lds;
  char* qs = lds + 8192;
  char* ks_ = lds + 8192 + 2304;
  const int idx = (lane & 8) ? (int)0xEEEEEEEE : 0x44444444;
  const __amdgpu_buffer_rsrc_t rflag = mkrsrc(ws + OFF_FLAG);
  const __amdgpu_buffer_rsrc_t rkx = mkrsrc(ws + OFF_KX);
  const __amdgpu_buffer_rsrc_t rmem = mkrsrc(ws + OFF_MEMX);
  const __amdgpu_buffer_rsrc_t rpex = mkrsrc(ws + OFF_PEX);
  const int rho = lane & 15;
  const int keyp = 8 * ((rho >> 2) & 1) + 4 * (rho >> 3) + (rho & 3);
  const u32 prho = (u32)((n & 7) >> 2) * 10240u, pi = n & 3;
  const u32 pxoff = prho + (u32)(((h >> 2) * 64) + 16 * (2 * (h & 1) + (gb >> 1)) + 8 * (gb & 1) + 4 * ((h >> 1) & 1) + pi) * 16u + (n >> 3) * 8u;
  const u32 pmoff = prho + 8192u + (u32)(h * 4 + pi) * 16u + (gb & 1) * 8u + (gb >> 1) * 4u;
  int D1 = w == 0 ? 3400 : (w == 1 ? 3300 : 4050); u32 tref = now32() - 8000u, seen = 0xFFFFFFFFu;
  STAMP(1);
  for (int s = 0; s < NSEG; s++) {
    u32x4 kx[2][4];
    if (w == 0) {
      if (!wait_progress(rflag, 4096u + h * 128u, s + 1, seen)) return;
#pragma unroll
      for (int tt = 0; tt < 2; tt++)
#pragma unroll
        for (int k4 = 0; k4 < 4; k4++)
          kx[tt][k4] = ld16_sc1(rkx, (u32)(((32 * s + 16 * tt + keyp) * NH + h) * DH + 32 * k4 + 8 * gb) * 2u);
    }
    f32x4 acc[4];
#pragma unroll
    for (int i = 0; i < 4; i++) { acc[i] = (f32x4){0.f, 0.f, 0.f, 0.f}; asm volatile("" : "+v"(acc[i])); }
    wait_until(tref + D1);
    {
      u32 off[2] = {(u32)s * 8192u + w * 2048u, (u32)s * 8192u + w * 2048u + 1024u};
      u32x4 a[2];
      u32 tiss = 0;
      u32 nsw = poll_chunks<2>(rmem, off, a, lane * 16u, tiss);
      if (!nsw) return;
      if (s > 2) ADAPT(D1, nsw);
      *(u32x4*)(memL + w * 2048 + lane * 16) = a[0];
      *(u32x4*)(memL + w * 2048 + 1024 + lane * 16) = a[1];
    }
    __syncthreads();
    STAMP(16 + 4 * s);
    bf16x8 af[8];
#pragma unroll
    for (int t = 0; t < 8; t++) af[t] = *(const bf16x8*)(memL + t * 1024 + lane * 16);
    __builtin_amdgcn_sched_barrier(0);
    const int ecol = 32 * w + 16 * (lane >> 5) + n, er0 = 4 * ((lane >> 4) & 1);
#pragma unroll
    for (int t = 0; t < 8; t++) { acc[0] = SMF(af[t], wf[0][t], acc[0], idx); acc[1] = SMF(af[t], wf[1][t], acc[1], idx); }
    __builtin_amdgcn_sched_barrier(0);
#pragma unroll
    for (int t = 0; t < 8; t++) {
      acc[2] = SMF(af[t], wf[2][t], acc[2], idx); acc[3] = SMF(af[t], wf[3][t], acc[3], idx);
      if (t >= 2 && t < 6) {
        const int j = t - 2;
        auto r = __builtin_amdgcn_permlane32_swap(f2u(acc[0][j]), f2u(acc[1][j]), false, false);
        *(__bf16*)(qs + (er0 + j) * 272 + ecol * 2) = (__bf16)(u2f(r[0]) + u2f(r[1]));
      }
      __builtin_amdgcn_sched_barrier(0);
    }
#pragma unroll
    for (int j = 0; j < 4; j++) {
      auto r = __builtin_amdgcn_permlane32_swap(f2u(acc[2][j]), f2u(acc[3][j]), false, false);
      *(__bf16*)(ks_ + (er0 + j) * 272 + ecol * 2) = (__bf16)(u2f(r[0]) + u2f(r[1]));
    }
    __syncthreads();
    STAMP(17 + 4 * s);
    if (w == 0 && s < NSEG - 1) {
      f32x4 sc[3];
#pragma unroll
      for (int i = 0; i < 3; i++) { sc[i] = (f32x4){0.f, 0.f, 0.f, 0.f}; asm volatile("" : "+v"(sc[i])); }
      bf16x8 qf[4], kf[4];
#pragma unroll
      for (int k4 = 0; k4 < 4; k4++) {
        qf[k4] = *(const bf16x8*)(qs + (lane & 7) * 272 + (32 * k4 + 8 * gb) * 2);
        kf[k4] = *(const bf16x8*)(ks_ + (lane & 7) * 272 + (32 * k4 + 8 * gb) * 2);
      }
      __builtin_amdgcn_sched_barrier(0);
#pragma unroll
      for (int k4 = 0; k4 < 4; k4++) {
        sc[0] = MFMA16(as_bf8(kx[0][k4]), qf[k4], sc[0]);
        sc[1] = MFMA16(as_bf8(kx[1][k4]), qf[k4], sc[1]);
        sc[2] = MFMA16(kf[k4], qf[k4], sc[2]);
      }
      const float sl = 0.08838834764831845f * 1.4426950408889634f;
      const bool hiq = n >= 8, hig = gb >= 2;
      float p[6];
#pragma unroll
      for (int j = 0; j < 4; j++) p[j] = hiq ? sc[1][j] : sc[0][j];
      p[4] = hig ? sc[2][2] : sc[2][0];
      p[5] = hig ? sc[2][3] : sc[2][1];
      float mx = max2r(max3r(p[0], p[1], p[2]), max3r(p[3], p[4], p[5]));
      mx = max2r(mx, u2f((u32)__builtin_amdgcn_mov_dpp((int)f2u(mx), 0x128, 0xF, 0xF, true)));
      { auto r = __builtin_amdgcn_permlane16_swap(f2u(mx), f2u(mx), false, false);
        mx = max2r(u2f(r[0]), u2f(r[1])); }
      { auto r = __builtin_amdgcn_permlane32_swap(f2u(mx), f2u(mx), false, false);
        mx = max2r(u2f(r[0]), u2f(r[1])); }
      const float nb = -mx * sl;
#pragma unroll
      for (int j = 0; j < 6; j++) p[j] = __builtin_amdgcn_exp2f(__builtin_fmaf(p[j], sl, nb));
      float sum = (p[0] + p[1]) + (p[2] + p[3]) + (hiq ? 0.f : (p[4] + p[5]));
      sum += u2f((u32)__builtin_amdgcn_mov_dpp((int)f2u(sum), 0x128, 0xF, 0xF, true));
      { auto r = __builtin_amdgcn_permlane16_swap(f2u(sum), f2u(sum), false, false);
        sum = u2f(r[0]) + u2f(r[1]); }
      { auto r = __builtin_amdgcn_permlane32_swap(f2u(sum), f2u(sum), false, false);
        sum = u2f(r[0]) + u2f(r[1]); }
      const float inv = __builtin_amdgcn_rcpf(sum);
      u32x2 px = {sane(pk2(p[0] * inv, p[1] * inv)), sane(pk2(p[2] * inv, p[3] * inv))};
      st8_sc1(px, rpex, (u32)s * 20480u + pxoff);
      if (!hiq) st4_sc1(sane(pk2(p[4] * inv, p[5] * inv)), rpex, (u32)s * 20480u + pmoff);
      STAMP(18 + 4 * s);
    } else if (w == 1) {
      const __amdgpu_buffer_rsrc_t rkm = mkrsrc(ws + OFF_KMEM);
#pragma unroll
      for (int j = 0; j < 2; j++) {
        int u = j * 64 + lane, r = u >> 4, c = u & 15;
        st16_sc1(*(const u32x4*)(ks_ + r * 272 + c * 16), rkm, (u32)((s * NH + h) * 1024 + r * 128 + c * 8) * 2u);
      }
      asm volatile("s_waitcnt vmcnt(0)" ::: "memory");
      if (lane == 0) st4_sc1((u32)(s + 1), rflag, OFF_KMF + h * 64u);
    }
    tref = now32();
  }
}

__device__ __forceinline__ void role_column(const u16* __restrict__ Wv, const u16* __restrict__ Wo, char* ws, char* lds, int c, int rho) {
  const int tid = threadIdx.x, lane = tid & 63, w = tid >> 6;
  const int n = lane & 15, gb = lane >> 4, cc = lane & 7;
  bf16x16 wf[4][8];
  wvo_task(Wv, Wo, ws, lds, c * 2 + rho);
  {
    u32 seenw = 0xFFFFFFFFu;
    if (!wait_progress(mkrsrc(ws + OFF_FLAG), OFF_WVF + (u32)((lane & 31) * 4 + (c >> 4)) * 16u, 1u, seenw)) return;
    const __amdgpu_buffer_rsrc_t rsrc = mkrsrc(ws + OFF_WVO + (size_t)(c * 4 + w) * 65536);
#pragma unroll
    for (int tl = 0; tl < 4; tl++)
#pragma unroll
      for (int t = 0; t < 8; t++)
        wf[tl][t] = as_bf16(ld16_sc1(rsrc, (u32)(((tl * 8 + t) * 2) * 64 + lane) * 16u), ld16_sc1(rsrc, (u32)(((tl * 8 + t) * 2 + 1) * 64 + lane) * 16u));
  }
  char* memL = lds;
  char* yl = lds + 8192 + w * 1024;
  float* red = (float*)(lds + 12288);
  const int idx = (lane & 8) ? (int)0xEEEEEEEE : 0x44444444;
  const __amdgpu_buffer_rsrc_t rflag = mkrsrc(ws + OFF_FLAG);
  const __amdgpu_buffer_rsrc_t rmem = mkrsrc(ws + OFF_MEMX);
  const __amdgpu_buffer_rsrc_t rpex = mkrsrc(ws + OFF_PEX);
  int D1 = w == 0 ? 750 : (w == 1 ? 850 : 1130), D2 = 2800; u32 tref = now32(), seen = 0xFFFFFFFFu;
  STAMP(1);
  for (int s = 0; s < NSEG; s++) {
    wait_until(tref + D1);
    {
      u32 off[2] = {(u32)s * 8192u + w * 2048u, (u32)s * 8192u + w * 2048u + 1024u};
      u32x4 a[2];
      u32 tiss = 0;
      u32 nsw = poll_chunks<2>(rmem, off, a, lane * 16u, tiss);
      if (!nsw) return;
      if (s > 2) ADAPT(D1, nsw);
      tref = now32();
      *(u32x4*)(memL + w * 2048 + lane * 16) = a[0];
      *(u32x4*)(memL + w * 2048 + 1024 + lane * 16) = a[1];
    }
    u32x4 uf[4][2];
    {
      if (!wait_progress(rflag, (u32)(8 * w + (lane & 7)) * 128u, s + 1, seen)) return;
      const __amdgpu_buffer_rsrc_t ru = mkrsrc(ws + OFF_UF + ((size_t)s * 64 + c) * 16384);
#pragma unroll
      for (int m = 0; m < 4; m++)
#pragma unroll
        for (int z = 0; z < 2; z++) uf[m][z] = ld16_sc1(ru, (u32)((((4 * w + m) * 2 + z) * 4 + gb) * 8 + cc) * 16u);
    }
    __syncthreads();
    STAMP(16 + 4 * s);
    bf16x16 yb;
    {
      f32x4 acc[4];
#pragma unroll
      for (int i = 0; i < 4; i++) acc[i] = (f32x4){0.f, 0.f, 0.f, 0.f};
      bf16x8 af[8];
#pragma unroll
      for (int t = 0; t < 8; t++) af[t] = *(const bf16x8*)(memL + t * 1024 + lane * 16);
      __builtin_amdgcn_sched_barrier(0);
#pragma unroll
      for (int t = 0; t < 8; t++)
#pragma unroll
        for (int i = 0; i < 4; i++) acc[i] = SMF(af[t], wf[i][t], acc[i], idx);
#pragma unroll
      for (int pr = 0; pr < 2; pr++) {
        float v[4];
#pragma unroll
        for (int j = 0; j < 4; j++) {
          auto r = __builtin_amdgcn_permlane32_swap(f2u(acc[2 * pr][j]), f2u(acc[2 * pr + 1][j]), false, false);
          v[j] = u2f(r[0]) + u2f(r[1]);
        }
        const int hl = 2 * (2 * pr + (lane >> 5)) + (n >> 3);
        u32x2 pv = {pk2(v[0], v[1]), pk2(v[2], v[3])};
        *(u32x2*)(yl + (hl * 8 + cc) * 16 + 8 * ((lane >> 4) & 1)) = pv;
      }
      asm volatile("s_waitcnt lgkmcnt(0)" ::: "memory");
      u32x4 hz[2];
#pragma unroll
      for (int z = 0; z < 2; z++) {
        const int ga = 2 * z + (gb >> 1);
        u32x2 a = *(const u32x2*)(yl + ((2 * ga) * 8 + cc) * 16 + 8 * (gb & 1));
        u32x2 b = *(const u32x2*)(yl + ((2 * ga + 1) * 8 + cc) * 16 + 8 * (gb & 1));
        hz[z] = (u32x4){a.x, b.x, a.y, b.y};
      }
      yb = as_bf16(hz[0], hz[1]);
      if (rho == 0) __builtin_amdgcn_raw_buffer_store_b128(*(const u32x4*)(yl + lane * 16), mkrsrc(ws + OFF_Y), (u32)(s * 64 + c) * 4096u + w * 1024u + lane * 16u, 0, 0);
    }
    STAMP(17 + 4 * s);
    if (s == NSEG - 1) break;
    wait_until(tref + D2);
    {
      const u32 pb = (u32)s * 20480u + rho * 10240u;
      u32 off[3] = {pb + (2 * w) * 1024u + lane * 16u, pb + (2 * w + 1) * 1024u + lane * 16u,
                    pb + 8192u + (u32)((8 * w + 2 * gb + ((lane >> 3) & 1)) * 4 + (lane & 3)) * 16u};
      u32x4 g[3];
      u32 tiss = 0;
      u32 nsw = poll_chunks<3>(rpex, off, g, 0u, tiss);
      if (!nsw) return;
      if (s > 2) ADAPT(D2, nsw);
      f32x4 dA = {0.f, 0.f, 0.f, 0.f}, dB = {0.f, 0.f, 0.f, 0.f};
      dA = SMF(as_bf8(g[0]), as_bf16(uf[0][0], uf[0][1]), dA, idx);
      dB = SMF(as_bf8(g[0]), as_bf16(uf[1][0], uf[1][1]), dB, idx);
      dA = SMF(as_bf8(g[1]), as_bf16(uf[2][0], uf[2][1]), dA, idx);
      dB = SMF(as_bf8(g[1]), as_bf16(uf[3][0], uf[3][1]), dB, idx);
      dA = SMF(as_bf8(g[2]), yb, dA, idx);
      float* rd = red + (s & 1) * 512;
      if (n < 8) {
#pragma unroll
        for (int j = 0; j < 4; j++) rd[(j * 8 + cc) * 16 + 4 * w + gb] = (gb & 1) ? dB[j] : dA[j];
      }
    }
    __syncthreads();
    STAMP(18 + 4 * s);
    if (w == 0) {
      const float* rd = red + (s & 1) * 512 + lane * 8;
      f32x4 a = *(const f32x4*)rd, b = *(const f32x4*)(rd + 4);
      float v = ((a[0] + a[1]) + (a[2] + a[3])) + ((b[0] + b[1]) + (b[2] + b[3]));
      v += u2f((u32)__builtin_amdgcn_mov_dpp((int)f2u(v), 0xB1, 0xF, 0xF, true));
      float o = u2f((u32)__builtin_amdgcn_mov_dpp((int)f2u(v), 0x4E, 0xF, 0xF, true));
      if (!(lane & 3)) st4_sc1(sane(pk2(v, o)), rmem, (u32)(s + 1) * 8192u + c * 128u + rho * 64u + (lane >> 2) * 4u);
      STAMP(19 + 4 * s);
    }
    tref = now32();
  }
}

__device__ __forceinline__ void load_h(u32x4 (&r)[8], const char* ws, int s) {
  const __amdgpu_buffer_rsrc_t rh = mkrsrc(ws + OFF_H);
#pragma unroll
  for (int j = 0; j < 8; j++) r[j] = ld16_sc1(rh, (u32)s * 32768u + (j * 256 + threadIdx.x) * 16u);
}
__device__ __forceinline__ void put_h(char* dst, const u32x4 (&r)[8]) {
#pragma unroll
  for (int j = 0; j < 8; j++) {
    int u = j * 256 + threadIdx.x;
    *(u32x4*)(dst + (u >> 6) * 1040 + (u & 63) * 16) = r[j];
  }
}

template <bool SC1>
__device__ __forceinline__ void store_T_tile(const f32x16& acc, char* base, int tokrow, int h, int w, int lane) {
  const __amdgpu_buffer_rsrc_t r = mkrsrc(base);
#pragma unroll
  for (int qp = 0; qp < 2; qp++) {
    u32 ax = pk2(acc[8 * qp + 0], acc[8 * qp + 1]), ay = pk2(acc[8 * qp + 2], acc[8 * qp + 3]);
    u32 bx = pk2(acc[8 * qp + 4], acc[8 * qp + 5]), by = pk2(acc[8 * qp + 6], acc[8 * qp + 7]);
    auto r0 = __builtin_amdgcn_permlane32_swap(ax, bx, false, false);
    auto r1 = __builtin_amdgcn_permlane32_swap(ay, by, false, false);
    u32x4 v = {(u32)r0[0], (u32)r1[0], (u32)r0[1], (u32)r1[1]};
    u32 off = (u32)(((tokrow + (lane & 31)) * NH + h) * DH + 32 * w + 16 * qp + 8 * (lane >> 5)) * 2u;
    if (SC1) __builtin_amdgcn_raw_buffer_store_b128(v, r, off, 0, 16);
    else __builtin_amdgcn_raw_buffer_store_b128(v, r, off, 0, 0);
  }
}

__device__ __forceinline__ void role_vu(const u16* __restrict__ Wv, const u16* __restrict__ Wo,
                                        char* ws, char* lds, int h) {
  const int tid = threadIdx.x, lane = tid & 63, w = tid >> 6, x = lane & 31, hh = lane >> 5;
  bf16x8 wv[32], wo[4][8];
  dma_tile<256>(lds, Wv + h * DH, DM, NH * DH);
  dma_wait();
#pragma unroll
  for (int ks = 0; ks < 32; ks++) wv[ks] = frag8(lds, 16 * ks + 8 * hh, 32 * w + (x & 16), 256, lane);
  __syncthreads();
  dma_tile<1024>(lds, Wo + (size_t)h * DH * DM, DH, DM);
  dma_wait();
#pragma unroll
  for (int nt = 0; nt < 4; nt++)
#pragma unroll
    for (int ks = 0; ks < 8; ks++) wo[nt][ks] = frag8(lds, 16 * ks + 8 * hh, 128 * w + 32 * nt + (x & 16), 1024, lane);
  __syncthreads();
  char* vs = lds + 66560;
  const __amdgpu_buffer_rsrc_t rflag = mkrsrc(ws + OFF_FLAG);
  u32x4 hr[8];
  { u32 seenh = 0xFFFFFFFFu; if (!wait_progress(rflag, OFF_DONE + (lane & 31) * 64u, 1u, seenh)) return; }
  load_h(hr, ws, 0);
  put_h(lds, hr);
  load_h(hr, ws, 1);
  __syncthreads();
  for (int s = 0; s < NSEG; s++) {
    const char* hb = lds + (s & 1) * 33280;
    f32x16 av;
#pragma unroll
    for (int i = 0; i < 16; i++) av[i] = 0.f;
    {
      bf16x8 hf[2][8];
#pragma unroll
      for (int k = 0; k < 8; k++) hf[0][k] = *(const bf16x8*)(hb + x * 1040 + (16 * k + 8 * hh) * 2);
#pragma unroll
      for (int ch = 0; ch < 4; ch++) {
        if (ch < 3) {
#pragma unroll
          for (int k = 0; k < 8; k++) hf[(ch + 1) & 1][k] = *(const bf16x8*)(hb + x * 1040 + (16 * (8 * ch + 8 + k) + 8 * hh) * 2);
        }
        __builtin_amdgcn_sched_barrier(0);
#pragma unroll
        for (int k = 0; k < 8; k++) av = MFMA32(wv[8 * ch + k], hf[ch & 1][k], av);
        __builtin_amdgcn_sched_barrier(0);
      }
    }
#pragma unroll
    for (int q = 0; q < 4; q++) {
      u32x2 pv = {pk2(av[4 * q], av[4 * q + 1]), pk2(av[4 * q + 2], av[4 * q + 3])};
      *(u32x2*)(vs + x * 272 + (32 * w + 8 * q + 4 * hh) * 2) = pv;
    }
    put_h(lds + ((s + 1) & 1) * 33280, hr);
    asm volatile("s_waitcnt vmcnt(0)" ::: "memory");
    __syncthreads();
    if (tid == 0 && s > 4) st4_sc1((u32)s, rflag, h * 128u);
    bf16x8 vf[8];
#pragma unroll
    for (int ks = 0; ks < 8; ks++) vf[ks] = *(const bf16x8*)(vs + x * 272 + (16 * ks + 8 * hh) * 2);
    const __amdgpu_buffer_rsrc_t ru = mkrsrc(ws + OFF_UF + (size_t)s * 64 * 16384);
#pragma unroll
    for (int nt = 0; nt < 4; nt++) {
      f32x16 au;
#pragma unroll
      for (int i = 0; i < 16; i++) au[i] = 0.f;
#pragma unroll
      for (int ks = 0; ks < 8; ks++) au = MFMA32(vf[ks], wo[nt][ks], au);
      const int d = 128 * w + 32 * nt + x, c = d >> 3, cc = d & 7;
#pragma unroll
      for (int xb = 0; xb < 2; xb++)
        st16_sc1(PACK_S(au, 8 * xb), ru, (u32)((((c * 16 + (h >> 1)) * 2 + (h & 1)) * 4 + 2 * hh + xb) * 8 + cc) * 16u);
    }
    if (s < 4 || s == NSEG - 1) asm volatile("s_waitcnt vmcnt(0)" ::: "memory");
    if (s == 30) { u32 seenh = 0xFFFFFFFFu; if (!wait_progress(rflag, OFF_DONE + lane * 64u, 1u, seenh)) return; }
    load_h(hr, ws, s + 2 < NSEG ? s + 2 : NSEG - 1);
    __syncthreads();
    if (tid == 0 && (s < 4 || s == NSEG - 1)) st4_sc1((u32)(s + 1), rflag, h * 128u);
    STAMP(16 + 4 * s);
  }
}

__device__ __forceinline__ void role_qk(const u16* __restrict__ Wq, const u16* __restrict__ Wk, char* ws, char* lds, int h) {
  const int tid = threadIdx.x, lane = tid & 63, w = tid >> 6, x = lane & 31, hh = lane >> 5;
  bf16x8 wq[32], wk[32];
  dma_tile<256>(lds, Wk + h * DH, DM, NH * DH);
  dma_wait();
#pragma unroll
  for (int ks = 0; ks < 32; ks++) wk[ks] = frag8(lds, 16 * ks + 8 * hh, 32 * w + (x & 16), 256, lane);
  __syncthreads();
  dma_tile<256>(lds, Wq + h * DH, DM, NH * DH);
  dma_wait();
#pragma unroll
  for (int ks = 0; ks < 32; ks++) wq[ks] = frag8(lds, 16 * ks + 8 * hh, 32 * w + (x & 16), 256, lane);
  __syncthreads();
  const __amdgpu_buffer_rsrc_t rflag = mkrsrc(ws + OFF_FLAG);
  u32x4 hr[8];
  { u32 seenh = 0xFFFFFFFFu; if (!wait_progress(rflag, OFF_DONE + (lane & 31) * 64u, 1u, seenh)) return; }
  load_h(hr, ws, 0);
  put_h(lds, hr);
  load_h(hr, ws, 1);
  __syncthreads();
  for (int s = 0; s < NSEG; s++) {
    const char* hb = lds + (s & 1) * 33280;
    f32x16 aq, ak;
#pragma unroll
    for (int i = 0; i < 16; i++) { aq[i] = 0.f; ak[i] = 0.f; }
    {
      bf16x8 hf[2][8];
#pragma unroll
      for (int k = 0; k < 8; k++) hf[0][k] = *(const bf16x8*)(hb + x * 1040 + (16 * k + 8 * hh) * 2);
#pragma unroll
      for (int ch = 0; ch < 4; ch++) {
        if (ch < 3) {
#pragma unroll
          for (int k = 0; k < 8; k++) hf[(ch + 1) & 1][k] = *(const bf16x8*)(hb + x * 1040 + (16 * (8 * ch + 8 + k) + 8 * hh) * 2);
        }
        __builtin_amdgcn_sched_barrier(0);
#pragma unroll
        for (int k = 0; k < 8; k++) { ak = MFMA32(wk[8 * ch + k], hf[ch & 1][k], ak); aq = MFMA32(wq[8 * ch + k], hf[ch & 1][k], aq); }
        __builtin_amdgcn_sched_barrier(0);
      }
    }
    put_h(lds + ((s + 1) & 1) * 33280, hr);
    asm volatile("s_waitcnt vmcnt(0)" ::: "memory");
    __syncthreads();
    if (tid == 0 && s > 4) st4_sc1((u32)s, rflag, 4096u + h * 128u);
    store_T_tile<true>(ak, ws + OFF_KX, 32 * s, h, w, lane);
    store_T_tile<true>(aq, ws + OFF_QX, 32 * s, h, w, lane);
    if (s < 4 || s == NSEG - 1) {
      asm volatile("s_waitcnt vmcnt(0)" ::: "memory");
      __syncthreads();
      if (tid == 0) st4_sc1((u32)(s + 1), rflag, 4096u + h * 128u);
    }
    if (s == 30) { u32 seenh = 0xFFFFFFFFu; if (!wait_progress(rflag, OFF_DONE + lane * 64u, 1u, seenh)) return; }
    load_h(hr, ws, s + 2 < NSEG ? s + 2 : NSEG - 1);
    STAMP(16 + 4 * s);
  }
}

__device__ __forceinline__ float xg_max(float v) {
  auto r = __builtin_amdgcn_permlane16_swap(f2u(v), f2u(v), false, false);
  v = fmaxf(u2f(r[0]), u2f(r[1]));
  auto r2 = __builtin_amdgcn_permlane32_swap(f2u(v), f2u(v), false, false);
  return fmaxf(u2f(r2[0]), u2f(r2[1]));
}
__device__ __forceinline__ float xg_sum(float v) {
  auto r = __builtin_amdgcn_permlane16_swap(f2u(v), f2u(v), false, false);
  v = u2f(r[0]) + u2f(r[1]);
  auto r2 = __builtin_amdgcn_permlane32_swap(f2u(v), f2u(v), false, false);
  return u2f(r2[0]) + u2f(r2[1]);
}
__device__ __forceinline__ void px_task(char* ws, int s, int hg) {
  const int tid = threadIdx.x, lane = tid & 63, w = tid >> 6;
  const int hd = hg * 4 + w, hl = w;
  const int n = lane & 15, g = lane >> 4;
  const __amdgpu_buffer_rsrc_t rq = mkrsrc(ws + OFF_QX + (size_t)s * 32 * 8192);
  const __amdgpu_buffer_rsrc_t rk = mkrsrc(ws + OFF_KX + (size_t)s * 32 * 8192);
  const __amdgpu_buffer_rsrc_t rkm = mkrsrc(ws + OFF_KMEM + (size_t)s * 65536);
  const int keyrow = 16 * ((n >> 2) & 1) + 4 * (n >> 3) + (n & 3);
  const int keyb = 16 * (g & 1) + 4 * (g >> 1);
  const float sl = 0.08838834764831845f * 1.4426950408889634f;
  bf16x8 qf[2][4], kf[2][4], mf[4];
  const __amdgpu_buffer_rsrc_t rflag = mkrsrc(ws + OFF_FLAG);
  { u32 seenq = 0xFFFFFFFFu; wait_progress(rflag, 4096u + hd * 128u, s + 1, seenq); }
#pragma unroll
  for (int ks = 0; ks < 4; ks++) {
#pragma unroll
    for (int t = 0; t < 2; t++) {
      qf[t][ks] = as_bf8(ld16_sc1(rq, (u32)(((16 * t + n) * NH + hd) * DH + 32 * ks + 8 * g) * 2u));
      kf[t][ks] = as_bf8(ld16_sc1(rk, (u32)(((8 * t + keyrow) * NH + hd) * DH + 32 * ks + 8 * g) * 2u));
    }
  }
  {
    u32 spins = 0, off = OFF_KMF + (u32)hd * 64u;
    while (true) {
      asm volatile("" : "+v"(off) :: "memory");
      u32 v = ld4_sc1(rflag, off);
      if ((int)v > s) break;
      __builtin_amdgcn_s_sleep(6);
      if (++spins > 800000u) break;
    }
  }
#pragma unroll
  for (int ks = 0; ks < 4; ks++) mf[ks] = as_bf8(ld16_sc1(rkm, (u32)(hd * 1024 + (n & 7) * 128 + 32 * ks + 8 * g) * 2u));
  const bool mvalid = (g >> 1) == (hl >> 1);
  u32x4* PXx = (u32x4*)(ws + OFF_PX);
  char* PXm = ws + OFF_PX + (4ull << 20);
#pragma unroll
  for (int qt = 0; qt < 2; qt++) {
    f32x4 sc[3];
#pragma unroll
    for (int i = 0; i < 3; i++) sc[i] = (f32x4){0.f, 0.f, 0.f, 0.f};
#pragma unroll
    for (int ks = 0; ks < 4; ks++) {
      sc[0] = MFMA16(kf[0][ks], qf[qt][ks], sc[0]);
      sc[1] = MFMA16(kf[1][ks], qf[qt][ks], sc[1]);
      sc[2] = MFMA16(mf[ks], qf[qt][ks], sc[2]);
    }
    const int qi = 16 * qt + n;
    float mx = -3.0e38f;
#pragma unroll
    for (int j = 0; j < 4; j++) {
      if (keyb + j > qi) sc[0][j] = -3.0e38f;
      if (keyb + 8 + j > qi) sc[1][j] = -3.0e38f;
      if (!mvalid) sc[2][j] = -3.0e38f;
      mx = fmaxf(mx, fmaxf(fmaxf(sc[0][j], sc[1][j]), sc[2][j]));
    }
    mx = xg_max(mx);
    float sum = 0.f;
#pragma unroll
    for (int i = 0; i < 3; i++)
#pragma unroll
      for (int j = 0; j < 4; j++) {
        float p = sc[i][j] > -1.0e38f ? __builtin_amdgcn_exp2f((sc[i][j] - mx) * sl) : 0.f;
        sc[i][j] = p; sum += p;
      }
    const float inv = __builtin_amdgcn_rcpf(xg_sum(sum));
    u32x4 pa = {pk2(sc[0][0] * inv, sc[0][1] * inv), pk2(sc[1][0] * inv, sc[1][1] * inv),
                pk2(sc[0][2] * inv, sc[0][3] * inv), pk2(sc[1][2] * inv, sc[1][3] * inv)};
    PXx[(((size_t)s * NH + hd) * 2 + qt) * 64 + lane] = pa;
    if (mvalid) {
      u32x2 pm = {pk2(sc[2][0] * inv, sc[2][1] * inv), pk2(sc[2][2] * inv, sc[2][3] * inv)};
      *(u32x2*)(PXm + ((((size_t)s * 8 + hg) * 2 + qt) * 64 + lane) * 16 + (hl & 1) * 8) = pm;
    }
  }
}

__global__ void __launch_bounds__(256, 1) k2_rec(const u16* __restrict__ Wq, const u16* __restrict__ Wk, const u16* __restrict__ Wv,
                                                  const u16* __restrict__ Wo, const u16* __restrict__ x, const u16* __restrict__ w1,
                                                  const u16* __restrict__ w2, const u16* __restrict__ cvec,
                                                  const u16* __restrict__ wmod, const u16* __restrict__ mem0, char* ws) {
  extern __shared__ __attribute__((aligned(16))) char lds[];
  const int b = blockIdx.x;
  STAMP(0);
  if (b < 32) role_help(x, w1, w2, cvec, wmod, mem0, ws, lds, b);
  else if (b < 64) role_score(Wq, Wk, ws, lds, b - 32);
  else if (b < 192) { const int q = b - 64; role_column(Wv, Wo, ws, lds, (q & 7) | ((q >> 4) << 3), (q >> 3) & 1); }
  else if (b < 224) role_vu(Wv, Wo, ws, lds, b - 192);
  else role_qk(Wq, Wk, ws, lds, b - 224);
}

__global__ void __launch_bounds__(512, 2) k3b_oproj(const u16* __restrict__ x, char* __restrict__ ws) {
  extern __shared__ __attribute__((aligned(16))) char lds[];
  const int b = blockIdx.x, tid = threadIdx.x, lane = tid & 63, w = tid >> 6;
  const int s = (b & 7) * 8 + (b >> 5), nq = (b >> 3) & 3;
  const int n = lane & 15, g = lane >> 4, cc = lane & 7;
  const u32x4* UF = (const u32x4*)(ws + OFF_UF) + (size_t)s * 64 * 1024;
  const char* Yg = ws + OFF_Y + (size_t)s * 64 * 4096;
  const u32x4* PXx = (const u32x4*)(ws + OFF_PX) + (size_t)s * NH * 128;
  const u32x4* PXm = (const u32x4*)(ws + OFF_PX + (4ull << 20)) + (size_t)s * 8 * 128;
  f32x4 acc[2][8];
#pragma unroll
  for (int i = 0; i < 2; i++)
#pragma unroll
    for (int j = 0; j < 8; j++) acc[i][j] = (f32x4){0.f, 0.f, 0.f, 0.f};
  u32x4 ub[4][8], pa[4][2], pm[2], yf[8];
#pragma unroll
  for (int hl = 0; hl < 4; hl++) {
    const int hd = 4 * w + hl;
#pragma unroll
    for (int nt = 0; nt < 8; nt++) ub[hl][nt] = __builtin_nontemporal_load(UF + (size_t)(16 * nq + 2 * nt + (n >> 3)) * 1024 + (hd * 4 + g) * 8 + cc);
    pa[hl][0] = PXx[(hd * 2) * 64 + lane];
    pa[hl][1] = PXx[(hd * 2 + 1) * 64 + lane];
  }
  pm[0] = PXm[(w * 2) * 64 + lane];
  pm[1] = PXm[(w * 2 + 1) * 64 + lane];
  {
    const int ha = 4 * w + 2 * (g >> 1);
#pragma unroll
    for (int nt = 0; nt < 8; nt++) {
      const int c = 16 * nq + 2 * nt + (n >> 3);
      u32x2 ya = __builtin_nontemporal_load((const u32x2*)(Yg + (size_t)c * 4096 + (ha * 8 + cc) * 16 + 8 * (g & 1)));
      u32x2 yb = __builtin_nontemporal_load((const u32x2*)(Yg + (size_t)c * 4096 + ((ha + 1) * 8 + cc) * 16 + 8 * (g & 1)));
      yf[nt] = (u32x4){ya.x, ya.y, yb.x, yb.y};
    }
  }
  __builtin_amdgcn_sched_barrier(0);
#pragma unroll
  for (int hl = 0; hl < 4; hl++)
#pragma unroll
    for (int nt = 0; nt < 8; nt++) {
      acc[0][nt] = MFMA16(as_bf8(pa[hl][0]), as_bf8(ub[hl][nt]), acc[0][nt]);
      acc[1][nt] = MFMA16(as_bf8(pa[hl][1]), as_bf8(ub[hl][nt]), acc[1][nt]);
    }
#pragma unroll
  for (int nt = 0; nt < 8; nt++) {
    acc[0][nt] = MFMA16(as_bf8(pm[0]), as_bf8(yf[nt]), acc[0][nt]);
    acc[1][nt] = MFMA16(as_bf8(pm[1]), as_bf8(yf[nt]), acc[1][nt]);
  }
  float* red = (float*)lds;
#pragma unroll
  for (int qt = 0; qt < 2; qt++)
#pragma unroll
    for (int nt = 0; nt < 8; nt++)
#pragma unroll
      for (int j = 0; j < 4; j++) red[(w * 32 + 16 * qt + 4 * g + j) * 132 + 16 * nt + n] = acc[qt][nt][j];
  __syncthreads();
  const float* mod = (const float*)(ws + OFF_MOD);
  u16* x1 = (u16*)(ws + OFF_X1);
  {
    const int r = tid >> 4, c8 = (tid & 15) * 8;
    float o[8];
#pragma unroll
    for (int j = 0; j < 8; j++) o[j] = 0.f;
#pragma unroll
    for (int ww = 0; ww < 8; ww++) {
      f32x4 a = *(const f32x4*)(red + (ww * 32 + r) * 132 + c8), bq = *(const f32x4*)(red + (ww * 32 + r) * 132 + c8 + 4);
#pragma unroll
      for (int j = 0; j < 4; j++) { o[j] += a[j]; o[4 + j] += bq[j]; }
    }
    const int col = 128 * nq + c8;
    const size_t go = (size_t)(32 * s + r) * DM + col;
    u32x4 xv = *(const u32x4*)(x + go);
    float xf[8] = {bf_lo(xv.x), bf_hi(xv.x), bf_lo(xv.y), bf_hi(xv.y), bf_lo(xv.z), bf_hi(xv.z), bf_lo(xv.w), bf_hi(xv.w)};
#pragma unroll
    for (int j = 0; j < 8; j++) o[j] = xf[j] + bfr(mod[1024 + col + j] * bfr(o[j]));
    u32x4 rv = {pk2(o[0], o[1]), pk2(o[2], o[3]), pk2(o[4], o[5]), pk2(o[6], o[7])};
    *(u32x4*)(x1 + go) = rv;
  }
}

__global__ void __launch_bounds__(512, 2) k4_mlp1(const u16* __restrict__ b1, char* __restrict__ ws) {
  extern __shared__ __attribute__((aligned(16))) char lds[];
  const int b = blockIdx.x, tid = threadIdx.x, lane = tid & 63, w = tid >> 6;
  const int jq = b >> 3, mt0 = (b & 7) * 4 + jq / 7, nb = jq % 7;
  const float* mod = (const float*)(ws + OFF_MOD);
  const u16* x1 = (const u16*)(ws + OFF_X1);
  const int n = lane & 15, g = lane >> 4;
  const f32x4 mv = *(const f32x4*)(mod + 1536 + (tid & 255) * 4);
  u32x4 xv[2][4];
#pragma unroll
  for (int it = 0; it < 2; it++)
#pragma unroll
    for (int j = 0; j < 4; j++) xv[it][j] = *(const u32x4*)(x1 + (size_t)(mt0 * 64 + it * 32 + w * 4 + g) * DM + (j * 16 + n) * 8);
  const u32x4* WF = (const u32x4*)(ws + OFF_W1F) + (size_t)(nb * 16 + w * 2) * 16 * 64 + lane;
  u32x4 bfm[2][16];
#pragma unroll
  for (int nt = 0; nt < 2; nt++)
#pragma unroll
    for (int ks = 0; ks < 8; ks++) bfm[nt][ks] = WF[(nt * 16 + ks) * 64];
  __builtin_amdgcn_sched_barrier(0);
  float* modl = (float*)(lds + 66560);
  if (tid < 256) *(f32x4*)(modl + tid * 4) = tid >= 128 ? mv + 1.f : mv;
  __syncthreads();
#pragma unroll
  for (int it = 0; it < 2; it++) {
    f32x2 f[16];
#pragma unroll
    for (int j = 0; j < 4; j++) {
      f[4 * j + 0] = (f32x2){bf_lo(xv[it][j].x), bf_hi(xv[it][j].x)}; f[4 * j + 1] = (f32x2){bf_lo(xv[it][j].y), bf_hi(xv[it][j].y)};
      f[4 * j + 2] = (f32x2){bf_lo(xv[it][j].z), bf_hi(xv[it][j].z)}; f[4 * j + 3] = (f32x2){bf_lo(xv[it][j].w), bf_hi(xv[it][j].w)};
    }
    f32x2 s2 = {0.f, 0.f};
#pragma unroll
    for (int i = 0; i < 16; i++) s2 += f[i];
    float m = row16_sum(s2.x + s2.y) * (1.f / DM);
    f32x2 q2 = {0.f, 0.f};
#pragma unroll
    for (int i = 0; i < 16; i++) { f[i] -= m; q2 += f[i] * f[i]; }
    float rs = rsqrtf(row16_sum(q2.x + q2.y) * (1.f / DM) + 1e-5f);
    s2 = (f32x2){0.f, 0.f};
#pragma unroll
    for (int j = 0; j < 4; j++) {
      const int c0 = (j * 16 + n) * 8;
      f32x4 c0v = *(const f32x4*)(modl + 512 + c0), c1v = *(const f32x4*)(modl + 512 + c0 + 4);
      f32x4 s0v = *(const f32x4*)(modl + c0), s1v = *(const f32x4*)(modl + c0 + 4);
      f[4 * j + 0] = f[4 * j + 0] * rs * (f32x2){c0v[0], c0v[1]} + (f32x2){s0v[0], s0v[1]};
      f[4 * j + 1] = f[4 * j + 1] * rs * (f32x2){c0v[2], c0v[3]} + (f32x2){s0v[2], s0v[3]};
      f[4 * j + 2] = f[4 * j + 2] * rs * (f32x2){c1v[0], c1v[1]} + (f32x2){s1v[0], s1v[1]};
      f[4 * j + 3] = f[4 * j + 3] * rs * (f32x2){c1v[2], c1v[3]} + (f32x2){s1v[2], s1v[3]};
      s2 += (f[4 * j + 0] + f[4 * j + 1]) + (f[4 * j + 2] + f[4 * j + 3]);
    }
    m = row16_sum(s2.x + s2.y) * (1.f / DM);
    q2 = (f32x2){0.f, 0.f};
#pragma unroll
    for (int i = 0; i < 16; i++) { f[i] -= m; q2 += f[i] * f[i]; }
    rs = rsqrtf(row16_sum(q2.x + q2.y) * (1.f / DM) + 1e-5f);
#pragma unroll
    for (int j = 0; j < 4; j++) {
      f32x2 o0 = f[4 * j] * rs, o1 = f[4 * j + 1] * rs, o2 = f[4 * j + 2] * rs, o3 = f[4 * j + 3] * rs;
      u32x4 o = {pk2(o0.x, o0.y), pk2(o1.x, o1.y), pk2(o2.x, o2.y), pk2(o3.x, o3.y)};
      *(u32x4*)(lds + (it * 32 + w * 4 + g) * 1040 + (j * 16 + n) * 16) = o;
    }
    __builtin_amdgcn_sched_barrier(0);
  }
#pragma unroll
  for (int nt = 0; nt < 2; nt++)
#pragma unroll
    for (int ks = 8; ks < 16; ks++) bfm[nt][ks] = WF[(nt * 16 + ks) * 64];
  __builtin_amdgcn_sched_barrier(0);
  __syncthreads();
  f32x4 acc[4][2];
#pragma unroll
  for (int i = 0; i < 4; i++)
#pragma unroll
    for (int j = 0; j < 2; j++) acc[i][j] = (f32x4){0.f, 0.f, 0.f, 0.f};
#pragma unroll
  for (int ks = 0; ks < 16; ks++) {
    bf16x8 af[4];
#pragma unroll
    for (int mt = 0; mt < 4; mt++) af[mt] = *(const bf16x8*)(lds + (16 * mt + n) * 1040 + (32 * ks + 8 * g) * 2);
#pragma unroll
    for (int mt = 0; mt < 4; mt++)
#pragma unroll
      for (int nt = 0; nt < 2; nt++) acc[mt][nt] = MFMA16(as_bf8(bfm[nt][ks]), af[mt], acc[mt][nt]);
    if (ks & 1) __builtin_amdgcn_sched_barrier(0);
  }
  u32x4* hidF = (u32x4*)(ws + OFF_HID);
  float bb[2][4];
#pragma unroll
  for (int nt = 0; nt < 2; nt++) {
    u32x2 bv = *(const u32x2*)(b1 + nb * 256 + w * 32 + 16 * nt + 4 * g);
    bb[nt][0] = bf_lo(bv.x); bb[nt][1] = bf_hi(bv.x); bb[nt][2] = bf_lo(bv.y); bb[nt][3] = bf_hi(bv.y);
  }
#pragma unroll
  for (int mt = 0; mt < 4; mt++) {
    float o[8];
#pragma unroll
    for (int nt = 0; nt < 2; nt++)
#pragma unroll
      for (int j = 0; j < 4; j++) {
        float u = bfr(bfr(acc[mt][nt][j]) + bb[nt][j]);
        float z = 0.7978845608028654f * (u + 0.044715f * u * u * u);
        float e = __builtin_amdgcn_exp2f(z * 2.8853900817779268f);
        float th = 1.f - 2.f * __builtin_amdgcn_rcpf(e + 1.f);
        o[4 * nt + j] = 0.5f * u * (1.f + th);
      }
    u32x4 ov = {pk2(o[0], o[1]), pk2(o[2], o[3]), pk2(o[4], o[5]), pk2(o[6], o[7])};
    hidF[((size_t)(mt0 * 4 + mt) * 56 + nb * 8 + w) * 64 + lane] = ov;
  }
}

__global__ void __launch_bounds__(512, 2) k5_mlp2(const u16* __restrict__ b2, char* __restrict__ ws, u16* __restrict__ out) {
  extern __shared__ __attribute__((aligned(16))) char lds[];
  const int b = blockIdx.x, tid = threadIdx.x, lane = tid & 63, w = tid >> 6;
  const int mt0 = (b & 7) * 4 + (b >> 6), nb = (b >> 3) & 7;
  const int n = lane & 15, g = lane >> 4;
  const u32x4* hidF = (const u32x4*)(ws + OFF_HID) + ((size_t)(mt0 * 4) * 56 + w * 7) * 64 + lane;
  const u32x4* WF = (const u32x4*)(ws + OFF_W2F) + ((size_t)(nb * 4) * 56 + w * 7) * 64 + lane;
  f32x4 acc[4][4];
#pragma unroll
  for (int i = 0; i < 4; i++)
#pragma unroll
    for (int j = 0; j < 4; j++) acc[i][j] = (f32x4){0.f, 0.f, 0.f, 0.f};
#pragma unroll
  for (int hb = 0; hb < 2; hb++) {
    const int nk = hb ? 3 : 4;
    u32x4 bfm[4][4]; bf16x8 af[4][4];
#pragma unroll
    for (int k = 0; k < 4; k++) if (k < nk) {
#pragma unroll
      for (int nt = 0; nt < 4; nt++) bfm[k][nt] = WF[(nt * 56 + hb * 4 + k) * 64];
#pragma unroll
      for (int mt = 0; mt < 4; mt++) af[k][mt] = as_bf8(hidF[(mt * 56 + hb * 4 + k) * 64]);
    }
    __builtin_amdgcn_sched_barrier(0);
#pragma unroll
    for (int k = 0; k < 4; k++) if (k < nk) {
#pragma unroll
      for (int mt = 0; mt < 4; mt++)
#pragma unroll
        for (int nt = 0; nt < 4; nt++) acc[mt][nt] = MFMA16(as_bf8(bfm[k][nt]), af[k][mt], acc[mt][nt]);
    }
  }
  float* red = (float*)lds;
#pragma unroll
  for (int mt = 0; mt < 4; mt++)
#pragma unroll
    for (int nt = 0; nt < 4; nt++) *(f32x4*)(red + (w * 64 + 16 * mt + n) * 68 + 16 * nt + 4 * g) = acc[mt][nt];
  __syncthreads();
  const float* mod = (const float*)(ws + OFF_MOD);
  const u16* x1 = (const u16*)(ws + OFF_X1);
  {
    const int r = tid >> 3, c8 = (tid & 7) * 8;
    float o[8];
#pragma unroll
    for (int j = 0; j < 8; j++) o[j] = 0.f;
#pragma unroll
    for (int ww = 0; ww < 8; ww++) {
      f32x4 a = *(const f32x4*)(red + (ww * 64 + r) * 68 + c8), bq = *(const f32x4*)(red + (ww * 64 + r) * 68 + c8 + 4);
#pragma unroll
      for (int j = 0; j < 4; j++) { o[j] += a[j]; o[4 + j] += bq[j]; }
    }
    const int col = 64 * nb + c8;
    const size_t go = (size_t)(mt0 * 64 + r) * DM + col;
    u32x4 xv = *(const u32x4*)(x1 + go), bv = *(const u32x4*)(b2 + col);
    float xf[8] = {bf_lo(xv.x), bf_hi(xv.x), bf_lo(xv.y), bf_hi(xv.y), bf_lo(xv.z), bf_hi(xv.z), bf_lo(xv.w), bf_hi(xv.w)};
    float bb[8] = {bf_lo(bv.x), bf_hi(bv.x), bf_lo(bv.y), bf_hi(bv.y), bf_lo(bv.z), bf_hi(bv.z), bf_lo(bv.w), bf_hi(bv.w)};
#pragma unroll
    for (int j = 0; j < 8; j++) o[j] = xf[j] + bfr(mod[2560 + col + j] * bfr(bfr(o[j]) + bb[j]));
    u32x4 rv = {pk2(o[0], o[1]), pk2(o[2], o[3]), pk2(o[4], o[5]), pk2(o[6], o[7])};
    *(u32x4*)(out + go) = rv;
  }
}

extern "C" void kernel_launch(void* const* d_in, const int* in_sizes, int n_in, void* d_out, int out_size, void* d_ws,
                              size_t ws_size, hipStream_t stream) {
  const u16* x = (const u16*)d_in[0];
  const u16* mem0 = (const u16*)d_in[1];
  const u16* Wq = (const u16*)d_in[2];
  const u16* Wk = (const u16*)d_in[3];
  const u16* Wv = (const u16*)d_in[4];
  const u16* Wo = (const u16*)d_in[5];
  const u16* w1 = (const u16*)d_in[8];
  const u16* b1 = (const u16*)d_in[9];
  const u16* w2 = (const u16*)d_in[10];
  const u16* b2 = (const u16*)d_in[11];
  const u16* cvec = (const u16*)d_in[12];
  const u16* wmod = (const u16*)d_in[13];
  char* ws = (char*)d_ws;
  hipFuncSetAttribute((const void*)k2_rec, hipFuncAttributeMaxDynamicSharedMemorySize, 135168);
  hipMemsetAsync(ws, 0xFF, OFF_H, stream);
  k2_rec<<<256, 256, 135168, stream>>>(Wq, Wk, Wv, Wo, x, w1, w2, cvec, wmod, mem0, ws);
  hipFuncSetAttribute((const void*)k3b_oproj, hipFuncAttributeMaxDynamicSharedMemorySize, 135168);
  k3b_oproj<<<256, 512, 135168, stream>>>(x, ws);
  hipFuncSetAttribute((const void*)k4_mlp1, hipFuncAttributeMaxDynamicSharedMemorySize, 70656);
  hipFuncSetAttribute((const void*)k5_mlp2, hipFuncAttributeMaxDynamicSharedMemorySize, 139264);
  k4_mlp1<<<224, 512, 70656, stream>>>(b1, ws);
  k5_mlp2<<<256, 512, 139264, stream>>>(b2, ws, (u16*)d_out);
}
```

```cpp
#include <hip/hip_runtime.h>
#include <hip/hip_bf16.h>
#include <stdint.h>

typedef unsigned int u32;
typedef unsigned short u16;
typedef unsigned long long u64;
typedef __attribute__((ext_vector_type(2))) __bf16 bf16x2;
typedef __attribute__((ext_vector_type(8))) __bf16 bf16x8;
typedef __attribute__((ext_vector_type(16))) __bf16 bf16x16;
typedef __attribute__((ext_vector_type(2))) float f32x2;
typedef __attribute__((ext_vector_type(4))) float f32x4;
typedef __attribute__((ext_vector_type(16))) float f32x16;
typedef __attribute__((ext_vector_type(2))) u32 u32x2;
typedef __attribute__((ext_vector_type(4))) u32 u32x4;
typedef __attribute__((ext_vector_type(8))) u32 u32x8;

#define NTOK 2048
#define DM 512
#define NH 32
#define DH 128
#define NSEG 64
#define FF 1792
#define SENT 0xFFFFFFFFu

#define OFF_FLAG 0ull
#define OFF_DONE 8192u
#define OFF_KMF 12288u
#define OFF_WVF 14336u
#define OFF_MDF 28672u
#define OFF_MOD 16384ull
#define OFF_MEMX 32768ull
#define OFF_PEX 0x90000ull
#define OFF_H (2ull << 20)
#define OFF_KMEM (4ull << 20)
#define OFF_WVO (8ull << 20)
#define OFF_KX (24ull << 20)
#define OFF_QX (40ull << 20)
#define OFF_Y (56ull << 20)
#define OFF_UF (72ull << 20)
#define OFF_PX (136ull << 20)
#define OFF_X1 (142ull << 20)
#define OFF_Y2 (146ull << 20)
#define OFF_HID (148ull << 20)
#define OFF_W1F (156ull << 20)
#define OFF_W2F (158ull << 20)
#define OFF_DBG (200ull << 20)
#define STAMP(slot) do { } while (0)

__device__ __forceinline__ u32 pk2(float a, float b) {
  bf16x2 v; v[0] = (__bf16)a; v[1] = (__bf16)b; return __builtin_bit_cast(u32, v);
}
__device__ __forceinline__ u32 f2u(float x) { return __builtin_bit_cast(u32, x); }
__device__ __forceinline__ float u2f(u32 x) { return __builtin_bit_cast(float, x); }
__device__ __forceinline__ float bf_lo(u32 x) { return __builtin_bit_cast(float, x << 16); }
__device__ __forceinline__ float bf_hi(u32 x) { return __builtin_bit_cast(float, x & 0xffff0000u); }
__device__ __forceinline__ float bfr(float a) { return (float)(__bf16)a; }
__device__ __forceinline__ __amdgpu_buffer_rsrc_t mkrsrc(const void* p) {
  return __builtin_amdgcn_make_buffer_rsrc((void*)p, 0, 0x7fffffff, 0x00020000);
}
__device__ __forceinline__ u32x4 ld16_sc1(__amdgpu_buffer_rsrc_t r, u32 off) { return __builtin_amdgcn_raw_buffer_load_b128(r, off, 0, 16); }
__device__ __forceinline__ u32 ld4_sc1(__amdgpu_buffer_rsrc_t r, u32 off) { return __builtin_amdgcn_raw_buffer_load_b32(r, off, 0, 16); }
__device__ __forceinline__ void st16_sc1(u32x4 v, __amdgpu_buffer_rsrc_t r, u32 off) { __builtin_amdgcn_raw_buffer_store_b128(v, r, off, 0, 16); }
__device__ __forceinline__ void st8_sc1(u32x2 v, __amdgpu_buffer_rsrc_t r, u32 off) { __builtin_amdgcn_raw_buffer_store_b64(v, r, off, 0, 16); }
__device__ __forceinline__ void st4_sc1(u32 v, __amdgpu_buffer_rsrc_t r, u32 off) { __builtin_amdgcn_raw_buffer_store_b32(v, r, off, 0, 16); }
__device__ __forceinline__ u32 badm(u32x4 a) { return (u32)(a.x == SENT) | (u32)(a.y == SENT) | (u32)(a.z == SENT) | (u32)(a.w == SENT); }
__device__ __forceinline__ u32 sane(u32 x) { return x < 0xFFFFFFFEu ? x : 0xFFFFFFFEu; }
__device__ __forceinline__ u32 now32() { return (u32)__builtin_amdgcn_s_memtime(); }
__device__ __forceinline__ void wait_until(u32 t) { while ((int)(t - now32()) > 0) { } }
__device__ __forceinline__ bf16x8 as_bf8(u32x4 v) { return __builtin_bit_cast(bf16x8, v); }
__device__ __forceinline__ bf16x16 as_bf16(u32x4 a, u32x4 b) {
  u32x8 v = {a.x, a.y, a.z, a.w, b.x, b.y, b.z, b.w}; return __builtin_bit_cast(bf16x16, v);
}
__device__ __forceinline__ float max3r(float a, float b, float c) { float r; asm("v_max3_f32 %0, %1, %2, %3" : "=v"(r) : "v"(a), "v"(b), "v"(c)); return r; }
__device__ __forceinline__ float max2r(float a, float b) { float r; asm("v_max_f32 %0, %1, %2" : "=v"(r) : "v"(a), "v"(b)); return r; }
#define MFMA16(a, b, c) __builtin_amdgcn_mfma_f32_16x16x32_bf16((a), (b), (c), 0, 0, 0)
#define MFMA32(a, b, c) __builtin_amdgcn_mfma_f32_32x32x16_bf16((a), (b), (c), 0, 0, 0)
#define SMF(a, b, c, idx) __builtin_amdgcn_smfmac_f32_16x16x64_bf16((a), (b), (c), (idx), 0, 0)

template <int N>
__device__ __forceinline__ u32 poll_chunks(__amdgpu_buffer_rsrc_t r, const u32 (&off)[N], u32x4 (&a)[N], u32 lane16, u32& tiss) {
  u32 spins = 0;
  while (true) {
    asm volatile("" : "+v"(lane16) :: "memory");
    if (spins) tiss = now32();
    u32 bad = 0;
#pragma unroll
    for (int j = 0; j < N; j++) a[j] = ld16_sc1(r, off[j] + lane16);
#pragma unroll
    for (int j = 0; j < N; j++) bad |= badm(a[j]);
    ++spins;
    if (!__any(bad)) return spins;
    if (spins > 400000u) return 0;
  }
}

typedef __attribute__((ext_vector_type(4))) short s16x4;
typedef __attribute__((address_space(3))) s16x4 lds_s16x4;
template <int ROWB>
__device__ __forceinline__ void dma_tile(char* lds, const u16* src, int rows, int gstride) {
  constexpr int LPR = ROWB / 16;
  const int total = rows * LPR;
  for (int u = threadIdx.x; u < total; u += 256) {
    const int r = u / LPR, c = u % LPR;
    __builtin_amdgcn_global_load_lds((const u32*)(src + (size_t)r * gstride + c * 8), (u32*)(lds + u * 16), 16, 0, 0);
  }
}
__device__ __forceinline__ void dma_wait() { asm volatile("s_waitcnt vmcnt(0)" ::: "memory"); __syncthreads(); }
__device__ __forceinline__ u32x2 tr4(const char* lds, int row0, int col0, int pitch, int lane) {
  const char* p = lds + (row0 + ((lane & 15) >> 2)) * pitch + (col0 + 4 * (lane & 3)) * 2;
  s16x4 v = __builtin_amdgcn_ds_read_tr16_b64_v4i16((lds_s16x4*)p);
  return __builtin_bit_cast(u32x2, v);
}
__device__ __forceinline__ bf16x8 frag8(const char* lds, int k0, int col0, int pitch, int lane) {
  u32x2 a = tr4(lds, k0, col0, pitch, lane), b = tr4(lds, k0 + 4, col0, pitch, lane);
  return as_bf8((u32x4){a.x, a.y, b.x, b.y});
}
__device__ __forceinline__ u32x4 frag8s(const char* lds, int k0, int col0, int pitch, int lane) {
  u32x2 a = tr4(lds, k0, col0, pitch, lane), b = tr4(lds, k0 + 8, col0, pitch, lane);
  return (u32x4){a.x, b.x, a.y, b.y};
}
#define PACK_S(acc, b) (u32x4){pk2((acc)[(b)], (acc)[(b) + 1]), pk2((acc)[(b) + 4], (acc)[(b) + 5]), pk2((acc)[(b) + 2], (acc)[(b) + 3]), pk2((acc)[(b) + 6], (acc)[(b) + 7])}

__device__ __forceinline__ void wvo_task(const u16* __restrict__ Wv, const u16* __restrict__ Wo, char* ws, char* lds, int task) {
  const int tid = threadIdx.x, lane = tid & 63, w = tid >> 6;
  const int h = task >> 2, nq = task & 3, x = lane & 31, hh = lane >> 5;
  dma_tile<256>(lds, Wo + (size_t)h * DH * DM + nq * 128, DH, DM);
  bf16x8 a[4][8];
#pragma unroll
  for (int mt = 0; mt < 4; mt++)
#pragma unroll
    for (int ks = 0; ks < 8; ks++)
      a[mt][ks] = *(const bf16x8*)(Wv + ((size_t)(128 * w + 32 * mt + x) * NH + h) * DH + 16 * ks + 8 * hh);
  dma_wait();
  const __amdgpu_buffer_rsrc_t rd = mkrsrc(ws + OFF_WVO);
#pragma unroll
  for (int nt = 0; nt < 4; nt++) {
    bf16x8 bw[8];
#pragma unroll
    for (int ks = 0; ks < 8; ks++) bw[ks] = frag8(lds, 16 * ks + 8 * hh, 32 * nt + (x & 16), 256, lane);
#pragma unroll
    for (int mt = 0; mt < 4; mt++) {
      f32x16 acc;
#pragma unroll
      for (int i = 0; i < 16; i++) acc[i] = 0.f;
#pragma unroll
      for (int ks = 0; ks < 8; ks++) acc = MFMA32(a[mt][ks], bw[ks], acc);
      const int d = 128 * nq + 32 * nt + x, c = d >> 3, cc = d & 7;
#pragma unroll
      for (int xb = 0; xb < 2; xb++) {
        const int dp = 128 * w + 32 * mt + 16 * xb, t = dp >> 6, ga = (dp >> 4) & 3, z = ga >> 1, gb = 2 * (ga & 1) + hh;
        const int lp = (h & 1) * 8 + cc + 16 * gb;
        u32 o = (u32)(((((c * 4 + (h >> 3)) * 4 + ((h >> 1) & 3)) * 8 + t) * 2 + z) * 64 + lp);
        st16_sc1(PACK_S(acc, 8 * xb), rd, o * 16u);
      }
    }
  }
  asm volatile("s_waitcnt vmcnt(0)" ::: "memory");
  __syncthreads();
  if (tid == 0) st4_sc1(1u, mkrsrc(ws + OFF_FLAG), OFF_WVF + task * 16u);
}

__device__ __forceinline__ void mod_cols(const u16* __restrict__ cvec, const u16* __restrict__ wmod, char* ws, char* lds, int col0) {
  const int tid = threadIdx.x, cp = tid & 15, ksl = tid >> 4;
  float* red = (float*)lds;
  u32 wv[32];
#pragma unroll
  for (int k = 0; k < 32; k++) wv[k] = *(const u32*)(wmod + (size_t)(ksl * 32 + k) * 3072 + col0 + cp * 2);
  float* sl_ = (float*)(lds + 2048);
  __syncthreads();
  {
    u32 cw = *(const u32*)(cvec + tid * 2);
    float c0 = bf_lo(cw), c1 = bf_hi(cw);
    sl_[tid * 2] = bfr(c0 / (1.f + __expf(-c0)));
    sl_[tid * 2 + 1] = bfr(c1 / (1.f + __expf(-c1)));
  }
  __syncthreads();
  float a0 = 0.f, a1 = 0.f;
#pragma unroll
  for (int k4 = 0; k4 < 8; k4++) {
    f32x4 sv = *(const f32x4*)(sl_ + ksl * 32 + k4 * 4);
#pragma unroll
    for (int j = 0; j < 4; j++) { a0 += sv[j] * bf_lo(wv[k4 * 4 + j]); a1 += sv[j] * bf_hi(wv[k4 * 4 + j]); }
  }
  red[ksl * 32 + cp * 2] = a0; red[ksl * 32 + cp * 2 + 1] = a1;
  __syncthreads();
  if (tid < 32) {
    float sacc = 0.f;
#pragma unroll
    for (int k = 0; k < 16; k++) sacc += red[k * 32 + tid];
    st4_sc1(f2u(bfr(sacc)), mkrsrc(ws + OFF_MOD), (u32)(col0 + tid) * 4u);
  }
}

__device__ __forceinline__ float row16_sum(float v) {
  v += u2f((u32)__builtin_amdgcn_mov_dpp((int)f2u(v), 0x128, 0xF, 0xF, true));
  v += u2f((u32)__builtin_amdgcn_mov_dpp((int)f2u(v), 0x124, 0xF, 0xF, true));
  v += u2f((u32)__builtin_amdgcn_mov_dpp((int)f2u(v), 0x122, 0xF, 0xF, true));
  v += u2f((u32)__builtin_amdgcn_mov_dpp((int)f2u(v), 0x121, 0xF, 0xF, true));
  return v;
}

__device__ __forceinline__ bool wait_progress(__amdgpu_buffer_rsrc_t rf, u32 off, u32 need, u32& seen) {
  u32 spins = 0;
  if (__all((int)seen >= (int)need)) return true;
  while (true) {
    asm volatile("" : "+v"(off) :: "memory");
    seen = ld4_sc1(rf, off);
    if (__all((int)seen >= (int)need)) return true;
    __builtin_amdgcn_s_sleep(2);
    if (++spins > 400000u) return false;
  }
}

__device__ __forceinline__ void px_task(char* ws, int s, int hg);
__device__ __forceinline__ void role_help(const u16* __restrict__ x, const u16* __restrict__ w1, const u16* __restrict__ w2,
                                          const u16* __restrict__ cvec, const u16* __restrict__ wmod, const u16* __restrict__ mem0,
                                          char* ws, char* lds, int a0) {
  const int tid = threadIdx.x, lane = tid & 63, w = tid >> 6, n = lane & 15, g = lane >> 4;
  u32x4 xpre[2][2][4];
#pragma unroll
  for (int sg = 0; sg < 2; sg++)
#pragma unroll
    for (int it = 0; it < 2; it++)
#pragma unroll
      for (int j = 0; j < 4; j++) xpre[sg][it][j] = *(const u32x4*)(x + (size_t)((a0 + 32 * sg) * 32 + it * 16 + w * 4 + g) * DM + (j * 16 + n) * 8);
  if (a0 == 31) {
    for (int i = tid; i < 512; i += 256) {
      u32x4 v = *(const u32x4*)(mem0 + (i & 7) * DM + (i >> 3) * 8);
      v.x = sane(v.x); v.y = sane(v.y); v.z = sane(v.z); v.w = sane(v.w);
      st16_sc1(v, mkrsrc(ws + OFF_MEMX), (u32)i * 16u);
    }
  }
  mod_cols(cvec, wmod, ws, lds, a0 * 32);
  asm volatile("s_waitcnt vmcnt(0)" ::: "memory");
  __syncthreads();
  if (tid == 0) st4_sc1(1u, mkrsrc(ws + OFF_FLAG), OFF_MDF + a0 * 16u);
  f32x2 fz[2][2][16];
#pragma unroll
  for (int sg = 0; sg < 2; sg++)
#pragma unroll
    for (int it = 0; it < 2; it++) {
      f32x2* f = fz[sg][it];
#pragma unroll
      for (int j = 0; j < 4; j++) {
        const u32x4 v = xpre[sg][it][j];
        f[4 * j + 0] = (f32x2){bf_lo(v.x), bf_hi(v.x)}; f[4 * j + 1] = (f32x2){bf_lo(v.y), bf_hi(v.y)};
        f[4 * j + 2] = (f32x2){bf_lo(v.z), bf_hi(v.z)}; f[4 * j + 3] = (f32x2){bf_lo(v.w), bf_hi(v.w)};
      }
      f32x2 s2 = {0.f, 0.f};
#pragma unroll
      for (int i = 0; i < 16; i++) s2 += f[i];
      const float m = row16_sum(s2.x + s2.y) * (1.f / DM);
      f32x2 q2 = {0.f, 0.f};
#pragma unroll
      for (int i = 0; i < 16; i++) { f[i] -= m; q2 += f[i] * f[i]; }
      const float rs = rsqrtf(row16_sum(q2.x + q2.y) * (1.f / DM) + 1e-5f);
#pragma unroll
      for (int i = 0; i < 16; i++) f[i] *= rs;
    }
  { u32 seenm = 0xFFFFFFFFu; if (!wait_progress(mkrsrc(ws + OFF_FLAG), OFF_MDF + (lane & 31) * 16u, 1u, seenm)) return; }
  float* modl = (float*)(lds + 8192);
  {
    u32x4 mv = ld16_sc1(mkrsrc(ws + OFF_MOD), (u32)tid * 16u);
    f32x4 mf = {u2f(mv.x), u2f(mv.y), u2f(mv.z), u2f(mv.w)};
    *(f32x4*)(modl + tid * 4) = tid >= 128 ? mf + 1.f : mf;
  }
  __syncthreads();
  const __amdgpu_buffer_rsrc_t rh = mkrsrc(ws + OFF_H);
#pragma unroll
  for (int sg = 0; sg < 2; sg++) {
    const int a = a0 + 32 * sg;
#pragma unroll
    for (int it = 0; it < 2; it++) {
      const f32x2* f = fz[sg][it];
#pragma unroll
      for (int j = 0; j < 4; j++) {
        const int c0 = (j * 16 + n) * 8;
        f32x4 c0v = *(const f32x4*)(modl + 512 + c0), c1v = *(const f32x4*)(modl + 512 + c0 + 4);
        f32x4 s0v = *(const f32x4*)(modl + c0), s1v = *(const f32x4*)(modl + c0 + 4);
        f32x2 o0 = f[4 * j + 0] * (f32x2){c0v[0], c0v[1]} + (f32x2){s0v[0], s0v[1]};
        f32x2 o1 = f[4 * j + 1] * (f32x2){c0v[2], c0v[3]} + (f32x2){s0v[2], s0v[3]};
        f32x2 o2 = f[4 * j + 2] * (f32x2){c1v[0], c1v[1]} + (f32x2){s1v[0], s1v[1]};
        f32x2 o3 = f[4 * j + 3] * (f32x2){c1v[2], c1v[3]} + (f32x2){s1v[2], s1v[3]};
        u32x4 r = {pk2(o0.x, o0.y), pk2(o1.x, o1.y), pk2(o2.x, o2.y), pk2(o3.x, o3.y)};
        st16_sc1(r, rh, (u32)((a * 32 + it * 16 + w * 4 + g) * DM + c0) * 2u);
      }
    }
    asm volatile("s_waitcnt vmcnt(0)" ::: "memory");
    __syncthreads();
    if (tid == 0) st4_sc1(1u, mkrsrc(ws + OFF_FLAG), OFF_DONE + a * 64u);
  }
  const int a = a0;
  mod_cols(cvec, wmod, ws, lds, 1024 + a * 64);
  mod_cols(cvec, wmod, ws, lds, 1024 + a * 64 + 32);
  __syncthreads();
  if (a < 28) {
    dma_tile<128>(lds, w1 + a * 64, DM, FF);
    dma_wait();
    u32x4* dst = (u32x4*)(ws + OFF_W1F);
    for (int ks = 0; ks < 16; ks++) {
      bf16x8 v = frag8(lds, 32 * ks + 8 * (lane >> 4), 16 * w, 128, lane);
      dst[((size_t)(a * 4 + w) * 16 + ks) * 64 + lane] = __builtin_bit_cast(u32x4, v);
    }
  }
  __syncthreads();
  {
    const int cb = a >> 2, kq = a & 3;
    dma_tile<128>(lds, w2 + (size_t)kq * 448 * DM + cb * 64, 448, DM);
    dma_wait();
    u32x4* dst = (u32x4*)(ws + OFF_W2F);
    for (int ks = 0; ks < 14; ks++) {
      u32x2 va = tr4(lds, 32 * ks + 4 * (lane >> 4), 16 * w, 128, lane), vb = tr4(lds, 32 * ks + 16 + 4 * (lane >> 4), 16 * w, 128, lane);
      dst[((size_t)(cb * 4 + w) * 56 + kq * 14 + ks) * 64 + lane] = (u32x4){va.x, va.y, vb.x, vb.y};
    }
  }
  {
    const __amdgpu_buffer_rsrc_t rflag = mkrsrc(ws + OFF_FLAG);
    const int hg = a & 7;
    for (int s = a >> 3; s < NSEG; s += 4) {
      px_task(ws, s, hg);
    }
  }
}

#define AD_DEC 3
#define AD_INC 60
#define ADAPT(D, n) do { if ((n) == 1) D -= AD_DEC; else D += AD_INC; if (D < 0) D = 0; } while (0)

__device__ __forceinline__ void role_score(const u16* __restrict__ Wq, const u16* __restrict__ Wk, char* ws,
                                           char* lds, int h) {
  const int tid = threadIdx.x, lane = tid & 63, w = tid >> 6;
  const int n = lane & 15, gb = lane >> 4;
  bf16x16 wf[4][8];
#pragma unroll
  for (int mat = 0; mat < 2; mat++) {
    __syncthreads();
    dma_tile<256>(lds, (mat ? Wk : Wq) + h * DH, DM, NH * DH);
    dma_wait();
#pragma unroll
    for (int tt = 0; tt < 2; tt++)
#pragma unroll
      for (int t = 0; t < 8; t++) {
        const int col = 32 * w + 16 * tt;
        u32x4 lo = frag8s(lds, 64 * t + 16 * (gb >> 1) + 4 * (gb & 1), col, 256, lane);
        u32x4 hi = frag8s(lds, 64 * t + 16 * (2 + (gb >> 1)) + 4 * (gb & 1), col, 256, lane);
        wf[mat * 2 + tt][t] = as_bf16(lo, hi);
      }
  }
  __syncthreads();
  char* memL = lds;
  char* qs = lds + 8192;
  char* ks_ = lds + 8192 + 2304;
  const int idx = (lane & 8) ? (int)0xEEEEEEEE : 0x44444444;
  const __amdgpu_buffer_rsrc_t rflag = mkrsrc(ws + OFF_FLAG);
  const __amdgpu_buffer_rsrc_t rkx = mkrsrc(ws + OFF_KX);
  const __amdgpu_buffer_rsrc_t rmem = mkrsrc(ws + OFF_MEMX);
  const __amdgpu_buffer_rsrc_t rpex = mkrsrc(ws + OFF_PEX);
  const int rho = lane & 15;
  const int keyp = 8 * ((rho >> 2) & 1) + 4 * (rho >> 3) + (rho & 3);
  const u32 prho = (u32)((n & 7) >> 2) * 10240u, pi = n & 3;
  const u32 pxoff = prho + (u32)(((h >> 2) * 64) + 16 * (2 * (h & 1) + (gb >> 1)) + 8 * (gb & 1) + 4 * ((h >> 1) & 1) + pi) * 16u + (n >> 3) * 8u;
  const u32 pmoff = prho + 8192u + (u32)(h * 4 + pi) * 16u + (gb & 1) * 8u + (gb >> 1) * 4u;
#define DS_OFF 0
#define DC_OFF 0
  int D1 = (w == 0 ? 3400 : (w == 1 ? 3300 : 4050)) - DS_OFF; u32 tref = now32() - 8000u, seen = 0xFFFFFFFFu;
  STAMP(1);
  for (int s = 0; s < NSEG; s++) {
    u32x4 kx[2][4];
    if (w == 0) {
      if (!wait_progress(rflag, 4096u + h * 128u, s + 1, seen)) return;
#pragma unroll
      for (int tt = 0; tt < 2; tt++)
#pragma unroll
        for (int k4 = 0; k4 < 4; k4++)
          kx[tt][k4] = ld16_sc1(rkx, (u32)(((32 * s + 16 * tt + keyp) * NH + h) * DH + 32 * k4 + 8 * gb) * 2u);
    }
    f32x4 acc[4];
#pragma unroll
    for (int i = 0; i < 4; i++) { acc[i] = (f32x4){0.f, 0.f, 0.f, 0.f}; asm volatile("" : "+v"(acc[i])); }
    wait_until(tref + D1);
    {
      u32 off[2] = {(u32)s * 8192u + w * 2048u, (u32)s * 8192u + w * 2048u + 1024u};
      u32x4 a[2];
      u32 tiss = 0;
      u32 nsw = poll_chunks<2>(rmem, off, a, lane * 16u, tiss);
      if (!nsw) return;
      if (s > 2) ADAPT(D1, nsw);
      *(u32x4*)(memL + w * 2048 + lane * 16) = a[0];
      *(u32x4*)(memL + w * 2048 + 1024 + lane * 16) = a[1];
    }
    __syncthreads();
    STAMP(16 + 4 * s);
    bf16x8 af[8];
#pragma unroll
    for (int t = 0; t < 8; t++) af[t] = *(const bf16x8*)(memL + t * 1024 + lane * 16);
    __builtin_amdgcn_sched_barrier(0);
    const int ecol = 32 * w + 16 * (lane >> 5) + n, er0 = 4 * ((lane >> 4) & 1);
#pragma unroll
    for (int t = 0; t < 8; t++) { acc[0] = SMF(af[t], wf[0][t], acc[0], idx); acc[1] = SMF(af[t], wf[1][t], acc[1], idx); }
    __builtin_amdgcn_sched_barrier(0);
#pragma unroll
    for (int t = 0; t < 8; t++) {
      acc[2] = SMF(af[t], wf[2][t], acc[2], idx); acc[3] = SMF(af[t], wf[3][t], acc[3], idx);
      if (t >= 2 && t < 6) {
        const int j = t - 2;
        auto r = __builtin_amdgcn_permlane32_swap(f2u(acc[0][j]), f2u(acc[1][j]), false, false);
        *(__bf16*)(qs + (er0 + j) * 272 + ecol * 2) = (__bf16)(u2f(r[0]) + u2f(r[1]));
      }
      __builtin_amdgcn_sched_barrier(0);
    }
#pragma unroll
    for (int j = 0; j < 4; j++) {
      auto r = __builtin_amdgcn_permlane32_swap(f2u(acc[2][j]), f2u(acc[3][j]), false, false);
      *(__bf16*)(ks_ + (er0 + j) * 272 + ecol * 2) = (__bf16)(u2f(r[0]) + u2f(r[1]));
    }
    __syncthreads();
    STAMP(17 + 4 * s);
    if (w == 0 && s < NSEG - 1) {
      f32x4 sc[3];
#pragma unroll
      for (int i = 0; i < 3; i++) { sc[i] = (f32x4){0.f, 0.f, 0.f, 0.f}; asm volatile("" : "+v"(sc[i])); }
      bf16x8 qf[4], kf[4];
#pragma unroll
      for (int k4 = 0; k4 < 4; k4++) {
        qf[k4] = *(const bf16x8*)(qs + (lane & 7) * 272 + (32 * k4 + 8 * gb) * 2);
        kf[k4] = *(const bf16x8*)(ks_ + (lane & 7) * 272 + (32 * k4 + 8 * gb) * 2);
      }
      __builtin_amdgcn_sched_barrier(0);
#pragma unroll
      for (int k4 = 0; k4 < 4; k4++) {
        sc[0] = MFMA16(as_bf8(kx[0][k4]), qf[k4], sc[0]);
        sc[1] = MFMA16(as_bf8(kx[1][k4]), qf[k4], sc[1]);
        sc[2] = MFMA16(kf[k4], qf[k4], sc[2]);
      }
      const float sl = 0.08838834764831845f * 1.4426950408889634f;
      const bool hiq = n >= 8, hig = gb >= 2;
      float p[6];
#pragma unroll
      for (int j = 0; j < 4; j++) p[j] = hiq ? sc[1][j] : sc[0][j];
      p[4] = hig ? sc[2][2] : sc[2][0];
      p[5] = hig ? sc[2][3] : sc[2][1];
      float mx = max2r(max3r(p[0], p[1], p[2]), max3r(p[3], p[4], p[5]));
      mx = max2r(mx, u2f((u32)__builtin_amdgcn_mov_dpp((int)f2u(mx), 0x128, 0xF, 0xF, true)));
      { auto r = __builtin_amdgcn_permlane16_swap(f2u(mx), f2u(mx), false, false);
        mx = max2r(u2f(r[0]), u2f(r[1])); }
      { auto r = __builtin_amdgcn_permlane32_swap(f2u(mx), f2u(mx), false, false);
        mx = max2r(u2f(r[0]), u2f(r[1])); }
      const float nb = -mx * sl;
#pragma unroll
      for (int j = 0; j < 6; j++) p[j] = __builtin_amdgcn_exp2f(__builtin_fmaf(p[j], sl, nb));
      float sum = (p[0] + p[1]) + (p[2] + p[3]) + (hiq ? 0.f : (p[4] + p[5]));
      sum += u2f((u32)__builtin_amdgcn_mov_dpp((int)f2u(sum), 0x128, 0xF, 0xF, true));
      { auto r = __builtin_amdgcn_permlane16_swap(f2u(sum), f2u(sum), false, false);
        sum = u2f(r[0]) + u2f(r[1]); }
      { auto r = __builtin_amdgcn_permlane32_swap(f2u(sum), f2u(sum), false, false);
        sum = u2f(r[0]) + u2f(r[1]); }
      const float inv = __builtin_amdgcn_rcpf(sum);
      u32x2 px = {sane(pk2(p[0] * inv, p[1] * inv)), sane(pk2(p[2] * inv, p[3] * inv))};
      st8_sc1(px, rpex, (u32)s * 20480u + pxoff);
      if (!hiq) st4_sc1(sane(pk2(p[4] * inv, p[5] * inv)), rpex, (u32)s * 20480u + pmoff);
      STAMP(18 + 4 * s);
    } else if (w == 1) {
      const __amdgpu_buffer_rsrc_t rkm = mkrsrc(ws + OFF_KMEM);
#pragma unroll
      for (int j = 0; j < 2; j++) {
        int u = j * 64 + lane, r = u >> 4, c = u & 15;
        st16_sc1(*(const u32x4*)(ks_ + r * 272 + c * 16), rkm, (u32)((s * NH + h) * 1024 + r * 128 + c * 8) * 2u);
      }
      asm volatile("s_waitcnt vmcnt(0)" ::: "memory");
      if (lane == 0) st4_sc1((u32)(s + 1), rflag, OFF_KMF + h * 64u);
    }
    tref = now32();
  }
}

__device__ __forceinline__ void role_column(const u16* __restrict__ Wv, const u16* __restrict__ Wo, char* ws, char* lds, int c, int rho) {
  const int tid = threadIdx.x, lane = tid & 63, w = tid >> 6;
  const int n = lane & 15, gb = lane >> 4, cc = lane & 7;
  bf16x16 wf[4][8];
  wvo_task(Wv, Wo, ws, lds, c * 2 + rho);
  {
    u32 seenw = 0xFFFFFFFFu;
    if (!wait_progress(mkrsrc(ws + OFF_FLAG), OFF_WVF + (u32)((lane & 31) * 4 + (c >> 4)) * 16u, 1u, seenw)) return;
    const __amdgpu_buffer_rsrc_t rsrc = mkrsrc(ws + OFF_WVO + (size_t)(c * 4 + w) * 65536);
#pragma unroll
    for (int tl = 0; tl < 4; tl++)
#pragma unroll
      for (int t = 0; t < 8; t++)
        wf[tl][t] = as_bf16(ld16_sc1(rsrc, (u32)(((tl * 8 + t) * 2) * 64 + lane) * 16u), ld16_sc1(rsrc, (u32)(((tl * 8 + t) * 2 + 1) * 64 + lane) * 16u));
  }
  char* memL = lds;
  char* yl = lds + 8192 + w * 1024;
  float* red = (float*)(lds + 12288);
  const int idx = (lane & 8) ? (int)0xEEEEEEEE : 0x44444444;
  const __amdgpu_buffer_rsrc_t rflag = mkrsrc(ws + OFF_FLAG);
  const __amdgpu_buffer_rsrc_t rmem = mkrsrc(ws + OFF_MEMX);
  const __amdgpu_buffer_rsrc_t rpex = mkrsrc(ws + OFF_PEX);
  int D1 = w == 0 ? 750 : (w == 1 ? 850 : 1130), D2 = 2800 - DC_OFF; u32 tref = now32(), seen = 0xFFFFFFFFu;
  STAMP(1);
  for (int s = 0; s < NSEG; s++) {
    wait_until(tref + D1);
    {
      u32 off[2] = {(u32)s * 8192u + w * 2048u, (u32)s * 8192u + w * 2048u + 1024u};
      u32x4 a[2];
      u32 tiss = 0;
      u32 nsw = poll_chunks<2>(rmem, off, a, lane * 16u, tiss);
      if (!nsw) return;
      if (s > 2) ADAPT(D1, nsw);
      tref = now32();
      *(u32x4*)(memL + w * 2048 + lane * 16) = a[0];
      *(u32x4*)(memL + w * 2048 + 1024 + lane * 16) = a[1];
    }
    u32x4 uf[4][2];
    {
      if (!wait_progress(rflag, (u32)(8 * w + (lane & 7)) * 128u, s + 1, seen)) return;
      const __amdgpu_buffer_rsrc_t ru = mkrsrc(ws + OFF_UF + ((size_t)s * 64 + c) * 16384);
#pragma unroll
      for (int m = 0; m < 4; m++)
#pragma unroll
        for (int z = 0; z < 2; z++) uf[m][z] = ld16_sc1(ru, (u32)((((4 * w + m) * 2 + z) * 4 + gb) * 8 + cc) * 16u);
    }
    __syncthreads();
    STAMP(16 + 4 * s);
    bf16x16 yb;
    {
      f32x4 acc[4];
#pragma unroll
      for (int i = 0; i < 4; i++) acc[i] = (f32x4){0.f, 0.f, 0.f, 0.f};
      bf16x8 af[8];
#pragma unroll
      for (int t = 0; t < 8; t++) af[t] = *(const bf16x8*)(memL + t * 1024 + lane * 16);
      __builtin_amdgcn_sched_barrier(0);
#pragma unroll
      for (int t = 0; t < 8; t++)
#pragma unroll
        for (int i = 0; i < 4; i++) acc[i] = SMF(af[t], wf[i][t], acc[i], idx);
#pragma unroll
      for (int pr = 0; pr < 2; pr++) {
        float v[4];
#pragma unroll
        for (int j = 0; j < 4; j++) {
          auto r = __builtin_amdgcn_permlane32_swap(f2u(acc[2 * pr][j]), f2u(acc[2 * pr + 1][j]), false, false);
          v[j] = u2f(r[0]) + u2f(r[1]);
        }
        const int hl = 2 * (2 * pr + (lane >> 5)) + (n >> 3);
        u32x2 pv = {pk2(v[0], v[1]), pk2(v[2], v[3])};
        *(u32x2*)(yl + (hl * 8 + cc) * 16 + 8 * ((lane >> 4) & 1)) = pv;
      }
      asm volatile("s_waitcnt lgkmcnt(0)" ::: "memory");
      u32x4 hz[2];
#pragma unroll
      for (int z = 0; z < 2; z++) {
        const int ga = 2 * z + (gb >> 1);
        u32x2 a = *(const u32x2*)(yl + ((2 * ga) * 8 + cc) * 16 + 8 * (gb & 1));
        u32x2 b = *(const u32x2*)(yl + ((2 * ga + 1) * 8 + cc) * 16 + 8 * (gb & 1));
        hz[z] = (u32x4){a.x, b.x, a.y, b.y};
      }
      yb = as_bf16(hz[0], hz[1]);
      if (rho == 0) __builtin_amdgcn_raw_buffer_store_b128(*(const u32x4*)(yl + lane * 16), mkrsrc(ws + OFF_Y), (u32)(s * 64 + c) * 4096u + w * 1024u + lane * 16u, 0, 0);
    }
    STAMP(17 + 4 * s);
    if (s == NSEG - 1) break;
    bf16x16 ub16[4];
#pragma unroll
    for (int m = 0; m < 4; m++) { ub16[m] = as_bf16(uf[m][0], uf[m][1]); asm volatile("" : "+v"(ub16[m])); }
    asm volatile("" : "+v"(yb));
    f32x4 dA = {0.f, 0.f, 0.f, 0.f}, dB = {0.f, 0.f, 0.f, 0.f};
    asm volatile("" : "+v"(dA), "+v"(dB));
    wait_until(tref + D2);
    {
      const u32 pb = (u32)s * 20480u + rho * 10240u;
      u32 off[3] = {pb + (2 * w) * 1024u + lane * 16u, pb + (2 * w + 1) * 1024u + lane * 16u,
                    pb + 8192u + (u32)((8 * w + 2 * gb + ((lane >> 3) & 1)) * 4 + (lane & 3)) * 16u};
      u32x4 g[3];
      u32 tiss = 0;
      u32 nsw = poll_chunks<3>(rpex, off, g, 0u, tiss);
      if (!nsw) return;
      if (s > 2) ADAPT(D2, nsw);
      dA = SMF(as_bf8(g[0]), ub16[0], dA, idx);
      dB = SMF(as_bf8(g[0]), ub16[1], dB, idx);
      dA = SMF(as_bf8(g[1]), ub16[2], dA, idx);
      dB = SMF(as_bf8(g[1]), ub16[3], dB, idx);
      dA = SMF(as_bf8(g[2]), yb, dA, idx);
      float* rd = red + (s & 1) * 512;
      if (n < 8) {
#pragma unroll
        for (int j = 0; j < 4; j++) rd[(j * 8 + cc) * 16 + 4 * w + gb] = (gb & 1) ? dB[j] : dA[j];
      }
    }
    __syncthreads();
    STAMP(18 + 4 * s);
    if (w == 0) {
      const float* rd = red + (s & 1) * 512 + lane * 8;
      f32x4 a = *(const f32x4*)rd, b = *(const f32x4*)(rd + 4);
      float v = ((a[0] + a[1]) + (a[2] + a[3])) + ((b[0] + b[1]) + (b[2] + b[3]));
      v += u2f((u32)__builtin_amdgcn_mov_dpp((int)f2u(v), 0xB1, 0xF, 0xF, true));
      float o = u2f((u32)__builtin_amdgcn_mov_dpp((int)f2u(v), 0x4E, 0xF, 0xF, true));
      if (!(lane & 3)) st4_sc1(sane(pk2(v, o)), rmem, (u32)(s + 1) * 8192u + c * 128u + rho * 64u + (lane >> 2) * 4u);
      STAMP(19 + 4 * s);
    }
    tref = now32();
  }
}

__device__ __forceinline__ void load_h(u32x4 (&r)[8], const char* ws, int s) {
  const __amdgpu_buffer_rsrc_t rh = mkrsrc(ws + OFF_H);
#pragma unroll
  for (int j = 0; j < 8; j++) r[j] = ld16_sc1(rh, (u32)s * 32768u + (j * 256 + threadIdx.x) * 16u);
}
__device__ __forceinline__ void put_h(char* dst, const u32x4 (&r)[8]) {
#pragma unroll
  for (int j = 0; j < 8; j++) {
    int u = j * 256 + threadIdx.x;
    *(u32x4*)(dst + (u >> 6) * 1040 + (u & 63) * 16) = r[j];
  }
}

template <bool SC1>
__device__ __forceinline__ void store_T_tile(const f32x16& acc, char* base, int tokrow, int h, int w, int lane) {
  const __amdgpu_buffer_rsrc_t r = mkrsrc(base);
#pragma unroll
  for (int qp = 0; qp < 2; qp++) {
    u32 ax = pk2(acc[8 * qp + 0], acc[8 * qp + 1]), ay = pk2(acc[8 * qp + 2], acc[8 * qp + 3]);
    u32 bx = pk2(acc[8 * qp + 4], acc[8 * qp + 5]), by = pk2(acc[8 * qp + 6], acc[8 * qp + 7]);
    auto r0 = __builtin_amdgcn_permlane32_swap(ax, bx, false, false);
    auto r1 = __builtin_amdgcn_permlane32_swap(ay, by, false, false);
    u32x4 v = {(u32)r0[0], (u32)r1[0], (u32)r0[1], (u32)r1[1]};
    u32 off = (u32)(((tokrow + (lane & 31)) * NH + h) * DH + 32 * w + 16 * qp + 8 * (lane >> 5)) * 2u;
    if (SC1) __builtin_amdgcn_raw_buffer_store_b128(v, r, off, 0, 16);
    else __builtin_amdgcn_raw_buffer_store_b128(v, r, off, 0, 0);
  }
}

__device__ __forceinline__ void role_vu(const u16* __restrict__ Wv, const u16* __restrict__ Wo,
                                        char* ws, char* lds, int h) {
  const int tid = threadIdx.x, lane = tid & 63, w = tid >> 6, x = lane & 31, hh = lane >> 5;
  bf16x8 wv[32], wo[4][8];
  dma_tile<256>(lds, Wv + h * DH, DM, NH * DH);
  dma_wait();
#pragma unroll
  for (int ks = 0; ks < 32; ks++) wv[ks] = frag8(lds, 16 * ks + 8 * hh, 32 * w + (x & 16), 256, lane);
  __syncthreads();
  dma_tile<1024>(lds, Wo + (size_t)h * DH * DM, DH, DM);
  dma_wait();
#pragma unroll
  for (int nt = 0; nt < 4; nt++)
#pragma unroll
    for (int ks = 0; ks < 8; ks++) wo[nt][ks] = frag8(lds, 16 * ks + 8 * hh, 128 * w + 32 * nt + (x & 16), 1024, lane);
  __syncthreads();
  char* vs = lds + 66560;
  const __amdgpu_buffer_rsrc_t rflag = mkrsrc(ws + OFF_FLAG);
  u32x4 hr[8];
  { u32 seenh = 0xFFFFFFFFu; if (!wait_progress(rflag, OFF_DONE + (lane & 31) * 64u, 1u, seenh)) return; }
  load_h(hr, ws, 0);
  put_h(lds, hr);
  load_h(hr, ws, 1);
  __syncthreads();
  for (int s = 0; s < NSEG; s++) {
    const char* hb = lds + (s & 1) * 33280;
    f32x16 av;
#pragma unroll
    for (int i = 0; i < 16; i++) av[i] = 0.f;
    {
      bf16x8 hf[2][8];
#pragma unroll
      for (int k = 0; k < 8; k++) hf[0][k] = *(const bf16x8*)(hb + x * 1040 + (16 * k + 8 * hh) * 2);
#pragma unroll
      for (int ch = 0; ch < 4; ch++) {
        if (ch < 3) {
#pragma unroll
          for (int k = 0; k < 8; k++) hf[(ch + 1) & 1][k] = *(const bf16x8*)(hb + x * 1040 + (16 * (8 * ch + 8 + k) + 8 * hh) * 2);
        }
        __builtin_amdgcn_sched_barrier(0);
#pragma unroll
        for (int k = 0; k < 8; k++) av = MFMA32(wv[8 * ch + k], hf[ch & 1][k], av);
        __builtin_amdgcn_sched_barrier(0);
      }
    }
#pragma unroll
    for (int q = 0; q < 4; q++) {
      u32x2 pv = {pk2(av[4 * q], av[4 * q + 1]), pk2(av[4 * q + 2], av[4 * q + 3])};
      *(u32x2*)(vs + x * 272 + (32 * w + 8 * q + 4 * hh) * 2) = pv;
    }
    put_h(lds + ((s + 1) & 1) * 33280, hr);
    asm volatile("s_waitcnt vmcnt(0)" ::: "memory");
    __syncthreads();
    if (tid == 0 && s > 4) st4_sc1((u32)s, rflag, h * 128u);
    bf16x8 vf[8];
#pragma unroll
    for (int ks = 0; ks < 8; ks++) vf[ks] = *(const bf16x8*)(vs + x * 272 + (16 * ks + 8 * hh) * 2);
    const __amdgpu_buffer_rsrc_t ru = mkrsrc(ws + OFF_UF + (size_t)s * 64 * 16384);
#pragma unroll
    for (int nt = 0; nt < 4; nt++) {
      f32x16 au;
#pragma unroll
      for (int i = 0; i < 16; i++) au[i] = 0.f;
#pragma unroll
      for (int ks = 0; ks < 8; ks++) au = MFMA32(vf[ks], wo[nt][ks], au);
      const int d = 128 * w + 32 * nt + x, c = d >> 3, cc = d & 7;
#pragma unroll
      for (int xb = 0; xb < 2; xb++)
        st16_sc1(PACK_S(au, 8 * xb), ru, (u32)((((c * 16 + (h >> 1)) * 2 + (h & 1)) * 4 + 2 * hh + xb) * 8 + cc) * 16u);
    }
    if (s < 4 || s == NSEG - 1) asm volatile("s_waitcnt vmcnt(0)" ::: "memory");
    if (s == 30) { u32 seenh = 0xFFFFFFFFu; if (!wait_progress(rflag, OFF_DONE + lane * 64u, 1u, seenh)) return; }
    load_h(hr, ws, s + 2 < NSEG ? s + 2 : NSEG - 1);
    __syncthreads();
    if (tid == 0 && (s < 4 || s == NSEG - 1)) st4_sc1((u32)(s + 1), rflag, h * 128u);
    STAMP(16 + 4 * s);
  }
}

__device__ __forceinline__ void role_qk(const u16* __restrict__ Wq, const u16* __restrict__ Wk, char* ws, char* lds, int h) {
  const int tid = threadIdx.x, lane = tid & 63, w = tid >> 6, x = lane & 31, hh = lane >> 5;
  bf16x8 wq[32], wk[32];
  dma_tile<256>(lds, Wk + h * DH, DM, NH * DH);
  dma_wait();
#pragma unroll
  for (int ks = 0; ks < 32; ks++) wk[ks] = frag8(lds, 16 * ks + 8 * hh, 32 * w + (x & 16), 256, lane);
  __syncthreads();
  dma_tile<256>(lds, Wq + h * DH, DM, NH * DH);
  dma_wait();
#pragma unroll
  for (int ks = 0; ks < 32; ks++) wq[ks] = frag8(lds, 16 * ks + 8 * hh, 32 * w + (x & 16), 256, lane);
  __syncthreads();
  const __amdgpu_buffer_rsrc_t rflag = mkrsrc(ws + OFF_FLAG);
  u32x4 hr[8];
  { u32 seenh = 0xFFFFFFFFu; if (!wait_progress(rflag, OFF_DONE + (lane & 31) * 64u, 1u, seenh)) return; }
  load_h(hr, ws, 0);
  put_h(lds, hr);
  load_h(hr, ws, 1);
  __syncthreads();
  for (int s = 0; s < NSEG; s++) {
    const char* hb = lds + (s & 1) * 33280;
    f32x16 aq, ak;
#pragma unroll
    for (int i = 0; i < 16; i++) { aq[i] = 0.f; ak[i] = 0.f; }
    {
      bf16x8 hf[2][8];
#pragma unroll
      for (int k = 0; k < 8; k++) hf[0][k] = *(const bf16x8*)(hb + x * 1040 + (16 * k + 8 * hh) * 2);
#pragma unroll
      for (int ch = 0; ch < 4; ch++) {
        if (ch < 3) {
#pragma unroll
          for (int k = 0; k < 8; k++) hf[(ch + 1) & 1][k] = *(const bf16x8*)(hb + x * 1040 + (16 * (8 * ch + 8 + k) + 8 * hh) * 2);
        }
        __builtin_amdgcn_sched_barrier(0);
#pragma unroll
        for (int k = 0; k < 8; k++) { ak = MFMA32(wk[8 * ch + k], hf[ch & 1][k], ak); aq = MFMA32(wq[8 * ch + k], hf[ch & 1][k], aq); }
        __builtin_amdgcn_sched_barrier(0);
      }
    }
    put_h(lds + ((s + 1) & 1) * 33280, hr);
    asm volatile("s_waitcnt vmcnt(0)" ::: "memory");
    __syncthreads();
    if (tid == 0 && s > 4) st4_sc1((u32)s, rflag, 4096u + h * 128u);
    store_T_tile<true>(ak, ws + OFF_KX, 32 * s, h, w, lane);
    store_T_tile<true>(aq, ws + OFF_QX, 32 * s, h, w, lane);
    if (s < 4 || s == NSEG - 1) {
      asm volatile("s_waitcnt vmcnt(0)" ::: "memory");
      __syncthreads();
      if (tid == 0) st4_sc1((u32)(s + 1), rflag, 4096u + h * 128u);
    }
    if (s == 30) { u32 seenh = 0xFFFFFFFFu; if (!wait_progress(rflag, OFF_DONE + lane * 64u, 1u, seenh)) return; }
    load_h(hr, ws, s + 2 < NSEG ? s + 2 : NSEG - 1);
    STAMP(16 + 4 * s);
  }
}

__device__ __forceinline__ float xg_max(float v) {
  auto r = __builtin_amdgcn_permlane16_swap(f2u(v), f2u(v), false, false);
  v = fmaxf(u2f(r[0]), u2f(r[1]));
  auto r2 = __builtin_amdgcn_permlane32_swap(f2u(v), f2u(v), false, false);
  return fmaxf(u2f(r2[0]), u2f(r2[1]));
}
__device__ __forceinline__ float xg_sum(float v) {
  auto r = __builtin_amdgcn_permlane16_swap(f2u(v), f2u(v), false, false);
  v = u2f(r[0]) + u2f(r[1]);
  auto r2 = __builtin_amdgcn_permlane32_swap(f2u(v), f2u(v), false, false);
  return u2f(r2[0]) + u2f(r2[1]);
}
__device__ __forceinline__ void px_task(char* ws, int s, int hg) {
  const int tid = threadIdx.x, lane = tid & 63, w = tid >> 6;
  const int hd = hg * 4 + w, hl = w;
  const int n = lane & 15, g = lane >> 4;
  const __amdgpu_buffer_rsrc_t rq = mkrsrc(ws + OFF_QX + (size_t)s * 32 * 8192);
  const __amdgpu_buffer_rsrc_t rk = mkrsrc(ws + OFF_KX + (size_t)s * 32 * 8192);
  const __amdgpu_buffer_rsrc_t rkm = mkrsrc(ws + OFF_KMEM + (size_t)s * 65536);
  const int keyrow = 16 * ((n >> 2) & 1) + 4 * (n >> 3) + (n & 3);
  const int keyb = 16 * (g & 1) + 4 * (g >> 1);
  const float sl = 0.08838834764831845f * 1.4426950408889634f;
  bf16x8 qf[2][4], kf[2][4], mf[4];
  const __amdgpu_buffer_rsrc_t rflag = mkrsrc(ws + OFF_FLAG);
  { u32 seenq = 0xFFFFFFFFu; wait_progress(rflag, 4096u + hd * 128u, s + 1, seenq); }
#pragma unroll
  for (int ks = 0; ks < 4; ks++) {
#pragma unroll
    for (int t = 0; t < 2; t++) {
      qf[t][ks] = as_bf8(ld16_sc1(rq, (u32)(((16 * t + n) * NH + hd) * DH + 32 * ks + 8 * g) * 2u));
      kf[t][ks] = as_bf8(ld16_sc1(rk, (u32)(((8 * t + keyrow) * NH + hd) * DH + 32 * ks + 8 * g) * 2u));
    }
  }
  {
    u32 spins = 0, off = OFF_KMF + (u32)hd * 64u;
    while (true) {
      asm volatile("" : "+v"(off) :: "memory");
      u32 v = ld4_sc1(rflag, off);
      if ((int)v > s) break;
      __builtin_amdgcn_s_sleep(6);
      if (++spins > 800000u) break;
    }
  }
#pragma unroll
  for (int ks = 0; ks < 4; ks++) mf[ks] = as_bf8(ld16_sc1(rkm, (u32)(hd * 1024 + (n & 7) * 128 + 32 * ks + 8 * g) * 2u));
  const bool mvalid = (g >> 1) == (hl >> 1);
  u32x4* PXx = (u32x4*)(ws + OFF_PX);
  char* PXm = ws + OFF_PX + (4ull << 20);
#pragma unroll
  for (int qt = 0; qt < 2; qt++) {
    f32x4 sc[3];
#pragma unroll
    for (int i = 0; i < 3; i++) sc[i] = (f32x4){0.f, 0.f, 0.f, 0.f};
#pragma unroll
    for (int ks = 0; ks < 4; ks++) {
      sc[0] = MFMA16(kf[0][ks], qf[qt][ks], sc[0]);
      sc[1] = MFMA16(kf[1][ks], qf[qt][ks], sc[1]);
      sc[2] = MFMA16(mf[ks], qf[qt][ks], sc[2]);
    }
    const int qi = 16 * qt + n;
    float mx = -3.0e38f;
#pragma unroll
    for (int j = 0; j < 4; j++) {
      if (keyb + j > qi) sc[0][j] = -3.0e38f;
      if (keyb + 8 + j > qi) sc[1][j] = -3.0e38f;
      if (!mvalid) sc[2][j] = -3.0e38f;
      mx = fmaxf(mx, fmaxf(fmaxf(sc[0][j], sc[1][j]), sc[2][j]));
    }
    mx = xg_max(mx);
    float sum = 0.f;
#pragma unroll
    for (int i = 0; i < 3; i++)
#pragma unroll
      for (int j = 0; j < 4; j++) {
        float p = sc[i][j] > -1.0e38f ? __builtin_amdgcn_exp2f((sc[i][j] - mx) * sl) : 0.f;
        sc[i][j] = p; sum += p;
      }
    const float inv = __builtin_amdgcn_rcpf(xg_sum(sum));
    u32x4 pa = {pk2(sc[0][0] * inv, sc[0][1] * inv), pk2(sc[1][0] * inv, sc[1][1] * inv),
                pk2(sc[0][2] * inv, sc[0][3] * inv), pk2(sc[1][2] * inv, sc[1][3] * inv)};
    PXx[(((size_t)s * NH + hd) * 2 + qt) * 64 + lane] = pa;
    if (mvalid) {
      u32x2 pm = {pk2(sc[2][0] * inv, sc[2][1] * inv), pk2(sc[2][2] * inv, sc[2][3] * inv)};
      *(u32x2*)(PXm + ((((size_t)s * 8 + hg) * 2 + qt) * 64 + lane) * 16 + (hl & 1) * 8) = pm;
    }
  }
}

__global__ void __launch_bounds__(256, 1) k2_rec(const u16* __restrict__ Wq, const u16* __restrict__ Wk, const u16* __restrict__ Wv,
                                                  const u16* __restrict__ Wo, const u16* __restrict__ x, const u16* __restrict__ w1,
                                                  const u16* __restrict__ w2, const u16* __restrict__ cvec,
                                                  const u16* __restrict__ wmod, const u16* __restrict__ mem0, char* ws) {
  extern __shared__ __attribute__((aligned(16))) char lds[];
  const int b = blockIdx.x;
  STAMP(0);
  if (b < 32) role_help(x, w1, w2, cvec, wmod, mem0, ws, lds, b);
  else if (b < 64) role_score(Wq, Wk, ws, lds, b - 32);
  else if (b < 192) { const int q = b - 64; role_column(Wv, Wo, ws, lds, (q & 7) | ((q >> 4) << 3), (q >> 3) & 1); }
  else if (b < 224) role_vu(Wv, Wo, ws, lds, b - 192);
  else role_qk(Wq, Wk, ws, lds, b - 224);
}

__global__ void __launch_bounds__(512, 2) k3b_oproj(const u16* __restrict__ x, char* __restrict__ ws) {
  extern __shared__ __attribute__((aligned(16))) char lds[];
  const int b = blockIdx.x, tid = threadIdx.x, lane = tid & 63, w = tid >> 6;
  const int s = (b & 7) * 8 + (b >> 5), nq = (b >> 3) & 3;
  const int n = lane & 15, g = lane >> 4, cc = lane & 7;
  const u32x4* UF = (const u32x4*)(ws + OFF_UF) + (size_t)s * 64 * 1024;
  const char* Yg = ws + OFF_Y + (size_t)s * 64 * 4096;
  const u32x4* PXx = (const u32x4*)(ws + OFF_PX) + (size_t)s * NH * 128;
  const u32x4* PXm = (const u32x4*)(ws + OFF_PX + (4ull << 20)) + (size_t)s * 8 * 128;
  f32x4 acc[2][8];
#pragma unroll
  for (int i = 0; i < 2; i++)
#pragma unroll
    for (int j = 0; j < 8; j++) acc[i][j] = (f32x4){0.f, 0.f, 0.f, 0.f};
  u32x4 ub[4][8], pa[4][2], pm[2], yf[8];
#pragma unroll
  for (int hl = 0; hl < 4; hl++) {
    const int hd = 4 * w + hl;
#pragma unroll
    for (int nt = 0; nt < 8; nt++) ub[hl][nt] = __builtin_nontemporal_load(UF + (size_t)(16 * nq + 2 * nt + (n >> 3)) * 1024 + (hd * 4 + g) * 8 + cc);
    pa[hl][0] = PXx[(hd * 2) * 64 + lane];
    pa[hl][1] = PXx[(hd * 2 + 1) * 64 + lane];
  }
  pm[0] = PXm[(w * 2) * 64 + lane];
  pm[1] = PXm[(w * 2 + 1) * 64 + lane];
  {
    const int ha = 4 * w + 2 * (g >> 1);
#pragma unroll
    for (int nt = 0; nt < 8; nt++) {
      const int c = 16 * nq + 2 * nt + (n >> 3);
      u32x2 ya = __builtin_nontemporal_load((const u32x2*)(Yg + (size_t)c * 4096 + (ha * 8 + cc) * 16 + 8 * (g & 1)));
      u32x2 yb = __builtin_nontemporal_load((const u32x2*)(Yg + (size_t)c * 4096 + ((ha + 1) * 8 + cc) * 16 + 8 * (g & 1)));
      yf[nt] = (u32x4){ya.x, ya.y, yb.x, yb.y};
    }
  }
  __builtin_amdgcn_sched_barrier(0);
#pragma unroll
  for (int hl = 0; hl < 4; hl++)
#pragma unroll
    for (int nt = 0; nt < 8; nt++) {
      acc[0][nt] = MFMA16(as_bf8(pa[hl][0]), as_bf8(ub[hl][nt]), acc[0][nt]);
      acc[1][nt] = MFMA16(as_bf8(pa[hl][1]), as_bf8(ub[hl][nt]), acc[1][nt]);
    }
#pragma unroll
  for (int nt = 0; nt < 8; nt++) {
    acc[0][nt] = MFMA16(as_bf8(pm[0]), as_bf8(yf[nt]), acc[0][nt]);
    acc[1][nt] = MFMA16(as_bf8(pm[1]), as_bf8(yf[nt]), acc[1][nt]);
  }
  float* red = (float*)lds;
#pragma unroll
  for (int qt = 0; qt < 2; qt++)
#pragma unroll
    for (int nt = 0; nt < 8; nt++)
#pragma unroll
      for (int j = 0; j < 4; j++) red[(w * 32 + 16 * qt + 4 * g + j) * 132 + 16 * nt + n] = acc[qt][nt][j];
  __syncthreads();
  const float* mod = (const float*)(ws + OFF_MOD);
  u16* x1 = (u16*)(ws + OFF_X1);
  {
    const int r = tid >> 4, c8 = (tid & 15) * 8;
    float o[8];
#pragma unroll
    for (int j = 0; j < 8; j++) o[j] = 0.f;
#pragma unroll
    for (int ww = 0; ww < 8; ww++) {
      f32x4 a = *(const f32x4*)(red + (ww * 32 + r) * 132 + c8), bq = *(const f32x4*)(red + (ww * 32 + r) * 132 + c8 + 4);
#pragma unroll
      for (int j = 0; j < 4; j++) { o[j] += a[j]; o[4 + j] += bq[j]; }
    }
    const int col = 128 * nq + c8;
    const size_t go = (size_t)(32 * s + r) * DM + col;
    u32x4 xv = *(const u32x4*)(x + go);
    float xf[8] = {bf_lo(xv.x), bf_hi(xv.x), bf_lo(xv.y), bf_hi(xv.y), bf_lo(xv.z), bf_hi(xv.z), bf_lo(xv.w), bf_hi(xv.w)};
#pragma unroll
    for (int j = 0; j < 8; j++) o[j] = xf[j] + bfr(mod[1024 + col + j] * bfr(o[j]));
    u32x4 rv = {pk2(o[0], o[1]), pk2(o[2], o[3]), pk2(o[4], o[5]), pk2(o[6], o[7])};
    *(u32x4*)(x1 + go) = rv;
  }
}

__global__ void __launch_bounds__(512, 2) k4_mlp1(const u16* __restrict__ b1, char* __restrict__ ws) {
  extern __shared__ __attribute__((aligned(16))) char lds[];
  const int b = blockIdx.x, tid = threadIdx.x, lane = tid & 63, w = tid >> 6;
  const int jq = b >> 3, mt0 = (b & 7) * 4 + jq / 7, nb = jq % 7;
  const float* mod = (const float*)(ws + OFF_MOD);
  const u16* x1 = (const u16*)(ws + OFF_X1);
  const int n = lane & 15, g = lane >> 4;
  const f32x4 mv = *(const f32x4*)(mod + 1536 + (tid & 255) * 4);
  u32x4 xv[2][4];
#pragma unroll
  for (int it = 0; it < 2; it++)
#pragma unroll
    for (int j = 0; j < 4; j++) xv[it][j] = *(const u32x4*)(x1 + (size_t)(mt0 * 64 + it * 32 + w * 4 + g) * DM + (j * 16 + n) * 8);
  const u32x4* WF = (const u32x4*)(ws + OFF_W1F) + (size_t)(nb * 16 + w * 2) * 16 * 64 + lane;
  u32x4 bfm[2][16];
#pragma unroll
  for (int nt = 0; nt < 2; nt++)
#pragma unroll
    for (int ks = 0; ks < 8; ks++) bfm[nt][ks] = WF[(nt * 16 + ks) * 64];
  __builtin_amdgcn_sched_barrier(0);
  float* modl = (float*)(lds + 66560);
  if (tid < 256) *(f32x4*)(modl + tid * 4) = tid >= 128 ? mv + 1.f : mv;
  __syncthreads();
#pragma unroll
  for (int it = 0; it < 2; it++) {
    f32x2 f[16];
#pragma unroll
    for (int j = 0; j < 4; j++) {
      f[4 * j + 0] = (f32x2){bf_lo(xv[it][j].x), bf_hi(xv[it][j].x)}; f[4 * j + 1] = (f32x2){bf_lo(xv[it][j].y), bf_hi(xv[it][j].y)};
      f[4 * j + 2] = (f32x2){bf_lo(xv[it][j].z), bf_hi(xv[it][j].z)}; f[4 * j + 3] = (f32x2){bf_lo(xv[it][j].w), bf_hi(xv[it][j].w)};
    }
    f32x2 s2 = {0.f, 0.f};
#pragma unroll
    for (int i = 0; i < 16; i++) s2 += f[i];
    float m = row16_sum(s2.x + s2.y) * (1.f / DM);
    f32x2 q2 = {0.f, 0.f};
#pragma unroll
    for (int i = 0; i < 16; i++) { f[i] -= m; q2 += f[i] * f[i]; }
    float rs = rsqrtf(row16_sum(q2.x + q2.y) * (1.f / DM) + 1e-5f);
    s2 = (f32x2){0.f, 0.f};
#pragma unroll
    for (int j = 0; j < 4; j++) {
      const int c0 = (j * 16 + n) * 8;
      f32x4 c0v = *(const f32x4*)(modl + 512 + c0), c1v = *(const f32x4*)(modl + 512 + c0 + 4);
      f32x4 s0v = *(const f32x4*)(modl + c0), s1v = *(const f32x4*)(modl + c0 + 4);
      f[4 * j + 0] = f[4 * j + 0] * rs * (f32x2){c0v[0], c0v[1]} + (f32x2){s0v[0], s0v[1]};
      f[4 * j + 1] = f[4 * j + 1] * rs * (f32x2){c0v[2], c0v[3]} + (f32x2){s0v[2], s0v[3]};
      f[4 * j + 2] = f[4 * j + 2] * rs * (f32x2){c1v[0], c1v[1]} + (f32x2){s1v[0], s1v[1]};
      f[4 * j + 3] = f[4 * j + 3] * rs * (f32x2){c1v[2], c1v[3]} + (f32x2){s1v[2], s1v[3]};
      s2 += (f[4 * j + 0] + f[4 * j + 1]) + (f[4 * j + 2] + f[4 * j + 3]);
    }
    m = row16_sum(s2.x + s2.y) * (1.f / DM);
    q2 = (f32x2){0.f, 0.f};
#pragma unroll
    for (int i = 0; i < 16; i++) { f[i] -= m; q2 += f[i] * f[i]; }
    rs = rsqrtf(row16_sum(q2.x + q2.y) * (1.f / DM) + 1e-5f);
#pragma unroll
    for (int j = 0; j < 4; j++) {
      f32x2 o0 = f[4 * j] * rs, o1 = f[4 * j + 1] * rs, o2 = f[4 * j + 2] * rs, o3 = f[4 * j + 3] * rs;
      u32x4 o = {pk2(o0.x, o0.y), pk2(o1.x, o1.y), pk2(o2.x, o2.y), pk2(o3.x, o3.y)};
      *(u32x4*)(lds + (it * 32 + w * 4 + g) * 1040 + (j * 16 + n) * 16) = o;
    }
    __builtin_amdgcn_sched_barrier(0);
  }
#pragma unroll
  for (int nt = 0; nt < 2; nt++)
#pragma unroll
    for (int ks = 8; ks < 16; ks++) bfm[nt][ks] = WF[(nt * 16 + ks) * 64];
  __builtin_amdgcn_sched_barrier(0);
  __syncthreads();
  f32x4 acc[4][2];
#pragma unroll
  for (int i = 0; i < 4; i++)
#pragma unroll
    for (int j = 0; j < 2; j++) acc[i][j] = (f32x4){0.f, 0.f, 0.f, 0.f};
#pragma unroll
  for (int ks = 0; ks < 16; ks++) {
    bf16x8 af[4];
#pragma unroll
    for (int mt = 0; mt < 4; mt++) af[mt] = *(const bf16x8*)(lds + (16 * mt + n) * 1040 + (32 * ks + 8 * g) * 2);
#pragma unroll
    for (int mt = 0; mt < 4; mt++)
#pragma unroll
      for (int nt = 0; nt < 2; nt++) acc[mt][nt] = MFMA16(as_bf8(bfm[nt][ks]), af[mt], acc[mt][nt]);
    if (ks & 1) __builtin_amdgcn_sched_barrier(0);
  }
  u32x4* hidF = (u32x4*)(ws + OFF_HID);
  float bb[2][4];
#pragma unroll
  for (int nt = 0; nt < 2; nt++) {
    u32x2 bv = *(const u32x2*)(b1 + nb * 256 + w * 32 + 16 * nt + 4 * g);
    bb[nt][0] = bf_lo(bv.x); bb[nt][1] = bf_hi(bv.x); bb[nt][2] = bf_lo(bv.y); bb[nt][3] = bf_hi(bv.y);
  }
#pragma unroll
  for (int mt = 0; mt < 4; mt++) {
    float o[8];
#pragma unroll
    for (int nt = 0; nt < 2; nt++)
#pragma unroll
      for (int j = 0; j < 4; j++) {
        float u = bfr(bfr(acc[mt][nt][j]) + bb[nt][j]);
        float z = 0.7978845608028654f * (u + 0.044715f * u * u * u);
        float e = __builtin_amdgcn_exp2f(z * 2.8853900817779268f);
        float th = 1.f - 2.f * __builtin_amdgcn_rcpf(e + 1.f);
        o[4 * nt + j] = 0.5f * u * (1.f + th);
      }
    u32x4 ov = {pk2(o[0], o[1]), pk2(o[2], o[3]), pk2(o[4], o[5]), pk2(o[6], o[7])};
    hidF[((size_t)(mt0 * 4 + mt) * 56 + nb * 8 + w) * 64 + lane] = ov;
  }
}

__global__ void __launch_bounds__(512, 2) k5_mlp2(const u16* __restrict__ b2, char* __restrict__ ws, u16* __restrict__ out) {
  extern __shared__ __attribute__((aligned(16))) char lds[];
  const int b = blockIdx.x, tid = threadIdx.x, lane = tid & 63, w = tid >> 6;
  const int mt0 = (b & 7) * 4 + (b >> 6), nb = (b >> 3) & 7;
  const int n = lane & 15, g = lane >> 4;
  const u32x4* hidF = (const u32x4*)(ws + OFF_HID) + ((size_t)(mt0 * 4) * 56 + w * 7) * 64 + lane;
  const u32x4* WF = (const u32x4*)(ws + OFF_W2F) + ((size_t)(nb * 4) * 56 + w * 7) * 64 + lane;
  f32x4 acc[4][4];
#pragma unroll
  for (int i = 0; i < 4; i++)
#pragma unroll
    for (int j = 0; j < 4; j++) acc[i][j] = (f32x4){0.f, 0.f, 0.f, 0.f};
#pragma unroll
  for (int hb = 0; hb < 2; hb++) {
    const int nk = hb ? 3 : 4;
    u32x4 bfm[4][4]; bf16x8 af[4][4];
#pragma unroll
    for (int k = 0; k < 4; k++) if (k < nk) {
#pragma unroll
      for (int nt = 0; nt < 4; nt++) bfm[k][nt] = WF[(nt * 56 + hb * 4 + k) * 64];
#pragma unroll
      for (int mt = 0; mt < 4; mt++) af[k][mt] = as_bf8(hidF[(mt * 56 + hb * 4 + k) * 64]);
    }
    __builtin_amdgcn_sched_barrier(0);
#pragma unroll
    for (int k = 0; k < 4; k++) if (k < nk) {
#pragma unroll
      for (int mt = 0; mt < 4; mt++)
#pragma unroll
        for (int nt = 0; nt < 4; nt++) acc[mt][nt] = MFMA16(as_bf8(bfm[k][nt]), af[k][mt], acc[mt][nt]);
    }
  }
  float* red = (float*)lds;
#pragma unroll
  for (int mt = 0; mt < 4; mt++)
#pragma unroll
    for (int nt = 0; nt < 4; nt++) *(f32x4*)(red + (w * 64 + 16 * mt + n) * 68 + 16 * nt + 4 * g) = acc[mt][nt];
  __syncthreads();
  const float* mod = (const float*)(ws + OFF_MOD);
  const u16* x1 = (const u16*)(ws + OFF_X1);
  {
    const int r = tid >> 3, c8 = (tid & 7) * 8;
    float o[8];
#pragma unroll
    for (int j = 0; j < 8; j++) o[j] = 0.f;
#pragma unroll
    for (int ww = 0; ww < 8; ww++) {
      f32x4 a = *(const f32x4*)(red + (ww * 64 + r) * 68 + c8), bq = *(const f32x4*)(red + (ww * 64 + r) * 68 + c8 + 4);
#pragma unroll
      for (int j = 0; j < 4; j++) { o[j] += a[j]; o[4 + j] += bq[j]; }
    }
    const int col = 64 * nb + c8;
    const size_t go = (size_t)(mt0 * 64 + r) * DM + col;
    u32x4 xv = *(const u32x4*)(x1 + go), bv = *(const u32x4*)(b2 + col);
    float xf[8] = {bf_lo(xv.x), bf_hi(xv.x), bf_lo(xv.y), bf_hi(xv.y), bf_lo(xv.z), bf_hi(xv.z), bf_lo(xv.w), bf_hi(xv.w)};
    float bb[8] = {bf_lo(bv.x), bf_hi(bv.x), bf_lo(bv.y), bf_hi(bv.y), bf_lo(bv.z), bf_hi(bv.z), bf_lo(bv.w), bf_hi(bv.w)};
#pragma unroll
    for (int j = 0; j < 8; j++) o[j] = xf[j] + bfr(mod[2560 + col + j] * bfr(bfr(o[j]) + bb[j]));
    u32x4 rv = {pk2(o[0], o[1]), pk2(o[2], o[3]), pk2(o[4], o[5]), pk2(o[6], o[7])};
    *(u32x4*)(out + go) = rv;
  }
}

extern "C" void kernel_launch(void* const* d_in, const int* in_sizes, int n_in, void* d_out, int out_size, void* d_ws,
                              size_t ws_size, hipStream_t stream) {
  const u16* x = (const u16*)d_in[0];
  const u16* mem0 = (const u16*)d_in[1];
  const u16* Wq = (const u16*)d_in[2];
  const u16* Wk = (const u16*)d_in[3];
  const u16* Wv = (const u16*)d_in[4];
  const u16* Wo = (const u16*)d_in[5];
  const u16* w1 = (const u16*)d_in[8];
  const u16* b1 = (const u16*)d_in[9];
  const u16* w2 = (const u16*)d_in[10];
  const u16* b2 = (const u16*)d_in[11];
  const u16* cvec = (const u16*)d_in[12];
  const u16* wmod = (const u16*)d_in[13];
  char* ws = (char*)d_ws;
  hipFuncSetAttribute((const void*)k2_rec, hipFuncAttributeMaxDynamicSharedMemorySize, 135168);
  hipMemsetAsync(ws, 0xFF, OFF_H, stream);
  k2_rec<<<256, 256, 135168, stream>>>(Wq, Wk, Wv, Wo, x, w1, w2, cvec, wmod, mem0, ws);
  hipFuncSetAttribute((const void*)k3b_oproj, hipFuncAttributeMaxDynamicSharedMemorySize, 135168);
  k3b_oproj<<<256, 512, 135168, stream>>>(x, ws);
  hipFuncSetAttribute((const void*)k4_mlp1, hipFuncAttributeMaxDynamicSharedMemorySize, 70656);
  hipFuncSetAttribute((const void*)k5_mlp2, hipFuncAttributeMaxDynamicSharedMemorySize, 139264);
  k4_mlp1<<<224, 512, 70656, stream>>>(b1, ws);
  k5_mlp2<<<256, 512, 139264, stream>>>(b2, ws, (u16*)d_out);
}
```

```cpp
#include <hip/hip_runtime.h>
#include <hip/hip_bf16.h>
#include <stdint.h>

typedef unsigned int u32;
typedef unsigned short u16;
typedef unsigned long long u64;
typedef __attribute__((ext_vector_type(2))) __bf16 bf16x2;
typedef __attribute__((ext_vector_type(8))) __bf16 bf16x8;
typedef __attribute__((ext_vector_type(16))) __bf16 bf16x16;
typedef __attribute__((ext_vector_type(2))) float f32x2;
typedef __attribute__((ext_vector_type(4))) float f32x4;
typedef __attribute__((ext_vector_type(16))) float f32x16;
typedef __attribute__((ext_vector_type(2))) u32 u32x2;
typedef __attribute__((ext_vector_type(4))) u32 u32x4;
typedef __attribute__((ext_vector_type(8))) u32 u32x8;

#define NTOK 2048
#define DM 512
#define NH 32
#define DH 128
#define NSEG 64
#define FF 1792
#define SENT 0xFFFFFFFFu

#define OFF_FLAG 0ull
#define OFF_DONE 8192u
#define OFF_KMF 12288u
#define OFF_WVF 14336u
#define OFF_MDF 28672u
#define OFF_MOD 16384ull
#define OFF_MEMX 32768ull
#define OFF_PEX 0x90000ull
#define OFF_H (2ull << 20)
#define OFF_KMEM (4ull << 20)
#define OFF_WVO (8ull << 20)
#define OFF_KX (24ull << 20)
#define OFF_QX (40ull << 20)
#define OFF_Y (56ull << 20)
#define OFF_UF (72ull << 20)
#define OFF_PX (136ull << 20)
#define OFF_X1 (142ull << 20)
#define OFF_Y2 (146ull << 20)
#define OFF_HID (148ull << 20)
#define OFF_W1F (156ull << 20)
#define OFF_W2F (158ull << 20)
#define OFF_DBG (200ull << 20)
#define STAMP(slot) do { } while (0)

__device__ __forceinline__ u32 pk2(float a, float b) {
  bf16x2 v; v[0] = (__bf16)a; v[1] = (__bf16)b; return __builtin_bit_cast(u32, v);
}
__device__ __forceinline__ u32 f2u(float x) { return __builtin_bit_cast(u32, x); }
__device__ __forceinline__ float u2f(u32 x) { return __builtin_bit_cast(float, x); }
__device__ __forceinline__ float bf_lo(u32 x) { return __builtin_bit_cast(float, x << 16); }
__device__ __forceinline__ float bf_hi(u32 x) { return __builtin_bit_cast(float, x & 0xffff0000u); }
__device__ __forceinline__ float bfr(float a) { return (float)(__bf16)a; }
__device__ __forceinline__ __amdgpu_buffer_rsrc_t mkrsrc(const void* p) {
  return __builtin_amdgcn_make_buffer_rsrc((void*)p, 0, 0x7fffffff, 0x00020000);
}
__device__ __forceinline__ u32x4 ld16_sc1(__amdgpu_buffer_rsrc_t r, u32 off) { return __builtin_amdgcn_raw_buffer_load_b128(r, off, 0, 16); }
__device__ __forceinline__ u32 ld4_sc1(__amdgpu_buffer_rsrc_t r, u32 off) { return __builtin_amdgcn_raw_buffer_load_b32(r, off, 0, 16); }
__device__ __forceinline__ void st16_sc1(u32x4 v, __amdgpu_buffer_rsrc_t r, u32 off) { __builtin_amdgcn_raw_buffer_store_b128(v, r, off, 0, 16); }
__device__ __forceinline__ void st8_sc1(u32x2 v, __amdgpu_buffer_rsrc_t r, u32 off) { __builtin_amdgcn_raw_buffer_store_b64(v, r, off, 0, 16); }
__device__ __forceinline__ void st4_sc1(u32 v, __amdgpu_buffer_rsrc_t r, u32 off) { __builtin_amdgcn_raw_buffer_store_b32(v, r, off, 0, 16); }
__device__ __forceinline__ u32 badm(u32x4 a) { return (u32)(a.x == SENT) | (u32)(a.y == SENT) | (u32)(a.z == SENT) | (u32)(a.w == SENT); }
__device__ __forceinline__ u32 sane(u32 x) { return x < 0xFFFFFFFEu ? x : 0xFFFFFFFEu; }
__device__ __forceinline__ u32 now32() { return (u32)__builtin_amdgcn_s_memtime(); }
__device__ __forceinline__ void wait_until(u32 t) { while ((int)(t - now32()) > 0) { } }
__device__ __forceinline__ bf16x8 as_bf8(u32x4 v) { return __builtin_bit_cast(bf16x8, v); }
__device__ __forceinline__ bf16x16 as_bf16(u32x4 a, u32x4 b) {
  u32x8 v = {a.x, a.y, a.z, a.w, b.x, b.y, b.z, b.w}; return __builtin_bit_cast(bf16x16, v);
}
__device__ __forceinline__ float max3r(float a, float b, float c) { float r; asm("v_max3_f32 %0, %1, %2, %3" : "=v"(r) : "v"(a), "v"(b), "v"(c)); return r; }
__device__ __forceinline__ float max2r(float a, float b) { float r; asm("v_max_f32 %0, %1, %2" : "=v"(r) : "v"(a), "v"(b)); return r; }
#define MFMA16(a, b, c) __builtin_amdgcn_mfma_f32_16x16x32_bf16((a), (b), (c), 0, 0, 0)
#define MFMA32(a, b, c) __builtin_amdgcn_mfma_f32_32x32x16_bf16((a), (b), (c), 0, 0, 0)
#define SMF(a, b, c, idx) __builtin_amdgcn_smfmac_f32_16x16x64_bf16((a), (b), (c), (idx), 0, 0)

__device__ __forceinline__ u32 umax3(u32 a, u32 b, u32 c) { u32 m = a > b ? a : b; return m > c ? m : c; }
template <int N>
__device__ __forceinline__ u32 poll_chunks(__amdgpu_buffer_rsrc_t r, const u32 (&off)[N], u32x4 (&a)[N], u32 lane16) {
  u32 spins = 0;
  while (true) {
    asm volatile("" : "+v"(lane16) :: "memory");
#pragma unroll
    for (int j = 0; j < N; j++) a[j] = ld16_sc1(r, off[j] + lane16);
    u32 m = 0;
#pragma unroll
    for (int j = 0; j < N; j++) { m = umax3(m, a[j].x, a[j].y); m = umax3(m, a[j].z, a[j].w); }
    ++spins;
    if (!__any(m == SENT)) return spins;
    if (spins > 400000u) return 0;
  }
}

typedef __attribute__((ext_vector_type(4))) short s16x4;
typedef __attribute__((address_space(3))) s16x4 lds_s16x4;
template <int ROWB>
__device__ __forceinline__ void dma_tile(char* lds, const u16* src, int rows, int gstride) {
  constexpr int LPR = ROWB / 16;
  const int total = rows * LPR;
  for (int u = threadIdx.x; u < total; u += 256) {
    const int r = u / LPR, c = u % LPR;
    __builtin_amdgcn_global_load_lds((const u32*)(src + (size_t)r * gstride + c * 8), (u32*)(lds + u * 16), 16, 0, 0);
  }
}
__device__ __forceinline__ void dma_wait() { asm volatile("s_waitcnt vmcnt(0)" ::: "memory"); __syncthreads(); }
__device__ __forceinline__ u32x2 tr4(const char* lds, int row0, int col0, int pitch, int lane) {
  const char* p = lds + (row0 + ((lane & 15) >> 2)) * pitch + (col0 + 4 * (lane & 3)) * 2;
  s16x4 v = __builtin_amdgcn_ds_read_tr16_b64_v4i16((lds_s16x4*)p);
  return __builtin_bit_cast(u32x2, v);
}
__device__ __forceinline__ bf16x8 frag8(const char* lds, int k0, int col0, int pitch, int lane) {
  u32x2 a = tr4(lds, k0, col0, pitch, lane), b = tr4(lds, k0 + 4, col0, pitch, lane);
  return as_bf8((u32x4){a.x, a.y, b.x, b.y});
}
__device__ __forceinline__ u32x4 frag8s(const char* lds, int k0, int col0, int pitch, int lane) {
  u32x2 a = tr4(lds, k0, col0, pitch, lane), b = tr4(lds, k0 + 8, col0, pitch, lane);
  return (u32x4){a.x, b.x, a.y, b.y};
}
#define PACK_S(acc, b) (u32x4){pk2((acc)[(b)], (acc)[(b) + 1]), pk2((acc)[(b) + 4], (acc)[(b) + 5]), pk2((acc)[(b) + 2], (acc)[(b) + 3]), pk2((acc)[(b) + 6], (acc)[(b) + 7])}

__device__ __forceinline__ void wvo_task(const u16* __restrict__ Wv, const u16* __restrict__ Wo, char* ws, char* lds, int task) {
  const int tid = threadIdx.x, lane = tid & 63, w = tid >> 6;
  const int h = task >> 2, nq = task & 3, x = lane & 31, hh = lane >> 5;
  dma_tile<256>(lds, Wo + (size_t)h * DH * DM + nq * 128, DH, DM);
  bf16x8 a[4][8];
#pragma unroll
  for (int mt = 0; mt < 4; mt++)
#pragma unroll
    for (int ks = 0; ks < 8; ks++)
      a[mt][ks] = *(const bf16x8*)(Wv + ((size_t)(128 * w + 32 * mt + x) * NH + h) * DH + 16 * ks + 8 * hh);
  dma_wait();
  const __amdgpu_buffer_rsrc_t rd = mkrsrc(ws + OFF_WVO);
#pragma unroll
  for (int nt = 0; nt < 4; nt++) {
    bf16x8 bw[8];
#pragma unroll
    for (int ks = 0; ks < 8; ks++) bw[ks] = frag8(lds, 16 * ks + 8 * hh, 32 * nt + (x & 16), 256, lane);
#pragma unroll
    for (int mt = 0; mt < 4; mt++) {
      f32x16 acc;
#pragma unroll
      for (int i = 0; i < 16; i++) acc[i] = 0.f;
#pragma unroll
      for (int ks = 0; ks < 8; ks++) acc = MFMA32(a[mt][ks], bw[ks], acc);
      const int d = 128 * nq + 32 * nt + x, c = d >> 3, cc = d & 7;
#pragma unroll
      for (int xb = 0; xb < 2; xb++) {
        const int dp = 128 * w + 32 * mt + 16 * xb, t = dp >> 6, ga = (dp >> 4) & 3, z = ga >> 1, gb = 2 * (ga & 1) + hh;
        const int lp = (h & 1) * 8 + cc + 16 * gb;
        u32 o = (u32)(((((c * 4 + (h >> 3)) * 4 + ((h >> 1) & 3)) * 8 + t) * 2 + z) * 64 + lp);
        st16_sc1(PACK_S(acc, 8 * xb), rd, o * 16u);
      }
    }
  }
  asm volatile("s_waitcnt vmcnt(0)" ::: "memory");
  __syncthreads();
  if (tid == 0) st4_sc1(1u, mkrsrc(ws + OFF_FLAG), OFF_WVF + task * 16u);
}

__device__ __forceinline__ void mod_cols(const u16* __restrict__ cvec, const u16* __restrict__ wmod, char* ws, char* lds, int col0) {
  const int tid = threadIdx.x, cp = tid & 15, ksl = tid >> 4;
  float* red = (float*)lds;
  u32 wv[32];
#pragma unroll
  for (int k = 0; k < 32; k++) wv[k] = *(const u32*)(wmod + (size_t)(ksl * 32 + k) * 3072 + col0 + cp * 2);
  float* sl_ = (float*)(lds + 2048);
  __syncthreads();
  {
    u32 cw = *(const u32*)(cvec + tid * 2);
    float c0 = bf_lo(cw), c1 = bf_hi(cw);
    sl_[tid * 2] = bfr(c0 / (1.f + __expf(-c0)));
    sl_[tid * 2 + 1] = bfr(c1 / (1.f + __expf(-c1)));
  }
  __syncthreads();
  float a0 = 0.f, a1 = 0.f;
#pragma unroll
  for (int k4 = 0; k4 < 8; k4++) {
    f32x4 sv = *(const f32x4*)(sl_ + ksl * 32 + k4 * 4);
#pragma unroll
    for (int j = 0; j < 4; j++) { a0 += sv[j] * bf_lo(wv[k4 * 4 + j]); a1 += sv[j] * bf_hi(wv[k4 * 4 + j]); }
  }
  red[ksl * 32 + cp * 2] = a0; red[ksl * 32 + cp * 2 + 1] = a1;
  __syncthreads();
  if (tid < 32) {
    float sacc = 0.f;
#pragma unroll
    for (int k = 0; k < 16; k++) sacc += red[k * 32 + tid];
    st4_sc1(f2u(bfr(sacc)), mkrsrc(ws + OFF_MOD), (u32)(col0 + tid) * 4u);
  }
}

__device__ __forceinline__ float row16_sum(float v) {
  v += u2f((u32)__builtin_amdgcn_mov_dpp((int)f2u(v), 0x128, 0xF, 0xF, true));
  v += u2f((u32)__builtin_amdgcn_mov_dpp((int)f2u(v), 0x124, 0xF, 0xF, true));
  v += u2f((u32)__builtin_amdgcn_mov_dpp((int)f2u(v), 0x122, 0xF, 0xF, true));
  v += u2f((u32)__builtin_amdgcn_mov_dpp((int)f2u(v), 0x121, 0xF, 0xF, true));
  return v;
}

__device__ __forceinline__ bool wait_progress(__amdgpu_buffer_rsrc_t rf, u32 off, u32 need, u32& seen) {
  u32 spins = 0;
  if (__all((int)seen >= (int)need)) return true;
  while (true) {
    asm volatile("" : "+v"(off) :: "memory");
    seen = ld4_sc1(rf, off);
    if (__all((int)seen >= (int)need)) return true;
    __builtin_amdgcn_s_sleep(2);
    if (++spins > 400000u) return false;
  }
}

__device__ __forceinline__ void px_task(char* ws, int s, int hg);
__device__ __forceinline__ void role_help(const u16* __restrict__ x, const u16* __restrict__ w1, const u16* __restrict__ w2,
                                          const u16* __restrict__ cvec, const u16* __restrict__ wmod, const u16* __restrict__ mem0,
                                          char* ws, char* lds, int a0) {
  const int tid = threadIdx.x, lane = tid & 63, w = tid >> 6, n = lane & 15, g = lane >> 4;
  u32x4 xpre[2][2][4];
#pragma unroll
  for (int sg = 0; sg < 2; sg++)
#pragma unroll
    for (int it = 0; it < 2; it++)
#pragma unroll
      for (int j = 0; j < 4; j++) xpre[sg][it][j] = *(const u32x4*)(x + (size_t)((a0 + 32 * sg) * 32 + it * 16 + w * 4 + g) * DM + (j * 16 + n) * 8);
  if (a0 == 31) {
    for (int i = tid; i < 512; i += 256) {
      u32x4 v = *(const u32x4*)(mem0 + (i & 7) * DM + (i >> 3) * 8);
      v.x = sane(v.x); v.y = sane(v.y); v.z = sane(v.z); v.w = sane(v.w);
      st16_sc1(v, mkrsrc(ws + OFF_MEMX), (u32)i * 16u);
    }
  }
  mod_cols(cvec, wmod, ws, lds, a0 * 32);
  asm volatile("s_waitcnt vmcnt(0)" ::: "memory");
  __syncthreads();
  if (tid == 0) st4_sc1(1u, mkrsrc(ws + OFF_FLAG), OFF_MDF + a0 * 16u);
  f32x2 fz[2][2][16];
#pragma unroll
  for (int sg = 0; sg < 2; sg++)
#pragma unroll
    for (int it = 0; it < 2; it++) {
      f32x2* f = fz[sg][it];
#pragma unroll
      for (int j = 0; j < 4; j++) {
        const u32x4 v = xpre[sg][it][j];
        f[4 * j + 0] = (f32x2){bf_lo(v.x), bf_hi(v.x)}; f[4 * j + 1] = (f32x2){bf_lo(v.y), bf_hi(v.y)};
        f[4 * j + 2] = (f32x2){bf_lo(v.z), bf_hi(v.z)}; f[4 * j + 3] = (f32x2){bf_lo(v.w), bf_hi(v.w)};
      }
      f32x2 s2 = {0.f, 0.f};
#pragma unroll
      for (int i = 0; i < 16; i++) s2 += f[i];
      const float m = row16_sum(s2.x + s2.y) * (1.f / DM);
      f32x2 q2 = {0.f, 0.f};
#pragma unroll
      for (int i = 0; i < 16; i++) { f[i] -= m; q2 += f[i] * f[i]; }
      const float rs = rsqrtf(row16_sum(q2.x + q2.y) * (1.f / DM) + 1e-5f);
#pragma unroll
      for (int i = 0; i < 16; i++) f[i] *= rs;
    }
  { u32 seenm = 0xFFFFFFFFu; if (!wait_progress(mkrsrc(ws + OFF_FLAG), OFF_MDF + (lane & 31) * 16u, 1u, seenm)) return; }
  float* modl = (float*)(lds + 8192);
  {
    u32x4 mv = ld16_sc1(mkrsrc(ws + OFF_MOD), (u32)tid * 16u);
    f32x4 mf = {u2f(mv.x), u2f(mv.y), u2f(mv.z), u2f(mv.w)};
    *(f32x4*)(modl + tid * 4) = tid >= 128 ? mf + 1.f : mf;
  }
  __syncthreads();
  const __amdgpu_buffer_rsrc_t rh = mkrsrc(ws + OFF_H);
#pragma unroll
  for (int sg = 0; sg < 2; sg++) {
    const int a = a0 + 32 * sg;
#pragma unroll
    for (int it = 0; it < 2; it++) {
      const f32x2* f = fz[sg][it];
#pragma unroll
      for (int j = 0; j < 4; j++) {
        const int c0 = (j * 16 + n) * 8;
        f32x4 c0v = *(const f32x4*)(modl + 512 + c0), c1v = *(const f32x4*)(modl + 512 + c0 + 4);
        f32x4 s0v = *(const f32x4*)(modl + c0), s1v = *(const f32x4*)(modl + c0 + 4);
        f32x2 o0 = f[4 * j + 0] * (f32x2){c0v[0], c0v[1]} + (f32x2){s0v[0], s0v[1]};
        f32x2 o1 = f[4 * j + 1] * (f32x2){c0v[2], c0v[3]} + (f32x2){s0v[2], s0v[3]};
        f32x2 o2 = f[4 * j + 2] * (f32x2){c1v[0], c1v[1]} + (f32x2){s1v[0], s1v[1]};
        f32x2 o3 = f[4 * j + 3] * (f32x2){c1v[2], c1v[3]} + (f32x2){s1v[2], s1v[3]};
        u32x4 r = {pk2(o0.x, o0.y), pk2(o1.x, o1.y), pk2(o2.x, o2.y), pk2(o3.x, o3.y)};
        st16_sc1(r, rh, (u32)((a * 32 + it * 16 + w * 4 + g) * DM + c0) * 2u);
      }
    }
    asm volatile("s_waitcnt vmcnt(0)" ::: "memory");
    __syncthreads();
    if (tid == 0) st4_sc1(1u, mkrsrc(ws + OFF_FLAG), OFF_DONE + a * 64u);
  }
  const int a = a0;
  mod_cols(cvec, wmod, ws, lds, 1024 + a * 64);
  mod_cols(cvec, wmod, ws, lds, 1024 + a * 64 + 32);
  __syncthreads();
  if (a < 28) {
    dma_tile<128>(lds, w1 + a * 64, DM, FF);
    dma_wait();
    u32x4* dst = (u32x4*)(ws + OFF_W1F);
    for (int ks = 0; ks < 16; ks++) {
      bf16x8 v = frag8(lds, 32 * ks + 8 * (lane >> 4), 16 * w, 128, lane);
      dst[((size_t)(a * 4 + w) * 16 + ks) * 64 + lane] = __builtin_bit_cast(u32x4, v);
    }
  }
  __syncthreads();
  {
    const int cb = a >> 2, kq = a & 3;
    dma_tile<128>(lds, w2 + (size_t)kq * 448 * DM + cb * 64, 448, DM);
    dma_wait();
    u32x4* dst = (u32x4*)(ws + OFF_W2F);
    for (int ks = 0; ks < 14; ks++) {
      u32x2 va = tr4(lds, 32 * ks + 4 * (lane >> 4), 16 * w, 128, lane), vb = tr4(lds, 32 * ks + 16 + 4 * (lane >> 4), 16 * w, 128, lane);
      dst[((size_t)(cb * 4 + w) * 56 + kq * 14 + ks) * 64 + lane] = (u32x4){va.x, va.y, vb.x, vb.y};
    }
  }
  {
    const __amdgpu_buffer_rsrc_t rflag = mkrsrc(ws + OFF_FLAG);
    const int hg = a & 7;
    for (int s = a >> 3; s < NSEG; s += 4) {
      px_task(ws, s, hg);
    }
  }
}

#define AD_DEC 5
#define AD_INC 120
#define ADAPT(D, n) do { if ((n) == 1) D -= AD_DEC; else D += AD_INC; if (D < 0) D = 0; } while (0)

__device__ __forceinline__ void role_score(const u16* __restrict__ Wq, const u16* __restrict__ Wk, char* ws,
                                           char* lds, int h) {
  const int tid = threadIdx.x, lane = tid & 63, w = tid >> 6;
  const int n = lane & 15, gb = lane >> 4;
  bf16x16 wf[4][8];
#pragma unroll
  for (int mat = 0; mat < 2; mat++) {
    __syncthreads();
    dma_tile<256>(lds, (mat ? Wk : Wq) + h * DH, DM, NH * DH);
    dma_wait();
#pragma unroll
    for (int tt = 0; tt < 2; tt++)
#pragma unroll
      for (int t = 0; t < 8; t++) {
        const int col = 32 * w + 16 * tt;
        u32x4 lo = frag8s(lds, 64 * t + 16 * (gb >> 1) + 4 * (gb & 1), col, 256, lane);
        u32x4 hi = frag8s(lds, 64 * t + 16 * (2 + (gb >> 1)) + 4 * (gb & 1), col, 256, lane);
        wf[mat * 2 + tt][t] = as_bf16(lo, hi);
      }
  }
  __syncthreads();
  char* memL = lds;
  char* qs = lds + 8192;
  char* ks_ = lds + 8192 + 2304;
  const int idx = (lane & 8) ? (int)0xEEEEEEEE : 0x44444444;
  const __amdgpu_buffer_rsrc_t rflag = mkrsrc(ws + OFF_FLAG);
  const __amdgpu_buffer_rsrc_t rkx = mkrsrc(ws + OFF_KX);
  const __amdgpu_buffer_rsrc_t rmem = mkrsrc(ws + OFF_MEMX);
  const __amdgpu_buffer_rsrc_t rpex = mkrsrc(ws + OFF_PEX);
  const int rho = lane & 15;
  const int keyp = 8 * ((rho >> 2) & 1) + 4 * (rho >> 3) + (rho & 3);
  const u32 prho = (u32)((n & 7) >> 2) * 10240u, pi = n & 3;
  const u32 pxoff = prho + (u32)(((h >> 2) * 64) + 16 * (2 * (h & 1) + (gb >> 1)) + 8 * (gb & 1) + 4 * ((h >> 1) & 1) + pi) * 16u + (n >> 3) * 8u;
  const u32 pmoff = prho + 8192u + (u32)(h * 4 + pi) * 16u + (gb & 1) * 8u + (gb >> 1) * 4u;
#define DS_OFF 350
#define DC_OFF 180
  int D1 = (w == 0 ? 3400 : (w == 1 ? 3300 : 4050)) - DS_OFF; u32 tref = now32() - 8000u, seen = 0xFFFFFFFFu;
  STAMP(1);
  for (int s = 0; s < NSEG; s++) {
    u32x4 kx[2][4];
    u32 nsw1;
    if (w == 0) {
      if (!wait_progress(rflag, 4096u + h * 128u, s + 1, seen)) return;
#pragma unroll
      for (int tt = 0; tt < 2; tt++)
#pragma unroll
        for (int k4 = 0; k4 < 4; k4++)
          kx[tt][k4] = ld16_sc1(rkx, (u32)(((32 * s + 16 * tt + keyp) * NH + h) * DH + 32 * k4 + 8 * gb) * 2u);
    }
    f32x4 acc[4];
#pragma unroll
    for (int i = 0; i < 4; i++) { acc[i] = (f32x4){0.f, 0.f, 0.f, 0.f}; asm volatile("" : "+v"(acc[i])); }
    u32 pxo = (u32)s * 20480u + pxoff, pmo = (u32)s * 20480u + pmoff;
    asm volatile("" : "+v"(pxo), "+v"(pmo));
    wait_until(tref + D1);
    {
      u32 off[2] = {(u32)s * 8192u + w * 2048u, (u32)s * 8192u + w * 2048u + 1024u};
      u32x4 a[2];
      nsw1 = poll_chunks<2>(rmem, off, a, lane * 16u);
      *(u32x4*)(memL + w * 2048 + lane * 16) = a[0];
      *(u32x4*)(memL + w * 2048 + 1024 + lane * 16) = a[1];
    }
    __syncthreads();
    STAMP(16 + 4 * s);
    bf16x8 af[8];
#pragma unroll
    for (int t = 0; t < 8; t++) af[t] = *(const bf16x8*)(memL + t * 1024 + lane * 16);
    __builtin_amdgcn_sched_barrier(0);
    const int ecol = 32 * w + 16 * (lane >> 5) + n, er0 = 4 * ((lane >> 4) & 1);
#pragma unroll
    for (int t = 0; t < 8; t++) { acc[0] = SMF(af[t], wf[0][t], acc[0], idx); acc[1] = SMF(af[t], wf[1][t], acc[1], idx); }
    __builtin_amdgcn_sched_barrier(0);
#pragma unroll
    for (int t = 0; t < 8; t++) {
      acc[2] = SMF(af[t], wf[2][t], acc[2], idx); acc[3] = SMF(af[t], wf[3][t], acc[3], idx);
      if (t >= 2 && t < 6) {
        const int j = t - 2;
        auto r = __builtin_amdgcn_permlane32_swap(f2u(acc[0][j]), f2u(acc[1][j]), false, false);
        *(__bf16*)(qs + (er0 + j) * 272 + ecol * 2) = (__bf16)(u2f(r[0]) + u2f(r[1]));
      }
      __builtin_amdgcn_sched_barrier(0);
    }
#pragma unroll
    for (int j = 0; j < 4; j++) {
      auto r = __builtin_amdgcn_permlane32_swap(f2u(acc[2][j]), f2u(acc[3][j]), false, false);
      *(__bf16*)(ks_ + (er0 + j) * 272 + ecol * 2) = (__bf16)(u2f(r[0]) + u2f(r[1]));
    }
    __syncthreads();
    STAMP(17 + 4 * s);
    if (w == 0 && s < NSEG - 1) {
      f32x4 sc[3];
#pragma unroll
      for (int i = 0; i < 3; i++) { sc[i] = (f32x4){0.f, 0.f, 0.f, 0.f}; asm volatile("" : "+v"(sc[i])); }
      bf16x8 qf[4], kf[4];
#pragma unroll
      for (int k4 = 0; k4 < 4; k4++) {
        qf[k4] = *(const bf16x8*)(qs + (lane & 7) * 272 + (32 * k4 + 8 * gb) * 2);
        kf[k4] = *(const bf16x8*)(ks_ + (lane & 7) * 272 + (32 * k4 + 8 * gb) * 2);
      }
      __builtin_amdgcn_sched_barrier(0);
#pragma unroll
      for (int k4 = 0; k4 < 4; k4++) {
        asm volatile("s_nop 1\n\tv_mfma_f32_16x16x32_bf16 %0, %3, %6, %0\n\tv_mfma_f32_16x16x32_bf16 %1, %4, %6, %1\n\tv_mfma_f32_16x16x32_bf16 %2, %5, %6, %2"
                     : "+v"(sc[0]), "+v"(sc[1]), "+v"(sc[2]) : "v"(kx[0][k4]), "v"(kx[1][k4]), "v"(kf[k4]), "v"(qf[k4]));
      }
      asm volatile("s_nop 7\n\ts_nop 3" : "+v"(sc[0]), "+v"(sc[1]), "+v"(sc[2]));
      const float sl = 0.08838834764831845f * 1.4426950408889634f;
      const bool hiq = n >= 8, hig = gb >= 2;
      float p[6];
#pragma unroll
      for (int j = 0; j < 4; j++) p[j] = hiq ? sc[1][j] : sc[0][j];
      p[4] = hig ? sc[2][2] : sc[2][0];
      p[5] = hig ? sc[2][3] : sc[2][1];
      float mx = max2r(max3r(p[0], p[1], p[2]), max3r(p[3], p[4], p[5]));
      mx = max2r(mx, u2f((u32)__builtin_amdgcn_mov_dpp((int)f2u(mx), 0x128, 0xF, 0xF, true)));
      { auto r = __builtin_amdgcn_permlane16_swap(f2u(mx), f2u(mx), false, false);
        mx = max2r(u2f(r[0]), u2f(r[1])); }
      { auto r = __builtin_amdgcn_permlane32_swap(f2u(mx), f2u(mx), false, false);
        mx = max2r(u2f(r[0]), u2f(r[1])); }
      const float nb = -mx * sl;
#pragma unroll
      for (int j = 0; j < 6; j++) p[j] = __builtin_amdgcn_exp2f(__builtin_fmaf(p[j], sl, nb));
      float sum = (p[0] + p[1]) + (p[2] + p[3]) + (hiq ? 0.f : (p[4] + p[5]));
      sum += u2f((u32)__builtin_amdgcn_mov_dpp((int)f2u(sum), 0x128, 0xF, 0xF, true));
      { auto r = __builtin_amdgcn_permlane16_swap(f2u(sum), f2u(sum), false, false);
        sum = u2f(r[0]) + u2f(r[1]); }
      { auto r = __builtin_amdgcn_permlane32_swap(f2u(sum), f2u(sum), false, false);
        sum = u2f(r[0]) + u2f(r[1]); }
      const float inv = __builtin_amdgcn_rcpf(sum);
      u32x2 px = {sane(pk2(p[0] * inv, p[1] * inv)), sane(pk2(p[2] * inv, p[3] * inv))};
      st8_sc1(px, rpex, pxo);
      if (!hiq) st4_sc1(sane(pk2(p[4] * inv, p[5] * inv)), rpex, pmo);
      STAMP(18 + 4 * s);
    } else if (w == 1) {
      const __amdgpu_buffer_rsrc_t rkm = mkrsrc(ws + OFF_KMEM);
#pragma unroll
      for (int j = 0; j < 2; j++) {
        int u = j * 64 + lane, r = u >> 4, c = u & 15;
        st16_sc1(*(const u32x4*)(ks_ + r * 272 + c * 16), rkm, (u32)((s * NH + h) * 1024 + r * 128 + c * 8) * 2u);
      }
      asm volatile("s_waitcnt vmcnt(0)" ::: "memory");
      if (lane == 0) st4_sc1((u32)(s + 1), rflag, OFF_KMF + h * 64u);
    }
    tref = now32();
    if (!nsw1) return;
    if (s > 2) ADAPT(D1, nsw1);
  }
}

__device__ __forceinline__ void role_column(const u16* __restrict__ Wv, const u16* __restrict__ Wo, char* ws, char* lds, int c, int rho) {
  const int tid = threadIdx.x, lane = tid & 63, w = tid >> 6;
  const int n = lane & 15, gb = lane >> 4, cc = lane & 7;
  bf16x16 wf[4][8];
  wvo_task(Wv, Wo, ws, lds, c * 2 + rho);
  {
    u32 seenw = 0xFFFFFFFFu;
    if (!wait_progress(mkrsrc(ws + OFF_FLAG), OFF_WVF + (u32)((lane & 31) * 4 + (c >> 4)) * 16u, 1u, seenw)) return;
    const __amdgpu_buffer_rsrc_t rsrc = mkrsrc(ws + OFF_WVO + (size_t)(c * 4 + w) * 65536);
#pragma unroll
    for (int tl = 0; tl < 4; tl++)
#pragma unroll
      for (int t = 0; t < 8; t++)
        wf[tl][t] = as_bf16(ld16_sc1(rsrc, (u32)(((tl * 8 + t) * 2) * 64 + lane) * 16u), ld16_sc1(rsrc, (u32)(((tl * 8 + t) * 2 + 1) * 64 + lane) * 16u));
  }
  char* memL = lds;
  char* yl = lds + 8192 + w * 1024;
  float* red = (float*)(lds + 12288);
  const int idx = (lane & 8) ? (int)0xEEEEEEEE : 0x44444444;
  const __amdgpu_buffer_rsrc_t rflag = mkrsrc(ws + OFF_FLAG);
  const __amdgpu_buffer_rsrc_t rmem = mkrsrc(ws + OFF_MEMX);
  const __amdgpu_buffer_rsrc_t rpex = mkrsrc(ws + OFF_PEX);
  int D1 = w == 0 ? 750 : (w == 1 ? 850 : 1130), D2 = 2800 - DC_OFF; u32 tref = now32(), seen = 0xFFFFFFFFu;
  STAMP(1);
  for (int s = 0; s < NSEG; s++) {
    wait_until(tref + D1);
    {
      u32 off[2] = {(u32)s * 8192u + w * 2048u, (u32)s * 8192u + w * 2048u + 1024u};
      u32x4 a[2];
      u32 nsw = poll_chunks<2>(rmem, off, a, lane * 16u);
      if (!nsw) return;
      if (s > 2) ADAPT(D1, nsw);
      tref = now32();
      *(u32x4*)(memL + w * 2048 + lane * 16) = a[0];
      *(u32x4*)(memL + w * 2048 + 1024 + lane * 16) = a[1];
    }
    u32x4 uf[4][2];
    {
      if (!wait_progress(rflag, (u32)(8 * w + (lane & 7)) * 128u, s + 1, seen)) return;
      const __amdgpu_buffer_rsrc_t ru = mkrsrc(ws + OFF_UF + ((size_t)s * 64 + c) * 16384);
#pragma unroll
      for (int m = 0; m < 4; m++)
#pragma unroll
        for (int z = 0; z < 2; z++) uf[m][z] = ld16_sc1(ru, (u32)((((4 * w + m) * 2 + z) * 4 + gb) * 8 + cc) * 16u);
    }
    __syncthreads();
    STAMP(16 + 4 * s);
    bf16x16 yb;
    {
      f32x4 acc[4];
#pragma unroll
      for (int i = 0; i < 4; i++) acc[i] = (f32x4){0.f, 0.f, 0.f, 0.f};
      bf16x8 af[8];
#pragma unroll
      for (int t = 0; t < 8; t++) af[t] = *(const bf16x8*)(memL + t * 1024 + lane * 16);
      __builtin_amdgcn_sched_barrier(0);
#pragma unroll
      for (int t = 0; t < 8; t++)
#pragma unroll
        for (int i = 0; i < 4; i++) acc[i] = SMF(af[t], wf[i][t], acc[i], idx);
#pragma unroll
      for (int pr = 0; pr < 2; pr++) {
        float v[4];
#pragma unroll
        for (int j = 0; j < 4; j++) {
          auto r = __builtin_amdgcn_permlane32_swap(f2u(acc[2 * pr][j]), f2u(acc[2 * pr + 1][j]), false, false);
          v[j] = u2f(r[0]) + u2f(r[1]);
        }
        const int hl = 2 * (2 * pr + (lane >> 5)) + (n >> 3);
        u32x2 pv = {pk2(v[0], v[1]), pk2(v[2], v[3])};
        *(u32x2*)(yl + (hl * 8 + cc) * 16 + 8 * ((lane >> 4) & 1)) = pv;
      }
      asm volatile("s_waitcnt lgkmcnt(0)" ::: "memory");
      u32x4 hz[2];
#pragma unroll
      for (int z = 0; z < 2; z++) {
        const int ga = 2 * z + (gb >> 1);
        u32x2 a = *(const u32x2*)(yl + ((2 * ga) * 8 + cc) * 16 + 8 * (gb & 1));
        u32x2 b = *(const u32x2*)(yl + ((2 * ga + 1) * 8 + cc) * 16 + 8 * (gb & 1));
        hz[z] = (u32x4){a.x, b.x, a.y, b.y};
      }
      yb = as_bf16(hz[0], hz[1]);
      if (rho == 0) __builtin_amdgcn_raw_buffer_store_b128(*(const u32x4*)(yl + lane * 16), mkrsrc(ws + OFF_Y), (u32)(s * 64 + c) * 4096u + w * 1024u + lane * 16u, 0, 0);
    }
    STAMP(17 + 4 * s);
    if (s == NSEG - 1) break;
    bf16x16 ub16[4];
#pragma unroll
    for (int m = 0; m < 4; m++) { ub16[m] = as_bf16(uf[m][0], uf[m][1]); asm volatile("" : "+v"(ub16[m])); }
    asm volatile("" : "+v"(yb));
    f32x4 dA = {0.f, 0.f, 0.f, 0.f}, dB = {0.f, 0.f, 0.f, 0.f};
    u32 rdw = (u32)(12288 + ((s & 1) * 512 + cc * 16 + 4 * w + gb) * 4), rdr = (u32)(12288 + ((s & 1) * 512 + lane * 8) * 4);
    u32 mso = (u32)(s + 1) * 8192u + c * 128u + rho * 64u + (lane >> 2) * 4u;
    asm volatile("" : "+v"(dA), "+v"(dB), "+v"(rdw), "+v"(rdr), "+v"(mso));
    u32 nsw2;
    wait_until(tref + D2);
    {
      const u32 pb = (u32)s * 20480u + rho * 10240u;
      u32 off[3] = {pb + (2 * w) * 1024u + lane * 16u, pb + (2 * w + 1) * 1024u + lane * 16u,
                    pb + 8192u + (u32)((8 * w + 2 * gb + ((lane >> 3) & 1)) * 4 + (lane & 3)) * 16u};
      u32x4 g[3];
      nsw2 = poll_chunks<3>(rpex, off, g, 0u);
      dA = SMF(as_bf8(g[0]), ub16[0], dA, idx);
      dB = SMF(as_bf8(g[0]), ub16[1], dB, idx);
      dA = SMF(as_bf8(g[1]), ub16[2], dA, idx);
      dB = SMF(as_bf8(g[1]), ub16[3], dB, idx);
      dA = SMF(as_bf8(g[2]), yb, dA, idx);
      if (n < 8) {
#pragma unroll
        for (int j = 0; j < 4; j++) *(float*)(lds + rdw + j * 512) = (gb & 1) ? dB[j] : dA[j];
      }
    }
    __syncthreads();
    STAMP(18 + 4 * s);
    if (w == 0) {
      f32x4 a = *(const f32x4*)(lds + rdr), b = *(const f32x4*)(lds + rdr + 16);
      float v = ((a[0] + a[1]) + (a[2] + a[3])) + ((b[0] + b[1]) + (b[2] + b[3]));
      v += u2f((u32)__builtin_amdgcn_mov_dpp((int)f2u(v), 0xB1, 0xF, 0xF, true));
      float o = u2f((u32)__builtin_amdgcn_mov_dpp((int)f2u(v), 0x4E, 0xF, 0xF, true));
      if (!(lane & 3)) st4_sc1(sane(pk2(v, o)), rmem, mso);
      STAMP(19 + 4 * s);
    }
    tref = now32();
    if (!nsw2) return;
    if (s > 2) ADAPT(D2, nsw2);
  }
}

__device__ __forceinline__ void load_h(u32x4 (&r)[8], const char* ws, int s) {
  const __amdgpu_buffer_rsrc_t rh = mkrsrc(ws + OFF_H);
#pragma unroll
  for (int j = 0; j < 8; j++) r[j] = ld16_sc1(rh, (u32)s * 32768u + (j * 256 + threadIdx.x) * 16u);
}
__device__ __forceinline__ void put_h(char* dst, const u32x4 (&r)[8]) {
#pragma unroll
  for (int j = 0; j < 8; j++) {
    int u = j * 256 + threadIdx.x;
    *(u32x4*)(dst + (u >> 6) * 1040 + (u & 63) * 16) = r[j];
  }
}

template <bool SC1>
__device__ __forceinline__ void store_T_tile(const f32x16& acc, char* base, int tokrow, int h, int w, int lane) {
  const __amdgpu_buffer_rsrc_t r = mkrsrc(base);
#pragma unroll
  for (int qp = 0; qp < 2; qp++) {
    u32 ax = pk2(acc[8 * qp + 0], acc[8 * qp + 1]), ay = pk2(acc[8 * qp + 2], acc[8 * qp + 3]);
    u32 bx = pk2(acc[8 * qp + 4], acc[8 * qp + 5]), by = pk2(acc[8 * qp + 6], acc[8 * qp + 7]);
    auto r0 = __builtin_amdgcn_permlane32_swap(ax, bx, false, false);
    auto r1 = __builtin_amdgcn_permlane32_swap(ay, by, false, false);
    u32x4 v = {(u32)r0[0], (u32)r1[0], (u32)r0[1], (u32)r1[1]};
    u32 off = (u32)(((tokrow + (lane & 31)) * NH + h) * DH + 32 * w + 16 * qp + 8 * (lane >> 5)) * 2u;
    if (SC1) __builtin_amdgcn_raw_buffer_store_b128(v, r, off, 0, 16);
    else __builtin_amdgcn_raw_buffer_store_b128(v, r, off, 0, 0);
  }
}

__device__ __forceinline__ void role_vu(const u16* __restrict__ Wv, const u16* __restrict__ Wo,
                                        char* ws, char* lds, int h) {
  const int tid = threadIdx.x, lane = tid & 63, w = tid >> 6, x = lane & 31, hh = lane >> 5;
  bf16x8 wv[32], wo[4][8];
  dma_tile<256>(lds, Wv + h * DH, DM, NH * DH);
  dma_wait();
#pragma unroll
  for (int ks = 0; ks < 32; ks++) wv[ks] = frag8(lds, 16 * ks + 8 * hh, 32 * w + (x & 16), 256, lane);
  __syncthreads();
  dma_tile<1024>(lds, Wo + (size_t)h * DH * DM, DH, DM);
  dma_wait();
#pragma unroll
  for (int nt = 0; nt < 4; nt++)
#pragma unroll
    for (int ks = 0; ks < 8; ks++) wo[nt][ks] = frag8(lds, 16 * ks + 8 * hh, 128 * w + 32 * nt + (x & 16), 1024, lane);
  __syncthreads();
  char* vs = lds + 66560;
  const __amdgpu_buffer_rsrc_t rflag = mkrsrc(ws + OFF_FLAG);
  u32x4 hr[8];
  { u32 seenh = 0xFFFFFFFFu; if (!wait_progress(rflag, OFF_DONE + (lane & 31) * 64u, 1u, seenh)) return; }
  load_h(hr, ws, 0);
  put_h(lds, hr);
  load_h(hr, ws, 1);
  __syncthreads();
  for (int s = 0; s < NSEG; s++) {
    const char* hb = lds + (s & 1) * 33280;
    f32x16 av;
#pragma unroll
    for (int i = 0; i < 16; i++) av[i] = 0.f;
    {
      bf16x8 hf[2][8];
#pragma unroll
      for (int k = 0; k < 8; k++) hf[0][k] = *(const bf16x8*)(hb + x * 1040 + (16 * k + 8 * hh) * 2);
#pragma unroll
      for (int ch = 0; ch < 4; ch++) {
        if (ch < 3) {
#pragma unroll
          for (int k = 0; k < 8; k++) hf[(ch + 1) & 1][k] = *(const bf16x8*)(hb + x * 1040 + (16 * (8 * ch + 8 + k) + 8 * hh) * 2);
        }
        __builtin_amdgcn_sched_barrier(0);
#pragma unroll
        for (int k = 0; k < 8; k++) av = MFMA32(wv[8 * ch + k], hf[ch & 1][k], av);
        __builtin_amdgcn_sched_barrier(0);
      }
    }
#pragma unroll
    for (int q = 0; q < 4; q++) {
      u32x2 pv = {pk2(av[4 * q], av[4 * q + 1]), pk2(av[4 * q + 2], av[4 * q + 3])};
      *(u32x2*)(vs + x * 272 + (32 * w + 8 * q + 4 * hh) * 2) = pv;
    }
    put_h(lds + ((s + 1) & 1) * 33280, hr);
    asm volatile("s_waitcnt vmcnt(0)" ::: "memory");
    __syncthreads();
    if (tid == 0 && s > 4) st4_sc1((u32)s, rflag, h * 128u);
    bf16x8 vf[8];
#pragma unroll
    for (int ks = 0; ks < 8; ks++) vf[ks] = *(const bf16x8*)(vs + x * 272 + (16 * ks + 8 * hh) * 2);
    const __amdgpu_buffer_rsrc_t ru = mkrsrc(ws + OFF_UF + (size_t)s * 64 * 16384);
#pragma unroll
    for (int nt = 0; nt < 4; nt++) {
      f32x16 au;
#pragma unroll
      for (int i = 0; i < 16; i++) au[i] = 0.f;
#pragma unroll
      for (int ks = 0; ks < 8; ks++) au = MFMA32(vf[ks], wo[nt][ks], au);
      const int d = 128 * w + 32 * nt + x, c = d >> 3, cc = d & 7;
#pragma unroll
      for (int xb = 0; xb < 2; xb++)
        st16_sc1(PACK_S(au, 8 * xb), ru, (u32)((((c * 16 + (h >> 1)) * 2 + (h & 1)) * 4 + 2 * hh + xb) * 8 + cc) * 16u);
    }
    if (s < 4 || s == NSEG - 1) asm volatile("s_waitcnt vmcnt(0)" ::: "memory");
    if (s == 30) { u32 seenh = 0xFFFFFFFFu; if (!wait_progress(rflag, OFF_DONE + lane * 64u, 1u, seenh)) return; }
    load_h(hr, ws, s + 2 < NSEG ? s + 2 : NSEG - 1);
    __syncthreads();
    if (tid == 0 && (s < 4 || s == NSEG - 1)) st4_sc1((u32)(s + 1), rflag, h * 128u);
    STAMP(16 + 4 * s);
  }
}

__device__ __forceinline__ void role_qk(const u16* __restrict__ Wq, const u16* __restrict__ Wk, char* ws, char* lds, int h) {
  const int tid = threadIdx.x, lane = tid & 63, w = tid >> 6, x = lane & 31, hh = lane >> 5;
  bf16x8 wq[32], wk[32];
  dma_tile<256>(lds, Wk + h * DH, DM, NH * DH);
  dma_wait();
#pragma unroll
  for (int ks = 0; ks < 32; ks++) wk[ks] = frag8(lds, 16 * ks + 8 * hh, 32 * w + (x & 16), 256, lane);
  __syncthreads();
  dma_tile<256>(lds, Wq + h * DH, DM, NH * DH);
  dma_wait();
#pragma unroll
  for (int ks = 0; ks < 32; ks++) wq[ks] = frag8(lds, 16 * ks + 8 * hh, 32 * w + (x & 16), 256, lane);
  __syncthreads();
  const __amdgpu_buffer_rsrc_t rflag = mkrsrc(ws + OFF_FLAG);
  u32x4 hr[8];
  { u32 seenh = 0xFFFFFFFFu; if (!wait_progress(rflag, OFF_DONE + (lane & 31) * 64u, 1u, seenh)) return; }
  load_h(hr, ws, 0);
  put_h(lds, hr);
  load_h(hr, ws, 1);
  __syncthreads();
  for (int s = 0; s < NSEG; s++) {
    const char* hb = lds + (s & 1) * 33280;
    f32x16 aq, ak;
#pragma unroll
    for (int i = 0; i < 16; i++) { aq[i] = 0.f; ak[i] = 0.f; }
    {
      bf16x8 hf[2][8];
#pragma unroll
      for (int k = 0; k < 8; k++) hf[0][k] = *(const bf16x8*)(hb + x * 1040 + (16 * k + 8 * hh) * 2);
#pragma unroll
      for (int ch = 0; ch < 4; ch++) {
        if (ch < 3) {
#pragma unroll
          for (int k = 0; k < 8; k++) hf[(ch + 1) & 1][k] = *(const bf16x8*)(hb + x * 1040 + (16 * (8 * ch + 8 + k) + 8 * hh) * 2);
        }
        __builtin_amdgcn_sched_barrier(0);
#pragma unroll
        for (int k = 0; k < 8; k++) { ak = MFMA32(wk[8 * ch + k], hf[ch & 1][k], ak); aq = MFMA32(wq[8 * ch + k], hf[ch & 1][k], aq); }
        __builtin_amdgcn_sched_barrier(0);
      }
    }
    put_h(lds + ((s + 1) & 1) * 33280, hr);
    asm volatile("s_waitcnt vmcnt(0)" ::: "memory");
    __syncthreads();
    if (tid == 0 && s > 4) st4_sc1((u32)s, rflag, 4096u + h * 128u);
    store_T_tile<true>(ak, ws + OFF_KX, 32 * s, h, w, lane);
    store_T_tile<true>(aq, ws + OFF_QX, 32 * s, h, w, lane);
    if (s < 4 || s == NSEG - 1) {
      asm volatile("s_waitcnt vmcnt(0)" ::: "memory");
      __syncthreads();
      if (tid == 0) st4_sc1((u32)(s + 1), rflag, 4096u + h * 128u);
    }
    if (s == 30) { u32 seenh = 0xFFFFFFFFu; if (!wait_progress(rflag, OFF_DONE + lane * 64u, 1u, seenh)) return; }
    load_h(hr, ws, s + 2 < NSEG ? s + 2 : NSEG - 1);
    STAMP(16 + 4 * s);
  }
}

__device__ __forceinline__ float xg_max(float v) {
  auto r = __builtin_amdgcn_permlane16_swap(f2u(v), f2u(v), false, false);
  v = fmaxf(u2f(r[0]), u2f(r[1]));
  auto r2 = __builtin_amdgcn_permlane32_swap(f2u(v), f2u(v), false, false);
  return fmaxf(u2f(r2[0]), u2f(r2[1]));
}
__device__ __forceinline__ float xg_sum(float v) {
  auto r = __builtin_amdgcn_permlane16_swap(f2u(v), f2u(v), false, false);
  v = u2f(r[0]) + u2f(r[1]);
  auto r2 = __builtin_amdgcn_permlane32_swap(f2u(v), f2u(v), false, false);
  return u2f(r2[0]) + u2f(r2[1]);
}
__device__ __forceinline__ void px_task(char* ws, int s, int hg) {
  const int tid = threadIdx.x, lane = tid & 63, w = tid >> 6;
  const int hd = hg * 4 + w, hl = w;
  const int n = lane & 15, g = lane >> 4;
  const __amdgpu_buffer_rsrc_t rq = mkrsrc(ws + OFF_QX + (size_t)s * 32 * 8192);
  const __amdgpu_buffer_rsrc_t rk = mkrsrc(ws + OFF_KX + (size_t)s * 32 * 8192);
  const __amdgpu_buffer_rsrc_t rkm = mkrsrc(ws + OFF_KMEM + (size_t)s * 65536);
  const int keyrow = 16 * ((n >> 2) & 1) + 4 * (n >> 3) + (n & 3);
  const int keyb = 16 * (g & 1) + 4 * (g >> 1);
  const float sl = 0.08838834764831845f * 1.4426950408889634f;
  bf16x8 qf[2][4], kf[2][4], mf[4];
  const __amdgpu_buffer_rsrc_t rflag = mkrsrc(ws + OFF_FLAG);
  { u32 seenq = 0xFFFFFFFFu; wait_progress(rflag, 4096u + hd * 128u, s + 1, seenq); }
#pragma unroll
  for (int ks = 0; ks < 4; ks++) {
#pragma unroll
    for (int t = 0; t < 2; t++) {
      qf[t][ks] = as_bf8(ld16_sc1(rq, (u32)(((16 * t + n) * NH + hd) * DH + 32 * ks + 8 * g) * 2u));
      kf[t][ks] = as_bf8(ld16_sc1(rk, (u32)(((8 * t + keyrow) * NH + hd) * DH + 32 * ks + 8 * g) * 2u));
    }
  }
  {
    u32 spins = 0, off = OFF_KMF + (u32)hd * 64u;
    while (true) {
      asm volatile("" : "+v"(off) :: "memory");
      u32 v = ld4_sc1(rflag, off);
      if ((int)v > s) break;
      __builtin_amdgcn_s_sleep(6);
      if (++spins > 800000u) break;
    }
  }
#pragma unroll
  for (int ks = 0; ks < 4; ks++) mf[ks] = as_bf8(ld16_sc1(rkm, (u32)(hd * 1024 + (n & 7) * 128 + 32 * ks + 8 * g) * 2u));
  const bool mvalid = (g >> 1) == (hl >> 1);
  u32x4* PXx = (u32x4*)(ws + OFF_PX);
  char* PXm = ws + OFF_PX + (4ull << 20);
#pragma unroll
  for (int qt = 0; qt < 2; qt++) {
    f32x4 sc[3];
#pragma unroll
    for (int i = 0; i < 3; i++) sc[i] = (f32x4){0.f, 0.f, 0.f, 0.f};
#pragma unroll
    for (int ks = 0; ks < 4; ks++) {
      sc[0] = MFMA16(kf[0][ks], qf[qt][ks], sc[0]);
      sc[1] = MFMA16(kf[1][ks], qf[qt][ks], sc[1]);
      sc[2] = MFMA16(mf[ks], qf[qt][ks], sc[2]);
    }
    const int qi = 16 * qt + n;
    float mx = -3.0e38f;
#pragma unroll
    for (int j = 0; j < 4; j++) {
      if (keyb + j > qi) sc[0][j] = -3.0e38f;
      if (keyb + 8 + j > qi) sc[1][j] = -3.0e38f;
      if (!mvalid) sc[2][j] = -3.0e38f;
      mx = fmaxf(mx, fmaxf(fmaxf(sc[0][j], sc[1][j]), sc[2][j]));
    }
    mx = xg_max(mx);
    float sum = 0.f;
#pragma unroll
    for (int i = 0; i < 3; i++)
#pragma unroll
      for (int j = 0; j < 4; j++) {
        float p = sc[i][j] > -1.0e38f ? __builtin_amdgcn_exp2f((sc[i][j] - mx) * sl) : 0.f;
        sc[i][j] = p; sum += p;
      }
    const float inv = __builtin_amdgcn_rcpf(xg_sum(sum));
    u32x4 pa = {pk2(sc[0][0] * inv, sc[0][1] * inv), pk2(sc[1][0] * inv, sc[1][1] * inv),
                pk2(sc[0][2] * inv, sc[0][3] * inv), pk2(sc[1][2] * inv, sc[1][3] * inv)};
    PXx[(((size_t)s * NH + hd) * 2 + qt) * 64 + lane] = pa;
    if (mvalid) {
      u32x2 pm = {pk2(sc[2][0] * inv, sc[2][1] * inv), pk2(sc[2][2] * inv, sc[2][3] * inv)};
      *(u32x2*)(PXm + ((((size_t)s * 8 + hg) * 2 + qt) * 64 + lane) * 16 + (hl & 1) * 8) = pm;
    }
  }
}

__global__ void __launch_bounds__(256, 1) k2_rec(const u16* __restrict__ Wq, const u16* __restrict__ Wk, const u16* __restrict__ Wv,
                                                  const u16* __restrict__ Wo, const u16* __restrict__ x, const u16* __restrict__ w1,
                                                  const u16* __restrict__ w2, const u16* __restrict__ cvec,
                                                  const u16* __restrict__ wmod, const u16* __restrict__ mem0, char* ws) {
  extern __shared__ __attribute__((aligned(16))) char lds[];
  const int b = blockIdx.x;
  STAMP(0);
  if (b < 32) role_help(x, w1, w2, cvec, wmod, mem0, ws, lds, b);
  else if (b < 64) role_score(Wq, Wk, ws, lds, b - 32);
  else if (b < 192) { const int q = b - 64; role_column(Wv, Wo, ws, lds, (q & 7) | ((q >> 4) << 3), (q >> 3) & 1); }
  else if (b < 224) role_vu(Wv, Wo, ws, lds, b - 192);
  else role_qk(Wq, Wk, ws, lds, b - 224);
}

__global__ void __launch_bounds__(512, 2) k3b_oproj(const u16* __restrict__ x, char* __restrict__ ws) {
  extern __shared__ __attribute__((aligned(16))) char lds[];
  const int b = blockIdx.x, tid = threadIdx.x, lane = tid & 63, w = tid >> 6;
  const int s = (b & 7) * 8 + (b >> 5), nq = (b >> 3) & 3;
  const int n = lane & 15, g = lane >> 4, cc = lane & 7;
  const u32x4* UF = (const u32x4*)(ws + OFF_UF) + (size_t)s * 64 * 1024;
  const char* Yg = ws + OFF_Y + (size_t)s * 64 * 4096;
  const u32x4* PXx = (const u32x4*)(ws + OFF_PX) + (size_t)s * NH * 128;
  const u32x4* PXm = (const u32x4*)(ws + OFF_PX + (4ull << 20)) + (size_t)s * 8 * 128;
  f32x4 acc[2][8];
#pragma unroll
  for (int i = 0; i < 2; i++)
#pragma unroll
    for (int j = 0; j < 8; j++) acc[i][j] = (f32x4){0.f, 0.f, 0.f, 0.f};
  u32x4 ub[4][8], pa[4][2], pm[2], yf[8];
#pragma unroll
  for (int hl = 0; hl < 4; hl++) {
    const int hd = 4 * w + hl;
#pragma unroll
    for (int nt = 0; nt < 8; nt++) ub[hl][nt] = __builtin_nontemporal_load(UF + (size_t)(16 * nq + 2 * nt + (n >> 3)) * 1024 + (hd * 4 + g) * 8 + cc);
    pa[hl][0] = PXx[(hd * 2) * 64 + lane];
    pa[hl][1] = PXx[(hd * 2 + 1) * 64 + lane];
  }
  pm[0] = PXm[(w * 2) * 64 + lane];
  pm[1] = PXm[(w * 2 + 1) * 64 + lane];
  {
    const int ha = 4 * w + 2 * (g >> 1);
#pragma unroll
    for (int nt = 0; nt < 8; nt++) {
      const int c = 16 * nq + 2 * nt + (n >> 3);
      u32x2 ya = __builtin_nontemporal_load((const u32x2*)(Yg + (size_t)c * 4096 + (ha * 8 + cc) * 16 + 8 * (g & 1)));
      u32x2 yb = __builtin_nontemporal_load((const u32x2*)(Yg + (size_t)c * 4096 + ((ha + 1) * 8 + cc) * 16 + 8 * (g & 1)));
      yf[nt] = (u32x4){ya.x, ya.y, yb.x, yb.y};
    }
  }
  __builtin_amdgcn_sched_barrier(0);
#pragma unroll
  for (int hl = 0; hl < 4; hl++)
#pragma unroll
    for (int nt = 0; nt < 8; nt++) {
      acc[0][nt] = MFMA16(as_bf8(pa[hl][0]), as_bf8(ub[hl][nt]), acc[0][nt]);
      acc[1][nt] = MFMA16(as_bf8(pa[hl][1]), as_bf8(ub[hl][nt]), acc[1][nt]);
    }
#pragma unroll
  for (int nt = 0; nt < 8; nt++) {
    acc[0][nt] = MFMA16(as_bf8(pm[0]), as_bf8(yf[nt]), acc[0][nt]);
    acc[1][nt] = MFMA16(as_bf8(pm[1]), as_bf8(yf[nt]), acc[1][nt]);
  }
  float* red = (float*)lds;
#pragma unroll
  for (int qt = 0; qt < 2; qt++)
#pragma unroll
    for (int nt = 0; nt < 8; nt++)
#pragma unroll
      for (int j = 0; j < 4; j++) red[(w * 32 + 16 * qt + 4 * g + j) * 132 + 16 * nt + n] = acc[qt][nt][j];
  __syncthreads();
  const float* mod = (const float*)(ws + OFF_MOD);
  u16* x1 = (u16*)(ws + OFF_X1);
  {
    const int r = tid >> 4, c8 = (tid & 15) * 8;
    float o[8];
#pragma unroll
    for (int j = 0; j < 8; j++) o[j] = 0.f;
#pragma unroll
    for (int ww = 0; ww < 8; ww++) {
      f32x4 a = *(const f32x4*)(red + (ww * 32 + r) * 132 + c8), bq = *(const f32x4*)(red + (ww * 32 + r) * 132 + c8 + 4);
#pragma unroll
      for (int j = 0; j < 4; j++) { o[j] += a[j]; o[4 + j] += bq[j]; }
    }
    const int col = 128 * nq + c8;
    const size_t go = (size_t)(32 * s + r) * DM + col;
    u32x4 xv = *(const u32x4*)(x + go);
    float xf[8] = {bf_lo(xv.x), bf_hi(xv.x), bf_lo(xv.y), bf_hi(xv.y), bf_lo(xv.z), bf_hi(xv.z), bf_lo(xv.w), bf_hi(xv.w)};
#pragma unroll
    for (int j = 0; j < 8; j++) o[j] = xf[j] + bfr(mod[1024 + col + j] * bfr(o[j]));
    u32x4 rv = {pk2(o[0], o[1]), pk2(o[2], o[3]), pk2(o[4], o[5]), pk2(o[6], o[7])};
    *(u32x4*)(x1 + go) = rv;
  }
}

__global__ void __launch_bounds__(512, 2) k4_mlp1(const u16* __restrict__ b1, char* __restrict__ ws) {
  extern __shared__ __attribute__((aligned(16))) char lds[];
  const int b = blockIdx.x, tid = threadIdx.x, lane = tid & 63, w = tid >> 6;
  const int jq = b >> 3, mt0 = (b & 7) * 4 + jq / 7, nb = jq % 7;
  const float* mod = (const float*)(ws + OFF_MOD);
  const u16* x1 = (const u16*)(ws + OFF_X1);
  const int n = lane & 15, g = lane >> 4;
  const f32x4 mv = *(const f32x4*)(mod + 1536 + (tid & 255) * 4);
  u32x4 xv[2][4];
#pragma unroll
  for (int it = 0; it < 2; it++)
#pragma unroll
    for (int j = 0; j < 4; j++) xv[it][j] = *(const u32x4*)(x1 + (size_t)(mt0 * 64 + it * 32 + w * 4 + g) * DM + (j * 16 + n) * 8);
  const u32x4* WF = (const u32x4*)(ws + OFF_W1F) + (size_t)(nb * 16 + w * 2) * 16 * 64 + lane;
  u32x4 bfm[2][16];
#pragma unroll
  for (int nt = 0; nt < 2; nt++)
#pragma unroll
    for (int ks = 0; ks < 8; ks++) bfm[nt][ks] = WF[(nt * 16 + ks) * 64];
  __builtin_amdgcn_sched_barrier(0);
  float* modl = (float*)(lds + 66560);
  if (tid < 256) *(f32x4*)(modl + tid * 4) = tid >= 128 ? mv + 1.f : mv;
  __syncthreads();
#pragma unroll
  for (int it = 0; it < 2; it++) {
    f32x2 f[16];
#pragma unroll
    for (int j = 0; j < 4; j++) {
      f[4 * j + 0] = (f32x2){bf_lo(xv[it][j].x), bf_hi(xv[it][j].x)}; f[4 * j + 1] = (f32x2){bf_lo(xv[it][j].y), bf_hi(xv[it][j].y)};
      f[4 * j + 2] = (f32x2){bf_lo(xv[it][j].z), bf_hi(xv[it][j].z)}; f[4 * j + 3] = (f32x2){bf_lo(xv[it][j].w), bf_hi(xv[it][j].w)};
    }
    f32x2 s2 = {0.f, 0.f};
#pragma unroll
    for (int i = 0; i < 16; i++) s2 += f[i];
    float m = row16_sum(s2.x + s2.y) * (1.f / DM);
    f32x2 q2 = {0.f, 0.f};
#pragma unroll
    for (int i = 0; i < 16; i++) { f[i] -= m; q2 += f[i] * f[i]; }
    float rs = rsqrtf(row16_sum(q2.x + q2.y) * (1.f / DM) + 1e-5f);
    s2 = (f32x2){0.f, 0.f};
#pragma unroll
    for (int j = 0; j < 4; j++) {
      const int c0 = (j * 16 + n) * 8;
      f32x4 c0v = *(const f32x4*)(modl + 512 + c0), c1v = *(const f32x4*)(modl + 512 + c0 + 4);
      f32x4 s0v = *(const f32x4*)(modl + c0), s1v = *(const f32x4*)(modl + c0 + 4);
      f[4 * j + 0] = f[4 * j + 0] * rs * (f32x2){c0v[0], c0v[1]} + (f32x2){s0v[0], s0v[1]};
      f[4 * j + 1] = f[4 * j + 1] * rs * (f32x2){c0v[2], c0v[3]} + (f32x2){s0v[2], s0v[3]};
      f[4 * j + 2] = f[4 * j + 2] * rs * (f32x2){c1v[0], c1v[1]} + (f32x2){s1v[0], s1v[1]};
      f[4 * j + 3] = f[4 * j + 3] * rs * (f32x2){c1v[2], c1v[3]} + (f32x2){s1v[2], s1v[3]};
      s2 += (f[4 * j + 0] + f[4 * j + 1]) + (f[4 * j + 2] + f[4 * j + 3]);
    }
    m = row16_sum(s2.x + s2.y) * (1.f / DM);
    q2 = (f32x2){0.f, 0.f};
#pragma unroll
    for (int i = 0; i < 16; i++) { f[i] -= m; q2 += f[i] * f[i]; }
    rs = rsqrtf(row16_sum(q2.x + q2.y) * (1.f / DM) + 1e-5f);
#pragma unroll
    for (int j = 0; j < 4; j++) {
      f32x2 o0 = f[4 * j] * rs, o1 = f[4 * j + 1] * rs, o2 = f[4 * j + 2] * rs, o3 = f[4 * j + 3] * rs;
      u32x4 o = {pk2(o0.x, o0.y), pk2(o1.x, o1.y), pk2(o2.x, o2.y), pk2(o3.x, o3.y)};
      *(u32x4*)(lds + (it * 32 + w * 4 + g) * 1040 + (j * 16 + n) * 16) = o;
    }
    __builtin_amdgcn_sched_barrier(0);
  }
#pragma unroll
  for (int nt = 0; nt < 2; nt++)
#pragma unroll
    for (int ks = 8; ks < 16; ks++) bfm[nt][ks] = WF[(nt * 16 + ks) * 64];
  __builtin_amdgcn_sched_barrier(0);
  __syncthreads();
  f32x4 acc[4][2];
#pragma unroll
  for (int i = 0; i < 4; i++)
#pragma unroll
    for (int j = 0; j < 2; j++) acc[i][j] = (f32x4){0.f, 0.f, 0.f, 0.f};
#pragma unroll
  for (int ks = 0; ks < 16; ks++) {
    bf16x8 af[4];
#pragma unroll
    for (int mt = 0; mt < 4; mt++) af[mt] = *(const bf16x8*)(lds + (16 * mt + n) * 1040 + (32 * ks + 8 * g) * 2);
#pragma unroll
    for (int mt = 0; mt < 4; mt++)
#pragma unroll
      for (int nt = 0; nt < 2; nt++) acc[mt][nt] = MFMA16(as_bf8(bfm[nt][ks]), af[mt], acc[mt][nt]);
    if (ks & 1) __builtin_amdgcn_sched_barrier(0);
  }
  u32x4* hidF = (u32x4*)(ws + OFF_HID);
  float bb[2][4];
#pragma unroll
  for (int nt = 0; nt < 2; nt++) {
    u32x2 bv = *(const u32x2*)(b1 + nb * 256 + w * 32 + 16 * nt + 4 * g);
    bb[nt][0] = bf_lo(bv.x); bb[nt][1] = bf_hi(bv.x); bb[nt][2] = bf_lo(bv.y); bb[nt][3] = bf_hi(bv.y);
  }
#pragma unroll
  for (int mt = 0; mt < 4; mt++) {
    float o[8];
#pragma unroll
    for (int nt = 0; nt < 2; nt++)
#pragma unroll
      for (int j = 0; j < 4; j++) {
        float u = bfr(bfr(acc[mt][nt][j]) + bb[nt][j]);
        float z = 0.7978845608028654f * (u + 0.044715f * u * u * u);
        float e = __builtin_amdgcn_exp2f(z * 2.8853900817779268f);
        float th = 1.f - 2.f * __builtin_amdgcn_rcpf(e + 1.f);
        o[4 * nt + j] = 0.5f * u * (1.f + th);
      }
    u32x4 ov = {pk2(o[0], o[1]), pk2(o[2], o[3]), pk2(o[4], o[5]), pk2(o[6], o[7])};
    hidF[((size_t)(mt0 * 4 + mt) * 56 + nb * 8 + w) * 64 + lane] = ov;
  }
}

__global__ void __launch_bounds__(512, 2) k5_mlp2(const u16* __restrict__ b2, char* __restrict__ ws, u16* __restrict__ out) {
  extern __shared__ __attribute__((aligned(16))) char lds[];
  const int b = blockIdx.x, tid = threadIdx.x, lane = tid & 63, w = tid >> 6;
  const int mt0 = (b & 7) * 4 + (b >> 6), nb = (b >> 3) & 7;
  const int n = lane & 15, g = lane >> 4;
  const u32x4* hidF = (const u32x4*)(ws + OFF_HID) + ((size_t)(mt0 * 4) * 56 + w * 7) * 64 + lane;
  const u32x4* WF = (const u32x4*)(ws + OFF_W2F) + ((size_t)(nb * 4) * 56 + w * 7) * 64 + lane;
  f32x4 acc[4][4];
#pragma unroll
  for (int i = 0; i < 4; i++)
#pragma unroll
    for (int j = 0; j < 4; j++) acc[i][j] = (f32x4){0.f, 0.f, 0.f, 0.f};
#pragma unroll
  for (int hb = 0; hb < 2; hb++) {
    const int nk = hb ? 3 : 4;
    u32x4 bfm[4][4]; bf16x8 af[4][4];
#pragma unroll
    for (int k = 0; k < 4; k++) if (k < nk) {
#pragma unroll
      for (int nt = 0; nt < 4; nt++) bfm[k][nt] = WF[(nt * 56 + hb * 4 + k) * 64];
#pragma unroll
      for (int mt = 0; mt < 4; mt++) af[k][mt] = as_bf8(hidF[(mt * 56 + hb * 4 + k) * 64]);
    }
    __builtin_amdgcn_sched_barrier(0);
#pragma unroll
    for (int k = 0; k < 4; k++) if (k < nk) {
#pragma unroll
      for (int mt = 0; mt < 4; mt++)
#pragma unroll
        for (int nt = 0; nt < 4; nt++) acc[mt][nt] = MFMA16(as_bf8(bfm[k][nt]), af[k][mt], acc[mt][nt]);
    }
  }
  float* red = (float*)lds;
#pragma unroll
  for (int mt = 0; mt < 4; mt++)
#pragma unroll
    for (int nt = 0; nt < 4; nt++) *(f32x4*)(red + (w * 64 + 16 * mt + n) * 68 + 16 * nt + 4 * g) = acc[mt][nt];
  __syncthreads();
  const float* mod = (const float*)(ws + OFF_MOD);
  const u16* x1 = (const u16*)(ws + OFF_X1);
  {
    const int r = tid >> 3, c8 = (tid & 7) * 8;
    float o[8];
#pragma unroll
    for (int j = 0; j < 8; j++) o[j] = 0.f;
#pragma unroll
    for (int ww = 0; ww < 8; ww++) {
      f32x4 a = *(const f32x4*)(red + (ww * 64 + r) * 68 + c8), bq = *(const f32x4*)(red + (ww * 64 + r) * 68 + c8 + 4);
#pragma unroll
      for (int j = 0; j < 4; j++) { o[j] += a[j]; o[4 + j] += bq[j]; }
    }
    const int col = 64 * nb + c8;
    const size_t go = (size_t)(mt0 * 64 + r) * DM + col;
    u32x4 xv = *(const u32x4*)(x1 + go), bv = *(const u32x4*)(b2 + col);
    float xf[8] = {bf_lo(xv.x), bf_hi(xv.x), bf_lo(xv.y), bf_hi(xv.y), bf_lo(xv.z), bf_hi(xv.z), bf_lo(xv.w), bf_hi(xv.w)};
    float bb[8] = {bf_lo(bv.x), bf_hi(bv.x), bf_lo(bv.y), bf_hi(bv.y), bf_lo(bv.z), bf_hi(bv.z), bf_lo(bv.w), bf_hi(bv.w)};
#pragma unroll
    for (int j = 0; j < 8; j++) o[j] = xf[j] + bfr(mod[2560 + col + j] * bfr(bfr(o[j]) + bb[j]));
    u32x4 rv = {pk2(o[0], o[1]), pk2(o[2], o[3]), pk2(o[4], o[5]), pk2(o[6], o[7])};
    *(u32x4*)(out + go) = rv;
  }
}

extern "C" void kernel_launch(void* const* d_in, const int* in_sizes, int n_in, void* d_out, int out_size, void* d_ws,
                              size_t ws_size, hipStream_t stream) {
  const u16* x = (const u16*)d_in[0];
  const u16* mem0 = (const u16*)d_in[1];
  const u16* Wq = (const u16*)d_in[2];
  const u16* Wk = (const u16*)d_in[3];
  const u16* Wv = (const u16*)d_in[4];
  const u16* Wo = (const u16*)d_in[5];
  const u16* w1 = (const u16*)d_in[8];
  const u16* b1 = (const u16*)d_in[9];
  const u16* w2 = (const u16*)d_in[10];
  const u16* b2 = (const u16*)d_in[11];
  const u16* cvec = (const u16*)d_in[12];
  const u16* wmod = (const u16*)d_in[13];
  char* ws = (char*)d_ws;
  hipFuncSetAttribute((const void*)k2_rec, hipFuncAttributeMaxDynamicSharedMemorySize, 135168);
  hipMemsetAsync(ws, 0xFF, OFF_H, stream);
  k2_rec<<<256, 256, 135168, stream>>>(Wq, Wk, Wv, Wo, x, w1, w2, cvec, wmod, mem0, ws);
  hipFuncSetAttribute((const void*)k3b_oproj, hipFuncAttributeMaxDynamicSharedMemorySize, 135168);
  k3b_oproj<<<256, 512, 135168, stream>>>(x, ws);
  hipFuncSetAttribute((const void*)k4_mlp1, hipFuncAttributeMaxDynamicSharedMemorySize, 70656);
  hipFuncSetAttribute((const void*)k5_mlp2, hipFuncAttributeMaxDynamicSharedMemorySize, 139264);
  k4_mlp1<<<224, 512, 70656, stream>>>(b1, ws);
  k5_mlp2<<<256, 512, 139264, stream>>>(b2, ws, (u16*)d_out);
}
```

```cpp
#include <hip/hip_runtime.h>
#include <hip/hip_bf16.h>
#include <stdint.h>

typedef unsigned int u32;
typedef unsigned short u16;
typedef unsigned long long u64;
typedef __attribute__((ext_vector_type(2))) __bf16 bf16x2;
typedef __attribute__((ext_vector_type(8))) __bf16 bf16x8;
typedef __attribute__((ext_vector_type(16))) __bf16 bf16x16;
typedef __attribute__((ext_vector_type(2))) float f32x2;
typedef __attribute__((ext_vector_type(4))) float f32x4;
typedef __attribute__((ext_vector_type(16))) float f32x16;
typedef __attribute__((ext_vector_type(2))) u32 u32x2;
typedef __attribute__((ext_vector_type(4))) u32 u32x4;
typedef __attribute__((ext_vector_type(8))) u32 u32x8;

#define NTOK 2048
#define DM 512
#define NH 32
#define DH 128
#define NSEG 64
#define FF 1792
#define SENT 0xFFFFFFFFu

#define OFF_FLAG 0ull
#define OFF_DONE 8192u
#define OFF_KMF 12288u
#define OFF_WVF 14336u
#define OFF_MDF 28672u
#define OFF_X1F 29696u
#define OFF_HDF 30720u
#define OFF_MOD 16384ull
#define OFF_MEMX 32768ull
#define OFF_PEX 0x90000ull
#define OFF_H (2ull << 20)
#define OFF_KMEM (4ull << 20)
#define OFF_WVO (8ull << 20)
#define OFF_KX (24ull << 20)
#define OFF_QX (40ull << 20)
#define OFF_Y (56ull << 20)
#define OFF_UF (72ull << 20)
#define OFF_PX (136ull << 20)
#define OFF_X1 (142ull << 20)
#define OFF_Y2 (146ull << 20)
#define OFF_HID (148ull << 20)
#define OFF_W1F (156ull << 20)
#define OFF_W2F (158ull << 20)
#define OFF_DBG (200ull << 20)
#define STAMP(slot) do { } while (0)

__device__ __forceinline__ u32 pk2(float a, float b) {
  bf16x2 v; v[0] = (__bf16)a; v[1] = (__bf16)b; return __builtin_bit_cast(u32, v);
}
__device__ __forceinline__ u32 f2u(float x) { return __builtin_bit_cast(u32, x); }
__device__ __forceinline__ float u2f(u32 x) { return __builtin_bit_cast(float, x); }
__device__ __forceinline__ float bf_lo(u32 x) { return __builtin_bit_cast(float, x << 16); }
__device__ __forceinline__ float bf_hi(u32 x) { return __builtin_bit_cast(float, x & 0xffff0000u); }
__device__ __forceinline__ float bfr(float a) { return (float)(__bf16)a; }
__device__ __forceinline__ __amdgpu_buffer_rsrc_t mkrsrc(const void* p) {
  return __builtin_amdgcn_make_buffer_rsrc((void*)p, 0, 0x7fffffff, 0x00020000);
}
__device__ __forceinline__ u32x4 ld16_sc1(__amdgpu_buffer_rsrc_t r, u32 off) { return __builtin_amdgcn_raw_buffer_load_b128(r, off, 0, 16); }
__device__ __forceinline__ u32 ld4_sc1(__amdgpu_buffer_rsrc_t r, u32 off) { return __builtin_amdgcn_raw_buffer_load_b32(r, off, 0, 16); }
__device__ __forceinline__ void st16_sc1(u32x4 v, __amdgpu_buffer_rsrc_t r, u32 off) { __builtin_amdgcn_raw_buffer_store_b128(v, r, off, 0, 16); }
__device__ __forceinline__ void st8_sc1(u32x2 v, __amdgpu_buffer_rsrc_t r, u32 off) { __builtin_amdgcn_raw_buffer_store_b64(v, r, off, 0, 16); }
__device__ __forceinline__ void st4_sc1(u32 v, __amdgpu_buffer_rsrc_t r, u32 off) { __builtin_amdgcn_raw_buffer_store_b32(v, r, off, 0, 16); }
__device__ __forceinline__ u32 badm(u32x4 a) { return (u32)(a.x == SENT) | (u32)(a.y == SENT) | (u32)(a.z == SENT) | (u32)(a.w == SENT); }
__device__ __forceinline__ u32 sane(u32 x) { return x < 0xFFFFFFFEu ? x : 0xFFFFFFFEu; }
__device__ __forceinline__ u32 now32() { return (u32)__builtin_amdgcn_s_memtime(); }
__device__ __forceinline__ void wait_until(u32 t) { while ((int)(t - now32()) > 0) { } }
__device__ __forceinline__ bf16x8 as_bf8(u32x4 v) { return __builtin_bit_cast(bf16x8, v); }
__device__ __forceinline__ bf16x16 as_bf16(u32x4 a, u32x4 b) {
  u32x8 v = {a.x, a.y, a.z, a.w, b.x, b.y, b.z, b.w}; return __builtin_bit_cast(bf16x16, v);
}
__device__ __forceinline__ float max3r(float a, float b, float c) { float r; asm("v_max3_f32 %0, %1, %2, %3" : "=v"(r) : "v"(a), "v"(b), "v"(c)); return r; }
__device__ __forceinline__ float max2r(float a, float b) { float r; asm("v_max_f32 %0, %1, %2" : "=v"(r) : "v"(a), "v"(b)); return r; }
#define MFMA16(a, b, c) __builtin_amdgcn_mfma_f32_16x16x32_bf16((a), (b), (c), 0, 0, 0)
#define MFMA32(a, b, c) __builtin_amdgcn_mfma_f32_32x32x16_bf16((a), (b), (c), 0, 0, 0)
#define SMF(a, b, c, idx) __builtin_amdgcn_smfmac_f32_16x16x64_bf16((a), (b), (c), (idx), 0, 0)

__device__ __forceinline__ u32 umax3(u32 a, u32 b, u32 c) { u32 m = a > b ? a : b; return m > c ? m : c; }
template <int N>
__device__ __forceinline__ u32 poll_chunks(__amdgpu_buffer_rsrc_t r, const u32 (&off)[N], u32x4 (&a)[N], u32 lane16) {
  u32 spins = 0;
  while (true) {
    asm volatile("" : "+v"(lane16) :: "memory");
#pragma unroll
    for (int j = 0; j < N; j++) a[j] = ld16_sc1(r, off[j] + lane16);
    u32 m = 0;
#pragma unroll
    for (int j = 0; j < N; j++) { m = umax3(m, a[j].x, a[j].y); m = umax3(m, a[j].z, a[j].w); }
    ++spins;
    if (!__any(m == SENT)) return spins;
    if (spins > 400000u) return 0;
  }
}

typedef __attribute__((ext_vector_type(4))) short s16x4;
typedef __attribute__((address_space(3))) s16x4 lds_s16x4;
template <int ROWB>
__device__ __forceinline__ void dma_tile(char* lds, const u16* src, int rows, int gstride) {
  constexpr int LPR = ROWB / 16;
  const int total = rows * LPR;
  for (int u = threadIdx.x; u < total; u += 256) {
    const int r = u / LPR, c = u % LPR;
    __builtin_amdgcn_global_load_lds((const u32*)(src + (size_t)r * gstride + c * 8), (u32*)(lds + u * 16), 16, 0, 0);
  }
}
__device__ __forceinline__ void dma_wait() { asm volatile("s_waitcnt vmcnt(0)" ::: "memory"); __syncthreads(); }
__device__ __forceinline__ u32x2 tr4(const char* lds, int row0, int col0, int pitch, int lane) {
  const char* p = lds + (row0 + ((lane & 15) >> 2)) * pitch + (col0 + 4 * (lane & 3)) * 2;
  s16x4 v = __builtin_amdgcn_ds_read_tr16_b64_v4i16((lds_s16x4*)p);
  return __builtin_bit_cast(u32x2, v);
}
__device__ __forceinline__ bf16x8 frag8(const char* lds, int k0, int col0, int pitch, int lane) {
  u32x2 a = tr4(lds, k0, col0, pitch, lane), b = tr4(lds, k0 + 4, col0, pitch, lane);
  return as_bf8((u32x4){a.x, a.y, b.x, b.y});
}
__device__ __forceinline__ u32x4 frag8s(const char* lds, int k0, int col0, int pitch, int lane) {
  u32x2 a = tr4(lds, k0, col0, pitch, lane), b = tr4(lds, k0 + 8, col0, pitch, lane);
  return (u32x4){a.x, b.x, a.y, b.y};
}
#define PACK_S(acc, b) (u32x4){pk2((acc)[(b)], (acc)[(b) + 1]), pk2((acc)[(b) + 4], (acc)[(b) + 5]), pk2((acc)[(b) + 2], (acc)[(b) + 3]), pk2((acc)[(b) + 6], (acc)[(b) + 7])}

__device__ __forceinline__ void wvo_task(const u16* __restrict__ Wv, const u16* __restrict__ Wo, char* ws, char* lds, int task) {
  const int tid = threadIdx.x, lane = tid & 63, w = tid >> 6;
  const int h = task >> 2, nq = task & 3, x = lane & 31, hh = lane >> 5;
  dma_tile<256>(lds, Wo + (size_t)h * DH * DM + nq * 128, DH, DM);
  bf16x8 a[4][8];
#pragma unroll
  for (int mt = 0; mt < 4; mt++)
#pragma unroll
    for (int ks = 0; ks < 8; ks++)
      a[mt][ks] = *(const bf16x8*)(Wv + ((size_t)(128 * w + 32 * mt + x) * NH + h) * DH + 16 * ks + 8 * hh);
  dma_wait();
  const __amdgpu_buffer_rsrc_t rd = mkrsrc(ws + OFF_WVO);
#pragma unroll
  for (int nt = 0; nt < 4; nt++) {
    bf16x8 bw[8];
#pragma unroll
    for (int ks = 0; ks < 8; ks++) bw[ks] = frag8(lds, 16 * ks + 8 * hh, 32 * nt + (x & 16), 256, lane);
#pragma unroll
    for (int mt = 0; mt < 4; mt++) {
      f32x16 acc;
#pragma unroll
      for (int i = 0; i < 16; i++) acc[i] = 0.f;
#pragma unroll
      for (int ks = 0; ks < 8; ks++) acc = MFMA32(a[mt][ks], bw[ks], acc);
      const int d = 128 * nq + 32 * nt + x, c = d >> 3, cc = d & 7;
#pragma unroll
      for (int xb = 0; xb < 2; xb++) {
        const int dp = 128 * w + 32 * mt + 16 * xb, t = dp >> 6, ga = (dp >> 4) & 3, z = ga >> 1, gb = 2 * (ga & 1) + hh;
        const int lp = (h & 1) * 8 + cc + 16 * gb;
        u32 o = (u32)(((((c * 4 + (h >> 3)) * 4 + ((h >> 1) & 3)) * 8 + t) * 2 + z) * 64 + lp);
        st16_sc1(PACK_S(acc, 8 * xb), rd, o * 16u);
      }
    }
  }
  asm volatile("s_waitcnt vmcnt(0)" ::: "memory");
  __syncthreads();
  if (tid == 0) st4_sc1(1u, mkrsrc(ws + OFF_FLAG), OFF_WVF + task * 16u);
}

__device__ __forceinline__ void mod_cols(const u16* __restrict__ cvec, const u16* __restrict__ wmod, char* ws, char* lds, int col0) {
  const int tid = threadIdx.x, cp = tid & 15, ksl = tid >> 4;
  float* red = (float*)lds;
  u32 wv[32];
#pragma unroll
  for (int k = 0; k < 32; k++) wv[k] = *(const u32*)(wmod + (size_t)(ksl * 32 + k) * 3072 + col0 + cp * 2);
  float* sl_ = (float*)(lds + 2048);
  __syncthreads();
  {
    u32 cw = *(const u32*)(cvec + tid * 2);
    float c0 = bf_lo(cw), c1 = bf_hi(cw);
    sl_[tid * 2] = bfr(c0 / (1.f + __expf(-c0)));
    sl_[tid * 2 + 1] = bfr(c1 / (1.f + __expf(-c1)));
  }
  __syncthreads();
  float a0 = 0.f, a1 = 0.f;
#pragma unroll
  for (int k4 = 0; k4 < 8; k4++) {
    f32x4 sv = *(const f32x4*)(sl_ + ksl * 32 + k4 * 4);
#pragma unroll
    for (int j = 0; j < 4; j++) { a0 += sv[j] * bf_lo(wv[k4 * 4 + j]); a1 += sv[j] * bf_hi(wv[k4 * 4 + j]); }
  }
  red[ksl * 32 + cp * 2] = a0; red[ksl * 32 + cp * 2 + 1] = a1;
  __syncthreads();
  if (tid < 32) {
    float sacc = 0.f;
#pragma unroll
    for (int k = 0; k < 16; k++) sacc += red[k * 32 + tid];
    st4_sc1(f2u(bfr(sacc)), mkrsrc(ws + OFF_MOD), (u32)(col0 + tid) * 4u);
  }
}

__device__ __forceinline__ float row16_sum(float v) {
  v += u2f((u32)__builtin_amdgcn_mov_dpp((int)f2u(v), 0x128, 0xF, 0xF, true));
  v += u2f((u32)__builtin_amdgcn_mov_dpp((int)f2u(v), 0x124, 0xF, 0xF, true));
  v += u2f((u32)__builtin_amdgcn_mov_dpp((int)f2u(v), 0x122, 0xF, 0xF, true));
  v += u2f((u32)__builtin_amdgcn_mov_dpp((int)f2u(v), 0x121, 0xF, 0xF, true));
  return v;
}

__device__ __forceinline__ bool wait_progress(__amdgpu_buffer_rsrc_t rf, u32 off, u32 need, u32& seen) {
  u32 spins = 0;
  if (__all((int)seen >= (int)need)) return true;
  while (true) {
    asm volatile("" : "+v"(off) :: "memory");
    seen = ld4_sc1(rf, off);
    if (__all((int)seen >= (int)need)) return true;
    __builtin_amdgcn_s_sleep(2);
    if (++spins > 400000u) return false;
  }
}

__device__ __forceinline__ void px_task(char* ws, int s, int hg);
__device__ __forceinline__ void role_help(const u16* __restrict__ x, const u16* __restrict__ w1, const u16* __restrict__ w2,
                                          const u16* __restrict__ cvec, const u16* __restrict__ wmod, const u16* __restrict__ mem0,
                                          char* ws, char* lds, int a0) {
  const int tid = threadIdx.x, lane = tid & 63, w = tid >> 6, n = lane & 15, g = lane >> 4;
  u32x4 xpre[2][2][4];
#pragma unroll
  for (int sg = 0; sg < 2; sg++)
#pragma unroll
    for (int it = 0; it < 2; it++)
#pragma unroll
      for (int j = 0; j < 4; j++) xpre[sg][it][j] = *(const u32x4*)(x + (size_t)((a0 + 32 * sg) * 32 + it * 16 + w * 4 + g) * DM + (j * 16 + n) * 8);
  if (a0 == 31) {
    for (int i = tid; i < 512; i += 256) {
      u32x4 v = *(const u32x4*)(mem0 + (i & 7) * DM + (i >> 3) * 8);
      v.x = sane(v.x); v.y = sane(v.y); v.z = sane(v.z); v.w = sane(v.w);
      st16_sc1(v, mkrsrc(ws + OFF_MEMX), (u32)i * 16u);
    }
  }
  mod_cols(cvec, wmod, ws, lds, a0 * 32);
  asm volatile("s_waitcnt vmcnt(0)" ::: "memory");
  __syncthreads();
  if (tid == 0) st4_sc1(1u, mkrsrc(ws + OFF_FLAG), OFF_MDF + a0 * 16u);
  f32x2 fz[2][2][16];
#pragma unroll
  for (int sg = 0; sg < 2; sg++)
#pragma unroll
    for (int it = 0; it < 2; it++) {
      f32x2* f = fz[sg][it];
#pragma unroll
      for (int j = 0; j < 4; j++) {
        const u32x4 v = xpre[sg][it][j];
        f[4 * j + 0] = (f32x2){bf_lo(v.x), bf_hi(v.x)}; f[4 * j + 1] = (f32x2){bf_lo(v.y), bf_hi(v.y)};
        f[4 * j + 2] = (f32x2){bf_lo(v.z), bf_hi(v.z)}; f[4 * j + 3] = (f32x2){bf_lo(v.w), bf_hi(v.w)};
      }
      f32x2 s2 = {0.f, 0.f};
#pragma unroll
      for (int i = 0; i < 16; i++) s2 += f[i];
      const float m = row16_sum(s2.x + s2.y) * (1.f / DM);
      f32x2 q2 = {0.f, 0.f};
#pragma unroll
      for (int i = 0; i < 16; i++) { f[i] -= m; q2 += f[i] * f[i]; }
      const float rs = rsqrtf(row16_sum(q2.x + q2.y) * (1.f / DM) + 1e-5f);
#pragma unroll
      for (int i = 0; i < 16; i++) f[i] *= rs;
    }
  { u32 seenm = 0xFFFFFFFFu; if (!wait_progress(mkrsrc(ws + OFF_FLAG), OFF_MDF + (lane & 31) * 16u, 1u, seenm)) return; }
  float* modl = (float*)(lds + 8192);
  {
    u32x4 mv = ld16_sc1(mkrsrc(ws + OFF_MOD), (u32)tid * 16u);
    f32x4 mf = {u2f(mv.x), u2f(mv.y), u2f(mv.z), u2f(mv.w)};
    *(f32x4*)(modl + tid * 4) = tid >= 128 ? mf + 1.f : mf;
  }
  __syncthreads();
  const __amdgpu_buffer_rsrc_t rh = mkrsrc(ws + OFF_H);
#pragma unroll
  for (int sg = 0; sg < 2; sg++) {
    const int a = a0 + 32 * sg;
#pragma unroll
    for (int it = 0; it < 2; it++) {
      const f32x2* f = fz[sg][it];
#pragma unroll
      for (int j = 0; j < 4; j++) {
        const int c0 = (j * 16 + n) * 8;
        f32x4 c0v = *(const f32x4*)(modl + 512 + c0), c1v = *(const f32x4*)(modl + 512 + c0 + 4);
        f32x4 s0v = *(const f32x4*)(modl + c0), s1v = *(const f32x4*)(modl + c0 + 4);
        f32x2 o0 = f[4 * j + 0] * (f32x2){c0v[0], c0v[1]} + (f32x2){s0v[0], s0v[1]};
        f32x2 o1 = f[4 * j + 1] * (f32x2){c0v[2], c0v[3]} + (f32x2){s0v[2], s0v[3]};
        f32x2 o2 = f[4 * j + 2] * (f32x2){c1v[0], c1v[1]} + (f32x2){s1v[0], s1v[1]};
        f32x2 o3 = f[4 * j + 3] * (f32x2){c1v[2], c1v[3]} + (f32x2){s1v[2], s1v[3]};
        u32x4 r = {pk2(o0.x, o0.y), pk2(o1.x, o1.y), pk2(o2.x, o2.y), pk2(o3.x, o3.y)};
        st16_sc1(r, rh, (u32)((a * 32 + it * 16 + w * 4 + g) * DM + c0) * 2u);
      }
    }
    asm volatile("s_waitcnt vmcnt(0)" ::: "memory");
    __syncthreads();
    if (tid == 0) st4_sc1(1u, mkrsrc(ws + OFF_FLAG), OFF_DONE + a * 64u);
  }
  const int a = a0;
  mod_cols(cvec, wmod, ws, lds, 1024 + a * 64);
  mod_cols(cvec, wmod, ws, lds, 1024 + a * 64 + 32);
  __syncthreads();
  if (a < 28) {
    dma_tile<128>(lds, w1 + a * 64, DM, FF);
    dma_wait();
    u32x4* dst = (u32x4*)(ws + OFF_W1F);
    for (int ks = 0; ks < 16; ks++) {
      bf16x8 v = frag8(lds, 32 * ks + 8 * (lane >> 4), 16 * w, 128, lane);
      dst[((size_t)(a * 4 + w) * 16 + ks) * 64 + lane] = __builtin_bit_cast(u32x4, v);
    }
  }
  __syncthreads();
  {
    const int cb = a >> 2, kq = a & 3;
    dma_tile<128>(lds, w2 + (size_t)kq * 448 * DM + cb * 64, 448, DM);
    dma_wait();
    u32x4* dst = (u32x4*)(ws + OFF_W2F);
    for (int ks = 0; ks < 14; ks++) {
      u32x2 va = tr4(lds, 32 * ks + 4 * (lane >> 4), 16 * w, 128, lane), vb = tr4(lds, 32 * ks + 16 + 4 * (lane >> 4), 16 * w, 128, lane);
      dst[((size_t)(cb * 4 + w) * 56 + kq * 14 + ks) * 64 + lane] = (u32x4){va.x, va.y, vb.x, vb.y};
    }
  }
  {
    const __amdgpu_buffer_rsrc_t rflag = mkrsrc(ws + OFF_FLAG);
    const int hg = a & 7;
    for (int s = a >> 3; s < NSEG; s += 4) {
      px_task(ws, s, hg);
    }
  }
}

#define AD_DEC 3
#define AD_INC 60
#define ADAPT(D, n) do { if ((n) == 1) D -= AD_DEC; else D += AD_INC; if (D < 0) D = 0; } while (0)

__device__ __forceinline__ void role_score(const u16* __restrict__ Wq, const u16* __restrict__ Wk, char* ws,
                                           char* lds, int h) {
  const int tid = threadIdx.x, lane = tid & 63, w = tid >> 6;
  const int n = lane & 15, gb = lane >> 4;
  bf16x16 wf[4][8];
#pragma unroll
  for (int mat = 0; mat < 2; mat++) {
    __syncthreads();
    dma_tile<256>(lds, (mat ? Wk : Wq) + h * DH, DM, NH * DH);
    dma_wait();
#pragma unroll
    for (int tt = 0; tt < 2; tt++)
#pragma unroll
      for (int t = 0; t < 8; t++) {
        const int col = 32 * w + 16 * tt;
        u32x4 lo = frag8s(lds, 64 * t + 16 * (gb >> 1) + 4 * (gb & 1), col, 256, lane);
        u32x4 hi = frag8s(lds, 64 * t + 16 * (2 + (gb >> 1)) + 4 * (gb & 1), col, 256, lane);
        wf[mat * 2 + tt][t] = as_bf16(lo, hi);
      }
  }
  __syncthreads();
  char* memL = lds;
  char* qs = lds + 8192;
  char* ks_ = lds + 8192 + 2304;
  const int idx = (lane & 8) ? (int)0xEEEEEEEE : 0x44444444;
  const __amdgpu_buffer_rsrc_t rflag = mkrsrc(ws + OFF_FLAG);
  const __amdgpu_buffer_rsrc_t rkx = mkrsrc(ws + OFF_KX);
  const __amdgpu_buffer_rsrc_t rmem = mkrsrc(ws + OFF_MEMX);
  const __amdgpu_buffer_rsrc_t rpex = mkrsrc(ws + OFF_PEX);
  const int rho = lane & 15;
  const int keyp = 8 * ((rho >> 2) & 1) + 4 * (rho >> 3) + (rho & 3);
  const u32 prho = (u32)((n & 7) >> 2) * 10240u, pi = n & 3;
  const u32 pxoff = prho + (u32)(((h >> 2) * 64) + 16 * (2 * (h & 1) + (gb >> 1)) + 8 * (gb & 1) + 4 * ((h >> 1) & 1) + pi) * 16u + (n >> 3) * 8u;
  const u32 pmoff = prho + 8192u + (u32)(h * 4 + pi) * 16u + (gb & 1) * 8u + (gb >> 1) * 4u;
#define DS_OFF 350
#define DC_OFF 180
  int D1 = (w == 0 ? 3400 : (w == 1 ? 3300 : 4050)) - DS_OFF; u32 tref = now32() - 8000u, seen = 0xFFFFFFFFu;
  STAMP(1);
  for (int s = 0; s < NSEG; s++) {
    u32x4 kx[2][4];
    u32 nsw1;
    if (w == 0) {
      if (!wait_progress(rflag, 4096u + h * 128u, s + 1, seen)) return;
#pragma unroll
      for (int tt = 0; tt < 2; tt++)
#pragma unroll
        for (int k4 = 0; k4 < 4; k4++)
          kx[tt][k4] = ld16_sc1(rkx, (u32)(((32 * s + 16 * tt + keyp) * NH + h) * DH + 32 * k4 + 8 * gb) * 2u);
    }
    f32x4 acc[4];
#pragma unroll
    for (int i = 0; i < 4; i++) { acc[i] = (f32x4){0.f, 0.f, 0.f, 0.f}; asm volatile("" : "+v"(acc[i])); }
    u32 pxo = (u32)s * 20480u + pxoff, pmo = (u32)s * 20480u + pmoff;
    asm volatile("" : "+v"(pxo), "+v"(pmo));
    wait_until(tref + D1);
    {
      u32 off[2] = {(u32)s * 8192u + w * 2048u, (u32)s * 8192u + w * 2048u + 1024u};
      u32x4 a[2];
      nsw1 = poll_chunks<2>(rmem, off, a, lane * 16u);
      *(u32x4*)(memL + w * 2048 + lane * 16) = a[0];
      *(u32x4*)(memL + w * 2048 + 1024 + lane * 16) = a[1];
    }
    __syncthreads();
    STAMP(16 + 4 * s);
    bf16x8 af[8];
#pragma unroll
    for (int t = 0; t < 8; t++) af[t] = *(const bf16x8*)(memL + t * 1024 + lane * 16);
    __builtin_amdgcn_sched_barrier(0);
    const int ecol = 32 * w + 16 * (lane >> 5) + n, er0 = 4 * ((lane >> 4) & 1);
#pragma unroll
    for (int t = 0; t < 8; t++) { acc[0] = SMF(af[t], wf[0][t], acc[0], idx); acc[1] = SMF(af[t], wf[1][t], acc[1], idx); }
    __builtin_amdgcn_sched_barrier(0);
#pragma unroll
    for (int t = 0; t < 8; t++) {
      acc[2] = SMF(af[t], wf[2][t], acc[2], idx); acc[3] = SMF(af[t], wf[3][t], acc[3], idx);
      if (t >= 2 && t < 6) {
        const int j = t - 2;
        auto r = __builtin_amdgcn_permlane32_swap(f2u(acc[0][j]), f2u(acc[1][j]), false, false);
        *(__bf16*)(qs + (er0 + j) * 272 + ecol * 2) = (__bf16)(u2f(r[0]) + u2f(r[1]));
      }
      __builtin_amdgcn_sched_barrier(0);
    }
#pragma unroll
    for (int j = 0; j < 4; j++) {
      auto r = __builtin_amdgcn_permlane32_swap(f2u(acc[2][j]), f2u(acc[3][j]), false, false);
      *(__bf16*)(ks_ + (er0 + j) * 272 + ecol * 2) = (__bf16)(u2f(r[0]) + u2f(r[1]));
    }
    __syncthreads();
    STAMP(17 + 4 * s);
    if (w == 0 && s < NSEG - 1) {
      f32x4 sc[3];
#pragma unroll
      for (int i = 0; i < 3; i++) { sc[i] = (f32x4){0.f, 0.f, 0.f, 0.f}; asm volatile("" : "+v"(sc[i])); }
      bf16x8 qf[4], kf[4];
#pragma unroll
      for (int k4 = 0; k4 < 4; k4++) {
        qf[k4] = *(const bf16x8*)(qs + (lane & 7) * 272 + (32 * k4 + 8 * gb) * 2);
        kf[k4] = *(const bf16x8*)(ks_ + (lane & 7) * 272 + (32 * k4 + 8 * gb) * 2);
      }
      __builtin_amdgcn_sched_barrier(0);
#pragma unroll
      for (int k4 = 0; k4 < 4; k4++) {
        asm volatile("s_nop 1\n\tv_mfma_f32_16x16x32_bf16 %0, %3, %6, %0\n\tv_mfma_f32_16x16x32_bf16 %1, %4, %6, %1\n\tv_mfma_f32_16x16x32_bf16 %2, %5, %6, %2"
                     : "+v"(sc[0]), "+v"(sc[1]), "+v"(sc[2]) : "v"(kx[0][k4]), "v"(kx[1][k4]), "v"(kf[k4]), "v"(qf[k4]));
      }
      asm volatile("s_nop 7\n\ts_nop 3" : "+v"(sc[0]), "+v"(sc[1]), "+v"(sc[2]));
      const float sl = 0.08838834764831845f * 1.4426950408889634f;
      const bool hiq = n >= 8, hig = gb >= 2;
      float p[6];
#pragma unroll
      for (int j = 0; j < 4; j++) p[j] = hiq ? sc[1][j] : sc[0][j];
      p[4] = hig ? sc[2][2] : sc[2][0];
      p[5] = hig ? sc[2][3] : sc[2][1];
      float mx = max2r(max3r(p[0], p[1], p[2]), max3r(p[3], p[4], p[5]));
      mx = max2r(mx, u2f((u32)__builtin_amdgcn_mov_dpp((int)f2u(mx), 0x128, 0xF, 0xF, true)));
      { auto r = __builtin_amdgcn_permlane16_swap(f2u(mx), f2u(mx), false, false);
        mx = max2r(u2f(r[0]), u2f(r[1])); }
      { auto r = __builtin_amdgcn_permlane32_swap(f2u(mx), f2u(mx), false, false);
        mx = max2r(u2f(r[0]), u2f(r[1])); }
      const float nb = -mx * sl;
#pragma unroll
      for (int j = 0; j < 6; j++) p[j] = __builtin_amdgcn_exp2f(__builtin_fmaf(p[j], sl, nb));
      float sum = (p[0] + p[1]) + (p[2] + p[3]) + (hiq ? 0.f : (p[4] + p[5]));
      sum += u2f((u32)__builtin_amdgcn_mov_dpp((int)f2u(sum), 0x128, 0xF, 0xF, true));
      { auto r = __builtin_amdgcn_permlane16_swap(f2u(sum), f2u(sum), false, false);
        sum = u2f(r[0]) + u2f(r[1]); }
      { auto r = __builtin_amdgcn_permlane32_swap(f2u(sum), f2u(sum), false, false);
        sum = u2f(r[0]) + u2f(r[1]); }
      const float inv = __builtin_amdgcn_rcpf(sum);
      u32x2 px = {sane(pk2(p[0] * inv, p[1] * inv)), sane(pk2(p[2] * inv, p[3] * inv))};
      st8_sc1(px, rpex, pxo);
      if (!hiq) st4_sc1(sane(pk2(p[4] * inv, p[5] * inv)), rpex, pmo);
      STAMP(18 + 4 * s);
    } else if (w == 1) {
      const __amdgpu_buffer_rsrc_t rkm = mkrsrc(ws + OFF_KMEM);
#pragma unroll
      for (int j = 0; j < 2; j++) {
        int u = j * 64 + lane, r = u >> 4, c = u & 15;
        st16_sc1(*(const u32x4*)(ks_ + r * 272 + c * 16), rkm, (u32)((s * NH + h) * 1024 + r * 128 + c * 8) * 2u);
      }
      asm volatile("s_waitcnt vmcnt(0)" ::: "memory");
      if (lane == 0) st4_sc1((u32)(s + 1), rflag, OFF_KMF + h * 64u);
    }
    tref = now32();
    if (!nsw1) return;
    if (s > 2) ADAPT(D1, nsw1);
  }
}

__device__ __forceinline__ void role_column(const u16* __restrict__ Wv, const u16* __restrict__ Wo, char* ws, char* lds, int c, int rho) {
  const int tid = threadIdx.x, lane = tid & 63, w = tid >> 6;
  const int n = lane & 15, gb = lane >> 4, cc = lane & 7;
  bf16x16 wf[4][8];
  wvo_task(Wv, Wo, ws, lds, c * 2 + rho);
  {
    u32 seenw = 0xFFFFFFFFu;
    if (!wait_progress(mkrsrc(ws + OFF_FLAG), OFF_WVF + (u32)((lane & 31) * 4 + (c >> 4)) * 16u, 1u, seenw)) return;
    const __amdgpu_buffer_rsrc_t rsrc = mkrsrc(ws + OFF_WVO + (size_t)(c * 4 + w) * 65536);
#pragma unroll
    for (int tl = 0; tl < 4; tl++)
#pragma unroll
      for (int t = 0; t < 8; t++)
        wf[tl][t] = as_bf16(ld16_sc1(rsrc, (u32)(((tl * 8 + t) * 2) * 64 + lane) * 16u), ld16_sc1(rsrc, (u32)(((tl * 8 + t) * 2 + 1) * 64 + lane) * 16u));
  }
  char* memL = lds;
  char* yl = lds + 8192 + w * 1024;
  float* red = (float*)(lds + 12288);
  const int idx = (lane & 8) ? (int)0xEEEEEEEE : 0x44444444;
  const __amdgpu_buffer_rsrc_t rflag = mkrsrc(ws + OFF_FLAG);
  const __amdgpu_buffer_rsrc_t rmem = mkrsrc(ws + OFF_MEMX);
  const __amdgpu_buffer_rsrc_t rpex = mkrsrc(ws + OFF_PEX);
  int D1 = w == 0 ? 750 : (w == 1 ? 850 : 1130), D2 = 2800 - DC_OFF; u32 tref = now32(), seen = 0xFFFFFFFFu;
  STAMP(1);
  for (int s = 0; s < NSEG; s++) {
    wait_until(tref + D1);
    {
      u32 off[2] = {(u32)s * 8192u + w * 2048u, (u32)s * 8192u + w * 2048u + 1024u};
      u32x4 a[2];
      u32 nsw = poll_chunks<2>(rmem, off, a, lane * 16u);
      if (!nsw) return;
      if (s > 2) ADAPT(D1, nsw);
      tref = now32();
      *(u32x4*)(memL + w * 2048 + lane * 16) = a[0];
      *(u32x4*)(memL + w * 2048 + 1024 + lane * 16) = a[1];
    }
    u32x4 uf[4][2];
    {
      if (!wait_progress(rflag, (u32)(8 * w + (lane & 7)) * 128u, s + 1, seen)) return;
      const __amdgpu_buffer_rsrc_t ru = mkrsrc(ws + OFF_UF + ((size_t)s * 64 + c) * 16384);
#pragma unroll
      for (int m = 0; m < 4; m++)
#pragma unroll
        for (int z = 0; z < 2; z++) uf[m][z] = ld16_sc1(ru, (u32)((((4 * w + m) * 2 + z) * 4 + gb) * 8 + cc) * 16u);
    }
    __syncthreads();
    STAMP(16 + 4 * s);
    bf16x16 yb;
    {
      f32x4 acc[4];
#pragma unroll
      for (int i = 0; i < 4; i++) acc[i] = (f32x4){0.f, 0.f, 0.f, 0.f};
      bf16x8 af[8];
#pragma unroll
      for (int t = 0; t < 8; t++) af[t] = *(const bf16x8*)(memL + t * 1024 + lane * 16);
      __builtin_amdgcn_sched_barrier(0);
#pragma unroll
      for (int t = 0; t < 8; t++)
#pragma unroll
        for (int i = 0; i < 4; i++) acc[i] = SMF(af[t], wf[i][t], acc[i], idx);
#pragma unroll
      for (int pr = 0; pr < 2; pr++) {
        float v[4];
#pragma unroll
        for (int j = 0; j < 4; j++) {
          auto r = __builtin_amdgcn_permlane32_swap(f2u(acc[2 * pr][j]), f2u(acc[2 * pr + 1][j]), false, false);
          v[j] = u2f(r[0]) + u2f(r[1]);
        }
        const int hl = 2 * (2 * pr + (lane >> 5)) + (n >> 3);
        u32x2 pv = {pk2(v[0], v[1]), pk2(v[2], v[3])};
        *(u32x2*)(yl + (hl * 8 + cc) * 16 + 8 * ((lane >> 4) & 1)) = pv;
      }
      asm volatile("s_waitcnt lgkmcnt(0)" ::: "memory");
      u32x4 hz[2];
#pragma unroll
      for (int z = 0; z < 2; z++) {
        const int ga = 2 * z + (gb >> 1);
        u32x2 a = *(const u32x2*)(yl + ((2 * ga) * 8 + cc) * 16 + 8 * (gb & 1));
        u32x2 b = *(const u32x2*)(yl + ((2 * ga + 1) * 8 + cc) * 16 + 8 * (gb & 1));
        hz[z] = (u32x4){a.x, b.x, a.y, b.y};
      }
      yb = as_bf16(hz[0], hz[1]);
      if (rho == 0) __builtin_amdgcn_raw_buffer_store_b128(*(const u32x4*)(yl + lane * 16), mkrsrc(ws + OFF_Y), (u32)(s * 64 + c) * 4096u + w * 1024u + lane * 16u, 0, 0);
    }
    STAMP(17 + 4 * s);
    if (s == NSEG - 1) break;
    bf16x16 ub16[4];
#pragma unroll
    for (int m = 0; m < 4; m++) { ub16[m] = as_bf16(uf[m][0], uf[m][1]); asm volatile("" : "+v"(ub16[m])); }
    asm volatile("" : "+v"(yb));
    f32x4 dA = {0.f, 0.f, 0.f, 0.f}, dB = {0.f, 0.f, 0.f, 0.f};
    u32 rdw = (u32)(12288 + ((s & 1) * 512 + cc * 16 + 4 * w + gb) * 4), rdr = (u32)(12288 + ((s & 1) * 512 + lane * 8) * 4);
    u32 mso = (u32)(s + 1) * 8192u + c * 128u + rho * 64u + (lane >> 2) * 4u;
    asm volatile("" : "+v"(dA), "+v"(dB), "+v"(rdw), "+v"(rdr), "+v"(mso));
    u32 nsw2;
    wait_until(tref + D2);
    {
      const u32 pb = (u32)s * 20480u + rho * 10240u;
      u32 off[3] = {pb + (2 * w) * 1024u + lane * 16u, pb + (2 * w + 1) * 1024u + lane * 16u,
                    pb + 8192u + (u32)((8 * w + 2 * gb + ((lane >> 3) & 1)) * 4 + (lane & 3)) * 16u};
      u32x4 g[3];
      nsw2 = poll_chunks<3>(rpex, off, g, 0u);
      dA = SMF(as_bf8(g[0]), ub16[0], dA, idx);
      dB = SMF(as_bf8(g[0]), ub16[1], dB, idx);
      dA = SMF(as_bf8(g[1]), ub16[2], dA, idx);
      dB = SMF(as_bf8(g[1]), ub16[3], dB, idx);
      dA = SMF(as_bf8(g[2]), yb, dA, idx);
      if (n < 8) {
#pragma unroll
        for (int j = 0; j < 4; j++) *(float*)(lds + rdw + j * 512) = (gb & 1) ? dB[j] : dA[j];
      }
    }
    __syncthreads();
    STAMP(18 + 4 * s);
    if (w == 0) {
      f32x4 a = *(const f32x4*)(lds + rdr), b = *(const f32x4*)(lds + rdr + 16);
      float v = ((a[0] + a[1]) + (a[2] + a[3])) + ((b[0] + b[1]) + (b[2] + b[3]));
      v += u2f((u32)__builtin_amdgcn_mov_dpp((int)f2u(v), 0xB1, 0xF, 0xF, true));
      float o = u2f((u32)__builtin_amdgcn_mov_dpp((int)f2u(v), 0x4E, 0xF, 0xF, true));
      if (!(lane & 3)) st4_sc1(sane(pk2(v, o)), rmem, mso);
      STAMP(19 + 4 * s);
    }
    tref = now32();
    if (!nsw2) return;
    if (s > 2) ADAPT(D2, nsw2);
  }
}

__device__ __forceinline__ void load_h(u32x4 (&r)[8], const char* ws, int s) {
  const __amdgpu_buffer_rsrc_t rh = mkrsrc(ws + OFF_H);
#pragma unroll
  for (int j = 0; j < 8; j++) r[j] = ld16_sc1(rh, (u32)s * 32768u + (j * 256 + threadIdx.x) * 16u);
}
__device__ __forceinline__ void put_h(char* dst, const u32x4 (&r)[8]) {
#pragma unroll
  for (int j = 0; j < 8; j++) {
    int u = j * 256 + threadIdx.x;
    *(u32x4*)(dst + (u >> 6) * 1040 + (u & 63) * 16) = r[j];
  }
}

template <bool SC1>
__device__ __forceinline__ void store_T_tile(const f32x16& acc, char* base, int tokrow, int h, int w, int lane) {
  const __amdgpu_buffer_rsrc_t r = mkrsrc(base);
#pragma unroll
  for (int qp = 0; qp < 2; qp++) {
    u32 ax = pk2(acc[8 * qp + 0], acc[8 * qp + 1]), ay = pk2(acc[8 * qp + 2], acc[8 * qp + 3]);
    u32 bx = pk2(acc[8 * qp + 4], acc[8 * qp + 5]), by = pk2(acc[8 * qp + 6], acc[8 * qp + 7]);
    auto r0 = __builtin_amdgcn_permlane32_swap(ax, bx, false, false);
    auto r1 = __builtin_amdgcn_permlane32_swap(ay, by, false, false);
    u32x4 v = {(u32)r0[0], (u32)r1[0], (u32)r0[1], (u32)r1[1]};
    u32 off = (u32)(((tokrow + (lane & 31)) * NH + h) * DH + 32 * w + 16 * qp + 8 * (lane >> 5)) * 2u;
    if (SC1) __builtin_amdgcn_raw_buffer_store_b128(v, r, off, 0, 16);
    else __builtin_amdgcn_raw_buffer_store_b128(v, r, off, 0, 0);
  }
}

__device__ __forceinline__ void role_vu(const u16* __restrict__ Wv, const u16* __restrict__ Wo,
                                        char* ws, char* lds, int h) {
  const int tid = threadIdx.x, lane = tid & 63, w = tid >> 6, x = lane & 31, hh = lane >> 5;
  bf16x8 wv[32], wo[4][8];
  dma_tile<256>(lds, Wv + h * DH, DM, NH * DH);
  dma_wait();
#pragma unroll
  for (int ks = 0; ks < 32; ks++) wv[ks] = frag8(lds, 16 * ks + 8 * hh, 32 * w + (x & 16), 256, lane);
  __syncthreads();
  dma_tile<1024>(lds, Wo + (size_t)h * DH * DM, DH, DM);
  dma_wait();
#pragma unroll
  for (int nt = 0; nt < 4; nt++)
#pragma unroll
    for (int ks = 0; ks < 8; ks++) wo[nt][ks] = frag8(lds, 16 * ks + 8 * hh, 128 * w + 32 * nt + (x & 16), 1024, lane);
  __syncthreads();
  char* vs = lds + 66560;
  const __amdgpu_buffer_rsrc_t rflag = mkrsrc(ws + OFF_FLAG);
  u32x4 hr[8];
  { u32 seenh = 0xFFFFFFFFu; if (!wait_progress(rflag, OFF_DONE + (lane & 31) * 64u, 1u, seenh)) return; }
  load_h(hr, ws, 0);
  put_h(lds, hr);
  load_h(hr, ws, 1);
  __syncthreads();
  for (int s = 0; s < NSEG; s++) {
    const char* hb = lds + (s & 1) * 33280;
    f32x16 av;
#pragma unroll
    for (int i = 0; i < 16; i++) av[i] = 0.f;
    {
      bf16x8 hf[2][8];
#pragma unroll
      for (int k = 0; k < 8; k++) hf[0][k] = *(const bf16x8*)(hb + x * 1040 + (16 * k + 8 * hh) * 2);
#pragma unroll
      for (int ch = 0; ch < 4; ch++) {
        if (ch < 3) {
#pragma unroll
          for (int k = 0; k < 8; k++) hf[(ch + 1) & 1][k] = *(const bf16x8*)(hb + x * 1040 + (16 * (8 * ch + 8 + k) + 8 * hh) * 2);
        }
        __builtin_amdgcn_sched_barrier(0);
#pragma unroll
        for (int k = 0; k < 8; k++) av = MFMA32(wv[8 * ch + k], hf[ch & 1][k], av);
        __builtin_amdgcn_sched_barrier(0);
      }
    }
#pragma unroll
    for (int q = 0; q < 4; q++) {
      u32x2 pv = {pk2(av[4 * q], av[4 * q + 1]), pk2(av[4 * q + 2], av[4 * q + 3])};
      *(u32x2*)(vs + x * 272 + (32 * w + 8 * q + 4 * hh) * 2) = pv;
    }
    put_h(lds + ((s + 1) & 1) * 33280, hr);
    asm volatile("s_waitcnt vmcnt(0)" ::: "memory");
    __syncthreads();
    if (tid == 0 && s > 4) st4_sc1((u32)s, rflag, h * 128u);
    bf16x8 vf[8];
#pragma unroll
    for (int ks = 0; ks < 8; ks++) vf[ks] = *(const bf16x8*)(vs + x * 272 + (16 * ks + 8 * hh) * 2);
    const __amdgpu_buffer_rsrc_t ru = mkrsrc(ws + OFF_UF + (size_t)s * 64 * 16384);
#pragma unroll
    for (int nt = 0; nt < 4; nt++) {
      f32x16 au;
#pragma unroll
      for (int i = 0; i < 16; i++) au[i] = 0.f;
#pragma unroll
      for (int ks = 0; ks < 8; ks++) au = MFMA32(vf[ks], wo[nt][ks], au);
      const int d = 128 * w + 32 * nt + x, c = d >> 3, cc = d & 7;
#pragma unroll
      for (int xb = 0; xb < 2; xb++)
        st16_sc1(PACK_S(au, 8 * xb), ru, (u32)((((c * 16 + (h >> 1)) * 2 + (h & 1)) * 4 + 2 * hh + xb) * 8 + cc) * 16u);
    }
    if (s < 4 || s == NSEG - 1) asm volatile("s_waitcnt vmcnt(0)" ::: "memory");
    if (s == 30) { u32 seenh = 0xFFFFFFFFu; if (!wait_progress(rflag, OFF_DONE + lane * 64u, 1u, seenh)) return; }
    load_h(hr, ws, s + 2 < NSEG ? s + 2 : NSEG - 1);
    __syncthreads();
    if (tid == 0 && (s < 4 || s == NSEG - 1)) st4_sc1((u32)(s + 1), rflag, h * 128u);
    STAMP(16 + 4 * s);
  }
}

__device__ __forceinline__ void role_qk(const u16* __restrict__ Wq, const u16* __restrict__ Wk, char* ws, char* lds, int h) {
  const int tid = threadIdx.x, lane = tid & 63, w = tid >> 6, x = lane & 31, hh = lane >> 5;
  bf16x8 wq[32], wk[32];
  dma_tile<256>(lds, Wk + h * DH, DM, NH * DH);
  dma_wait();
#pragma unroll
  for (int ks = 0; ks < 32; ks++) wk[ks] = frag8(lds, 16 * ks + 8 * hh, 32 * w + (x & 16), 256, lane);
  __syncthreads();
  dma_tile<256>(lds, Wq + h * DH, DM, NH * DH);
  dma_wait();
#pragma unroll
  for (int ks = 0; ks < 32; ks++) wq[ks] = frag8(lds, 16 * ks + 8 * hh, 32 * w + (x & 16), 256, lane);
  __syncthreads();
  const __amdgpu_buffer_rsrc_t rflag = mkrsrc(ws + OFF_FLAG);
  u32x4 hr[8];
  { u32 seenh = 0xFFFFFFFFu; if (!wait_progress(rflag, OFF_DONE + (lane & 31) * 64u, 1u, seenh)) return; }
  load_h(hr, ws, 0);
  put_h(lds, hr);
  load_h(hr, ws, 1);
  __syncthreads();
  for (int s = 0; s < NSEG; s++) {
    const char* hb = lds + (s & 1) * 33280;
    f32x16 aq, ak;
#pragma unroll
    for (int i = 0; i < 16; i++) { aq[i] = 0.f; ak[i] = 0.f; }
    {
      bf16x8 hf[2][8];
#pragma unroll
      for (int k = 0; k < 8; k++) hf[0][k] = *(const bf16x8*)(hb + x * 1040 + (16 * k + 8 * hh) * 2);
#pragma unroll
      for (int ch = 0; ch < 4; ch++) {
        if (ch < 3) {
#pragma unroll
          for (int k = 0; k < 8; k++) hf[(ch + 1) & 1][k] = *(const bf16x8*)(hb + x * 1040 + (16 * (8 * ch + 8 + k) + 8 * hh) * 2);
        }
        __builtin_amdgcn_sched_barrier(0);
#pragma unroll
        for (int k = 0; k < 8; k++) { ak = MFMA32(wk[8 * ch + k], hf[ch & 1][k], ak); aq = MFMA32(wq[8 * ch + k], hf[ch & 1][k], aq); }
        __builtin_amdgcn_sched_barrier(0);
      }
    }
    put_h(lds + ((s + 1) & 1) * 33280, hr);
    asm volatile("s_waitcnt vmcnt(0)" ::: "memory");
    __syncthreads();
    if (tid == 0 && s > 4) st4_sc1((u32)s, rflag, 4096u + h * 128u);
    store_T_tile<true>(ak, ws + OFF_KX, 32 * s, h, w, lane);
    store_T_tile<true>(aq, ws + OFF_QX, 32 * s, h, w, lane);
    if (s < 4 || s == NSEG - 1) {
      asm volatile("s_waitcnt vmcnt(0)" ::: "memory");
      __syncthreads();
      if (tid == 0) st4_sc1((u32)(s + 1), rflag, 4096u + h * 128u);
    }
    if (s == 30) { u32 seenh = 0xFFFFFFFFu; if (!wait_progress(rflag, OFF_DONE + lane * 64u, 1u, seenh)) return; }
    load_h(hr, ws, s + 2 < NSEG ? s + 2 : NSEG - 1);
    STAMP(16 + 4 * s);
  }
}

__device__ __forceinline__ float xg_max(float v) {
  auto r = __builtin_amdgcn_permlane16_swap(f2u(v), f2u(v), false, false);
  v = fmaxf(u2f(r[0]), u2f(r[1]));
  auto r2 = __builtin_amdgcn_permlane32_swap(f2u(v), f2u(v), false, false);
  return fmaxf(u2f(r2[0]), u2f(r2[1]));
}
__device__ __forceinline__ float xg_sum(float v) {
  auto r = __builtin_amdgcn_permlane16_swap(f2u(v), f2u(v), false, false);
  v = u2f(r[0]) + u2f(r[1]);
  auto r2 = __builtin_amdgcn_permlane32_swap(f2u(v), f2u(v), false, false);
  return u2f(r2[0]) + u2f(r2[1]);
}
__device__ __forceinline__ void px_task(char* ws, int s, int hg) {
  const int tid = threadIdx.x, lane = tid & 63, w = tid >> 6;
  const int hd = hg * 4 + w, hl = w;
  const int n = lane & 15, g = lane >> 4;
  const __amdgpu_buffer_rsrc_t rq = mkrsrc(ws + OFF_QX + (size_t)s * 32 * 8192);
  const __amdgpu_buffer_rsrc_t rk = mkrsrc(ws + OFF_KX + (size_t)s * 32 * 8192);
  const __amdgpu_buffer_rsrc_t rkm = mkrsrc(ws + OFF_KMEM + (size_t)s * 65536);
  const int keyrow = 16 * ((n >> 2) & 1) + 4 * (n >> 3) + (n & 3);
  const int keyb = 16 * (g & 1) + 4 * (g >> 1);
  const float sl = 0.08838834764831845f * 1.4426950408889634f;
  bf16x8 qf[2][4], kf[2][4], mf[4];
  const __amdgpu_buffer_rsrc_t rflag = mkrsrc(ws + OFF_FLAG);
  { u32 seenq = 0xFFFFFFFFu; wait_progress(rflag, 4096u + hd * 128u, s + 1, seenq); }
#pragma unroll
  for (int ks = 0; ks < 4; ks++) {
#pragma unroll
    for (int t = 0; t < 2; t++) {
      qf[t][ks] = as_bf8(ld16_sc1(rq, (u32)(((16 * t + n) * NH + hd) * DH + 32 * ks + 8 * g) * 2u));
      kf[t][ks] = as_bf8(ld16_sc1(rk, (u32)(((8 * t + keyrow) * NH + hd) * DH + 32 * ks + 8 * g) * 2u));
    }
  }
  {
    u32 spins = 0, off = OFF_KMF + (u32)hd * 64u;
    while (true) {
      asm volatile("" : "+v"(off) :: "memory");
      u32 v = ld4_sc1(rflag, off);
      if ((int)v > s) break;
      __builtin_amdgcn_s_sleep(6);
      if (++spins > 800000u) break;
    }
  }
#pragma unroll
  for (int ks = 0; ks < 4; ks++) mf[ks] = as_bf8(ld16_sc1(rkm, (u32)(hd * 1024 + (n & 7) * 128 + 32 * ks + 8 * g) * 2u));
  const bool mvalid = (g >> 1) == (hl >> 1);
  u32x4* PXx = (u32x4*)(ws + OFF_PX);
  char* PXm = ws + OFF_PX + (4ull << 20);
#pragma unroll
  for (int qt = 0; qt < 2; qt++) {
    f32x4 sc[3];
#pragma unroll
    for (int i = 0; i < 3; i++) sc[i] = (f32x4){0.f, 0.f, 0.f, 0.f};
#pragma unroll
    for (int ks = 0; ks < 4; ks++) {
      sc[0] = MFMA16(kf[0][ks], qf[qt][ks], sc[0]);
      sc[1] = MFMA16(kf[1][ks], qf[qt][ks], sc[1]);
      sc[2] = MFMA16(mf[ks], qf[qt][ks], sc[2]);
    }
    const int qi = 16 * qt + n;
    float mx = -3.0e38f;
#pragma unroll
    for (int j = 0; j < 4; j++) {
      if (keyb + j > qi) sc[0][j] = -3.0e38f;
      if (keyb + 8 + j > qi) sc[1][j] = -3.0e38f;
      if (!mvalid) sc[2][j] = -3.0e38f;
      mx = fmaxf(mx, fmaxf(fmaxf(sc[0][j], sc[1][j]), sc[2][j]));
    }
    mx = xg_max(mx);
    float sum = 0.f;
#pragma unroll
    for (int i = 0; i < 3; i++)
#pragma unroll
      for (int j = 0; j < 4; j++) {
        float p = sc[i][j] > -1.0e38f ? __builtin_amdgcn_exp2f((sc[i][j] - mx) * sl) : 0.f;
        sc[i][j] = p; sum += p;
      }
    const float inv = __builtin_amdgcn_rcpf(xg_sum(sum));
    u32x4 pa = {pk2(sc[0][0] * inv, sc[0][1] * inv), pk2(sc[1][0] * inv, sc[1][1] * inv),
                pk2(sc[0][2] * inv, sc[0][3] * inv), pk2(sc[1][2] * inv, sc[1][3] * inv)};
    PXx[(((size_t)s * NH + hd) * 2 + qt) * 64 + lane] = pa;
    if (mvalid) {
      u32x2 pm = {pk2(sc[2][0] * inv, sc[2][1] * inv), pk2(sc[2][2] * inv, sc[2][3] * inv)};
      *(u32x2*)(PXm + ((((size_t)s * 8 + hg) * 2 + qt) * 64 + lane) * 16 + (hl & 1) * 8) = pm;
    }
  }
}

__global__ void __launch_bounds__(256, 1) k2_rec(const u16* __restrict__ Wq, const u16* __restrict__ Wk, const u16* __restrict__ Wv,
                                                  const u16* __restrict__ Wo, const u16* __restrict__ x, const u16* __restrict__ w1,
                                                  const u16* __restrict__ w2, const u16* __restrict__ cvec,
                                                  const u16* __restrict__ wmod, const u16* __restrict__ mem0, char* ws) {
  extern __shared__ __attribute__((aligned(16))) char lds[];
  const int b = blockIdx.x;
  STAMP(0);
  if (b < 32) role_help(x, w1, w2, cvec, wmod, mem0, ws, lds, b);
  else if (b < 64) role_score(Wq, Wk, ws, lds, b - 32);
  else if (b < 192) { const int q = b - 64; role_column(Wv, Wo, ws, lds, (q & 7) | ((q >> 4) << 3), (q >> 3) & 1); }
  else if (b < 224) role_vu(Wv, Wo, ws, lds, b - 192);
  else role_qk(Wq, Wk, ws, lds, b - 224);
}

__device__ __forceinline__ void ph_oproj(const u16* __restrict__ x, char* ws, char* lds) {
  const int b = blockIdx.x, tid = threadIdx.x, lane = tid & 63, w = tid >> 6;
  const int s = (b & 7) * 8 + (b >> 5), nq = (b >> 3) & 3;
  const int n = lane & 15, g = lane >> 4, cc = lane & 7;
  const u32x4* UF = (const u32x4*)(ws + OFF_UF) + (size_t)s * 64 * 1024;
  const char* Yg = ws + OFF_Y + (size_t)s * 64 * 4096;
  const u32x4* PXx = (const u32x4*)(ws + OFF_PX) + (size_t)s * NH * 128;
  const u32x4* PXm = (const u32x4*)(ws + OFF_PX + (4ull << 20)) + (size_t)s * 8 * 128;
  f32x4 acc[2][8];
#pragma unroll
  for (int i = 0; i < 2; i++)
#pragma unroll
    for (int j = 0; j < 8; j++) acc[i][j] = (f32x4){0.f, 0.f, 0.f, 0.f};
  u32x4 ub[4][8], pa[4][2], pm[2], yf[8];
#pragma unroll
  for (int hl = 0; hl < 4; hl++) {
    const int hd = 4 * w + hl;
#pragma unroll
    for (int nt = 0; nt < 8; nt++) ub[hl][nt] = __builtin_nontemporal_load(UF + (size_t)(16 * nq + 2 * nt + (n >> 3)) * 1024 + (hd * 4 + g) * 8 + cc);
    pa[hl][0] = PXx[(hd * 2) * 64 + lane];
    pa[hl][1] = PXx[(hd * 2 + 1) * 64 + lane];
  }
  pm[0] = PXm[(w * 2) * 64 + lane];
  pm[1] = PXm[(w * 2 + 1) * 64 + lane];
  {
    const int ha = 4 * w + 2 * (g >> 1);
#pragma unroll
    for (int nt = 0; nt < 8; nt++) {
      const int c = 16 * nq + 2 * nt + (n >> 3);
      u32x2 ya = __builtin_nontemporal_load((const u32x2*)(Yg + (size_t)c * 4096 + (ha * 8 + cc) * 16 + 8 * (g & 1)));
      u32x2 yb = __builtin_nontemporal_load((const u32x2*)(Yg + (size_t)c * 4096 + ((ha + 1) * 8 + cc) * 16 + 8 * (g & 1)));
      yf[nt] = (u32x4){ya.x, ya.y, yb.x, yb.y};
    }
  }
  __builtin_amdgcn_sched_barrier(0);
#pragma unroll
  for (int hl = 0; hl < 4; hl++)
#pragma unroll
    for (int nt = 0; nt < 8; nt++) {
      acc[0][nt] = MFMA16(as_bf8(pa[hl][0]), as_bf8(ub[hl][nt]), acc[0][nt]);
      acc[1][nt] = MFMA16(as_bf8(pa[hl][1]), as_bf8(ub[hl][nt]), acc[1][nt]);
    }
#pragma unroll
  for (int nt = 0; nt < 8; nt++) {
    acc[0][nt] = MFMA16(as_bf8(pm[0]), as_bf8(yf[nt]), acc[0][nt]);
    acc[1][nt] = MFMA16(as_bf8(pm[1]), as_bf8(yf[nt]), acc[1][nt]);
  }
  float* red = (float*)lds;
#pragma unroll
  for (int qt = 0; qt < 2; qt++)
#pragma unroll
    for (int nt = 0; nt < 8; nt++)
#pragma unroll
      for (int j = 0; j < 4; j++) red[(w * 32 + 16 * qt + 4 * g + j) * 132 + 16 * nt + n] = acc[qt][nt][j];
  __syncthreads();
  const float* mod = (const float*)(ws + OFF_MOD);
  {
    const int r = tid >> 4, c8 = (tid & 15) * 8;
    float o[8];
#pragma unroll
    for (int j = 0; j < 8; j++) o[j] = 0.f;
#pragma unroll
    for (int ww = 0; ww < 8; ww++) {
      f32x4 a = *(const f32x4*)(red + (ww * 32 + r) * 132 + c8), bq = *(const f32x4*)(red + (ww * 32 + r) * 132 + c8 + 4);
#pragma unroll
      for (int j = 0; j < 4; j++) { o[j] += a[j]; o[4 + j] += bq[j]; }
    }
    const int col = 128 * nq + c8;
    const size_t go = (size_t)(32 * s + r) * DM + col;
    u32x4 xv = *(const u32x4*)(x + go);
    float xf[8] = {bf_lo(xv.x), bf_hi(xv.x), bf_lo(xv.y), bf_hi(xv.y), bf_lo(xv.z), bf_hi(xv.z), bf_lo(xv.w), bf_hi(xv.w)};
#pragma unroll
    for (int j = 0; j < 8; j++) o[j] = xf[j] + bfr(mod[1024 + col + j] * bfr(o[j]));
    u32x4 rv = {pk2(o[0], o[1]), pk2(o[2], o[3]), pk2(o[4], o[5]), pk2(o[6], o[7])};
    st16_sc1(rv, mkrsrc(ws + OFF_X1), (u32)go * 2u);
  }
  asm volatile("s_waitcnt vmcnt(0)" ::: "memory");
  __syncthreads();
  if (tid == 0) st4_sc1(1u, mkrsrc(ws + OFF_FLAG), OFF_X1F + (u32)(s * 4 + nq) * 4u);
}

__device__ __forceinline__ void ph_mlp1(const u16* __restrict__ b1, char* ws, char* lds) {
  const int b = blockIdx.x, tid = threadIdx.x, lane = tid & 63, w = tid >> 6;
  const int jq = b >> 3, mt0 = (b & 7) * 4 + jq / 7, nb = jq % 7;
  const float* mod = (const float*)(ws + OFF_MOD);
  const int n = lane & 15, g = lane >> 4;
  const f32x4 mv = *(const f32x4*)(mod + 1536 + (tid & 255) * 4);
  const u32x4* WF = (const u32x4*)(ws + OFF_W1F) + (size_t)(nb * 16 + w * 2) * 16 * 64 + lane;
  u32x4 bfm[2][16];
#pragma unroll
  for (int nt = 0; nt < 2; nt++)
#pragma unroll
    for (int ks = 0; ks < 8; ks++) bfm[nt][ks] = WF[(nt * 16 + ks) * 64];
  __builtin_amdgcn_sched_barrier(0);
  { u32 seenx = 0xFFFFFFFFu; wait_progress(mkrsrc(ws + OFF_FLAG), OFF_X1F + (u32)(mt0 * 8 + (lane & 7)) * 4u, 1u, seenx); }
  u32x4 xv[2][4];
#pragma unroll
  for (int it = 0; it < 2; it++)
#pragma unroll
    for (int j = 0; j < 4; j++) xv[it][j] = ld16_sc1(mkrsrc(ws + OFF_X1), (u32)((mt0 * 64 + it * 32 + w * 4 + g) * DM + (j * 16 + n) * 8) * 2u);
  __builtin_amdgcn_sched_barrier(0);
  float* modl = (float*)(lds + 66560);
  if (tid < 256) *(f32x4*)(modl + tid * 4) = tid >= 128 ? mv + 1.f : mv;
  __syncthreads();
#pragma unroll
  for (int it = 0; it < 2; it++) {
    f32x2 f[16];
#pragma unroll
    for (int j = 0; j < 4; j++) {
      f[4 * j + 0] = (f32x2){bf_lo(xv[it][j].x), bf_hi(xv[it][j].x)}; f[4 * j + 1] = (f32x2){bf_lo(xv[it][j].y), bf_hi(xv[it][j].y)};
      f[4 * j + 2] = (f32x2){bf_lo(xv[it][j].z), bf_hi(xv[it][j].z)}; f[4 * j + 3] = (f32x2){bf_lo(xv[it][j].w), bf_hi(xv[it][j].w)};
    }
    f32x2 s2 = {0.f, 0.f};
#pragma unroll
    for (int i = 0; i < 16; i++) s2 += f[i];
    float m = row16_sum(s2.x + s2.y) * (1.f / DM);
    f32x2 q2 = {0.f, 0.f};
#pragma unroll
    for (int i = 0; i < 16; i++) { f[i] -= m; q2 += f[i] * f[i]; }
    float rs = rsqrtf(row16_sum(q2.x + q2.y) * (1.f / DM) + 1e-5f);
    s2 = (f32x2){0.f, 0.f};
#pragma unroll
    for (int j = 0; j < 4; j++) {
      const int c0 = (j * 16 + n) * 8;
      f32x4 c0v = *(const f32x4*)(modl + 512 + c0), c1v = *(const f32x4*)(modl + 512 + c0 + 4);
      f32x4 s0v = *(const f32x4*)(modl + c0), s1v = *(const f32x4*)(modl + c0 + 4);
      f[4 * j + 0] = f[4 * j + 0] * rs * (f32x2){c0v[0], c0v[1]} + (f32x2){s0v[0], s0v[1]};
      f[4 * j + 1] = f[4 * j + 1] * rs * (f32x2){c0v[2], c0v[3]} + (f32x2){s0v[2], s0v[3]};
      f[4 * j + 2] = f[4 * j + 2] * rs * (f32x2){c1v[0], c1v[1]} + (f32x2){s1v[0], s1v[1]};
      f[4 * j + 3] = f[4 * j + 3] * rs * (f32x2){c1v[2], c1v[3]} + (f32x2){s1v[2], s1v[3]};
      s2 += (f[4 * j + 0] + f[4 * j + 1]) + (f[4 * j + 2] + f[4 * j + 3]);
    }
    m = row16_sum(s2.x + s2.y) * (1.f / DM);
    q2 = (f32x2){0.f, 0.f};
#pragma unroll
    for (int i = 0; i < 16; i++) { f[i] -= m; q2 += f[i] * f[i]; }
    rs = rsqrtf(row16_sum(q2.x + q2.y) * (1.f / DM) + 1e-5f);
#pragma unroll
    for (int j = 0; j < 4; j++) {
      f32x2 o0 = f[4 * j] * rs, o1 = f[4 * j + 1] * rs, o2 = f[4 * j + 2] * rs, o3 = f[4 * j + 3] * rs;
      u32x4 o = {pk2(o0.x, o0.y), pk2(o1.x, o1.y), pk2(o2.x, o2.y), pk2(o3.x, o3.y)};
      *(u32x4*)(lds + (it * 32 + w * 4 + g) * 1040 + (j * 16 + n) * 16) = o;
    }
    __builtin_amdgcn_sched_barrier(0);
  }
#pragma unroll
  for (int nt = 0; nt < 2; nt++)
#pragma unroll
    for (int ks = 8; ks < 16; ks++) bfm[nt][ks] = WF[(nt * 16 + ks) * 64];
  __builtin_amdgcn_sched_barrier(0);
  __syncthreads();
  f32x4 acc[4][2];
#pragma unroll
  for (int i = 0; i < 4; i++)
#pragma unroll
    for (int j = 0; j < 2; j++) acc[i][j] = (f32x4){0.f, 0.f, 0.f, 0.f};
#pragma unroll
  for (int ks = 0; ks < 16; ks++) {
    bf16x8 af[4];
#pragma unroll
    for (int mt = 0; mt < 4; mt++) af[mt] = *(const bf16x8*)(lds + (16 * mt + n) * 1040 + (32 * ks + 8 * g) * 2);
#pragma unroll
    for (int mt = 0; mt < 4; mt++)
#pragma unroll
      for (int nt = 0; nt < 2; nt++) acc[mt][nt] = MFMA16(as_bf8(bfm[nt][ks]), af[mt], acc[mt][nt]);
    if (ks & 1) __builtin_amdgcn_sched_barrier(0);
  }
  float bb[2][4];
#pragma unroll
  for (int nt = 0; nt < 2; nt++) {
    u32x2 bv = *(const u32x2*)(b1 + nb * 256 + w * 32 + 16 * nt + 4 * g);
    bb[nt][0] = bf_lo(bv.x); bb[nt][1] = bf_hi(bv.x); bb[nt][2] = bf_lo(bv.y); bb[nt][3] = bf_hi(bv.y);
  }
#pragma unroll
  for (int mt = 0; mt < 4; mt++) {
    float o[8];
#pragma unroll
    for (int nt = 0; nt < 2; nt++)
#pragma unroll
      for (int j = 0; j < 4; j++) {
        float u = bfr(bfr(acc[mt][nt][j]) + bb[nt][j]);
        float z = 0.7978845608028654f * (u + 0.044715f * u * u * u);
        float e = __builtin_amdgcn_exp2f(z * 2.8853900817779268f);
        float th = 1.f - 2.f * __builtin_amdgcn_rcpf(e + 1.f);
        o[4 * nt + j] = 0.5f * u * (1.f + th);
      }
    u32x4 ov = {pk2(o[0], o[1]), pk2(o[2], o[3]), pk2(o[4], o[5]), pk2(o[6], o[7])};
    st16_sc1(ov, mkrsrc(ws + OFF_HID), (u32)(((mt0 * 4 + mt) * 56 + nb * 8 + w) * 64 + lane) * 16u);
  }
  asm volatile("s_waitcnt vmcnt(0)" ::: "memory");
  __syncthreads();
  if (tid == 0) st4_sc1(1u, mkrsrc(ws + OFF_FLAG), OFF_HDF + (u32)(mt0 * 8 + nb) * 4u);
}

__device__ __forceinline__ void ph_mlp2(const u16* __restrict__ b2, char* ws, u16* __restrict__ out, char* lds) {
  const int b = blockIdx.x, tid = threadIdx.x, lane = tid & 63, w = tid >> 6;
  const int mt0 = (b & 7) * 4 + (b >> 6), nb = (b >> 3) & 7;
  const int n = lane & 15, g = lane >> 4;
  const __amdgpu_buffer_rsrc_t rhid = mkrsrc(ws + OFF_HID);
  const u32 hbase = (u32)(((mt0 * 4) * 56 + w * 7) * 64 + lane) * 16u;
  { u32 seenh2 = 0xFFFFFFFFu; const int fl = (lane & 7) < 7 ? (lane & 7) : 0; wait_progress(mkrsrc(ws + OFF_FLAG), OFF_HDF + (u32)(mt0 * 8 + fl) * 4u, 1u, seenh2); }
  const u32x4* WF = (const u32x4*)(ws + OFF_W2F) + ((size_t)(nb * 4) * 56 + w * 7) * 64 + lane;
  f32x4 acc[4][4];
#pragma unroll
  for (int i = 0; i < 4; i++)
#pragma unroll
    for (int j = 0; j < 4; j++) acc[i][j] = (f32x4){0.f, 0.f, 0.f, 0.f};
#pragma unroll
  for (int hb = 0; hb < 2; hb++) {
    const int nk = hb ? 3 : 4;
    u32x4 bfm[4][4]; bf16x8 af[4][4];
#pragma unroll
    for (int k = 0; k < 4; k++) if (k < nk) {
#pragma unroll
      for (int nt = 0; nt < 4; nt++) bfm[k][nt] = WF[(nt * 56 + hb * 4 + k) * 64];
#pragma unroll
      for (int mt = 0; mt < 4; mt++) af[k][mt] = as_bf8(ld16_sc1(rhid, hbase + (u32)((mt * 56 + hb * 4 + k) * 64) * 16u));
    }
    __builtin_amdgcn_sched_barrier(0);
#pragma unroll
    for (int k = 0; k < 4; k++) if (k < nk) {
#pragma unroll
      for (int mt = 0; mt < 4; mt++)
#pragma unroll
        for (int nt = 0; nt < 4; nt++) acc[mt][nt] = MFMA16(as_bf8(bfm[k][nt]), af[k][mt], acc[mt][nt]);
    }
  }
  float* red = (float*)lds;
#pragma unroll
  for (int mt = 0; mt < 4; mt++)
#pragma unroll
    for (int nt = 0; nt < 4; nt++) *(f32x4*)(red + (w * 64 + 16 * mt + n) * 68 + 16 * nt + 4 * g) = acc[mt][nt];
  __syncthreads();
  const float* mod = (const float*)(ws + OFF_MOD);
  {
    const int r = tid >> 3, c8 = (tid & 7) * 8;
    float o[8];
#pragma unroll
    for (int j = 0; j < 8; j++) o[j] = 0.f;
#pragma unroll
    for (int ww = 0; ww < 8; ww++) {
      f32x4 a = *(const f32x4*)(red + (ww * 64 + r) * 68 + c8), bq = *(const f32x4*)(red + (ww * 64 + r) * 68 + c8 + 4);
#pragma unroll
      for (int j = 0; j < 4; j++) { o[j] += a[j]; o[4 + j] += bq[j]; }
    }
    const int col = 64 * nb + c8;
    const size_t go = (size_t)(mt0 * 64 + r) * DM + col;
    u32x4 xv = ld16_sc1(mkrsrc(ws + OFF_X1), (u32)go * 2u), bv = *(const u32x4*)(b2 + col);
    float xf[8] = {bf_lo(xv.x), bf_hi(xv.x), bf_lo(xv.y), bf_hi(xv.y), bf_lo(xv.z), bf_hi(xv.z), bf_lo(xv.w), bf_hi(xv.w)};
    float bb[8] = {bf_lo(bv.x), bf_hi(bv.x), bf_lo(bv.y), bf_hi(bv.y), bf_lo(bv.z), bf_hi(bv.z), bf_lo(bv.w), bf_hi(bv.w)};
#pragma unroll
    for (int j = 0; j < 8; j++) o[j] = xf[j] + bfr(mod[2560 + col + j] * bfr(bfr(o[j]) + bb[j]));
    u32x4 rv = {pk2(o[0], o[1]), pk2(o[2], o[3]), pk2(o[4], o[5]), pk2(o[6], o[7])};
    *(u32x4*)(out + go) = rv;
  }
}

__global__ void __launch_bounds__(512, 2) k3_post(const u16* __restrict__ x, const u16* __restrict__ b1, const u16* __restrict__ b2,
                                                   char* ws, u16* __restrict__ out) {
  extern __shared__ __attribute__((aligned(16))) char lds[];
  ph_oproj(x, ws, lds);
  __syncthreads();
  if (blockIdx.x < 224) ph_mlp1(b1, ws, lds);
  __syncthreads();
  ph_mlp2(b2, ws, out, lds);
}

extern "C" void kernel_launch(void* const* d_in, const int* in_sizes, int n_in, void* d_out, int out_size, void* d_ws,
                              size_t ws_size, hipStream_t stream) {
  const u16* x = (const u16*)d_in[0];
  const u16* mem0 = (const u16*)d_in[1];
  const u16* Wq = (const u16*)d_in[2];
  const u16* Wk = (const u16*)d_in[3];
  const u16* Wv = (const u16*)d_in[4];
  const u16* Wo = (const u16*)d_in[5];
  const u16* w1 = (const u16*)d_in[8];
  const u16* b1 = (const u16*)d_in[9];
  const u16* w2 = (const u16*)d_in[10];
  const u16* b2 = (const u16*)d_in[11];
  const u16* cvec = (const u16*)d_in[12];
  const u16* wmod = (const u16*)d_in[13];
  char* ws = (char*)d_ws;
  hipFuncSetAttribute((const void*)k2_rec, hipFuncAttributeMaxDynamicSharedMemorySize, 135168);
  hipMemsetAsync(ws, 0xFF, OFF_H, stream);
  k2_rec<<<256, 256, 135168, stream>>>(Wq, Wk, Wv, Wo, x, w1, w2, cvec, wmod, mem0, ws);
  hipFuncSetAttribute((const void*)k3_post, hipFuncAttributeMaxDynamicSharedMemorySize, 139264);
  k3_post<<<256, 512, 139264, stream>>>(x, b1, b2, ws, (u16*)d_out);
}
```

```cpp
#include <hip/hip_runtime.h>
#include <hip/hip_bf16.h>
#include <stdint.h>

typedef unsigned int u32;
typedef unsigned short u16;
typedef unsigned long long u64;
typedef __attribute__((ext_vector_type(2))) __bf16 bf16x2;
typedef __attribute__((ext_vector_type(8))) __bf16 bf16x8;
typedef __attribute__((ext_vector_type(16))) __bf16 bf16x16;
typedef __attribute__((ext_vector_type(2))) float f32x2;
typedef __attribute__((ext_vector_type(4))) float f32x4;
typedef __attribute__((ext_vector_type(16))) float f32x16;
typedef __attribute__((ext_vector_type(2))) u32 u32x2;
typedef __attribute__((ext_vector_type(4))) u32 u32x4;
typedef __attribute__((ext_vector_type(8))) u32 u32x8;

#define NTOK 2048
#define DM 512
#define NH 32
#define DH 128
#define NSEG 64
#define FF 1792
#define SENT 0xFFFFFFFFu

#define OFF_FLAG 0ull
#define OFF_DONE 8192u
#define OFF_KMF 12288u
#define OFF_WVF 14336u
#define OFF_MDF 28672u
#define OFF_X1F 29696u
#define OFF_HDF 30720u
#define OFF_MOD 16384ull
#define OFF_MEMX 32768ull
#define OFF_PEX 0x90000ull
#define OFF_H (2ull << 20)
#define OFF_KMEM (4ull << 20)
#define OFF_WVO (8ull << 20)
#define OFF_KX (24ull << 20)
#define OFF_QX (40ull << 20)
#define OFF_Y (56ull << 20)
#define OFF_UF (72ull << 20)
#define OFF_PX (136ull << 20)
#define OFF_X1 (142ull << 20)
#define OFF_Y2 (146ull << 20)
#define OFF_HID (148ull << 20)
#define OFF_W1F (156ull << 20)
#define OFF_W2F (158ull << 20)
#define OFF_DBG (200ull << 20)
#define STAMP(slot) do { } while (0)

__device__ __forceinline__ u32 pk2(float a, float b) {
  bf16x2 v; v[0] = (__bf16)a; v[1] = (__bf16)b; return __builtin_bit_cast(u32, v);
}
__device__ __forceinline__ u32 f2u(float x) { return __builtin_bit_cast(u32, x); }
__device__ __forceinline__ float u2f(u32 x) { return __builtin_bit_cast(float, x); }
__device__ __forceinline__ float bf_lo(u32 x) { return __builtin_bit_cast(float, x << 16); }
__device__ __forceinline__ float bf_hi(u32 x) { return __builtin_bit_cast(float, x & 0xffff0000u); }
__device__ __forceinline__ float bfr(float a) { return (float)(__bf16)a; }
__device__ __forceinline__ __amdgpu_buffer_rsrc_t mkrsrc(const void* p) {
  return __builtin_amdgcn_make_buffer_rsrc((void*)p, 0, 0x7fffffff, 0x00020000);
}
__device__ __forceinline__ u32x4 ld16_sc1(__amdgpu_buffer_rsrc_t r, u32 off) { return __builtin_amdgcn_raw_buffer_load_b128(r, off, 0, 16); }
__device__ __forceinline__ u32 ld4_sc1(__amdgpu_buffer_rsrc_t r, u32 off) { return __builtin_amdgcn_raw_buffer_load_b32(r, off, 0, 16); }
__device__ __forceinline__ void st16_sc1(u32x4 v, __amdgpu_buffer_rsrc_t r, u32 off) { __builtin_amdgcn_raw_buffer_store_b128(v, r, off, 0, 16); }
__device__ __forceinline__ void st8_sc1(u32x2 v, __amdgpu_buffer_rsrc_t r, u32 off) { __builtin_amdgcn_raw_buffer_store_b64(v, r, off, 0, 16); }
__device__ __forceinline__ void st4_sc1(u32 v, __amdgpu_buffer_rsrc_t r, u32 off) { __builtin_amdgcn_raw_buffer_store_b32(v, r, off, 0, 16); }
__device__ __forceinline__ u32 badm(u32x4 a) { return (u32)(a.x == SENT) | (u32)(a.y == SENT) | (u32)(a.z == SENT) | (u32)(a.w == SENT); }
__device__ __forceinline__ u32 sane(u32 x) { return x < 0xFFFFFFFEu ? x : 0xFFFFFFFEu; }
__device__ __forceinline__ u32 now32() { return (u32)__builtin_amdgcn_s_memtime(); }
__device__ __forceinline__ void wait_until(u32 t) { while ((int)(t - now32()) > 0) { } }
__device__ __forceinline__ bf16x8 as_bf8(u32x4 v) { return __builtin_bit_cast(bf16x8, v); }
__device__ __forceinline__ bf16x16 as_bf16(u32x4 a, u32x4 b) {
  u32x8 v = {a.x, a.y, a.z, a.w, b.x, b.y, b.z, b.w}; return __builtin_bit_cast(bf16x16, v);
}
__device__ __forceinline__ float max3r(float a, float b, float c) { float r; asm("v_max3_f32 %0, %1, %2, %3" : "=v"(r) : "v"(a), "v"(b), "v"(c)); return r; }
__device__ __forceinline__ float max2r(float a, float b) { float r; asm("v_max_f32 %0, %1, %2" : "=v"(r) : "v"(a), "v"(b)); return r; }
#define MFMA16(a, b, c) __builtin_amdgcn_mfma_f32_16x16x32_bf16((a), (b), (c), 0, 0, 0)
#define MFMA32(a, b, c) __builtin_amdgcn_mfma_f32_32x32x16_bf16((a), (b), (c), 0, 0, 0)
#define SMF(a, b, c, idx) __builtin_amdgcn_smfmac_f32_16x16x64_bf16((a), (b), (c), (idx), 0, 0)

__device__ __forceinline__ u32 umax3(u32 a, u32 b, u32 c) { u32 m = a > b ? a : b; return m > c ? m : c; }
template <int N>
__device__ __forceinline__ u32 poll_chunks(__amdgpu_buffer_rsrc_t r, const u32 (&off)[N], u32x4 (&a)[N], u32 lane16) {
  u32 spins = 0;
  while (true) {
    asm volatile("" : "+v"(lane16) :: "memory");
#pragma unroll
    for (int j = 0; j < N; j++) a[j] = ld16_sc1(r, off[j] + lane16);
    u32 m = 0;
#pragma unroll
    for (int j = 0; j < N; j++) { m = umax3(m, a[j].x, a[j].y); m = umax3(m, a[j].z, a[j].w); }
    ++spins;
    if (!__any(m == SENT)) return spins;
    if (spins > 400000u) return 0;
  }
}

typedef __attribute__((ext_vector_type(4))) short s16x4;
typedef __attribute__((address_space(3))) s16x4 lds_s16x4;
template <int ROWB>
__device__ __forceinline__ void dma_tile(char* lds, const u16* src, int rows, int gstride) {
  constexpr int LPR = ROWB / 16;
  const int total = rows * LPR;
  for (int u = threadIdx.x; u < total; u += 256) {
    const int r = u / LPR, c = u % LPR;
    __builtin_amdgcn_global_load_lds((const u32*)(src + (size_t)r * gstride + c * 8), (u32*)(lds + u * 16), 16, 0, 0);
  }
}
__device__ __forceinline__ void dma_wait() { asm volatile("s_waitcnt vmcnt(0)" ::: "memory"); __syncthreads(); }
__device__ __forceinline__ u32x2 tr4(const char* lds, int row0, int col0, int pitch, int lane) {
  const char* p = lds + (row0 + ((lane & 15) >> 2)) * pitch + (col0 + 4 * (lane & 3)) * 2;
  s16x4 v = __builtin_amdgcn_ds_read_tr16_b64_v4i16((lds_s16x4*)p);
  return __builtin_bit_cast(u32x2, v);
}
__device__ __forceinline__ bf16x8 frag8(const char* lds, int k0, int col0, int pitch, int lane) {
  u32x2 a = tr4(lds, k0, col0, pitch, lane), b = tr4(lds, k0 + 4, col0, pitch, lane);
  return as_bf8((u32x4){a.x, a.y, b.x, b.y});
}
__device__ __forceinline__ u32x4 frag8s(const char* lds, int k0, int col0, int pitch, int lane) {
  u32x2 a = tr4(lds, k0, col0, pitch, lane), b = tr4(lds, k0 + 8, col0, pitch, lane);
  return (u32x4){a.x, b.x, a.y, b.y};
}
#define PACK_S(acc, b) (u32x4){pk2((acc)[(b)], (acc)[(b) + 1]), pk2((acc)[(b) + 4], (acc)[(b) + 5]), pk2((acc)[(b) + 2], (acc)[(b) + 3]), pk2((acc)[(b) + 6], (acc)[(b) + 7])}

__device__ __forceinline__ void wvo_task(const u16* __restrict__ Wv, const u16* __restrict__ Wo, char* ws, char* lds, int task) {
  const int tid = threadIdx.x, lane = tid & 63, w = tid >> 6;
  const int h = task >> 2, nq = task & 3, x = lane & 31, hh = lane >> 5;
  dma_tile<256>(lds, Wo + (size_t)h * DH * DM + nq * 128, DH, DM);
  bf16x8 a[4][8];
#pragma unroll
  for (int mt = 0; mt < 4; mt++)
#pragma unroll
    for (int ks = 0; ks < 8; ks++)
      a[mt][ks] = *(const bf16x8*)(Wv + ((size_t)(128 * w + 32 * mt + x) * NH + h) * DH + 16 * ks + 8 * hh);
  dma_wait();
  const __amdgpu_buffer_rsrc_t rd = mkrsrc(ws + OFF_WVO);
#pragma unroll
  for (int nt = 0; nt < 4; nt++) {
    bf16x8 bw[8];
#pragma unroll
    for (int ks = 0; ks < 8; ks++) bw[ks] = frag8(lds, 16 * ks + 8 * hh, 32 * nt + (x & 16), 256, lane);
#pragma unroll
    for (int mt = 0; mt < 4; mt++) {
      f32x16 acc;
#pragma unroll
      for (int i = 0; i < 16; i++) acc[i] = 0.f;
#pragma unroll
      for (int ks = 0; ks < 8; ks++) acc = MFMA32(a[mt][ks], bw[ks], acc);
      const int d = 128 * nq + 32 * nt + x, c = d >> 3, cc = d & 7;
#pragma unroll
      for (int xb = 0; xb < 2; xb++) {
        const int dp = 128 * w + 32 * mt + 16 * xb, t = dp >> 6, ga = (dp >> 4) & 3, z = ga >> 1, gb = 2 * (ga & 1) + hh;
        const int lp = (h & 1) * 8 + cc + 16 * gb;
        u32 o = (u32)(((((c * 4 + (h >> 3)) * 4 + ((h >> 1) & 3)) * 8 + t) * 2 + z) * 64 + lp);
        st16_sc1(PACK_S(acc, 8 * xb), rd, o * 16u);
      }
    }
  }
  asm volatile("s_waitcnt vmcnt(0)" ::: "memory");
  __syncthreads();
  if (tid == 0) st4_sc1(1u, mkrsrc(ws + OFF_FLAG), OFF_WVF + task * 16u);
}

__device__ __forceinline__ void mod_cols(const u16* __restrict__ cvec, const u16* __restrict__ wmod, char* ws, char* lds, int col0) {
  const int tid = threadIdx.x, cp = tid & 15, ksl = tid >> 4;
  float* red = (float*)lds;
  u32 wv[32];
#pragma unroll
  for (int k = 0; k < 32; k++) wv[k] = *(const u32*)(wmod + (size_t)(ksl * 32 + k) * 3072 + col0 + cp * 2);
  float* sl_ = (float*)(lds + 2048);
  __syncthreads();
  {
    u32 cw = *(const u32*)(cvec + tid * 2);
    float c0 = bf_lo(cw), c1 = bf_hi(cw);
    sl_[tid * 2] = bfr(c0 / (1.f + __expf(-c0)));
    sl_[tid * 2 + 1] = bfr(c1 / (1.f + __expf(-c1)));
  }
  __syncthreads();
  float a0 = 0.f, a1 = 0.f;
#pragma unroll
  for (int k4 = 0; k4 < 8; k4++) {
    f32x4 sv = *(const f32x4*)(sl_ + ksl * 32 + k4 * 4);
#pragma unroll
    for (int j = 0; j < 4; j++) { a0 += sv[j] * bf_lo(wv[k4 * 4 + j]); a1 += sv[j] * bf_hi(wv[k4 * 4 + j]); }
  }
  red[ksl * 32 + cp * 2] = a0; red[ksl * 32 + cp * 2 + 1] = a1;
  __syncthreads();
  if (tid < 32) {
    float sacc = 0.f;
#pragma unroll
    for (int k = 0; k < 16; k++) sacc += red[k * 32 + tid];
    st4_sc1(f2u(bfr(sacc)), mkrsrc(ws + OFF_MOD), (u32)(col0 + tid) * 4u);
  }
}

__device__ __forceinline__ float row16_sum(float v) {
  v += u2f((u32)__builtin_amdgcn_mov_dpp((int)f2u(v), 0x128, 0xF, 0xF, true));
  v += u2f((u32)__builtin_amdgcn_mov_dpp((int)f2u(v), 0x124, 0xF, 0xF, true));
  v += u2f((u32)__builtin_amdgcn_mov_dpp((int)f2u(v), 0x122, 0xF, 0xF, true));
  v += u2f((u32)__builtin_amdgcn_mov_dpp((int)f2u(v), 0x121, 0xF, 0xF, true));
  return v;
}

__device__ __forceinline__ bool wait_progress(__amdgpu_buffer_rsrc_t rf, u32 off, u32 need, u32& seen) {
  u32 spins = 0;
  if (__all((int)seen >= (int)need)) return true;
  while (true) {
    asm volatile("" : "+v"(off) :: "memory");
    seen = ld4_sc1(rf, off);
    if (__all((int)seen >= (int)need)) return true;
    __builtin_amdgcn_s_sleep(2);
    if (++spins > 400000u) return false;
  }
}

__device__ __forceinline__ void px_task(char* ws, int s, int hg);
__device__ __forceinline__ void role_help(const u16* __restrict__ x, const u16* __restrict__ w1, const u16* __restrict__ w2,
                                          const u16* __restrict__ cvec, const u16* __restrict__ wmod, const u16* __restrict__ mem0,
                                          char* ws, char* lds, int a0) {
  const int tid = threadIdx.x, lane = tid & 63, w = tid >> 6, n = lane & 15, g = lane >> 4;
  u32x4 xpre[2][2][4];
#pragma unroll
  for (int sg = 0; sg < 2; sg++)
#pragma unroll
    for (int it = 0; it < 2; it++)
#pragma unroll
      for (int j = 0; j < 4; j++) xpre[sg][it][j] = *(const u32x4*)(x + (size_t)((a0 + 32 * sg) * 32 + it * 16 + w * 4 + g) * DM + (j * 16 + n) * 8);
  if (a0 == 31) {
    for (int i = tid; i < 512; i += 256) {
      u32x4 v = *(const u32x4*)(mem0 + (i & 7) * DM + (i >> 3) * 8);
      v.x = sane(v.x); v.y = sane(v.y); v.z = sane(v.z); v.w = sane(v.w);
      st16_sc1(v, mkrsrc(ws + OFF_MEMX), (u32)i * 16u);
    }
  }
  mod_cols(cvec, wmod, ws, lds, a0 * 32);
  asm volatile("s_waitcnt vmcnt(0)" ::: "memory");
  __syncthreads();
  if (tid == 0) st4_sc1(1u, mkrsrc(ws + OFF_FLAG), OFF_MDF + a0 * 16u);
  f32x2 fz[2][2][16];
#pragma unroll
  for (int sg = 0; sg < 2; sg++)
#pragma unroll
    for (int it = 0; it < 2; it++) {
      f32x2* f = fz[sg][it];
#pragma unroll
      for (int j = 0; j < 4; j++) {
        const u32x4 v = xpre[sg][it][j];
        f[4 * j + 0] = (f32x2){bf_lo(v.x), bf_hi(v.x)}; f[4 * j + 1] = (f32x2){bf_lo(v.y), bf_hi(v.y)};
        f[4 * j + 2] = (f32x2){bf_lo(v.z), bf_hi(v.z)}; f[4 * j + 3] = (f32x2){bf_lo(v.w), bf_hi(v.w)};
      }
      f32x2 s2 = {0.f, 0.f};
#pragma unroll
      for (int i = 0; i < 16; i++) s2 += f[i];
      const float m = row16_sum(s2.x + s2.y) * (1.f / DM);
      f32x2 q2 = {0.f, 0.f};
#pragma unroll
      for (int i = 0; i < 16; i++) { f[i] -= m; q2 += f[i] * f[i]; }
      const float rs = rsqrtf(row16_sum(q2.x + q2.y) * (1.f / DM) + 1e-5f);
#pragma unroll
      for (int i = 0; i < 16; i++) f[i] *= rs;
    }
  { u32 seenm = 0xFFFFFFFFu; if (!wait_progress(mkrsrc(ws + OFF_FLAG), OFF_MDF + (lane & 31) * 16u, 1u, seenm)) return; }
  float* modl = (float*)(lds + 8192);
  {
    u32x4 mv = ld16_sc1(mkrsrc(ws + OFF_MOD), (u32)tid * 16u);
    f32x4 mf = {u2f(mv.x), u2f(mv.y), u2f(mv.z), u2f(mv.w)};
    *(f32x4*)(modl + tid * 4) = tid >= 128 ? mf + 1.f : mf;
  }
  __syncthreads();
  const __amdgpu_buffer_rsrc_t rh = mkrsrc(ws + OFF_H);
#pragma unroll
  for (int sg = 0; sg < 2; sg++) {
    const int a = a0 + 32 * sg;
#pragma unroll
    for (int it = 0; it < 2; it++) {
      const f32x2* f = fz[sg][it];
#pragma unroll
      for (int j = 0; j < 4; j++) {
        const int c0 = (j * 16 + n) * 8;
        f32x4 c0v = *(const f32x4*)(modl + 512 + c0), c1v = *(const f32x4*)(modl + 512 + c0 + 4);
        f32x4 s0v = *(const f32x4*)(modl + c0), s1v = *(const f32x4*)(modl + c0 + 4);
        f32x2 o0 = f[4 * j + 0] * (f32x2){c0v[0], c0v[1]} + (f32x2){s0v[0], s0v[1]};
        f32x2 o1 = f[4 * j + 1] * (f32x2){c0v[2], c0v[3]} + (f32x2){s0v[2], s0v[3]};
        f32x2 o2 = f[4 * j + 2] * (f32x2){c1v[0], c1v[1]} + (f32x2){s1v[0], s1v[1]};
        f32x2 o3 = f[4 * j + 3] * (f32x2){c1v[2], c1v[3]} + (f32x2){s1v[2], s1v[3]};
        u32x4 r = {pk2(o0.x, o0.y), pk2(o1.x, o1.y), pk2(o2.x, o2.y), pk2(o3.x, o3.y)};
        st16_sc1(r, rh, (u32)((a * 32 + it * 16 + w * 4 + g) * DM + c0) * 2u);
      }
    }
    asm volatile("s_waitcnt vmcnt(0)" ::: "memory");
    __syncthreads();
    if (tid == 0) st4_sc1(1u, mkrsrc(ws + OFF_FLAG), OFF_DONE + a * 64u);
  }
  const int a = a0;
  mod_cols(cvec, wmod, ws, lds, 1024 + a * 64);
  mod_cols(cvec, wmod, ws, lds, 1024 + a * 64 + 32);
  __syncthreads();
  if (a < 28) {
    dma_tile<128>(lds, w1 + a * 64, DM, FF);
    dma_wait();
    u32x4* dst = (u32x4*)(ws + OFF_W1F);
    for (int ks = 0; ks < 16; ks++) {
      bf16x8 v = frag8(lds, 32 * ks + 8 * (lane >> 4), 16 * w, 128, lane);
      dst[((size_t)(a * 4 + w) * 16 + ks) * 64 + lane] = __builtin_bit_cast(u32x4, v);
    }
  }
  __syncthreads();
  {
    const int cb = a >> 2, kq = a & 3;
    dma_tile<128>(lds, w2 + (size_t)kq * 448 * DM + cb * 64, 448, DM);
    dma_wait();
    u32x4* dst = (u32x4*)(ws + OFF_W2F);
    for (int ks = 0; ks < 14; ks++) {
      u32x2 va = tr4(lds, 32 * ks + 4 * (lane >> 4), 16 * w, 128, lane), vb = tr4(lds, 32 * ks + 16 + 4 * (lane >> 4), 16 * w, 128, lane);
      dst[((size_t)(cb * 4 + w) * 56 + kq * 14 + ks) * 64 + lane] = (u32x4){va.x, va.y, vb.x, vb.y};
    }
  }
  {
    const __amdgpu_buffer_rsrc_t rflag = mkrsrc(ws + OFF_FLAG);
    const int hg = a & 7;
    for (int s = a >> 3; s < NSEG; s += 4) {
      px_task(ws, s, hg);
    }
  }
}

#define AD_DEC 3
#define AD_INC 60
#define ADAPT(D, n) do { if ((n) == 1) D -= AD_DEC; else D += AD_INC; if (D < 0) D = 0; } while (0)

__device__ __forceinline__ void role_score(const u16* __restrict__ Wq, const u16* __restrict__ Wk, char* ws,
                                           char* lds, int h) {
  const int tid = threadIdx.x, lane = tid & 63, w = tid >> 6;
  const int n = lane & 15, gb = lane >> 4;
  bf16x16 wf[4][8];
#pragma unroll
  for (int mat = 0; mat < 2; mat++) {
    __syncthreads();
    dma_tile<256>(lds, (mat ? Wk : Wq) + h * DH, DM, NH * DH);
    dma_wait();
#pragma unroll
    for (int tt = 0; tt < 2; tt++)
#pragma unroll
      for (int t = 0; t < 8; t++) {
        const int col = 32 * w + 16 * tt;
        u32x4 lo = frag8s(lds, 64 * t + 16 * (gb >> 1) + 4 * (gb & 1), col, 256, lane);
        u32x4 hi = frag8s(lds, 64 * t + 16 * (2 + (gb >> 1)) + 4 * (gb & 1), col, 256, lane);
        wf[mat * 2 + tt][t] = as_bf16(lo, hi);
      }
  }
  __syncthreads();
  char* memL = lds;
  char* qs = lds + 8192;
  char* ks_ = lds + 8192 + 2304;
  const int idx = (lane & 8) ? (int)0xEEEEEEEE : 0x44444444;
  const __amdgpu_buffer_rsrc_t rflag = mkrsrc(ws + OFF_FLAG);
  const __amdgpu_buffer_rsrc_t rkx = mkrsrc(ws + OFF_KX);
  const __amdgpu_buffer_rsrc_t rmem = mkrsrc(ws + OFF_MEMX);
  const __amdgpu_buffer_rsrc_t rpex = mkrsrc(ws + OFF_PEX);
  const int rho = lane & 15;
  const int keyp = 8 * ((rho >> 2) & 1) + 4 * (rho >> 3) + (rho & 3);
  const u32 prho = (u32)((n & 7) >> 2) * 10240u, pi = n & 3;
  const u32 pxoff = prho + (u32)(((h >> 2) * 64) + 16 * (2 * (h & 1) + (gb >> 1)) + 8 * (gb & 1) + 4 * ((h >> 1) & 1) + pi) * 16u + (n >> 3) * 8u;
  const u32 pmoff = prho + 8192u + (u32)(h * 4 + pi) * 16u + (gb & 1) * 8u + (gb >> 1) * 4u;
#define DS_OFF 350
#define DC_OFF 180
  int D1 = (w == 0 ? 3400 : (w == 1 ? 3300 : 4050)) - DS_OFF; u32 tref = now32() - 8000u, seen = 0xFFFFFFFFu;
  STAMP(1);
  for (int s = 0; s < NSEG; s++) {
    u32x4 kx[2][4];
    u32 nsw1;
    if (w == 0) {
      if (!wait_progress(rflag, 4096u + h * 128u, s + 1, seen)) return;
#pragma unroll
      for (int tt = 0; tt < 2; tt++)
#pragma unroll
        for (int k4 = 0; k4 < 4; k4++)
          kx[tt][k4] = ld16_sc1(rkx, (u32)(((32 * s + 16 * tt + keyp) * NH + h) * DH + 32 * k4 + 8 * gb) * 2u);
    }
    f32x4 acc[4];
#pragma unroll
    for (int i = 0; i < 4; i++) { acc[i] = (f32x4){0.f, 0.f, 0.f, 0.f}; asm volatile("" : "+v"(acc[i])); }
    u32 pxo = (u32)s * 20480u + pxoff, pmo = (u32)s * 20480u + pmoff;
    asm volatile("" : "+v"(pxo), "+v"(pmo));
    wait_until(tref + D1);
    {
      u32 off[2] = {(u32)s * 8192u + w * 2048u, (u32)s * 8192u + w * 2048u + 1024u};
      u32x4 a[2];
      nsw1 = poll_chunks<2>(rmem, off, a, lane * 16u);
      *(u32x4*)(memL + w * 2048 + lane * 16) = a[0];
      *(u32x4*)(memL + w * 2048 + 1024 + lane * 16) = a[1];
    }
    __syncthreads();
    STAMP(16 + 4 * s);
    bf16x8 af[8];
#pragma unroll
    for (int t = 0; t < 8; t++) af[t] = *(const bf16x8*)(memL + t * 1024 + lane * 16);
    __builtin_amdgcn_sched_barrier(0);
    const int ecol = 32 * w + 16 * (lane >> 5) + n, er0 = 4 * ((lane >> 4) & 1);
#pragma unroll
    for (int t = 0; t < 8; t++) { acc[0] = SMF(af[t], wf[0][t], acc[0], idx); acc[1] = SMF(af[t], wf[1][t], acc[1], idx); }
    __builtin_amdgcn_sched_barrier(0);
#pragma unroll
    for (int t = 0; t < 8; t++) {
      acc[2] = SMF(af[t], wf[2][t], acc[2], idx); acc[3] = SMF(af[t], wf[3][t], acc[3], idx);
      if (t >= 2 && t < 6) {
        const int j = t - 2;
        auto r = __builtin_amdgcn_permlane32_swap(f2u(acc[0][j]), f2u(acc[1][j]), false, false);
        *(__bf16*)(qs + (er0 + j) * 272 + ecol * 2) = (__bf16)(u2f(r[0]) + u2f(r[1]));
      }
      __builtin_amdgcn_sched_barrier(0);
    }
#pragma unroll
    for (int j = 0; j < 4; j++) {
      auto r = __builtin_amdgcn_permlane32_swap(f2u(acc[2][j]), f2u(acc[3][j]), false, false);
      *(__bf16*)(ks_ + (er0 + j) * 272 + ecol * 2) = (__bf16)(u2f(r[0]) + u2f(r[1]));
    }
    __syncthreads();
    STAMP(17 + 4 * s);
    if (w == 0 && s < NSEG - 1) {
      f32x4 sc[3];
#pragma unroll
      for (int i = 0; i < 3; i++) { sc[i] = (f32x4){0.f, 0.f, 0.f, 0.f}; asm volatile("" : "+v"(sc[i])); }
      bf16x8 qf[4], kf[4];
#pragma unroll
      for (int k4 = 0; k4 < 4; k4++) {
        qf[k4] = *(const bf16x8*)(qs + (lane & 7) * 272 + (32 * k4 + 8 * gb) * 2);
        kf[k4] = *(const bf16x8*)(ks_ + (lane & 7) * 272 + (32 * k4 + 8 * gb) * 2);
      }
      __builtin_amdgcn_sched_barrier(0);
#pragma unroll
      for (int k4 = 0; k4 < 4; k4++) {
        asm volatile("s_nop 1\n\tv_mfma_f32_16x16x32_bf16 %0, %3, %6, %0\n\tv_mfma_f32_16x16x32_bf16 %1, %4, %6, %1\n\tv_mfma_f32_16x16x32_bf16 %2, %5, %6, %2"
                     : "+v"(sc[0]), "+v"(sc[1]), "+v"(sc[2]) : "v"(kx[0][k4]), "v"(kx[1][k4]), "v"(kf[k4]), "v"(qf[k4]));
      }
      asm volatile("s_nop 7\n\ts_nop 3" : "+v"(sc[0]), "+v"(sc[1]), "+v"(sc[2]));
      const float sl = 0.08838834764831845f * 1.4426950408889634f;
      const bool hiq = n >= 8, hig = gb >= 2;
      float p[6];
#pragma unroll
      for (int j = 0; j < 4; j++) p[j] = hiq ? sc[1][j] : sc[0][j];
      p[4] = hig ? sc[2][2] : sc[2][0];
      p[5] = hig ? sc[2][3] : sc[2][1];
      float mx = max2r(max3r(p[0], p[1], p[2]), max3r(p[3], p[4], p[5]));
      mx = max2r(mx, u2f((u32)__builtin_amdgcn_mov_dpp((int)f2u(mx), 0x128, 0xF, 0xF, true)));
      { auto r = __builtin_amdgcn_permlane16_swap(f2u(mx), f2u(mx), false, false);
        mx = max2r(u2f(r[0]), u2f(r[1])); }
      { auto r = __builtin_amdgcn_permlane32_swap(f2u(mx), f2u(mx), false, false);
        mx = max2r(u2f(r[0]), u2f(r[1])); }
      const float nb = -mx * sl;
#pragma unroll
      for (int j = 0; j < 6; j++) p[j] = __builtin_amdgcn_exp2f(__builtin_fmaf(p[j], sl, nb));
      float sum = (p[0] + p[1]) + (p[2] + p[3]) + (hiq ? 0.f : (p[4] + p[5]));
      sum += u2f((u32)__builtin_amdgcn_mov_dpp((int)f2u(sum), 0x128, 0xF, 0xF, true));
      { auto r = __builtin_amdgcn_permlane16_swap(f2u(sum), f2u(sum), false, false);
        sum = u2f(r[0]) + u2f(r[1]); }
      { auto r = __builtin_amdgcn_permlane32_swap(f2u(sum), f2u(sum), false, false);
        sum = u2f(r[0]) + u2f(r[1]); }
      const float inv = __builtin_amdgcn_rcpf(sum);
      u32x2 px = {sane(pk2(p[0] * inv, p[1] * inv)), sane(pk2(p[2] * inv, p[3] * inv))};
      st8_sc1(px, rpex, pxo);
      if (!hiq) st4_sc1(sane(pk2(p[4] * inv, p[5] * inv)), rpex, pmo);
      STAMP(18 + 4 * s);
    } else if (w == 1) {
      const __amdgpu_buffer_rsrc_t rkm = mkrsrc(ws + OFF_KMEM);
#pragma unroll
      for (int j = 0; j < 2; j++) {
        int u = j * 64 + lane, r = u >> 4, c = u & 15;
        st16_sc1(*(const u32x4*)(ks_ + r * 272 + c * 16), rkm, (u32)((s * NH + h) * 1024 + r * 128 + c * 8) * 2u);
      }
      asm volatile("s_waitcnt vmcnt(0)" ::: "memory");
      if (lane == 0) st4_sc1((u32)(s + 1), rflag, OFF_KMF + h * 64u);
    }
    tref = now32();
    if (!nsw1) return;
    if (s > 2) ADAPT(D1, nsw1);
  }
}

__device__ __forceinline__ void role_column(const u16* __restrict__ Wv, const u16* __restrict__ Wo, char* ws, char* lds, int c, int rho) {
  const int tid = threadIdx.x, lane = tid & 63, w = tid >> 6;
  const int n = lane & 15, gb = lane >> 4, cc = lane & 7;
  bf16x16 wf[4][8];
  wvo_task(Wv, Wo, ws, lds, c * 2 + rho);
  {
    u32 seenw = 0xFFFFFFFFu;
    if (!wait_progress(mkrsrc(ws + OFF_FLAG), OFF_WVF + (u32)((lane & 31) * 4 + (c >> 4)) * 16u, 1u, seenw)) return;
    const __amdgpu_buffer_rsrc_t rsrc = mkrsrc(ws + OFF_WVO + (size_t)(c * 4 + w) * 65536);
#pragma unroll
    for (int tl = 0; tl < 4; tl++)
#pragma unroll
      for (int t = 0; t < 8; t++)
        wf[tl][t] = as_bf16(ld16_sc1(rsrc, (u32)(((tl * 8 + t) * 2) * 64 + lane) * 16u), ld16_sc1(rsrc, (u32)(((tl * 8 + t) * 2 + 1) * 64 + lane) * 16u));
  }
  char* memL = lds;
  char* yl = lds + 8192 + w * 1024;
  float* red = (float*)(lds + 12288);
  const int idx = (lane & 8) ? (int)0xEEEEEEEE : 0x44444444;
  const __amdgpu_buffer_rsrc_t rflag = mkrsrc(ws + OFF_FLAG);
  const __amdgpu_buffer_rsrc_t rmem = mkrsrc(ws + OFF_MEMX);
  const __amdgpu_buffer_rsrc_t rpex = mkrsrc(ws + OFF_PEX);
  int D1 = w == 0 ? 750 : (w == 1 ? 850 : 1130), D2 = 2800 - DC_OFF; u32 tref = now32(), seen = 0xFFFFFFFFu;
  STAMP(1);
  for (int s = 0; s < NSEG; s++) {
    wait_until(tref + D1);
    {
      u32 off[2] = {(u32)s * 8192u + w * 2048u, (u32)s * 8192u + w * 2048u + 1024u};
      u32x4 a[2];
      u32 nsw = poll_chunks<2>(rmem, off, a, lane * 16u);
      if (!nsw) return;
      if (s > 2) ADAPT(D1, nsw);
      tref = now32();
      *(u32x4*)(memL + w * 2048 + lane * 16) = a[0];
      *(u32x4*)(memL + w * 2048 + 1024 + lane * 16) = a[1];
    }
    u32x4 uf[4][2];
    {
      if (!wait_progress(rflag, (u32)(8 * w + (lane & 7)) * 128u, s + 1, seen)) return;
      const __amdgpu_buffer_rsrc_t ru = mkrsrc(ws + OFF_UF + ((size_t)s * 64 + c) * 16384);
#pragma unroll
      for (int m = 0; m < 4; m++)
#pragma unroll
        for (int z = 0; z < 2; z++) uf[m][z] = ld16_sc1(ru, (u32)((((4 * w + m) * 2 + z) * 4 + gb) * 8 + cc) * 16u);
    }
    __syncthreads();
    STAMP(16 + 4 * s);
    bf16x16 yb;
    {
      f32x4 acc[4];
#pragma unroll
      for (int i = 0; i < 4; i++) acc[i] = (f32x4){0.f, 0.f, 0.f, 0.f};
      bf16x8 af[8];
#pragma unroll
      for (int t = 0; t < 8; t++) af[t] = *(const bf16x8*)(memL + t * 1024 + lane * 16);
      __builtin_amdgcn_sched_barrier(0);
#pragma unroll
      for (int t = 0; t < 8; t++)
#pragma unroll
        for (int i = 0; i < 4; i++) acc[i] = SMF(af[t], wf[i][t], acc[i], idx);
#pragma unroll
      for (int pr = 0; pr < 2; pr++) {
        float v[4];
#pragma unroll
        for (int j = 0; j < 4; j++) {
          auto r = __builtin_amdgcn_permlane32_swap(f2u(acc[2 * pr][j]), f2u(acc[2 * pr + 1][j]), false, false);
          v[j] = u2f(r[0]) + u2f(r[1]);
        }
        const int hl = 2 * (2 * pr + (lane >> 5)) + (n >> 3);
        u32x2 pv = {pk2(v[0], v[1]), pk2(v[2], v[3])};
        *(u32x2*)(yl + (hl * 8 + cc) * 16 + 8 * ((lane >> 4) & 1)) = pv;
      }
      asm volatile("s_waitcnt lgkmcnt(0)" ::: "memory");
      u32x4 hz[2];
#pragma unroll
      for (int z = 0; z < 2; z++) {
        const int ga = 2 * z + (gb >> 1);
        u32x2 a = *(const u32x2*)(yl + ((2 * ga) * 8 + cc) * 16 + 8 * (gb & 1));
        u32x2 b = *(const u32x2*)(yl + ((2 * ga + 1) * 8 + cc) * 16 + 8 * (gb & 1));
        hz[z] = (u32x4){a.x, b.x, a.y, b.y};
      }
      yb = as_bf16(hz[0], hz[1]);
      if (rho == 0) __builtin_amdgcn_raw_buffer_store_b128(*(const u32x4*)(yl + lane * 16), mkrsrc(ws + OFF_Y), (u32)(s * 64 + c) * 4096u + w * 1024u + lane * 16u, 0, 0);
    }
    STAMP(17 + 4 * s);
    if (s == NSEG - 1) break;
    bf16x16 ub16[4];
#pragma unroll
    for (int m = 0; m < 4; m++) { ub16[m] = as_bf16(uf[m][0], uf[m][1]); asm volatile("" : "+v"(ub16[m])); }
    asm volatile("" : "+v"(yb));
    f32x4 dA = {0.f, 0.f, 0.f, 0.f}, dB = {0.f, 0.f, 0.f, 0.f};
    u32 rdw = (u32)(12288 + ((s & 1) * 512 + cc * 16 + 4 * w + gb) * 4), rdr = (u32)(12288 + ((s & 1) * 512 + lane * 8) * 4);
    u32 mso = (u32)(s + 1) * 8192u + c * 128u + rho * 64u + (lane >> 2) * 4u;
    asm volatile("" : "+v"(dA), "+v"(dB), "+v"(rdw), "+v"(rdr), "+v"(mso));
    u32 nsw2;
    wait_until(tref + D2);
    {
      const u32 pb = (u32)s * 20480u + rho * 10240u;
      u32 off[3] = {pb + (2 * w) * 1024u + lane * 16u, pb + (2 * w + 1) * 1024u + lane * 16u,
                    pb + 8192u + (u32)((8 * w + 2 * gb + ((lane >> 3) & 1)) * 4 + (lane & 3)) * 16u};
      u32x4 g[3];
      nsw2 = poll_chunks<3>(rpex, off, g, 0u);
      dA = SMF(as_bf8(g[0]), ub16[0], dA, idx);
      dB = SMF(as_bf8(g[0]), ub16[1], dB, idx);
      dA = SMF(as_bf8(g[1]), ub16[2], dA, idx);
      dB = SMF(as_bf8(g[1]), ub16[3], dB, idx);
      dA = SMF(as_bf8(g[2]), yb, dA, idx);
      if (n < 8) {
#pragma unroll
        for (int j = 0; j < 4; j++) *(float*)(lds + rdw + j * 512) = (gb & 1) ? dB[j] : dA[j];
      }
    }
    __syncthreads();
    STAMP(18 + 4 * s);
    if (w == 0) {
      f32x4 a = *(const f32x4*)(lds + rdr), b = *(const f32x4*)(lds + rdr + 16);
      float v = ((a[0] + a[1]) + (a[2] + a[3])) + ((b[0] + b[1]) + (b[2] + b[3]));
      v += u2f((u32)__builtin_amdgcn_mov_dpp((int)f2u(v), 0xB1, 0xF, 0xF, true));
      float o = u2f((u32)__builtin_amdgcn_mov_dpp((int)f2u(v), 0x4E, 0xF, 0xF, true));
      if (!(lane & 3)) st4_sc1(sane(pk2(v, o)), rmem, mso);
      STAMP(19 + 4 * s);
    }
    tref = now32();
    if (!nsw2) return;
    if (s > 2) ADAPT(D2, nsw2);
  }
}

__device__ __forceinline__ void load_h(u32x4 (&r)[8], const char* ws, int s) {
  const __amdgpu_buffer_rsrc_t rh = mkrsrc(ws + OFF_H);
#pragma unroll
  for (int j = 0; j < 8; j++) r[j] = ld16_sc1(rh, (u32)s * 32768u + (j * 256 + threadIdx.x) * 16u);
}
__device__ __forceinline__ void put_h(char* dst, const u32x4 (&r)[8]) {
#pragma unroll
  for (int j = 0; j < 8; j++) {
    int u = j * 256 + threadIdx.x;
    *(u32x4*)(dst + (u >> 6) * 1040 + (u & 63) * 16) = r[j];
  }
}

template <bool SC1>
__device__ __forceinline__ void store_T_tile(const f32x16& acc, char* base, int tokrow, int h, int w, int lane) {
  const __amdgpu_buffer_rsrc_t r = mkrsrc(base);
#pragma unroll
  for (int qp = 0; qp < 2; qp++) {
    u32 ax = pk2(acc[8 * qp + 0], acc[8 * qp + 1]), ay = pk2(acc[8 * qp + 2], acc[8 * qp + 3]);
    u32 bx = pk2(acc[8 * qp + 4], acc[8 * qp + 5]), by = pk2(acc[8 * qp + 6], acc[8 * qp + 7]);
    auto r0 = __builtin_amdgcn_permlane32_swap(ax, bx, false, false);
    auto r1 = __builtin_amdgcn_permlane32_swap(ay, by, false, false);
    u32x4 v = {(u32)r0[0], (u32)r1[0], (u32)r0[1], (u32)r1[1]};
    u32 off = (u32)(((tokrow + (lane & 31)) * NH + h) * DH + 32 * w + 16 * qp + 8 * (lane >> 5)) * 2u;
    if (SC1) __builtin_amdgcn_raw_buffer_store_b128(v, r, off, 0, 16);
    else __builtin_amdgcn_raw_buffer_store_b128(v, r, off, 0, 0);
  }
}

__device__ __forceinline__ void role_vu(const u16* __restrict__ Wv, const u16* __restrict__ Wo,
                                        char* ws, char* lds, int h) {
  const int tid = threadIdx.x, lane = tid & 63, w = tid >> 6, x = lane & 31, hh = lane >> 5;
  bf16x8 wv[32], wo[4][8];
  dma_tile<256>(lds, Wv + h * DH, DM, NH * DH);
  dma_wait();
#pragma unroll
  for (int ks = 0; ks < 32; ks++) wv[ks] = frag8(lds, 16 * ks + 8 * hh, 32 * w + (x & 16), 256, lane);
  __syncthreads();
  dma_tile<1024>(lds, Wo + (size_t)h * DH * DM, DH, DM);
  dma_wait();
#pragma unroll
  for (int nt = 0; nt < 4; nt++)
#pragma unroll
    for (int ks = 0; ks < 8; ks++) wo[nt][ks] = frag8(lds, 16 * ks + 8 * hh, 128 * w + 32 * nt + (x & 16), 1024, lane);
  __syncthreads();
  char* vs = lds + 66560;
  const __amdgpu_buffer_rsrc_t rflag = mkrsrc(ws + OFF_FLAG);
  u32x4 hr[8];
  { u32 seenh = 0xFFFFFFFFu; if (!wait_progress(rflag, OFF_DONE + (lane & 31) * 64u, 1u, seenh)) return; }
  load_h(hr, ws, 0);
  put_h(lds, hr);
  load_h(hr, ws, 1);
  __syncthreads();
  for (int s = 0; s < NSEG; s++) {
    const char* hb = lds + (s & 1) * 33280;
    f32x16 av;
#pragma unroll
    for (int i = 0; i < 16; i++) av[i] = 0.f;
    {
      bf16x8 hf[2][8];
#pragma unroll
      for (int k = 0; k < 8; k++) hf[0][k] = *(const bf16x8*)(hb + x * 1040 + (16 * k + 8 * hh) * 2);
#pragma unroll
      for (int ch = 0; ch < 4; ch++) {
        if (ch < 3) {
#pragma unroll
          for (int k = 0; k < 8; k++) hf[(ch + 1) & 1][k] = *(const bf16x8*)(hb + x * 1040 + (16 * (8 * ch + 8 + k) + 8 * hh) * 2);
        }
        __builtin_amdgcn_sched_barrier(0);
#pragma unroll
        for (int k = 0; k < 8; k++) av = MFMA32(wv[8 * ch + k], hf[ch & 1][k], av);
        __builtin_amdgcn_sched_barrier(0);
      }
    }
#pragma unroll
    for (int q = 0; q < 4; q++) {
      u32x2 pv = {pk2(av[4 * q], av[4 * q + 1]), pk2(av[4 * q + 2], av[4 * q + 3])};
      *(u32x2*)(vs + x * 272 + (32 * w + 8 * q + 4 * hh) * 2) = pv;
    }
    put_h(lds + ((s + 1) & 1) * 33280, hr);
    asm volatile("s_waitcnt vmcnt(0)" ::: "memory");
    __syncthreads();
    if (tid == 0 && s > 4) st4_sc1((u32)s, rflag, h * 128u);
    bf16x8 vf[8];
#pragma unroll
    for (int ks = 0; ks < 8; ks++) vf[ks] = *(const bf16x8*)(vs + x * 272 + (16 * ks + 8 * hh) * 2);
    const __amdgpu_buffer_rsrc_t ru = mkrsrc(ws + OFF_UF + (size_t)s * 64 * 16384);
#pragma unroll
    for (int nt = 0; nt < 4; nt++) {
      f32x16 au;
#pragma unroll
      for (int i = 0; i < 16; i++) au[i] = 0.f;
#pragma unroll
      for (int ks = 0; ks < 8; ks++) au = MFMA32(vf[ks], wo[nt][ks], au);
      const int d = 128 * w + 32 * nt + x, c = d >> 3, cc = d & 7;
#pragma unroll
      for (int xb = 0; xb < 2; xb++)
        st16_sc1(PACK_S(au, 8 * xb), ru, (u32)((((c * 16 + (h >> 1)) * 2 + (h & 1)) * 4 + 2 * hh + xb) * 8 + cc) * 16u);
    }
    if (s < 4 || s == NSEG - 1) asm volatile("s_waitcnt vmcnt(0)" ::: "memory");
    if (s == 30) { u32 seenh = 0xFFFFFFFFu; if (!wait_progress(rflag, OFF_DONE + lane * 64u, 1u, seenh)) return; }
    load_h(hr, ws, s + 2 < NSEG ? s + 2 : NSEG - 1);
    __syncthreads();
    if (tid == 0 && (s < 4 || s == NSEG - 1)) st4_sc1((u32)(s + 1), rflag, h * 128u);
    STAMP(16 + 4 * s);
  }
}

__device__ __forceinline__ void role_qk(const u16* __restrict__ Wq, const u16* __restrict__ Wk, char* ws, char* lds, int h) {
  const int tid = threadIdx.x, lane = tid & 63, w = tid >> 6, x = lane & 31, hh = lane >> 5;
  bf16x8 wq[32], wk[32];
  dma_tile<256>(lds, Wk + h * DH, DM, NH * DH);
  dma_wait();
#pragma unroll
  for (int ks = 0; ks < 32; ks++) wk[ks] = frag8(lds, 16 * ks + 8 * hh, 32 * w + (x & 16), 256, lane);
  __syncthreads();
  dma_tile<256>(lds, Wq + h * DH, DM, NH * DH);
  dma_wait();
#pragma unroll
  for (int ks = 0; ks < 32; ks++) wq[ks] = frag8(lds, 16 * ks + 8 * hh, 32 * w + (x & 16), 256, lane);
  __syncthreads();
  const __amdgpu_buffer_rsrc_t rflag = mkrsrc(ws + OFF_FLAG);
  u32x4 hr[8];
  { u32 seenh = 0xFFFFFFFFu; if (!wait_progress(rflag, OFF_DONE + (lane & 31) * 64u, 1u, seenh)) return; }
  load_h(hr, ws, 0);
  put_h(lds, hr);
  load_h(hr, ws, 1);
  __syncthreads();
  for (int s = 0; s < NSEG; s++) {
    const char* hb = lds + (s & 1) * 33280;
    f32x16 aq, ak;
#pragma unroll
    for (int i = 0; i < 16; i++) { aq[i] = 0.f; ak[i] = 0.f; }
    {
      bf16x8 hf[2][8];
#pragma unroll
      for (int k = 0; k < 8; k++) hf[0][k] = *(const bf16x8*)(hb + x * 1040 + (16 * k + 8 * hh) * 2);
#pragma unroll
      for (int ch = 0; ch < 4; ch++) {
        if (ch < 3) {
#pragma unroll
          for (int k = 0; k < 8; k++) hf[(ch + 1) & 1][k] = *(const bf16x8*)(hb + x * 1040 + (16 * (8 * ch + 8 + k) + 8 * hh) * 2);
        }
        __builtin_amdgcn_sched_barrier(0);
#pragma unroll
        for (int k = 0; k < 8; k++) { ak = MFMA32(wk[8 * ch + k], hf[ch & 1][k], ak); aq = MFMA32(wq[8 * ch + k], hf[ch & 1][k], aq); }
        __builtin_amdgcn_sched_barrier(0);
      }
    }
    put_h(lds + ((s + 1) & 1) * 33280, hr);
    asm volatile("s_waitcnt vmcnt(0)" ::: "memory");
    __syncthreads();
    if (tid == 0 && s > 4) st4_sc1((u32)s, rflag, 4096u + h * 128u);
    store_T_tile<true>(ak, ws + OFF_KX, 32 * s, h, w, lane);
    store_T_tile<true>(aq, ws + OFF_QX, 32 * s, h, w, lane);
    if (s < 4 || s == NSEG - 1) {
      asm volatile("s_waitcnt vmcnt(0)" ::: "memory");
      __syncthreads();
      if (tid == 0) st4_sc1((u32)(s + 1), rflag, 4096u + h * 128u);
    }
    if (s == 30) { u32 seenh = 0xFFFFFFFFu; if (!wait_progress(rflag, OFF_DONE + lane * 64u, 1u, seenh)) return; }
    load_h(hr, ws, s + 2 < NSEG ? s + 2 : NSEG - 1);
    STAMP(16 + 4 * s);
  }
}

__device__ __forceinline__ float xg_max(float v) {
  auto r = __builtin_amdgcn_permlane16_swap(f2u(v), f2u(v), false, false);
  v = fmaxf(u2f(r[0]), u2f(r[1]));
  auto r2 = __builtin_amdgcn_permlane32_swap(f2u(v), f2u(v), false, false);
  return fmaxf(u2f(r2[0]), u2f(r2[1]));
}
__device__ __forceinline__ float xg_sum(float v) {
  auto r = __builtin_amdgcn_permlane16_swap(f2u(v), f2u(v), false, false);
  v = u2f(r[0]) + u2f(r[1]);
  auto r2 = __builtin_amdgcn_permlane32_swap(f2u(v), f2u(v), false, false);
  return u2f(r2[0]) + u2f(r2[1]);
}
__device__ __forceinline__ void px_task(char* ws, int s, int hg) {
  const int tid = threadIdx.x, lane = tid & 63, w = tid >> 6;
  const int hd = hg * 4 + w, hl = w;
  const int n = lane & 15, g = lane >> 4;
  const __amdgpu_buffer_rsrc_t rq = mkrsrc(ws + OFF_QX + (size_t)s * 32 * 8192);
  const __amdgpu_buffer_rsrc_t rk = mkrsrc(ws + OFF_KX + (size_t)s * 32 * 8192);
  const __amdgpu_buffer_rsrc_t rkm = mkrsrc(ws + OFF_KMEM + (size_t)s * 65536);
  const int keyrow = 16 * ((n >> 2) & 1) + 4 * (n >> 3) + (n & 3);
  const int keyb = 16 * (g & 1) + 4 * (g >> 1);
  const float sl = 0.08838834764831845f * 1.4426950408889634f;
  bf16x8 qf[2][4], kf[2][4], mf[4];
  const __amdgpu_buffer_rsrc_t rflag = mkrsrc(ws + OFF_FLAG);
  { u32 seenq = 0xFFFFFFFFu; wait_progress(rflag, 4096u + hd * 128u, s + 1, seenq); }
#pragma unroll
  for (int ks = 0; ks < 4; ks++) {
#pragma unroll
    for (int t = 0; t < 2; t++) {
      qf[t][ks] = as_bf8(ld16_sc1(rq, (u32)(((16 * t + n) * NH + hd) * DH + 32 * ks + 8 * g) * 2u));
      kf[t][ks] = as_bf8(ld16_sc1(rk, (u32)(((8 * t + keyrow) * NH + hd) * DH + 32 * ks + 8 * g) * 2u));
    }
  }
  {
    u32 spins = 0, off = OFF_KMF + (u32)hd * 64u;
    while (true) {
      asm volatile("" : "+v"(off) :: "memory");
      u32 v = ld4_sc1(rflag, off);
      if ((int)v > s) break;
      __builtin_amdgcn_s_sleep(6);
      if (++spins > 800000u) break;
    }
  }
#pragma unroll
  for (int ks = 0; ks < 4; ks++) mf[ks] = as_bf8(ld16_sc1(rkm, (u32)(hd * 1024 + (n & 7) * 128 + 32 * ks + 8 * g) * 2u));
  const bool mvalid = (g >> 1) == (hl >> 1);
  u32x4* PXx = (u32x4*)(ws + OFF_PX);
  char* PXm = ws + OFF_PX + (4ull << 20);
#pragma unroll
  for (int qt = 0; qt < 2; qt++) {
    f32x4 sc[3];
#pragma unroll
    for (int i = 0; i < 3; i++) sc[i] = (f32x4){0.f, 0.f, 0.f, 0.f};
#pragma unroll
    for (int ks = 0; ks < 4; ks++) {
      sc[0] = MFMA16(kf[0][ks], qf[qt][ks], sc[0]);
      sc[1] = MFMA16(kf[1][ks], qf[qt][ks], sc[1]);
      sc[2] = MFMA16(mf[ks], qf[qt][ks], sc[2]);
    }
    const int qi = 16 * qt + n;
    float mx = -3.0e38f;
#pragma unroll
    for (int j = 0; j < 4; j++) {
      if (keyb + j > qi) sc[0][j] = -3.0e38f;
      if (keyb + 8 + j > qi) sc[1][j] = -3.0e38f;
      if (!mvalid) sc[2][j] = -3.0e38f;
      mx = fmaxf(mx, fmaxf(fmaxf(sc[0][j], sc[1][j]), sc[2][j]));
    }
    mx = xg_max(mx);
    float sum = 0.f;
#pragma unroll
    for (int i = 0; i < 3; i++)
#pragma unroll
      for (int j = 0; j < 4; j++) {
        float p = sc[i][j] > -1.0e38f ? __builtin_amdgcn_exp2f((sc[i][j] - mx) * sl) : 0.f;
        sc[i][j] = p; sum += p;
      }
    const float inv = __builtin_amdgcn_rcpf(xg_sum(sum));
    u32x4 pa = {pk2(sc[0][0] * inv, sc[0][1] * inv), pk2(sc[1][0] * inv, sc[1][1] * inv),
                pk2(sc[0][2] * inv, sc[0][3] * inv), pk2(sc[1][2] * inv, sc[1][3] * inv)};
    PXx[(((size_t)s * NH + hd) * 2 + qt) * 64 + lane] = pa;
    if (mvalid) {
      u32x2 pm = {pk2(sc[2][0] * inv, sc[2][1] * inv), pk2(sc[2][2] * inv, sc[2][3] * inv)};
      *(u32x2*)(PXm + ((((size_t)s * 8 + hg) * 2 + qt) * 64 + lane) * 16 + (hl & 1) * 8) = pm;
    }
  }
}

__global__ void __launch_bounds__(256, 1) k2_rec(const u16* __restrict__ Wq, const u16* __restrict__ Wk, const u16* __restrict__ Wv,
                                                  const u16* __restrict__ Wo, const u16* __restrict__ x, const u16* __restrict__ w1,
                                                  const u16* __restrict__ w2, const u16* __restrict__ cvec,
                                                  const u16* __restrict__ wmod, const u16* __restrict__ mem0, char* ws) {
  extern __shared__ __attribute__((aligned(16))) char lds[];
  const int b = blockIdx.x;
  STAMP(0);
  if (b < 32) role_help(x, w1, w2, cvec, wmod, mem0, ws, lds, b);
  else if (b < 64) role_score(Wq, Wk, ws, lds, b - 32);
  else if (b < 192) { const int q = b - 64; role_column(Wv, Wo, ws, lds, (q & 7) | ((q >> 4) << 3), (q >> 3) & 1); }
  else if (b < 224) role_vu(Wv, Wo, ws, lds, b - 192);
  else role_qk(Wq, Wk, ws, lds, b - 224);
}

__device__ __forceinline__ void ph_oproj(const u16* __restrict__ x, char* ws, char* lds) {
  const int b = blockIdx.x, tid = threadIdx.x, lane = tid & 63, w = tid >> 6;
  const int s = (b & 7) * 8 + (b >> 5), nq = (b >> 3) & 3;
  const int n = lane & 15, g = lane >> 4, cc = lane & 7;
  const u32x4* UF = (const u32x4*)(ws + OFF_UF) + (size_t)s * 64 * 1024;
  const char* Yg = ws + OFF_Y + (size_t)s * 64 * 4096;
  const u32x4* PXx = (const u32x4*)(ws + OFF_PX) + (size_t)s * NH * 128;
  const u32x4* PXm = (const u32x4*)(ws + OFF_PX + (4ull << 20)) + (size_t)s * 8 * 128;
  f32x4 acc[2][8];
#pragma unroll
  for (int i = 0; i < 2; i++)
#pragma unroll
    for (int j = 0; j < 8; j++) acc[i][j] = (f32x4){0.f, 0.f, 0.f, 0.f};
  u32x4 ub[4][8], pa[4][2], pm[2], yf[8];
#pragma unroll
  for (int hl = 0; hl < 4; hl++) {
    const int hd = 4 * w + hl;
#pragma unroll
    for (int nt = 0; nt < 8; nt++) ub[hl][nt] = __builtin_nontemporal_load(UF + (size_t)(16 * nq + 2 * nt + (n >> 3)) * 1024 + (hd * 4 + g) * 8 + cc);
    pa[hl][0] = PXx[(hd * 2) * 64 + lane];
    pa[hl][1] = PXx[(hd * 2 + 1) * 64 + lane];
  }
  pm[0] = PXm[(w * 2) * 64 + lane];
  pm[1] = PXm[(w * 2 + 1) * 64 + lane];
  {
    const int ha = 4 * w + 2 * (g >> 1);
#pragma unroll
    for (int nt = 0; nt < 8; nt++) {
      const int c = 16 * nq + 2 * nt + (n >> 3);
      u32x2 ya = __builtin_nontemporal_load((const u32x2*)(Yg + (size_t)c * 4096 + (ha * 8 + cc) * 16 + 8 * (g & 1)));
      u32x2 yb = __builtin_nontemporal_load((const u32x2*)(Yg + (size_t)c * 4096 + ((ha + 1) * 8 + cc) * 16 + 8 * (g & 1)));
      yf[nt] = (u32x4){ya.x, ya.y, yb.x, yb.y};
    }
  }
  __builtin_amdgcn_sched_barrier(0);
#pragma unroll
  for (int hl = 0; hl < 4; hl++)
#pragma unroll
    for (int nt = 0; nt < 8; nt++) {
      acc[0][nt] = MFMA16(as_bf8(pa[hl][0]), as_bf8(ub[hl][nt]), acc[0][nt]);
      acc[1][nt] = MFMA16(as_bf8(pa[hl][1]), as_bf8(ub[hl][nt]), acc[1][nt]);
    }
#pragma unroll
  for (int nt = 0; nt < 8; nt++) {
    acc[0][nt] = MFMA16(as_bf8(pm[0]), as_bf8(yf[nt]), acc[0][nt]);
    acc[1][nt] = MFMA16(as_bf8(pm[1]), as_bf8(yf[nt]), acc[1][nt]);
  }
  float* red = (float*)lds;
#pragma unroll
  for (int qt = 0; qt < 2; qt++)
#pragma unroll
    for (int nt = 0; nt < 8; nt++)
#pragma unroll
      for (int j = 0; j < 4; j++) red[(w * 32 + 16 * qt + 4 * g + j) * 132 + 16 * nt + n] = acc[qt][nt][j];
  __syncthreads();
  const float* mod = (const float*)(ws + OFF_MOD);
  {
    const int r = tid >> 4, c8 = (tid & 15) * 8;
    float o[8];
#pragma unroll
    for (int j = 0; j < 8; j++) o[j] = 0.f;
#pragma unroll
    for (int ww = 0; ww < 8; ww++) {
      f32x4 a = *(const f32x4*)(red + (ww * 32 + r) * 132 + c8), bq = *(const f32x4*)(red + (ww * 32 + r) * 132 + c8 + 4);
#pragma unroll
      for (int j = 0; j < 4; j++) { o[j] += a[j]; o[4 + j] += bq[j]; }
    }
    const int col = 128 * nq + c8;
    const size_t go = (size_t)(32 * s + r) * DM + col;
    u32x4 xv = *(const u32x4*)(x + go);
    float xf[8] = {bf_lo(xv.x), bf_hi(xv.x), bf_lo(xv.y), bf_hi(xv.y), bf_lo(xv.z), bf_hi(xv.z), bf_lo(xv.w), bf_hi(xv.w)};
#pragma unroll
    for (int j = 0; j < 8; j++) o[j] = xf[j] + bfr(mod[1024 + col + j] * bfr(o[j]));
    u32x4 rv = {pk2(o[0], o[1]), pk2(o[2], o[3]), pk2(o[4], o[5]), pk2(o[6], o[7])};
    st16_sc1(rv, mkrsrc(ws + OFF_X1), (u32)go * 2u);
  }
  asm volatile("s_waitcnt vmcnt(0)" ::: "memory");
  __syncthreads();
  if (tid == 0) st4_sc1(1u, mkrsrc(ws + OFF_FLAG), OFF_X1F + (u32)(s * 4 + nq) * 4u);
}

__device__ __forceinline__ void ph_mlp1(const u16* __restrict__ b1, char* ws, char* lds) {
  const int b = blockIdx.x, tid = threadIdx.x, lane = tid & 63, w = tid >> 6;
  const int jq = b >> 3, mt0 = (b & 7) * 4 + jq / 7, nb = jq % 7;
  const float* mod = (const float*)(ws + OFF_MOD);
  const int n = lane & 15, g = lane >> 4;
  const f32x4 mv = *(const f32x4*)(mod + 1536 + (tid & 255) * 4);
  const u32x4* WF = (const u32x4*)(ws + OFF_W1F) + (size_t)(nb * 16 + w * 2) * 16 * 64 + lane;
  u32x4 bfm[2][16];
#pragma unroll
  for (int nt = 0; nt < 2; nt++)
#pragma unroll
    for (int ks = 0; ks < 8; ks++) bfm[nt][ks] = WF[(nt * 16 + ks) * 64];
  __builtin_amdgcn_sched_barrier(0);
  { u32 seenx = 0xFFFFFFFFu; wait_progress(mkrsrc(ws + OFF_FLAG), OFF_X1F + (u32)(mt0 * 8 + (lane & 7)) * 4u, 1u, seenx); }
  u32x4 xv[2][4];
#pragma unroll
  for (int it = 0; it < 2; it++)
#pragma unroll
    for (int j = 0; j < 4; j++) xv[it][j] = ld16_sc1(mkrsrc(ws + OFF_X1), (u32)((mt0 * 64 + it * 32 + w * 4 + g) * DM + (j * 16 + n) * 8) * 2u);
  __builtin_amdgcn_sched_barrier(0);
  float* modl = (float*)(lds + 66560);
  if (tid < 256) *(f32x4*)(modl + tid * 4) = tid >= 128 ? mv + 1.f : mv;
  __syncthreads();
#pragma unroll
  for (int it = 0; it < 2; it++) {
    f32x2 f[16];
#pragma unroll
    for (int j = 0; j < 4; j++) {
      f[4 * j + 0] = (f32x2){bf_lo(xv[it][j].x), bf_hi(xv[it][j].x)}; f[4 * j + 1] = (f32x2){bf_lo(xv[it][j].y), bf_hi(xv[it][j].y)};
      f[4 * j + 2] = (f32x2){bf_lo(xv[it][j].z), bf_hi(xv[it][j].z)}; f[4 * j + 3] = (f32x2){bf_lo(xv[it][j].w), bf_hi(xv[it][j].w)};
    }
    f32x2 s2 = {0.f, 0.f};
#pragma unroll
    for (int i = 0; i < 16; i++) s2 += f[i];
    float m = row16_sum(s2.x + s2.y) * (1.f / DM);
    f32x2 q2 = {0.f, 0.f};
#pragma unroll
    for (int i = 0; i < 16; i++) { f[i] -= m; q2 += f[i] * f[i]; }
    float rs = rsqrtf(row16_sum(q2.x + q2.y) * (1.f / DM) + 1e-5f);
    s2 = (f32x2){0.f, 0.f};
#pragma unroll
    for (int j = 0; j < 4; j++) {
      const int c0 = (j * 16 + n) * 8;
      f32x4 c0v = *(const f32x4*)(modl + 512 + c0), c1v = *(const f32x4*)(modl + 512 + c0 + 4);
      f32x4 s0v = *(const f32x4*)(modl + c0), s1v = *(const f32x4*)(modl + c0 + 4);
      f[4 * j + 0] = f[4 * j + 0] * rs * (f32x2){c0v[0], c0v[1]} + (f32x2){s0v[0], s0v[1]};
      f[4 * j + 1] = f[4 * j + 1] * rs * (f32x2){c0v[2], c0v[3]} + (f32x2){s0v[2], s0v[3]};
      f[4 * j + 2] = f[4 * j + 2] * rs * (f32x2){c1v[0], c1v[1]} + (f32x2){s1v[0], s1v[1]};
      f[4 * j + 3] = f[4 * j + 3] * rs * (f32x2){c1v[2], c1v[3]} + (f32x2){s1v[2], s1v[3]};
      s2 += (f[4 * j + 0] + f[4 * j + 1]) + (f[4 * j + 2] + f[4 * j + 3]);
    }
    m = row16_sum(s2.x + s2.y) * (1.f / DM);
    q2 = (f32x2){0.f, 0.f};
#pragma unroll
    for (int i = 0; i < 16; i++) { f[i] -= m; q2 += f[i] * f[i]; }
    rs = rsqrtf(row16_sum(q2.x + q2.y) * (1.f / DM) + 1e-5f);
#pragma unroll
    for (int j = 0; j < 4; j++) {
      f32x2 o0 = f[4 * j] * rs, o1 = f[4 * j + 1] * rs, o2 = f[4 * j + 2] * rs, o3 = f[4 * j + 3] * rs;
      u32x4 o = {pk2(o0.x, o0.y), pk2(o1.x, o1.y), pk2(o2.x, o2.y), pk2(o3.x, o3.y)};
      *(u32x4*)(lds + (it * 32 + w * 4 + g) * 1040 + (j * 16 + n) * 16) = o;
    }
    __builtin_amdgcn_sched_barrier(0);
  }
#pragma unroll
  for (int nt = 0; nt < 2; nt++)
#pragma unroll
    for (int ks = 8; ks < 16; ks++) bfm[nt][ks] = WF[(nt * 16 + ks) * 64];
  __builtin_amdgcn_sched_barrier(0);
  __syncthreads();
  f32x4 acc[4][2];
#pragma unroll
  for (int i = 0; i < 4; i++)
#pragma unroll
    for (int j = 0; j < 2; j++) acc[i][j] = (f32x4){0.f, 0.f, 0.f, 0.f};
#pragma unroll
  for (int ks = 0; ks < 16; ks++) {
    bf16x8 af[4];
#pragma unroll
    for (int mt = 0; mt < 4; mt++) af[mt] = *(const bf16x8*)(lds + (16 * mt + n) * 1040 + (32 * ks + 8 * g) * 2);
#pragma unroll
    for (int mt = 0; mt < 4; mt++)
#pragma unroll
      for (int nt = 0; nt < 2; nt++) acc[mt][nt] = MFMA16(as_bf8(bfm[nt][ks]), af[mt], acc[mt][nt]);
    if (ks & 1) __builtin_amdgcn_sched_barrier(0);
  }
  float bb[2][4];
#pragma unroll
  for (int nt = 0; nt < 2; nt++) {
    u32x2 bv = *(const u32x2*)(b1 + nb * 256 + w * 32 + 16 * nt + 4 * g);
    bb[nt][0] = bf_lo(bv.x); bb[nt][1] = bf_hi(bv.x); bb[nt][2] = bf_lo(bv.y); bb[nt][3] = bf_hi(bv.y);
  }
#pragma unroll
  for (int mt = 0; mt < 4; mt++) {
    float o[8];
#pragma unroll
    for (int nt = 0; nt < 2; nt++)
#pragma unroll
      for (int j = 0; j < 4; j++) {
        float u = bfr(bfr(acc[mt][nt][j]) + bb[nt][j]);
        float z = 0.7978845608028654f * (u + 0.044715f * u * u * u);
        float e = __builtin_amdgcn_exp2f(z * 2.8853900817779268f);
        float th = 1.f - 2.f * __builtin_amdgcn_rcpf(e + 1.f);
        o[4 * nt + j] = 0.5f * u * (1.f + th);
      }
    u32x4 ov = {pk2(o[0], o[1]), pk2(o[2], o[3]), pk2(o[4], o[5]), pk2(o[6], o[7])};
    st16_sc1(ov, mkrsrc(ws + OFF_HID), (u32)(((mt0 * 4 + mt) * 56 + nb * 8 + w) * 64 + lane) * 16u);
  }
  asm volatile("s_waitcnt vmcnt(0)" ::: "memory");
  __syncthreads();
  if (tid == 0) st4_sc1(1u, mkrsrc(ws + OFF_FLAG), OFF_HDF + (u32)(mt0 * 8 + nb) * 4u);
}

__device__ __forceinline__ void ph_mlp2(const u16* __restrict__ b2, char* ws, u16* __restrict__ out, char* lds) {
  const int b = blockIdx.x, tid = threadIdx.x, lane = tid & 63, w = tid >> 6;
  const int mt0 = (b & 7) * 4 + (b >> 6), nb = (b >> 3) & 7;
  const int n = lane & 15, g = lane >> 4;
  const __amdgpu_buffer_rsrc_t rhid = mkrsrc(ws + OFF_HID);
  const u32 hbase = (u32)(((mt0 * 4) * 56 + w * 7) * 64 + lane) * 16u;
  const u32x4* WF = (const u32x4*)(ws + OFF_W2F) + ((size_t)(nb * 4) * 56 + w * 7) * 64 + lane;
  f32x4 acc[4][4];
#pragma unroll
  for (int i = 0; i < 4; i++)
#pragma unroll
    for (int j = 0; j < 4; j++) acc[i][j] = (f32x4){0.f, 0.f, 0.f, 0.f};
  u32x4 bfm[7][4];
#pragma unroll
  for (int k = 0; k < 7; k++)
#pragma unroll
    for (int nt = 0; nt < 4; nt++) bfm[k][nt] = WF[(nt * 56 + k) * 64];
  __builtin_amdgcn_sched_barrier(0);
  { u32 seenh2 = 0xFFFFFFFFu; const int fl = (lane & 7) < 7 ? (lane & 7) : 0; wait_progress(mkrsrc(ws + OFF_FLAG), OFF_HDF + (u32)(mt0 * 8 + fl) * 4u, 1u, seenh2); }
#pragma unroll
  for (int hb = 0; hb < 2; hb++) {
    const int nk = hb ? 3 : 4;
    bf16x8 af[4][4];
#pragma unroll
    for (int k = 0; k < 4; k++) if (k < nk) {
#pragma unroll
      for (int mt = 0; mt < 4; mt++) af[k][mt] = as_bf8(ld16_sc1(rhid, hbase + (u32)((mt * 56 + hb * 4 + k) * 64) * 16u));
    }
    __builtin_amdgcn_sched_barrier(0);
#pragma unroll
    for (int k = 0; k < 4; k++) if (k < nk) {
#pragma unroll
      for (int mt = 0; mt < 4; mt++)
#pragma unroll
        for (int nt = 0; nt < 4; nt++) acc[mt][nt] = MFMA16(as_bf8(bfm[hb * 4 + k][nt]), af[k][mt], acc[mt][nt]);
    }
  }
  float* red = (float*)lds;
#pragma unroll
  for (int mt = 0; mt < 4; mt++)
#pragma unroll
    for (int nt = 0; nt < 4; nt++) *(f32x4*)(red + (w * 64 + 16 * mt + n) * 68 + 16 * nt + 4 * g) = acc[mt][nt];
  __syncthreads();
  const float* mod = (const float*)(ws + OFF_MOD);
  {
    const int r = tid >> 3, c8 = (tid & 7) * 8;
    float o[8];
#pragma unroll
    for (int j = 0; j < 8; j++) o[j] = 0.f;
#pragma unroll
    for (int ww = 0; ww < 8; ww++) {
      f32x4 a = *(const f32x4*)(red + (ww * 64 + r) * 68 + c8), bq = *(const f32x4*)(red + (ww * 64 + r) * 68 + c8 + 4);
#pragma unroll
      for (int j = 0; j < 4; j++) { o[j] += a[j]; o[4 + j] += bq[j]; }
    }
    const int col = 64 * nb + c8;
    const size_t go = (size_t)(mt0 * 64 + r) * DM + col;
    u32x4 xv = ld16_sc1(mkrsrc(ws + OFF_X1), (u32)go * 2u), bv = *(const u32x4*)(b2 + col);
    float xf[8] = {bf_lo(xv.x), bf_hi(xv.x), bf_lo(xv.y), bf_hi(xv.y), bf_lo(xv.z), bf_hi(xv.z), bf_lo(xv.w), bf_hi(xv.w)};
    float bb[8] = {bf_lo(bv.x), bf_hi(bv.x), bf_lo(bv.y), bf_hi(bv.y), bf_lo(bv.z), bf_hi(bv.z), bf_lo(bv.w), bf_hi(bv.w)};
#pragma unroll
    for (int j = 0; j < 8; j++) o[j] = xf[j] + bfr(mod[2560 + col + j] * bfr(bfr(o[j]) + bb[j]));
    u32x4 rv = {pk2(o[0], o[1]), pk2(o[2], o[3]), pk2(o[4], o[5]), pk2(o[6], o[7])};
    *(u32x4*)(out + go) = rv;
  }
}

__global__ void __launch_bounds__(512, 2) k3_post(const u16* __restrict__ x, const u16* __restrict__ b1, const u16* __restrict__ b2,
                                                   char* ws, u16* __restrict__ out) {
  extern __shared__ __attribute__((aligned(16))) char lds[];
  ph_oproj(x, ws, lds);
  __syncthreads();
  if (blockIdx.x < 224) ph_mlp1(b1, ws, lds);
  __syncthreads();
  ph_mlp2(b2, ws, out, lds);
}

extern "C" void kernel_launch(void* const* d_in, const int* in_sizes, int n_in, void* d_out, int out_size, void* d_ws,
                              size_t ws_size, hipStream_t stream) {
  const u16* x = (const u16*)d_in[0];
  const u16* mem0 = (const u16*)d_in[1];
  const u16* Wq = (const u16*)d_in[2];
  const u16* Wk = (const u16*)d_in[3];
  const u16* Wv = (const u16*)d_in[4];
  const u16* Wo = (const u16*)d_in[5];
  const u16* w1 = (const u16*)d_in[8];
  const u16* b1 = (const u16*)d_in[9];
  const u16* w2 = (const u16*)d_in[10];
  const u16* b2 = (const u16*)d_in[11];
  const u16* cvec = (const u16*)d_in[12];
  const u16* wmod = (const u16*)d_in[13];
  char* ws = (char*)d_ws;
  hipFuncSetAttribute((const void*)k2_rec, hipFuncAttributeMaxDynamicSharedMemorySize, 135168);
  hipMemsetAsync(ws, 0xFF, OFF_H, stream);
  k2_rec<<<256, 256, 135168, stream>>>(Wq, Wk, Wv, Wo, x, w1, w2, cvec, wmod, mem0, ws);
  hipFuncSetAttribute((const void*)k3_post, hipFuncAttributeMaxDynamicSharedMemorySize, 139264);
  k3_post<<<256, 512, 139264, stream>>>(x, b1, b2, ws, (u16*)d_out);
}
```

```cpp
#include <hip/hip_runtime.h>
#include <hip/hip_bf16.h>
#include <stdint.h>

typedef unsigned int u32;
typedef unsigned short u16;
typedef unsigned long long u64;
typedef __attribute__((ext_vector_type(2))) __bf16 bf16x2;
typedef __attribute__((ext_vector_type(8))) __bf16 bf16x8;
typedef __attribute__((ext_vector_type(16))) __bf16 bf16x16;
typedef __attribute__((ext_vector_type(2))) float f32x2;
typedef __attribute__((ext_vector_type(4))) float f32x4;
typedef __attribute__((ext_vector_type(16))) float f32x16;
typedef __attribute__((ext_vector_type(2))) u32 u32x2;
typedef __attribute__((ext_vector_type(4))) u32 u32x4;
typedef __attribute__((ext_vector_type(8))) u32 u32x8;

#define NTOK 2048
#define DM 512
#define NH 32
#define DH 128
#define NSEG 64
#define FF 1792
#define SENT 0xFFFFFFFFu

#define OFF_FLAG 0ull
#define OFF_DONE 8192u
#define OFF_KMF 12288u
#define OFF_WVF 14336u
#define OFF_MDF 28672u
#define OFF_X1F 29696u
#define OFF_HDF 30720u
#define OFF_MOD 16384ull
#define OFF_MEMX 32768ull
#define OFF_PEX 0x90000ull
#define OFF_H (2ull << 20)
#define OFF_KMEM (4ull << 20)
#define OFF_WVO (8ull << 20)
#define OFF_KX (24ull << 20)
#define OFF_QX (40ull << 20)
#define OFF_Y (56ull << 20)
#define OFF_UF (72ull << 20)
#define OFF_PX (136ull << 20)
#define OFF_X1 (142ull << 20)
#define OFF_Y2 (146ull << 20)
#define OFF_HID (148ull << 20)
#define OFF_W1F (156ull << 20)
#define OFF_W2F (158ull << 20)
#define OFF_DBG (200ull << 20)
#define STAMP(slot) do { } while (0)

__device__ __forceinline__ u32 pk2(float a, float b) {
  bf16x2 v; v[0] = (__bf16)a; v[1] = (__bf16)b; return __builtin_bit_cast(u32, v);
}
__device__ __forceinline__ u32 f2u(float x) { return __builtin_bit_cast(u32, x); }
__device__ __forceinline__ float u2f(u32 x) { return __builtin_bit_cast(float, x); }
__device__ __forceinline__ float bf_lo(u32 x) { return __builtin_bit_cast(float, x << 16); }
__device__ __forceinline__ float bf_hi(u32 x) { return __builtin_bit_cast(float, x & 0xffff0000u); }
__device__ __forceinline__ float bfr(float a) { return (float)(__bf16)a; }
__device__ __forceinline__ __amdgpu_buffer_rsrc_t mkrsrc(const void* p) {
  return __builtin_amdgcn_make_buffer_rsrc((void*)p, 0, 0x7fffffff, 0x00020000);
}
__device__ __forceinline__ u32x4 ld16_sc1(__amdgpu_buffer_rsrc_t r, u32 off) { return __builtin_amdgcn_raw_buffer_load_b128(r, off, 0, 16); }
__device__ __forceinline__ u32 ld4_sc1(__amdgpu_buffer_rsrc_t r, u32 off) { return __builtin_amdgcn_raw_buffer_load_b32(r, off, 0, 16); }
__device__ __forceinline__ void st16_sc1(u32x4 v, __amdgpu_buffer_rsrc_t r, u32 off) { __builtin_amdgcn_raw_buffer_store_b128(v, r, off, 0, 16); }
__device__ __forceinline__ void st8_sc1(u32x2 v, __amdgpu_buffer_rsrc_t r, u32 off) { __builtin_amdgcn_raw_buffer_store_b64(v, r, off, 0, 16); }
__device__ __forceinline__ void st4_sc1(u32 v, __amdgpu_buffer_rsrc_t r, u32 off) { __builtin_amdgcn_raw_buffer_store_b32(v, r, off, 0, 16); }
__device__ __forceinline__ u32 badm(u32x4 a) { return (u32)(a.x == SENT) | (u32)(a.y == SENT) | (u32)(a.z == SENT) | (u32)(a.w == SENT); }
__device__ __forceinline__ u32 sane(u32 x) { return x < 0xFFFFFFFEu ? x : 0xFFFFFFFEu; }
__device__ __forceinline__ u32 now32() { return (u32)__builtin_amdgcn_s_memtime(); }
__device__ __forceinline__ void wait_until(u32 t) { while ((int)(t - now32()) > 0) { } }
__device__ __forceinline__ bf16x8 as_bf8(u32x4 v) { return __builtin_bit_cast(bf16x8, v); }
__device__ __forceinline__ bf16x16 as_bf16(u32x4 a, u32x4 b) {
  u32x8 v = {a.x, a.y, a.z, a.w, b.x, b.y, b.z, b.w}; return __builtin_bit_cast(bf16x16, v);
}
__device__ __forceinline__ float max3r(float a, float b, float c) { float r; asm("v_max3_f32 %0, %1, %2, %3" : "=v"(r) : "v"(a), "v"(b), "v"(c)); return r; }
__device__ __forceinline__ float max2r(float a, float b) { float r; asm("v_max_f32 %0, %1, %2" : "=v"(r) : "v"(a), "v"(b)); return r; }
#define MFMA16(a, b, c) __builtin_amdgcn_mfma_f32_16x16x32_bf16((a), (b), (c), 0, 0, 0)
#define MFMA32(a, b, c) __builtin_amdgcn_mfma_f32_32x32x16_bf16((a), (b), (c), 0, 0, 0)
#define SMF(a, b, c, idx) __builtin_amdgcn_smfmac_f32_16x16x64_bf16((a), (b), (c), (idx), 0, 0)

__device__ __forceinline__ u32 umax3(u32 a, u32 b, u32 c) { u32 m = a > b ? a : b; return m > c ? m : c; }
template <int N>
__device__ __forceinline__ u32 poll_chunks(__amdgpu_buffer_rsrc_t r, const u32 (&off)[N], u32x4 (&a)[N], u32 lane16) {
  u32 spins = 0;
  while (true) {
    asm volatile("" : "+v"(lane16) :: "memory");
#pragma unroll
    for (int j = 0; j < N; j++) a[j] = ld16_sc1(r, off[j] + lane16);
    u32 m = 0;
#pragma unroll
    for (int j = 0; j < N; j++) { m = umax3(m, a[j].x, a[j].y); m = umax3(m, a[j].z, a[j].w); }
    ++spins;
    if (!__any(m == SENT)) return spins;
    if (spins > 400000u) return 0;
  }
}

typedef __attribute__((ext_vector_type(4))) short s16x4;
typedef __attribute__((address_space(3))) s16x4 lds_s16x4;
template <int ROWB>
__device__ __forceinline__ void dma_tile(char* lds, const u16* src, int rows, int gstride) {
  constexpr int LPR = ROWB / 16;
  const int total = rows * LPR;
  for (int u = threadIdx.x; u < total; u += 256) {
    const int r = u / LPR, c = u % LPR;
    __builtin_amdgcn_global_load_lds((const u32*)(src + (size_t)r * gstride + c * 8), (u32*)(lds + u * 16), 16, 0, 0);
  }
}
__device__ __forceinline__ void dma_wait() { asm volatile("s_waitcnt vmcnt(0)" ::: "memory"); __syncthreads(); }
__device__ __forceinline__ u32x2 tr4(const char* lds, int row0, int col0, int pitch, int lane) {
  const char* p = lds + (row0 + ((lane & 15) >> 2)) * pitch + (col0 + 4 * (lane & 3)) * 2;
  s16x4 v = __builtin_amdgcn_ds_read_tr16_b64_v4i16((lds_s16x4*)p);
  return __builtin_bit_cast(u32x2, v);
}
__device__ __forceinline__ bf16x8 frag8(const char* lds, int k0, int col0, int pitch, int lane) {
  u32x2 a = tr4(lds, k0, col0, pitch, lane), b = tr4(lds, k0 + 4, col0, pitch, lane);
  return as_bf8((u32x4){a.x, a.y, b.x, b.y});
}
__device__ __forceinline__ u32x4 frag8s(const char* lds, int k0, int col0, int pitch, int lane) {
  u32x2 a = tr4(lds, k0, col0, pitch, lane), b = tr4(lds, k0 + 8, col0, pitch, lane);
  return (u32x4){a.x, b.x, a.y, b.y};
}
#define PACK_S(acc, b) (u32x4){pk2((acc)[(b)], (acc)[(b) + 1]), pk2((acc)[(b) + 4], (acc)[(b) + 5]), pk2((acc)[(b) + 2], (acc)[(b) + 3]), pk2((acc)[(b) + 6], (acc)[(b) + 7])}

__device__ __forceinline__ void wvo_task(const u16* __restrict__ Wv, const u16* __restrict__ Wo, char* ws, char* lds, int task) {
  const int tid = threadIdx.x, lane = tid & 63, w = tid >> 6;
  const int h = task >> 2, nq = task & 3, x = lane & 31, hh = lane >> 5;
  dma_tile<256>(lds, Wo + (size_t)h * DH * DM + nq * 128, DH, DM);
  bf16x8 a[4][8];
#pragma unroll
  for (int mt = 0; mt < 4; mt++)
#pragma unroll
    for (int ks = 0; ks < 8; ks++)
      a[mt][ks] = *(const bf16x8*)(Wv + ((size_t)(128 * w + 32 * mt + x) * NH + h) * DH + 16 * ks + 8 * hh);
  dma_wait();
  const __amdgpu_buffer_rsrc_t rd = mkrsrc(ws + OFF_WVO);
#pragma unroll
  for (int nt = 0; nt < 4; nt++) {
    bf16x8 bw[8];
#pragma unroll
    for (int ks = 0; ks < 8; ks++) bw[ks] = frag8(lds, 16 * ks + 8 * hh, 32 * nt + (x & 16), 256, lane);
#pragma unroll
    for (int mt = 0; mt < 4; mt++) {
      f32x16 acc;
#pragma unroll
      for (int i = 0; i < 16; i++) acc[i] = 0.f;
#pragma unroll
      for (int ks = 0; ks < 8; ks++) acc = MFMA32(a[mt][ks], bw[ks], acc);
      const int d = 128 * nq + 32 * nt + x, c = d >> 3, cc = d & 7;
#pragma unroll
      for (int xb = 0; xb < 2; xb++) {
        const int dp = 128 * w + 32 * mt + 16 * xb, t = dp >> 6, ga = (dp >> 4) & 3, z = ga >> 1, gb = 2 * (ga & 1) + hh;
        const int lp = (h & 1) * 8 + cc + 16 * gb;
        u32 o = (u32)(((((c * 4 + (h >> 3)) * 4 + ((h >> 1) & 3)) * 8 + t) * 2 + z) * 64 + lp);
        st16_sc1(PACK_S(acc, 8 * xb), rd, o * 16u);
      }
    }
  }
  asm volatile("s_waitcnt vmcnt(0)" ::: "memory");
  __syncthreads();
  if (tid == 0) st4_sc1(1u, mkrsrc(ws + OFF_FLAG), OFF_WVF + task * 16u);
}

__device__ __forceinline__ void mod_cols(const u16* __restrict__ cvec, const u16* __restrict__ wmod, char* ws, char* lds, int col0) {
  const int tid = threadIdx.x, cp = tid & 15, ksl = tid >> 4;
  float* red = (float*)lds;
  u32 wv[32];
#pragma unroll
  for (int k = 0; k < 32; k++) wv[k] = *(const u32*)(wmod + (size_t)(ksl * 32 + k) * 3072 + col0 + cp * 2);
  float* sl_ = (float*)(lds + 2048);
  __syncthreads();
  {
    u32 cw = *(const u32*)(cvec + tid * 2);
    float c0 = bf_lo(cw), c1 = bf_hi(cw);
    sl_[tid * 2] = bfr(c0 / (1.f + __expf(-c0)));
    sl_[tid * 2 + 1] = bfr(c1 / (1.f + __expf(-c1)));
  }
  __syncthreads();
  float a0 = 0.f, a1 = 0.f;
#pragma unroll
  for (int k4 = 0; k4 < 8; k4++) {
    f32x4 sv = *(const f32x4*)(sl_ + ksl * 32 + k4 * 4);
#pragma unroll
    for (int j = 0; j < 4; j++) { a0 += sv[j] * bf_lo(wv[k4 * 4 + j]); a1 += sv[j] * bf_hi(wv[k4 * 4 + j]); }
  }
  red[ksl * 32 + cp * 2] = a0; red[ksl * 32 + cp * 2 + 1] = a1;
  __syncthreads();
  if (tid < 32) {
    float sacc = 0.f;
#pragma unroll
    for (int k = 0; k < 16; k++) sacc += red[k * 32 + tid];
    st4_sc1(f2u(bfr(sacc)), mkrsrc(ws + OFF_MOD), (u32)(col0 + tid) * 4u);
  }
}

__device__ __forceinline__ float row16_sum(float v) {
  v += u2f((u32)__builtin_amdgcn_mov_dpp((int)f2u(v), 0x128, 0xF, 0xF, true));
  v += u2f((u32)__builtin_amdgcn_mov_dpp((int)f2u(v), 0x124, 0xF, 0xF, true));
  v += u2f((u32)__builtin_amdgcn_mov_dpp((int)f2u(v), 0x122, 0xF, 0xF, true));
  v += u2f((u32)__builtin_amdgcn_mov_dpp((int)f2u(v), 0x121, 0xF, 0xF, true));
  return v;
}

__device__ __forceinline__ bool wait_progress(__amdgpu_buffer_rsrc_t rf, u32 off, u32 need, u32& seen) {
  u32 spins = 0;
  if (__all((int)seen >= (int)need)) return true;
  while (true) {
    asm volatile("" : "+v"(off) :: "memory");
    seen = ld4_sc1(rf, off);
    if (__all((int)seen >= (int)need)) return true;
    __builtin_amdgcn_s_sleep(2);
    if (++spins > 400000u) return false;
  }
}

__device__ __forceinline__ void px_task(char* ws, int s, int hg);
__device__ __forceinline__ void role_help(const u16* __restrict__ x, const u16* __restrict__ w1, const u16* __restrict__ w2,
                                          const u16* __restrict__ cvec, const u16* __restrict__ wmod, const u16* __restrict__ mem0,
                                          char* ws, char* lds, int a0) {
  const int tid = threadIdx.x, lane = tid & 63, w = tid >> 6, n = lane & 15, g = lane >> 4;
  u32x4 xpre[2][2][4];
#pragma unroll
  for (int sg = 0; sg < 2; sg++)
#pragma unroll
    for (int it = 0; it < 2; it++)
#pragma unroll
      for (int j = 0; j < 4; j++) xpre[sg][it][j] = *(const u32x4*)(x + (size_t)((a0 + 32 * sg) * 32 + it * 16 + w * 4 + g) * DM + (j * 16 + n) * 8);
  if (a0 == 31) {
    for (int i = tid; i < 512; i += 256) {
      u32x4 v = *(const u32x4*)(mem0 + (i & 7) * DM + (i >> 3) * 8);
      v.x = sane(v.x); v.y = sane(v.y); v.z = sane(v.z); v.w = sane(v.w);
      st16_sc1(v, mkrsrc(ws + OFF_MEMX), (u32)i * 16u);
    }
  }
  mod_cols(cvec, wmod, ws, lds, a0 * 32);
  asm volatile("s_waitcnt vmcnt(0)" ::: "memory");
  __syncthreads();
  if (tid == 0) st4_sc1(1u, mkrsrc(ws + OFF_FLAG), OFF_MDF + a0 * 16u);
  f32x2 fz[2][2][16];
#pragma unroll
  for (int sg = 0; sg < 2; sg++)
#pragma unroll
    for (int it = 0; it < 2; it++) {
      f32x2* f = fz[sg][it];
#pragma unroll
      for (int j = 0; j < 4; j++) {
        const u32x4 v = xpre[sg][it][j];
        f[4 * j + 0] = (f32x2){bf_lo(v.x), bf_hi(v.x)}; f[4 * j + 1] = (f32x2){bf_lo(v.y), bf_hi(v.y)};
        f[4 * j + 2] = (f32x2){bf_lo(v.z), bf_hi(v.z)}; f[4 * j + 3] = (f32x2){bf_lo(v.w), bf_hi(v.w)};
      }
      f32x2 s2 = {0.f, 0.f};
#pragma unroll
      for (int i = 0; i < 16; i++) s2 += f[i];
      const float m = row16_sum(s2.x + s2.y) * (1.f / DM);
      f32x2 q2 = {0.f, 0.f};
#pragma unroll
      for (int i = 0; i < 16; i++) { f[i] -= m; q2 += f[i] * f[i]; }
      const float rs = rsqrtf(row16_sum(q2.x + q2.y) * (1.f / DM) + 1e-5f);
#pragma unroll
      for (int i = 0; i < 16; i++) f[i] *= rs;
    }
  { u32 seenm = 0xFFFFFFFFu; if (!wait_progress(mkrsrc(ws + OFF_FLAG), OFF_MDF + (lane & 31) * 16u, 1u, seenm)) return; }
  float* modl = (float*)(lds + 8192);
  {
    u32x4 mv = ld16_sc1(mkrsrc(ws + OFF_MOD), (u32)tid * 16u);
    f32x4 mf = {u2f(mv.x), u2f(mv.y), u2f(mv.z), u2f(mv.w)};
    *(f32x4*)(modl + tid * 4) = tid >= 128 ? mf + 1.f : mf;
  }
  __syncthreads();
  const __amdgpu_buffer_rsrc_t rh = mkrsrc(ws + OFF_H);
#pragma unroll
  for (int sg = 0; sg < 2; sg++) {
    const int a = a0 + 32 * sg;
#pragma unroll
    for (int it = 0; it < 2; it++) {
      const f32x2* f = fz[sg][it];
#pragma unroll
      for (int j = 0; j < 4; j++) {
        const int c0 = (j * 16 + n) * 8;
        f32x4 c0v = *(const f32x4*)(modl + 512 + c0), c1v = *(const f32x4*)(modl + 512 + c0 + 4);
        f32x4 s0v = *(const f32x4*)(modl + c0), s1v = *(const f32x4*)(modl + c0 + 4);
        f32x2 o0 = f[4 * j + 0] * (f32x2){c0v[0], c0v[1]} + (f32x2){s0v[0], s0v[1]};
        f32x2 o1 = f[4 * j + 1] * (f32x2){c0v[2], c0v[3]} + (f32x2){s0v[2], s0v[3]};
        f32x2 o2 = f[4 * j + 2] * (f32x2){c1v[0], c1v[1]} + (f32x2){s1v[0], s1v[1]};
        f32x2 o3 = f[4 * j + 3] * (f32x2){c1v[2], c1v[3]} + (f32x2){s1v[2], s1v[3]};
        u32x4 r = {pk2(o0.x, o0.y), pk2(o1.x, o1.y), pk2(o2.x, o2.y), pk2(o3.x, o3.y)};
        st16_sc1(r, rh, (u32)((a * 32 + it * 16 + w * 4 + g) * DM + c0) * 2u);
      }
    }
    asm volatile("s_waitcnt vmcnt(0)" ::: "memory");
    __syncthreads();
    if (tid == 0) st4_sc1(1u, mkrsrc(ws + OFF_FLAG), OFF_DONE + a * 64u);
  }
  const int a = a0;
  mod_cols(cvec, wmod, ws, lds, 1024 + a * 64);
  mod_cols(cvec, wmod, ws, lds, 1024 + a * 64 + 32);
  __syncthreads();
  if (a < 28) {
    dma_tile<128>(lds, w1 + a * 64, DM, FF);
    dma_wait();
    u32x4* dst = (u32x4*)(ws + OFF_W1F);
    for (int ks = 0; ks < 16; ks++) {
      bf16x8 v = frag8(lds, 32 * ks + 8 * (lane >> 4), 16 * w, 128, lane);
      dst[((size_t)(a * 4 + w) * 16 + ks) * 64 + lane] = __builtin_bit_cast(u32x4, v);
    }
  }
  __syncthreads();
  {
    const int cb = a >> 2, kq = a & 3;
    dma_tile<128>(lds, w2 + (size_t)kq * 448 * DM + cb * 64, 448, DM);
    dma_wait();
    u32x4* dst = (u32x4*)(ws + OFF_W2F);
    for (int ks = 0; ks < 14; ks++) {
      u32x2 va = tr4(lds, 32 * ks + 4 * (lane >> 4), 16 * w, 128, lane), vb = tr4(lds, 32 * ks + 16 + 4 * (lane >> 4), 16 * w, 128, lane);
      dst[((size_t)(cb * 4 + w) * 56 + kq * 14 + ks) * 64 + lane] = (u32x4){va.x, va.y, vb.x, vb.y};
    }
  }
  {
    const __amdgpu_buffer_rsrc_t rflag = mkrsrc(ws + OFF_FLAG);
    const int hg = a & 7;
    for (int s = a >> 3; s < NSEG; s += 4) {
      px_task(ws, s, hg);
    }
  }
}

#define AD_DEC 3
#define AD_INC 60
#define ADAPT(D, n) do { if ((n) == 1) D -= AD_DEC; else D += AD_INC; if (D < 0) D = 0; } while (0)

__device__ __forceinline__ void role_score(const u16* __restrict__ Wq, const u16* __restrict__ Wk, char* ws,
                                           char* lds, int h) {
  const int tid = threadIdx.x, lane = tid & 63, w = tid >> 6;
  const int n = lane & 15, gb = lane >> 4;
  bf16x16 wf[4][8];
#pragma unroll
  for (int mat = 0; mat < 2; mat++) {
    __syncthreads();
    dma_tile<256>(lds, (mat ? Wk : Wq) + h * DH, DM, NH * DH);
    dma_wait();
#pragma unroll
    for (int tt = 0; tt < 2; tt++)
#pragma unroll
      for (int t = 0; t < 8; t++) {
        const int col = 32 * w + 16 * tt;
        const int ta = (t + 2 * w) & 7;
        u32x4 lo = frag8s(lds, 64 * ta + 16 * (gb >> 1) + 4 * (gb & 1), col, 256, lane);
        u32x4 hi = frag8s(lds, 64 * ta + 16 * (2 + (gb >> 1)) + 4 * (gb & 1), col, 256, lane);
        wf[mat * 2 + tt][t] = as_bf16(lo, hi);
      }
  }
  __syncthreads();
  char* memL = lds;
  char* qs = lds + 8192;
  char* ks_ = lds + 8192 + 2304;
  const int idx = (lane & 8) ? (int)0xEEEEEEEE : 0x44444444;
  const __amdgpu_buffer_rsrc_t rflag = mkrsrc(ws + OFF_FLAG);
  const __amdgpu_buffer_rsrc_t rkx = mkrsrc(ws + OFF_KX);
  const __amdgpu_buffer_rsrc_t rmem = mkrsrc(ws + OFF_MEMX);
  const __amdgpu_buffer_rsrc_t rpex = mkrsrc(ws + OFF_PEX);
  const int rho = lane & 15;
  const int keyp = 8 * ((rho >> 2) & 1) + 4 * (rho >> 3) + (rho & 3);
  const u32 prho = (u32)((n & 7) >> 2) * 10240u, pi = n & 3;
  const u32 pxoff = prho + (u32)(((h >> 2) * 64) + 16 * (2 * (h & 1) + (gb >> 1)) + 8 * (gb & 1) + 4 * ((h >> 1) & 1) + pi) * 16u + (n >> 3) * 8u;
  const u32 pmoff = prho + 8192u + (u32)(h * 4 + pi) * 16u + (gb & 1) * 8u + (gb >> 1) * 4u;
#define DS_OFF 350
#define DC_OFF 180
  int D1 = (w == 0 ? 3400 : (w == 1 ? 3300 : 4050)) - DS_OFF; u32 tref = now32() - 8000u, seen = 0xFFFFFFFFu;
  STAMP(1);
  for (int s = 0; s < NSEG; s++) {
    u32x4 kx[2][4];
    u32 nsw1;
    if (w == 0) {
      if (!wait_progress(rflag, 4096u + h * 128u, s + 1, seen)) return;
#pragma unroll
      for (int tt = 0; tt < 2; tt++)
#pragma unroll
        for (int k4 = 0; k4 < 4; k4++)
          kx[tt][k4] = ld16_sc1(rkx, (u32)(((32 * s + 16 * tt + keyp) * NH + h) * DH + 32 * k4 + 8 * gb) * 2u);
    }
    f32x4 acc[4];
#pragma unroll
    for (int i = 0; i < 4; i++) { acc[i] = (f32x4){0.f, 0.f, 0.f, 0.f}; asm volatile("" : "+v"(acc[i])); }
    u32 pxo = (u32)s * 20480u + pxoff, pmo = (u32)s * 20480u + pmoff;
    asm volatile("" : "+v"(pxo), "+v"(pmo));
    wait_until(tref + D1);
    {
      u32 off[2] = {(u32)s * 8192u + w * 2048u, (u32)s * 8192u + w * 2048u + 1024u};
      u32x4 a[2];
      nsw1 = poll_chunks<2>(rmem, off, a, lane * 16u);
      *(u32x4*)(memL + w * 2048 + lane * 16) = a[0];
      *(u32x4*)(memL + w * 2048 + 1024 + lane * 16) = a[1];
      __builtin_amdgcn_sched_barrier(0);
#pragma unroll
      for (int t = 0; t < 2; t++)
#pragma unroll
        for (int i = 0; i < 4; i++) acc[i] = SMF(as_bf8(a[t]), wf[i][t], acc[i], idx);
      __builtin_amdgcn_sched_barrier(0);
    }
    __syncthreads();
    STAMP(16 + 4 * s);
    bf16x8 af[8];
#pragma unroll
    for (int t = 2; t < 8; t++) af[t] = *(const bf16x8*)(memL + ((t + 2 * w) & 7) * 1024 + lane * 16);
    __builtin_amdgcn_sched_barrier(0);
    const int ecol = 32 * w + 16 * (lane >> 5) + n, er0 = 4 * ((lane >> 4) & 1);
#pragma unroll
    for (int t = 2; t < 8; t++) { acc[0] = SMF(af[t], wf[0][t], acc[0], idx); acc[1] = SMF(af[t], wf[1][t], acc[1], idx); }
    __builtin_amdgcn_sched_barrier(0);
#pragma unroll
    for (int t = 2; t < 8; t++) {
      acc[2] = SMF(af[t], wf[2][t], acc[2], idx); acc[3] = SMF(af[t], wf[3][t], acc[3], idx);
      if (t >= 3 && t < 7) {
        const int j = t - 3;
        auto r = __builtin_amdgcn_permlane32_swap(f2u(acc[0][j]), f2u(acc[1][j]), false, false);
        *(__bf16*)(qs + (er0 + j) * 272 + ecol * 2) = (__bf16)(u2f(r[0]) + u2f(r[1]));
      }
      __builtin_amdgcn_sched_barrier(0);
    }
#pragma unroll
    for (int j = 0; j < 4; j++) {
      auto r = __builtin_amdgcn_permlane32_swap(f2u(acc[2][j]), f2u(acc[3][j]), false, false);
      *(__bf16*)(ks_ + (er0 + j) * 272 + ecol * 2) = (__bf16)(u2f(r[0]) + u2f(r[1]));
    }
    __syncthreads();
    STAMP(17 + 4 * s);
    if (w == 0 && s < NSEG - 1) {
      f32x4 sc[3];
#pragma unroll
      for (int i = 0; i < 3; i++) { sc[i] = (f32x4){0.f, 0.f, 0.f, 0.f}; asm volatile("" : "+v"(sc[i])); }
      bf16x8 qf[4], kf[4];
#pragma unroll
      for (int k4 = 0; k4 < 4; k4++) {
        qf[k4] = *(const bf16x8*)(qs + (lane & 7) * 272 + (32 * k4 + 8 * gb) * 2);
        kf[k4] = *(const bf16x8*)(ks_ + (lane & 7) * 272 + (32 * k4 + 8 * gb) * 2);
      }
      __builtin_amdgcn_sched_barrier(0);
#pragma unroll
      for (int k4 = 0; k4 < 4; k4++) {
        asm volatile("s_nop 1\n\tv_mfma_f32_16x16x32_bf16 %0, %3, %6, %0\n\tv_mfma_f32_16x16x32_bf16 %1, %4, %6, %1\n\tv_mfma_f32_16x16x32_bf16 %2, %5, %6, %2"
                     : "+v"(sc[0]), "+v"(sc[1]), "+v"(sc[2]) : "v"(kx[0][k4]), "v"(kx[1][k4]), "v"(kf[k4]), "v"(qf[k4]));
      }
      asm volatile("s_nop 7\n\ts_nop 3" : "+v"(sc[0]), "+v"(sc[1]), "+v"(sc[2]));
      const float sl = 0.08838834764831845f * 1.4426950408889634f;
      const bool hiq = n >= 8, hig = gb >= 2;
      float p[6];
#pragma unroll
      for (int j = 0; j < 4; j++) p[j] = hiq ? sc[1][j] : sc[0][j];
      p[4] = hig ? sc[2][2] : sc[2][0];
      p[5] = hig ? sc[2][3] : sc[2][1];
      float mx = max2r(max3r(p[0], p[1], p[2]), max3r(p[3], p[4], p[5]));
      mx = max2r(mx, u2f((u32)__builtin_amdgcn_mov_dpp((int)f2u(mx), 0x128, 0xF, 0xF, true)));
      { auto r = __builtin_amdgcn_permlane16_swap(f2u(mx), f2u(mx), false, false);
        mx = max2r(u2f(r[0]), u2f(r[1])); }
      { auto r = __builtin_amdgcn_permlane32_swap(f2u(mx), f2u(mx), false, false);
        mx = max2r(u2f(r[0]), u2f(r[1])); }
      const float nb = -mx * sl;
#pragma unroll
      for (int j = 0; j < 6; j++) p[j] = __builtin_amdgcn_exp2f(__builtin_fmaf(p[j], sl, nb));
      float sum = (p[0] + p[1]) + (p[2] + p[3]) + (hiq ? 0.f : (p[4] + p[5]));
      sum += u2f((u32)__builtin_amdgcn_mov_dpp((int)f2u(sum), 0x128, 0xF, 0xF, true));
      { auto r = __builtin_amdgcn_permlane16_swap(f2u(sum), f2u(sum), false, false);
        sum = u2f(r[0]) + u2f(r[1]); }
      { auto r = __builtin_amdgcn_permlane32_swap(f2u(sum), f2u(sum), false, false);
        sum = u2f(r[0]) + u2f(r[1]); }
      const float inv = __builtin_amdgcn_rcpf(sum);
      u32x2 px = {sane(pk2(p[0] * inv, p[1] * inv)), sane(pk2(p[2] * inv, p[3] * inv))};
      st8_sc1(px, rpex, pxo);
      if (!hiq) st4_sc1(sane(pk2(p[4] * inv, p[5] * inv)), rpex, pmo);
      STAMP(18 + 4 * s);
    } else if (w == 1) {
      const __amdgpu_buffer_rsrc_t rkm = mkrsrc(ws + OFF_KMEM);
#pragma unroll
      for (int j = 0; j < 2; j++) {
        int u = j * 64 + lane, r = u >> 4, c = u & 15;
        st16_sc1(*(const u32x4*)(ks_ + r * 272 + c * 16), rkm, (u32)((s * NH + h) * 1024 + r * 128 + c * 8) * 2u);
      }
      asm volatile("s_waitcnt vmcnt(0)" ::: "memory");
      if (lane == 0) st4_sc1((u32)(s + 1), rflag, OFF_KMF + h * 64u);
    }
    tref = now32();
    if (!nsw1) return;
    if (s > 2) ADAPT(D1, nsw1);
  }
}

__device__ __forceinline__ void role_column(const u16* __restrict__ Wv, const u16* __restrict__ Wo, char* ws, char* lds, int c, int rho) {
  const int tid = threadIdx.x, lane = tid & 63, w = tid >> 6;
  const int n = lane & 15, gb = lane >> 4, cc = lane & 7;
  bf16x16 wf[4][8];
  wvo_task(Wv, Wo, ws, lds, c * 2 + rho);
  {
    u32 seenw = 0xFFFFFFFFu;
    if (!wait_progress(mkrsrc(ws + OFF_FLAG), OFF_WVF + (u32)((lane & 31) * 4 + (c >> 4)) * 16u, 1u, seenw)) return;
    const __amdgpu_buffer_rsrc_t rsrc = mkrsrc(ws + OFF_WVO + (size_t)(c * 4 + w) * 65536);
#pragma unroll
    for (int tl = 0; tl < 4; tl++)
#pragma unroll
      for (int t = 0; t < 8; t++)
        wf[tl][t] = as_bf16(ld16_sc1(rsrc, (u32)(((tl * 8 + t) * 2) * 64 + lane) * 16u), ld16_sc1(rsrc, (u32)(((tl * 8 + t) * 2 + 1) * 64 + lane) * 16u));
  }
  char* memL = lds;
  char* yl = lds + 8192 + w * 1024;
  float* red = (float*)(lds + 12288);
  const int idx = (lane & 8) ? (int)0xEEEEEEEE : 0x44444444;
  const __amdgpu_buffer_rsrc_t rflag = mkrsrc(ws + OFF_FLAG);
  const __amdgpu_buffer_rsrc_t rmem = mkrsrc(ws + OFF_MEMX);
  const __amdgpu_buffer_rsrc_t rpex = mkrsrc(ws + OFF_PEX);
  int D1 = w == 0 ? 750 : (w == 1 ? 850 : 1130), D2 = 2800 - DC_OFF; u32 tref = now32(), seen = 0xFFFFFFFFu;
  STAMP(1);
  for (int s = 0; s < NSEG; s++) {
    wait_until(tref + D1);
    {
      u32 off[2] = {(u32)s * 8192u + w * 2048u, (u32)s * 8192u + w * 2048u + 1024u};
      u32x4 a[2];
      u32 nsw = poll_chunks<2>(rmem, off, a, lane * 16u);
      if (!nsw) return;
      if (s > 2) ADAPT(D1, nsw);
      tref = now32();
      *(u32x4*)(memL + w * 2048 + lane * 16) = a[0];
      *(u32x4*)(memL + w * 2048 + 1024 + lane * 16) = a[1];
    }
    u32x4 uf[4][2];
    {
      if (!wait_progress(rflag, (u32)(8 * w + (lane & 7)) * 128u, s + 1, seen)) return;
      const __amdgpu_buffer_rsrc_t ru = mkrsrc(ws + OFF_UF + ((size_t)s * 64 + c) * 16384);
#pragma unroll
      for (int m = 0; m < 4; m++)
#pragma unroll
        for (int z = 0; z < 2; z++) uf[m][z] = ld16_sc1(ru, (u32)((((4 * w + m) * 2 + z) * 4 + gb) * 8 + cc) * 16u);
    }
    __syncthreads();
    STAMP(16 + 4 * s);
    bf16x16 yb;
    {
      f32x4 acc[4];
#pragma unroll
      for (int i = 0; i < 4; i++) acc[i] = (f32x4){0.f, 0.f, 0.f, 0.f};
      bf16x8 af[8];
#pragma unroll
      for (int t = 0; t < 8; t++) af[t] = *(const bf16x8*)(memL + t * 1024 + lane * 16);
      __builtin_amdgcn_sched_barrier(0);
#pragma unroll
      for (int t = 0; t < 8; t++)
#pragma unroll
        for (int i = 0; i < 4; i++) acc[i] = SMF(af[t], wf[i][t], acc[i], idx);
#pragma unroll
      for (int pr = 0; pr < 2; pr++) {
        float v[4];
#pragma unroll
        for (int j = 0; j < 4; j++) {
          auto r = __builtin_amdgcn_permlane32_swap(f2u(acc[2 * pr][j]), f2u(acc[2 * pr + 1][j]), false, false);
          v[j] = u2f(r[0]) + u2f(r[1]);
        }
        const int hl = 2 * (2 * pr + (lane >> 5)) + (n >> 3);
        u32x2 pv = {pk2(v[0], v[1]), pk2(v[2], v[3])};
        *(u32x2*)(yl + (hl * 8 + cc) * 16 + 8 * ((lane >> 4) & 1)) = pv;
      }
      asm volatile("s_waitcnt lgkmcnt(0)" ::: "memory");
      u32x4 hz[2];
#pragma unroll
      for (int z = 0; z < 2; z++) {
        const int ga = 2 * z + (gb >> 1);
        u32x2 a = *(const u32x2*)(yl + ((2 * ga) * 8 + cc) * 16 + 8 * (gb & 1));
        u32x2 b = *(const u32x2*)(yl + ((2 * ga + 1) * 8 + cc) * 16 + 8 * (gb & 1));
        hz[z] = (u32x4){a.x, b.x, a.y, b.y};
      }
      yb = as_bf16(hz[0], hz[1]);
      if (rho == 0) __builtin_amdgcn_raw_buffer_store_b128(*(const u32x4*)(yl + lane * 16), mkrsrc(ws + OFF_Y), (u32)(s * 64 + c) * 4096u + w * 1024u + lane * 16u, 0, 0);
    }
    STAMP(17 + 4 * s);
    if (s == NSEG - 1) break;
    bf16x16 ub16[4];
#pragma unroll
    for (int m = 0; m < 4; m++) { ub16[m] = as_bf16(uf[m][0], uf[m][1]); asm volatile("" : "+v"(ub16[m])); }
    asm volatile("" : "+v"(yb));
    f32x4 dA = {0.f, 0.f, 0.f, 0.f}, dB = {0.f, 0.f, 0.f, 0.f};
    u32 rdw = (u32)(12288 + ((s & 1) * 512 + cc * 16 + 4 * w + gb) * 4), rdr = (u32)(12288 + ((s & 1) * 512 + lane * 8) * 4);
    u32 mso = (u32)(s + 1) * 8192u + c * 128u + rho * 64u + (lane >> 2) * 4u;
    asm volatile("" : "+v"(dA), "+v"(dB), "+v"(rdw), "+v"(rdr), "+v"(mso));
    u32 nsw2;
    wait_until(tref + D2);
    {
      const u32 pb = (u32)s * 20480u + rho * 10240u;
      u32 off[3] = {pb + (2 * w) * 1024u + lane * 16u, pb + (2 * w + 1) * 1024u + lane * 16u,
                    pb + 8192u + (u32)((8 * w + 2 * gb + ((lane >> 3) & 1)) * 4 + (lane & 3)) * 16u};
      u32x4 g[3];
      nsw2 = poll_chunks<3>(rpex, off, g, 0u);
      dA = SMF(as_bf8(g[0]), ub16[0], dA, idx);
      dB = SMF(as_bf8(g[0]), ub16[1], dB, idx);
      dA = SMF(as_bf8(g[1]), ub16[2], dA, idx);
      dB = SMF(as_bf8(g[1]), ub16[3], dB, idx);
      dA = SMF(as_bf8(g[2]), yb, dA, idx);
      if (n < 8) {
#pragma unroll
        for (int j = 0; j < 4; j++) *(float*)(lds + rdw + j * 512) = (gb & 1) ? dB[j] : dA[j];
      }
    }
    __syncthreads();
    STAMP(18 + 4 * s);
    if (w == 0) {
      f32x4 a = *(const f32x4*)(lds + rdr), b = *(const f32x4*)(lds + rdr + 16);
      float v = ((a[0] + a[1]) + (a[2] + a[3])) + ((b[0] + b[1]) + (b[2] + b[3]));
      v += u2f((u32)__builtin_amdgcn_mov_dpp((int)f2u(v), 0xB1, 0xF, 0xF, true));
      float o = u2f((u32)__builtin_amdgcn_mov_dpp((int)f2u(v), 0x4E, 0xF, 0xF, true));
      if (!(lane & 3)) st4_sc1(sane(pk2(v, o)), rmem, mso);
      STAMP(19 + 4 * s);
    }
    tref = now32();
    if (!nsw2) return;
    if (s > 2) ADAPT(D2, nsw2);
  }
}

__device__ __forceinline__ void load_h(u32x4 (&r)[8], const char* ws, int s) {
  const __amdgpu_buffer_rsrc_t rh = mkrsrc(ws + OFF_H);
#pragma unroll
  for (int j = 0; j < 8; j++) r[j] = ld16_sc1(rh, (u32)s * 32768u + (j * 256 + threadIdx.x) * 16u);
}
__device__ __forceinline__ void put_h(char* dst, const u32x4 (&r)[8]) {
#pragma unroll
  for (int j = 0; j < 8; j++) {
    int u = j * 256 + threadIdx.x;
    *(u32x4*)(dst + (u >> 6) * 1040 + (u & 63) * 16) = r[j];
  }
}

template <bool SC1>
__device__ __forceinline__ void store_T_tile(const f32x16& acc, char* base, int tokrow, int h, int w, int lane) {
  const __amdgpu_buffer_rsrc_t r = mkrsrc(base);
#pragma unroll
  for (int qp = 0; qp < 2; qp++) {
    u32 ax = pk2(acc[8 * qp + 0], acc[8 * qp + 1]), ay = pk2(acc[8 * qp + 2], acc[8 * qp + 3]);
    u32 bx = pk2(acc[8 * qp + 4], acc[8 * qp + 5]), by = pk2(acc[8 * qp + 6], acc[8 * qp + 7]);
    auto r0 = __builtin_amdgcn_permlane32_swap(ax, bx, false, false);
    auto r1 = __builtin_amdgcn_permlane32_swap(ay, by, false, false);
    u32x4 v = {(u32)r0[0], (u32)r1[0], (u32)r0[1], (u32)r1[1]};
    u32 off = (u32)(((tokrow + (lane & 31)) * NH + h) * DH + 32 * w + 16 * qp + 8 * (lane >> 5)) * 2u;
    if (SC1) __builtin_amdgcn_raw_buffer_store_b128(v, r, off, 0, 16);
    else __builtin_amdgcn_raw_buffer_store_b128(v, r, off, 0, 0);
  }
}

__device__ __forceinline__ void role_vu(const u16* __restrict__ Wv, const u16* __restrict__ Wo,
                                        char* ws, char* lds, int h) {
  const int tid = threadIdx.x, lane = tid & 63, w = tid >> 6, x = lane & 31, hh = lane >> 5;
  bf16x8 wv[32], wo[4][8];
  dma_tile<256>(lds, Wv + h * DH, DM, NH * DH);
  dma_wait();
#pragma unroll
  for (int ks = 0; ks < 32; ks++) wv[ks] = frag8(lds, 16 * ks + 8 * hh, 32 * w + (x & 16), 256, lane);
  __syncthreads();
  dma_tile<1024>(lds, Wo + (size_t)h * DH * DM, DH, DM);
  dma_wait();
#pragma unroll
  for (int nt = 0; nt < 4; nt++)
#pragma unroll
    for (int ks = 0; ks < 8; ks++) wo[nt][ks] = frag8(lds, 16 * ks + 8 * hh, 128 * w + 32 * nt + (x & 16), 1024, lane);
  __syncthreads();
  char* vs = lds + 66560;
  const __amdgpu_buffer_rsrc_t rflag = mkrsrc(ws + OFF_FLAG);
  u32x4 hr[8];
  { u32 seenh = 0xFFFFFFFFu; if (!wait_progress(rflag, OFF_DONE + (lane & 31) * 64u, 1u, seenh)) return; }
  load_h(hr, ws, 0);
  put_h(lds, hr);
  load_h(hr, ws, 1);
  __syncthreads();
  for (int s = 0; s < NSEG; s++) {
    const char* hb = lds + (s & 1) * 33280;
    f32x16 av;
#pragma unroll
    for (int i = 0; i < 16; i++) av[i] = 0.f;
    {
      bf16x8 hf[2][8];
#pragma unroll
      for (int k = 0; k < 8; k++) hf[0][k] = *(const bf16x8*)(hb + x * 1040 + (16 * k + 8 * hh) * 2);
#pragma unroll
      for (int ch = 0; ch < 4; ch++) {
        if (ch < 3) {
#pragma unroll
          for (int k = 0; k < 8; k++) hf[(ch + 1) & 1][k] = *(const bf16x8*)(hb + x * 1040 + (16 * (8 * ch + 8 + k) + 8 * hh) * 2);
        }
        __builtin_amdgcn_sched_barrier(0);
#pragma unroll
        for (int k = 0; k < 8; k++) av = MFMA32(wv[8 * ch + k], hf[ch & 1][k], av);
        __builtin_amdgcn_sched_barrier(0);
      }
    }
#pragma unroll
    for (int q = 0; q < 4; q++) {
      u32x2 pv = {pk2(av[4 * q], av[4 * q + 1]), pk2(av[4 * q + 2], av[4 * q + 3])};
      *(u32x2*)(vs + x * 272 + (32 * w + 8 * q + 4 * hh) * 2) = pv;
    }
    put_h(lds + ((s + 1) & 1) * 33280, hr);
    asm volatile("s_waitcnt vmcnt(8)" ::: "memory");
    __syncthreads();
    if (tid == 0 && s > 5) st4_sc1((u32)(s - 1), rflag, h * 128u);
    if (s == 30) { u32 seenh = 0xFFFFFFFFu; if (!wait_progress(rflag, OFF_DONE + lane * 64u, 1u, seenh)) return; }
    load_h(hr, ws, s + 2 < NSEG ? s + 2 : NSEG - 1);
    bf16x8 vf[8];
#pragma unroll
    for (int ks = 0; ks < 8; ks++) vf[ks] = *(const bf16x8*)(vs + x * 272 + (16 * ks + 8 * hh) * 2);
    const __amdgpu_buffer_rsrc_t ru = mkrsrc(ws + OFF_UF + (size_t)s * 64 * 16384);
#pragma unroll
    for (int nt = 0; nt < 4; nt++) {
      f32x16 au;
#pragma unroll
      for (int i = 0; i < 16; i++) au[i] = 0.f;
#pragma unroll
      for (int ks = 0; ks < 8; ks++) au = MFMA32(vf[ks], wo[nt][ks], au);
      const int d = 128 * w + 32 * nt + x, c = d >> 3, cc = d & 7;
#pragma unroll
      for (int xb = 0; xb < 2; xb++)
        st16_sc1(PACK_S(au, 8 * xb), ru, (u32)((((c * 16 + (h >> 1)) * 2 + (h & 1)) * 4 + 2 * hh + xb) * 8 + cc) * 16u);
    }
    if (s < 4 || s == NSEG - 1) asm volatile("s_waitcnt vmcnt(0)" ::: "memory");
    __syncthreads();
    if (tid == 0 && (s < 4 || s == NSEG - 1)) st4_sc1((u32)(s + 1), rflag, h * 128u);
    STAMP(16 + 4 * s);
  }
}

__device__ __forceinline__ void role_qk(const u16* __restrict__ Wq, const u16* __restrict__ Wk, char* ws, char* lds, int h) {
  const int tid = threadIdx.x, lane = tid & 63, w = tid >> 6, x = lane & 31, hh = lane >> 5;
  bf16x8 wq[32], wk[32];
  dma_tile<256>(lds, Wk + h * DH, DM, NH * DH);
  dma_wait();
#pragma unroll
  for (int ks = 0; ks < 32; ks++) wk[ks] = frag8(lds, 16 * ks + 8 * hh, 32 * w + (x & 16), 256, lane);
  __syncthreads();
  dma_tile<256>(lds, Wq + h * DH, DM, NH * DH);
  dma_wait();
#pragma unroll
  for (int ks = 0; ks < 32; ks++) wq[ks] = frag8(lds, 16 * ks + 8 * hh, 32 * w + (x & 16), 256, lane);
  __syncthreads();
  const __amdgpu_buffer_rsrc_t rflag = mkrsrc(ws + OFF_FLAG);
  u32x4 hr[8];
  { u32 seenh = 0xFFFFFFFFu; if (!wait_progress(rflag, OFF_DONE + (lane & 31) * 64u, 1u, seenh)) return; }
  load_h(hr, ws, 0);
  put_h(lds, hr);
  load_h(hr, ws, 1);
  __syncthreads();
  for (int s = 0; s < NSEG; s++) {
    const char* hb = lds + (s & 1) * 33280;
    f32x16 aq, ak;
#pragma unroll
    for (int i = 0; i < 16; i++) { aq[i] = 0.f; ak[i] = 0.f; }
    {
      bf16x8 hf[2][8];
#pragma unroll
      for (int k = 0; k < 8; k++) hf[0][k] = *(const bf16x8*)(hb + x * 1040 + (16 * k + 8 * hh) * 2);
#pragma unroll
      for (int ch = 0; ch < 4; ch++) {
        if (ch < 3) {
#pragma unroll
          for (int k = 0; k < 8; k++) hf[(ch + 1) & 1][k] = *(const bf16x8*)(hb + x * 1040 + (16 * (8 * ch + 8 + k) + 8 * hh) * 2);
        }
        __builtin_amdgcn_sched_barrier(0);
#pragma unroll
        for (int k = 0; k < 8; k++) { ak = MFMA32(wk[8 * ch + k], hf[ch & 1][k], ak); aq = MFMA32(wq[8 * ch + k], hf[ch & 1][k], aq); }
        __builtin_amdgcn_sched_barrier(0);
      }
    }
    put_h(lds + ((s + 1) & 1) * 33280, hr);
    asm volatile("s_waitcnt vmcnt(4)" ::: "memory");
    __syncthreads();
    if (tid == 0 && s > 5) st4_sc1((u32)(s - 1), rflag, 4096u + h * 128u);
    if (s == 30) { u32 seenh = 0xFFFFFFFFu; if (!wait_progress(rflag, OFF_DONE + lane * 64u, 1u, seenh)) return; }
    load_h(hr, ws, s + 2 < NSEG ? s + 2 : NSEG - 1);
    store_T_tile<true>(ak, ws + OFF_KX, 32 * s, h, w, lane);
    store_T_tile<true>(aq, ws + OFF_QX, 32 * s, h, w, lane);
    if (s < 4 || s == NSEG - 1) {
      asm volatile("s_waitcnt vmcnt(0)" ::: "memory");
      __syncthreads();
      if (tid == 0) st4_sc1((u32)(s + 1), rflag, 4096u + h * 128u);
    }
    STAMP(16 + 4 * s);
  }
}

__device__ __forceinline__ float xg_max(float v) {
  auto r = __builtin_amdgcn_permlane16_swap(f2u(v), f2u(v), false, false);
  v = fmaxf(u2f(r[0]), u2f(r[1]));
  auto r2 = __builtin_amdgcn_permlane32_swap(f2u(v), f2u(v), false, false);
  return fmaxf(u2f(r2[0]), u2f(r2[1]));
}
__device__ __forceinline__ float xg_sum(float v) {
  auto r = __builtin_amdgcn_permlane16_swap(f2u(v), f2u(v), false, false);
  v = u2f(r[0]) + u2f(r[1]);
  auto r2 = __builtin_amdgcn_permlane32_swap(f2u(v), f2u(v), false, false);
  return u2f(r2[0]) + u2f(r2[1]);
}
__device__ __forceinline__ void px_task(char* ws, int s, int hg) {
  const int tid = threadIdx.x, lane = tid & 63, w = tid >> 6;
  const int hd = hg * 4 + w, hl = w;
  const int n = lane & 15, g = lane >> 4;
  const __amdgpu_buffer_rsrc_t rq = mkrsrc(ws + OFF_QX + (size_t)s * 32 * 8192);
  const __amdgpu_buffer_rsrc_t rk = mkrsrc(ws + OFF_KX + (size_t)s * 32 * 8192);
  const __amdgpu_buffer_rsrc_t rkm = mkrsrc(ws + OFF_KMEM + (size_t)s * 65536);
  const int keyrow = 16 * ((n >> 2) & 1) + 4 * (n >> 3) + (n & 3);
  const int keyb = 16 * (g & 1) + 4 * (g >> 1);
  const float sl = 0.08838834764831845f * 1.4426950408889634f;
  bf16x8 qf[2][4], kf[2][4], mf[4];
  const __amdgpu_buffer_rsrc_t rflag = mkrsrc(ws + OFF_FLAG);
  { u32 seenq = 0xFFFFFFFFu; wait_progress(rflag, 4096u + hd * 128u, s + 1, seenq); }
#pragma unroll
  for (int ks = 0; ks < 4; ks++) {
#pragma unroll
    for (int t = 0; t < 2; t++) {
      qf[t][ks] = as_bf8(ld16_sc1(rq, (u32)(((16 * t + n) * NH + hd) * DH + 32 * ks + 8 * g) * 2u));
      kf[t][ks] = as_bf8(ld16_sc1(rk, (u32)(((8 * t + keyrow) * NH + hd) * DH + 32 * ks + 8 * g) * 2u));
    }
  }
  {
    u32 spins = 0, off = OFF_KMF + (u32)hd * 64u;
    while (true) {
      asm volatile("" : "+v"(off) :: "memory");
      u32 v = ld4_sc1(rflag, off);
      if ((int)v > s) break;
      __builtin_amdgcn_s_sleep(6);
      if (++spins > 800000u) break;
    }
  }
#pragma unroll
  for (int ks = 0; ks < 4; ks++) mf[ks] = as_bf8(ld16_sc1(rkm, (u32)(hd * 1024 + (n & 7) * 128 + 32 * ks + 8 * g) * 2u));
  const bool mvalid = (g >> 1) == (hl >> 1);
  u32x4* PXx = (u32x4*)(ws + OFF_PX);
  char* PXm = ws + OFF_PX + (4ull << 20);
#pragma unroll
  for (int qt = 0; qt < 2; qt++) {
    f32x4 sc[3];
#pragma unroll
    for (int i = 0; i < 3; i++) sc[i] = (f32x4){0.f, 0.f, 0.f, 0.f};
#pragma unroll
    for (int ks = 0; ks < 4; ks++) {
      sc[0] = MFMA16(kf[0][ks], qf[qt][ks], sc[0]);
      sc[1] = MFMA16(kf[1][ks], qf[qt][ks], sc[1]);
      sc[2] = MFMA16(mf[ks], qf[qt][ks], sc[2]);
    }
    const int qi = 16 * qt + n;
    float mx = -3.0e38f;
#pragma unroll
    for (int j = 0; j < 4; j++) {
      if (keyb + j > qi) sc[0][j] = -3.0e38f;
      if (keyb + 8 + j > qi) sc[1][j] = -3.0e38f;
      if (!mvalid) sc[2][j] = -3.0e38f;
      mx = fmaxf(mx, fmaxf(fmaxf(sc[0][j], sc[1][j]), sc[2][j]));
    }
    mx = xg_max(mx);
    float sum = 0.f;
#pragma unroll
    for (int i = 0; i < 3; i++)
#pragma unroll
      for (int j = 0; j < 4; j++) {
        float p = sc[i][j] > -1.0e38f ? __builtin_amdgcn_exp2f((sc[i][j] - mx) * sl) : 0.f;
        sc[i][j] = p; sum += p;
      }
    const float inv = __builtin_amdgcn_rcpf(xg_sum(sum));
    u32x4 pa = {pk2(sc[0][0] * inv, sc[0][1] * inv), pk2(sc[1][0] * inv, sc[1][1] * inv),
                pk2(sc[0][2] * inv, sc[0][3] * inv), pk2(sc[1][2] * inv, sc[1][3] * inv)};
    PXx[(((size_t)s * NH + hd) * 2 + qt) * 64 + lane] = pa;
    if (mvalid) {
      u32x2 pm = {pk2(sc[2][0] * inv, sc[2][1] * inv), pk2(sc[2][2] * inv, sc[2][3] * inv)};
      *(u32x2*)(PXm + ((((size_t)s * 8 + hg) * 2 + qt) * 64 + lane) * 16 + (hl & 1) * 8) = pm;
    }
  }
}

__global__ void __launch_bounds__(256, 1) k2_rec(const u16* __restrict__ Wq, const u16* __restrict__ Wk, const u16* __restrict__ Wv,
                                                  const u16* __restrict__ Wo, const u16* __restrict__ x, const u16* __restrict__ w1,
                                                  const u16* __restrict__ w2, const u16* __restrict__ cvec,
                                                  const u16* __restrict__ wmod, const u16* __restrict__ mem0, char* ws) {
  extern __shared__ __attribute__((aligned(16))) char lds[];
  const int b = blockIdx.x;
  STAMP(0);
  if (b < 32) role_help(x, w1, w2, cvec, wmod, mem0, ws, lds, b);
  else if (b < 64) role_score(Wq, Wk, ws, lds, b - 32);
  else if (b < 192) { const int q = b - 64; role_column(Wv, Wo, ws, lds, (q & 7) | ((q >> 4) << 3), (q >> 3) & 1); }
  else if (b < 224) role_vu(Wv, Wo, ws, lds, b - 192);
  else role_qk(Wq, Wk, ws, lds, b - 224);
}

__device__ __forceinline__ void ph_oproj(const u16* __restrict__ x, char* ws, char* lds) {
  const int b = blockIdx.x, tid = threadIdx.x, lane = tid & 63, w = tid >> 6;
  const int s = (b & 7) * 8 + (b >> 5), nq = (b >> 3) & 3;
  const int n = lane & 15, g = lane >> 4, cc = lane & 7;
  const u32x4* UF = (const u32x4*)(ws + OFF_UF) + (size_t)s * 64 * 1024;
  const char* Yg = ws + OFF_Y + (size_t)s * 64 * 4096;
  const u32x4* PXx = (const u32x4*)(ws + OFF_PX) + (size_t)s * NH * 128;
  const u32x4* PXm = (const u32x4*)(ws + OFF_PX + (4ull << 20)) + (size_t)s * 8 * 128;
  f32x4 acc[2][8];
#pragma unroll
  for (int i = 0; i < 2; i++)
#pragma unroll
    for (int j = 0; j < 8; j++) acc[i][j] = (f32x4){0.f, 0.f, 0.f, 0.f};
  u32x4 ub[4][8], pa[4][2], pm[2], yf[8];
#pragma unroll
  for (int hl = 0; hl < 4; hl++) {
    const int hd = 4 * w + hl;
#pragma unroll
    for (int nt = 0; nt < 8; nt++) ub[hl][nt] = __builtin_nontemporal_load(UF + (size_t)(16 * nq + 2 * nt + (n >> 3)) * 1024 + (hd * 4 + g) * 8 + cc);
    pa[hl][0] = PXx[(hd * 2) * 64 + lane];
    pa[hl][1] = PXx[(hd * 2 + 1) * 64 + lane];
  }
  pm[0] = PXm[(w * 2) * 64 + lane];
  pm[1] = PXm[(w * 2 + 1) * 64 + lane];
  {
    const int ha = 4 * w + 2 * (g >> 1);
#pragma unroll
    for (int nt = 0; nt < 8; nt++) {
      const int c = 16 * nq + 2 * nt + (n >> 3);
      u32x2 ya = __builtin_nontemporal_load((const u32x2*)(Yg + (size_t)c * 4096 + (ha * 8 + cc) * 16 + 8 * (g & 1)));
      u32x2 yb = __builtin_nontemporal_load((const u32x2*)(Yg + (size_t)c * 4096 + ((ha + 1) * 8 + cc) * 16 + 8 * (g & 1)));
      yf[nt] = (u32x4){ya.x, ya.y, yb.x, yb.y};
    }
  }
  __builtin_amdgcn_sched_barrier(0);
#pragma unroll
  for (int hl = 0; hl < 4; hl++)
#pragma unroll
    for (int nt = 0; nt < 8; nt++) {
      acc[0][nt] = MFMA16(as_bf8(pa[hl][0]), as_bf8(ub[hl][nt]), acc[0][nt]);
      acc[1][nt] = MFMA16(as_bf8(pa[hl][1]), as_bf8(ub[hl][nt]), acc[1][nt]);
    }
#pragma unroll
  for (int nt = 0; nt < 8; nt++) {
    acc[0][nt] = MFMA16(as_bf8(pm[0]), as_bf8(yf[nt]), acc[0][nt]);
    acc[1][nt] = MFMA16(as_bf8(pm[1]), as_bf8(yf[nt]), acc[1][nt]);
  }
  float* red = (float*)lds;
#pragma unroll
  for (int qt = 0; qt < 2; qt++)
#pragma unroll
    for (int nt = 0; nt < 8; nt++)
#pragma unroll
      for (int j = 0; j < 4; j++) red[(w * 32 + 16 * qt + 4 * g + j) * 132 + 16 * nt + n] = acc[qt][nt][j];
  __syncthreads();
  const float* mod = (const float*)(ws + OFF_MOD);
  {
    const int r = tid >> 4, c8 = (tid & 15) * 8;
    float o[8];
#pragma unroll
    for (int j = 0; j < 8; j++) o[j] = 0.f;
#pragma unroll
    for (int ww = 0; ww < 8; ww++) {
      f32x4 a = *(const f32x4*)(red + (ww * 32 + r) * 132 + c8), bq = *(const f32x4*)(red + (ww * 32 + r) * 132 + c8 + 4);
#pragma unroll
      for (int j = 0; j < 4; j++) { o[j] += a[j]; o[4 + j] += bq[j]; }
    }
    const int col = 128 * nq + c8;
    const size_t go = (size_t)(32 * s + r) * DM + col;
    u32x4 xv = *(const u32x4*)(x + go);
    float xf[8] = {bf_lo(xv.x), bf_hi(xv.x), bf_lo(xv.y), bf_hi(xv.y), bf_lo(xv.z), bf_hi(xv.z), bf_lo(xv.w), bf_hi(xv.w)};
#pragma unroll
    for (int j = 0; j < 8; j++) o[j] = xf[j] + bfr(mod[1024 + col + j] * bfr(o[j]));
    u32x4 rv = {pk2(o[0], o[1]), pk2(o[2], o[3]), pk2(o[4], o[5]), pk2(o[6], o[7])};
    st16_sc1(rv, mkrsrc(ws + OFF_X1), (u32)go * 2u);
  }
  asm volatile("s_waitcnt vmcnt(0)" ::: "memory");
  __syncthreads();
  if (tid == 0) st4_sc1(1u, mkrsrc(ws + OFF_FLAG), OFF_X1F + (u32)(s * 4 + nq) * 4u);
}

__device__ __forceinline__ void ph_mlp1(const u16* __restrict__ b1, char* ws, char* lds) {
  const int b = blockIdx.x, tid = threadIdx.x, lane = tid & 63, w = tid >> 6;
  const int jq = b >> 3, mt0 = (b & 7) * 4 + jq / 7, nb = jq % 7;
  const float* mod = (const float*)(ws + OFF_MOD);
  const int n = lane & 15, g = lane >> 4;
  const f32x4 mv = *(const f32x4*)(mod + 1536 + (tid & 255) * 4);
  const u32x4* WF = (const u32x4*)(ws + OFF_W1F) + (size_t)(nb * 16 + w * 2) * 16 * 64 + lane;
  u32x4 bfm[2][16];
#pragma unroll
  for (int nt = 0; nt < 2; nt++)
#pragma unroll
    for (int ks = 0; ks < 8; ks++) bfm[nt][ks] = WF[(nt * 16 + ks) * 64];
  __builtin_amdgcn_sched_barrier(0);
  { u32 seenx = 0xFFFFFFFFu; wait_progress(mkrsrc(ws + OFF_FLAG), OFF_X1F + (u32)(mt0 * 8 + (lane & 7)) * 4u, 1u, seenx); }
  u32x4 xv[2][4];
#pragma unroll
  for (int it = 0; it < 2; it++)
#pragma unroll
    for (int j = 0; j < 4; j++) xv[it][j] = ld16_sc1(mkrsrc(ws + OFF_X1), (u32)((mt0 * 64 + it * 32 + w * 4 + g) * DM + (j * 16 + n) * 8) * 2u);
  __builtin_amdgcn_sched_barrier(0);
  float* modl = (float*)(lds + 66560);
  if (tid < 256) *(f32x4*)(modl + tid * 4) = tid >= 128 ? mv + 1.f : mv;
  __syncthreads();
#pragma unroll
  for (int it = 0; it < 2; it++) {
    f32x2 f[16];
#pragma unroll
    for (int j = 0; j < 4; j++) {
      f[4 * j + 0] = (f32x2){bf_lo(xv[it][j].x), bf_hi(xv[it][j].x)}; f[4 * j + 1] = (f32x2){bf_lo(xv[it][j].y), bf_hi(xv[it][j].y)};
      f[4 * j + 2] = (f32x2){bf_lo(xv[it][j].z), bf_hi(xv[it][j].z)}; f[4 * j + 3] = (f32x2){bf_lo(xv[it][j].w), bf_hi(xv[it][j].w)};
    }
    f32x2 s2 = {0.f, 0.f};
#pragma unroll
    for (int i = 0; i < 16; i++) s2 += f[i];
    float m = row16_sum(s2.x + s2.y) * (1.f / DM);
    f32x2 q2 = {0.f, 0.f};
#pragma unroll
    for (int i = 0; i < 16; i++) { f[i] -= m; q2 += f[i] * f[i]; }
    float rs = rsqrtf(row16_sum(q2.x + q2.y) * (1.f / DM) + 1e-5f);
    s2 = (f32x2){0.f, 0.f};
#pragma unroll
    for (int j = 0; j < 4; j++) {
      const int c0 = (j * 16 + n) * 8;
      f32x4 c0v = *(const f32x4*)(modl + 512 + c0), c1v = *(const f32x4*)(modl + 512 + c0 + 4);
      f32x4 s0v = *(const f32x4*)(modl + c0), s1v = *(const f32x4*)(modl + c0 + 4);
      f[4 * j + 0] = f[4 * j + 0] * rs * (f32x2){c0v[0], c0v[1]} + (f32x2){s0v[0], s0v[1]};
      f[4 * j + 1] = f[4 * j + 1] * rs * (f32x2){c0v[2], c0v[3]} + (f32x2){s0v[2], s0v[3]};
      f[4 * j + 2] = f[4 * j + 2] * rs * (f32x2){c1v[0], c1v[1]} + (f32x2){s1v[0], s1v[1]};
      f[4 * j + 3] = f[4 * j + 3] * rs * (f32x2){c1v[2], c1v[3]} + (f32x2){s1v[2], s1v[3]};
      s2 += (f[4 * j + 0] + f[4 * j + 1]) + (f[4 * j + 2] + f[4 * j + 3]);
    }
    m = row16_sum(s2.x + s2.y) * (1.f / DM);
    q2 = (f32x2){0.f, 0.f};
#pragma unroll
    for (int i = 0; i < 16; i++) { f[i] -= m; q2 += f[i] * f[i]; }
    rs = rsqrtf(row16_sum(q2.x + q2.y) * (1.f / DM) + 1e-5f);
#pragma unroll
    for (int j = 0; j < 4; j++) {
      f32x2 o0 = f[4 * j] * rs, o1 = f[4 * j + 1] * rs, o2 = f[4 * j + 2] * rs, o3 = f[4 * j + 3] * rs;
      u32x4 o = {pk2(o0.x, o0.y), pk2(o1.x, o1.y), pk2(o2.x, o2.y), pk2(o3.x, o3.y)};
      *(u32x4*)(lds + (it * 32 + w * 4 + g) * 1040 + (j * 16 + n) * 16) = o;
    }
    __builtin_amdgcn_sched_barrier(0);
  }
#pragma unroll
  for (int nt = 0; nt < 2; nt++)
#pragma unroll
    for (int ks = 8; ks < 16; ks++) bfm[nt][ks] = WF[(nt * 16 + ks) * 64];
  __builtin_amdgcn_sched_barrier(0);
  __syncthreads();
  f32x4 acc[4][2];
#pragma unroll
  for (int i = 0; i < 4; i++)
#pragma unroll
    for (int j = 0; j < 2; j++) acc[i][j] = (f32x4){0.f, 0.f, 0.f, 0.f};
#pragma unroll
  for (int ks = 0; ks < 16; ks++) {
    bf16x8 af[4];
#pragma unroll
    for (int mt = 0; mt < 4; mt++) af[mt] = *(const bf16x8*)(lds + (16 * mt + n) * 1040 + (32 * ks + 8 * g) * 2);
#pragma unroll
    for (int mt = 0; mt < 4; mt++)
#pragma unroll
      for (int nt = 0; nt < 2; nt++) acc[mt][nt] = MFMA16(as_bf8(bfm[nt][ks]), af[mt], acc[mt][nt]);
    if (ks & 1) __builtin_amdgcn_sched_barrier(0);
  }
  float bb[2][4];
#pragma unroll
  for (int nt = 0; nt < 2; nt++) {
    u32x2 bv = *(const u32x2*)(b1 + nb * 256 + w * 32 + 16 * nt + 4 * g);
    bb[nt][0] = bf_lo(bv.x); bb[nt][1] = bf_hi(bv.x); bb[nt][2] = bf_lo(bv.y); bb[nt][3] = bf_hi(bv.y);
  }
#pragma unroll
  for (int mt = 0; mt < 4; mt++) {
    float o[8];
#pragma unroll
    for (int nt = 0; nt < 2; nt++)
#pragma unroll
      for (int j = 0; j < 4; j++) {
        float u = bfr(bfr(acc[mt][nt][j]) + bb[nt][j]);
        float z = 0.7978845608028654f * (u + 0.044715f * u * u * u);
        float e = __builtin_amdgcn_exp2f(z * 2.8853900817779268f);
        float th = 1.f - 2.f * __builtin_amdgcn_rcpf(e + 1.f);
        o[4 * nt + j] = 0.5f * u * (1.f + th);
      }
    u32x4 ov = {pk2(o[0], o[1]), pk2(o[2], o[3]), pk2(o[4], o[5]), pk2(o[6], o[7])};
    st16_sc1(ov, mkrsrc(ws + OFF_HID), (u32)(((mt0 * 4 + mt) * 56 + nb * 8 + w) * 64 + lane) * 16u);
  }
  asm volatile("s_waitcnt vmcnt(0)" ::: "memory");
  __syncthreads();
  if (tid == 0) st4_sc1(1u, mkrsrc(ws + OFF_FLAG), OFF_HDF + (u32)(mt0 * 8 + nb) * 4u);
}

__device__ __forceinline__ void ph_mlp2(const u16* __restrict__ b2, char* ws, u16* __restrict__ out, char* lds) {
  const int b = blockIdx.x, tid = threadIdx.x, lane = tid & 63, w = tid >> 6;
  const int mt0 = (b & 7) * 4 + (b >> 6), nb = (b >> 3) & 7;
  const int n = lane & 15, g = lane >> 4;
  const __amdgpu_buffer_rsrc_t rhid = mkrsrc(ws + OFF_HID);
  const u32 hbase = (u32)(((mt0 * 4) * 56 + w * 7) * 64 + lane) * 16u;
  const u32x4* WF = (const u32x4*)(ws + OFF_W2F) + ((size_t)(nb * 4) * 56 + w * 7) * 64 + lane;
  f32x4 acc[4][4];
#pragma unroll
  for (int i = 0; i < 4; i++)
#pragma unroll
    for (int j = 0; j < 4; j++) acc[i][j] = (f32x4){0.f, 0.f, 0.f, 0.f};
  u32x4 bfm[7][4];
#pragma unroll
  for (int k = 0; k < 7; k++)
#pragma unroll
    for (int nt = 0; nt < 4; nt++) bfm[k][nt] = WF[(nt * 56 + k) * 64];
  __builtin_amdgcn_sched_barrier(0);
  { u32 seenh2 = 0xFFFFFFFFu; const int fl = (lane & 7) < 7 ? (lane & 7) : 0; wait_progress(mkrsrc(ws + OFF_FLAG), OFF_HDF + (u32)(mt0 * 8 + fl) * 4u, 1u, seenh2); }
#pragma unroll
  for (int hb = 0; hb < 2; hb++) {
    const int nk = hb ? 3 : 4;
    bf16x8 af[4][4];
#pragma unroll
    for (int k = 0; k < 4; k++) if (k < nk) {
#pragma unroll
      for (int mt = 0; mt < 4; mt++) af[k][mt] = as_bf8(ld16_sc1(rhid, hbase + (u32)((mt * 56 + hb * 4 + k) * 64) * 16u));
    }
    __builtin_amdgcn_sched_barrier(0);
#pragma unroll
    for (int k = 0; k < 4; k++) if (k < nk) {
#pragma unroll
      for (int mt = 0; mt < 4; mt++)
#pragma unroll
        for (int nt = 0; nt < 4; nt++) acc[mt][nt] = MFMA16(as_bf8(bfm[hb * 4 + k][nt]), af[k][mt], acc[mt][nt]);
    }
  }
  float* red = (float*)lds;
#pragma unroll
  for (int mt = 0; mt < 4; mt++)
#pragma unroll
    for (int nt = 0; nt < 4; nt++) *(f32x4*)(red + (w * 64 + 16 * mt + n) * 68 + 16 * nt + 4 * g) = acc[mt][nt];
  __syncthreads();
  const float* mod = (const float*)(ws + OFF_MOD);
  {
    const int r = tid >> 3, c8 = (tid & 7) * 8;
    float o[8];
#pragma unroll
    for (int j = 0; j < 8; j++) o[j] = 0.f;
#pragma unroll
    for (int ww = 0; ww < 8; ww++) {
      f32x4 a = *(const f32x4*)(red + (ww * 64 + r) * 68 + c8), bq = *(const f32x4*)(red + (ww * 64 + r) * 68 + c8 + 4);
#pragma unroll
      for (int j = 0; j < 4; j++) { o[j] += a[j]; o[4 + j] += bq[j]; }
    }
    const int col = 64 * nb + c8;
    const size_t go = (size_t)(mt0 * 64 + r) * DM + col;
    u32x4 xv = ld16_sc1(mkrsrc(ws + OFF_X1), (u32)go * 2u), bv = *(const u32x4*)(b2 + col);
    float xf[8] = {bf_lo(xv.x), bf_hi(xv.x), bf_lo(xv.y), bf_hi(xv.y), bf_lo(xv.z), bf_hi(xv.z), bf_lo(xv.w), bf_hi(xv.w)};
    float bb[8] = {bf_lo(bv.x), bf_hi(bv.x), bf_lo(bv.y), bf_hi(bv.y), bf_lo(bv.z), bf_hi(bv.z), bf_lo(bv.w), bf_hi(bv.w)};
#pragma unroll
    for (int j = 0; j < 8; j++) o[j] = xf[j] + bfr(mod[2560 + col + j] * bfr(bfr(o[j]) + bb[j]));
    u32x4 rv = {pk2(o[0], o[1]), pk2(o[2], o[3]), pk2(o[4], o[5]), pk2(o[6], o[7])};
    *(u32x4*)(out + go) = rv;
  }
}

__global__ void __launch_bounds__(512, 2) k3_post(const u16* __restrict__ x, const u16* __restrict__ b1, const u16* __restrict__ b2,
                                                   char* ws, u16* __restrict__ out) {
  extern __shared__ __attribute__((aligned(16))) char lds[];
  ph_oproj(x, ws, lds);
  __syncthreads();
  if (blockIdx.x < 224) ph_mlp1(b1, ws, lds);
  __syncthreads();
  ph_mlp2(b2, ws, out, lds);
}

extern "C" void kernel_launch(void* const* d_in, const int* in_sizes, int n_in, void* d_out, int out_size, void* d_ws,
                              size_t ws_size, hipStream_t stream) {
  const u16* x = (const u16*)d_in[0];
  const u16* mem0 = (const u16*)d_in[1];
  const u16* Wq = (const u16*)d_in[2];
  const u16* Wk = (const u16*)d_in[3];
  const u16* Wv = (const u16*)d_in[4];
  const u16* Wo = (const u16*)d_in[5];
  const u16* w1 = (const u16*)d_in[8];
  const u16* b1 = (const u16*)d_in[9];
  const u16* w2 = (const u16*)d_in[10];
  const u16* b2 = (const u16*)d_in[11];
  const u16* cvec = (const u16*)d_in[12];
  const u16* wmod = (const u16*)d_in[13];
  char* ws = (char*)d_ws;
  hipFuncSetAttribute((const void*)k2_rec, hipFuncAttributeMaxDynamicSharedMemorySize, 135168);
  hipMemsetAsync(ws, 0xFF, OFF_H, stream);
  k2_rec<<<256, 256, 135168, stream>>>(Wq, Wk, Wv, Wo, x, w1, w2, cvec, wmod, mem0, ws);
  hipFuncSetAttribute((const void*)k3_post, hipFuncAttributeMaxDynamicSharedMemorySize, 139264);
  k3_post<<<256, 512, 139264, stream>>>(x, b1, b2, ws, (u16*)d_out);
}
```

```cpp
#include <hip/hip_runtime.h>
#include <hip/hip_bf16.h>
#include <stdint.h>

typedef unsigned int u32;
typedef unsigned short u16;
typedef unsigned long long u64;
typedef __attribute__((ext_vector_type(2))) __bf16 bf16x2;
typedef __attribute__((ext_vector_type(8))) __bf16 bf16x8;
typedef __attribute__((ext_vector_type(16))) __bf16 bf16x16;
typedef __attribute__((ext_vector_type(2))) float f32x2;
typedef __attribute__((ext_vector_type(4))) float f32x4;
typedef __attribute__((ext_vector_type(16))) float f32x16;
typedef __attribute__((ext_vector_type(2))) u32 u32x2;
typedef __attribute__((ext_vector_type(4))) u32 u32x4;
typedef __attribute__((ext_vector_type(8))) u32 u32x8;

#define NTOK 2048
#define DM 512
#define NH 32
#define DH 128
#define NSEG 64
#define FF 1792
#define SENT 0xFFFFFFFFu

#define OFF_FLAG 0ull
#define OFF_DONE 8192u
#define OFF_KMF 12288u
#define OFF_WVF 14336u
#define OFF_MDF 28672u
#define OFF_X1F 29696u
#define OFF_HDF 30720u
#define OFF_MOD 16384ull
#define OFF_MEMX 32768ull
#define OFF_PEX 0x90000ull
#define OFF_H (2ull << 20)
#define OFF_KMEM (4ull << 20)
#define OFF_WVO (8ull << 20)
#define OFF_KX (24ull << 20)
#define OFF_QX (40ull << 20)
#define OFF_Y (56ull << 20)
#define OFF_UF (72ull << 20)
#define OFF_PX (136ull << 20)
#define OFF_X1 (142ull << 20)
#define OFF_Y2 (146ull << 20)
#define OFF_HID (148ull << 20)
#define OFF_W1F (156ull << 20)
#define OFF_W2F (158ull << 20)
#define OFF_DBG (200ull << 20)
#define STAMP(slot) do { } while (0)

__device__ __forceinline__ u32 pk2(float a, float b) {
  bf16x2 v; v[0] = (__bf16)a; v[1] = (__bf16)b; return __builtin_bit_cast(u32, v);
}
__device__ __forceinline__ u32 f2u(float x) { return __builtin_bit_cast(u32, x); }
__device__ __forceinline__ float u2f(u32 x) { return __builtin_bit_cast(float, x); }
__device__ __forceinline__ float bf_lo(u32 x) { return __builtin_bit_cast(float, x << 16); }
__device__ __forceinline__ float bf_hi(u32 x) { return __builtin_bit_cast(float, x & 0xffff0000u); }
__device__ __forceinline__ float bfr(float a) { return (float)(__bf16)a; }
__device__ __forceinline__ __amdgpu_buffer_rsrc_t mkrsrc(const void* p) {
  return __builtin_amdgcn_make_buffer_rsrc((void*)p, 0, 0x7fffffff, 0x00020000);
}
__device__ __forceinline__ u32x4 ld16_sc1(__amdgpu_buffer_rsrc_t r, u32 off) { return __builtin_amdgcn_raw_buffer_load_b128(r, off, 0, 16); }
__device__ __forceinline__ u32 ld4_sc1(__amdgpu_buffer_rsrc_t r, u32 off) { return __builtin_amdgcn_raw_buffer_load_b32(r, off, 0, 16); }
__device__ __forceinline__ void st16_sc1(u32x4 v, __amdgpu_buffer_rsrc_t r, u32 off) { __builtin_amdgcn_raw_buffer_store_b128(v, r, off, 0, 16); }
__device__ __forceinline__ void st8_sc1(u32x2 v, __amdgpu_buffer_rsrc_t r, u32 off) { __builtin_amdgcn_raw_buffer_store_b64(v, r, off, 0, 16); }
__device__ __forceinline__ void st4_sc1(u32 v, __amdgpu_buffer_rsrc_t r, u32 off) { __builtin_amdgcn_raw_buffer_store_b32(v, r, off, 0, 16); }
__device__ __forceinline__ u32 badm(u32x4 a) { return (u32)(a.x == SENT) | (u32)(a.y == SENT) | (u32)(a.z == SENT) | (u32)(a.w == SENT); }
__device__ __forceinline__ u32 sane(u32 x) { return x < 0xFFFFFFFEu ? x : 0xFFFFFFFEu; }
__device__ __forceinline__ u32 now32() { return (u32)__builtin_amdgcn_s_memtime(); }
__device__ __forceinline__ void wait_until(u32 t) { while ((int)(t - now32()) > 0) { } }
__device__ __forceinline__ bf16x8 as_bf8(u32x4 v) { return __builtin_bit_cast(bf16x8, v); }
__device__ __forceinline__ bf16x16 as_bf16(u32x4 a, u32x4 b) {
  u32x8 v = {a.x, a.y, a.z, a.w, b.x, b.y, b.z, b.w}; return __builtin_bit_cast(bf16x16, v);
}
__device__ __forceinline__ float max3r(float a, float b, float c) { float r; asm("v_max3_f32 %0, %1, %2, %3" : "=v"(r) : "v"(a), "v"(b), "v"(c)); return r; }
__device__ __forceinline__ float max2r(float a, float b) { float r; asm("v_max_f32 %0, %1, %2" : "=v"(r) : "v"(a), "v"(b)); return r; }
#define MFMA16(a, b, c) __builtin_amdgcn_mfma_f32_16x16x32_bf16((a), (b), (c), 0, 0, 0)
#define MFMA32(a, b, c) __builtin_amdgcn_mfma_f32_32x32x16_bf16((a), (b), (c), 0, 0, 0)
#define SMF(a, b, c, idx) __builtin_amdgcn_smfmac_f32_16x16x64_bf16((a), (b), (c), (idx), 0, 0)

__device__ __forceinline__ u32 umax3(u32 a, u32 b, u32 c) { u32 m = a > b ? a : b; return m > c ? m : c; }
template <int N>
__device__ __forceinline__ u32 poll_chunks(__amdgpu_buffer_rsrc_t r, const u32 (&off)[N], u32x4 (&a)[N], u32 lane16) {
  u32 spins = 0;
  while (true) {
    asm volatile("" : "+v"(lane16) :: "memory");
#pragma unroll
    for (int j = 0; j < N; j++) a[j] = ld16_sc1(r, off[j] + lane16);
    u32 m = 0;
#pragma unroll
    for (int j = 0; j < N; j++) { m = umax3(m, a[j].x, a[j].y); m = umax3(m, a[j].z, a[j].w); }
    ++spins;
    if (!__any(m == SENT)) return spins;
    if (spins > 400000u) return 0;
  }
}

typedef __attribute__((ext_vector_type(4))) short s16x4;
typedef __attribute__((address_space(3))) s16x4 lds_s16x4;
template <int ROWB>
__device__ __forceinline__ void dma_tile(char* lds, const u16* src, int rows, int gstride) {
  constexpr int LPR = ROWB / 16;
  const int total = rows * LPR;
  for (int u = threadIdx.x; u < total; u += 256) {
    const int r = u / LPR, c = u % LPR;
    __builtin_amdgcn_global_load_lds((const u32*)(src + (size_t)r * gstride + c * 8), (u32*)(lds + u * 16), 16, 0, 0);
  }
}
__device__ __forceinline__ void dma_wait() { asm volatile("s_waitcnt vmcnt(0)" ::: "memory"); __syncthreads(); }
__device__ __forceinline__ u32x2 tr4(const char* lds, int row0, int col0, int pitch, int lane) {
  const char* p = lds + (row0 + ((lane & 15) >> 2)) * pitch + (col0 + 4 * (lane & 3)) * 2;
  s16x4 v = __builtin_amdgcn_ds_read_tr16_b64_v4i16((lds_s16x4*)p);
  return __builtin_bit_cast(u32x2, v);
}
__device__ __forceinline__ bf16x8 frag8(const char* lds, int k0, int col0, int pitch, int lane) {
  u32x2 a = tr4(lds, k0, col0, pitch, lane), b = tr4(lds, k0 + 4, col0, pitch, lane);
  return as_bf8((u32x4){a.x, a.y, b.x, b.y});
}
__device__ __forceinline__ u32x4 frag8s(const char* lds, int k0, int col0, int pitch, int lane) {
  u32x2 a = tr4(lds, k0, col0, pitch, lane), b = tr4(lds, k0 + 8, col0, pitch, lane);
  return (u32x4){a.x, b.x, a.y, b.y};
}
#define PACK_S(acc, b) (u32x4){pk2((acc)[(b)], (acc)[(b) + 1]), pk2((acc)[(b) + 4], (acc)[(b) + 5]), pk2((acc)[(b) + 2], (acc)[(b) + 3]), pk2((acc)[(b) + 6], (acc)[(b) + 7])}

__device__ __forceinline__ void wvo_task(const u16* __restrict__ Wv, const u16* __restrict__ Wo, char* ws, char* lds, int task) {
  const int tid = threadIdx.x, lane = tid & 63, w = tid >> 6;
  const int h = task >> 2, nq = task & 3, x = lane & 31, hh = lane >> 5;
  dma_tile<256>(lds, Wo + (size_t)h * DH * DM + nq * 128, DH, DM);
  bf16x8 a[4][8];
#pragma unroll
  for (int mt = 0; mt < 4; mt++)
#pragma unroll
    for (int ks = 0; ks < 8; ks++)
      a[mt][ks] = *(const bf16x8*)(Wv + ((size_t)(128 * w + 32 * mt + x) * NH + h) * DH + 16 * ks + 8 * hh);
  dma_wait();
  const __amdgpu_buffer_rsrc_t rd = mkrsrc(ws + OFF_WVO);
#pragma unroll
  for (int nt = 0; nt < 4; nt++) {
    bf16x8 bw[8];
#pragma unroll
    for (int ks = 0; ks < 8; ks++) bw[ks] = frag8(lds, 16 * ks + 8 * hh, 32 * nt + (x & 16), 256, lane);
#pragma unroll
    for (int mt = 0; mt < 4; mt++) {
      f32x16 acc;
#pragma unroll
      for (int i = 0; i < 16; i++) acc[i] = 0.f;
#pragma unroll
      for (int ks = 0; ks < 8; ks++) acc = MFMA32(a[mt][ks], bw[ks], acc);
      const int d = 128 * nq + 32 * nt + x, c = d >> 3, cc = d & 7;
#pragma unroll
      for (int xb = 0; xb < 2; xb++) {
        const int dp = 128 * w + 32 * mt + 16 * xb, t = dp >> 6, ga = (dp >> 4) & 3, z = ga >> 1, gb = 2 * (ga & 1) + hh;
        const int lp = (h & 1) * 8 + cc + 16 * gb;
        u32 o = (u32)(((((c * 4 + (h >> 3)) * 4 + ((h >> 1) & 3)) * 8 + t) * 2 + z) * 64 + lp);
        st16_sc1(PACK_S(acc, 8 * xb), rd, o * 16u);
      }
    }
  }
  asm volatile("s_waitcnt vmcnt(0)" ::: "memory");
  __syncthreads();
  if (tid == 0) st4_sc1(1u, mkrsrc(ws + OFF_FLAG), OFF_WVF + task * 16u);
}

__device__ __forceinline__ void mod_cols(const u16* __restrict__ cvec, const u16* __restrict__ wmod, char* ws, char* lds, int col0) {
  const int tid = threadIdx.x, cp = tid & 15, ksl = tid >> 4;
  float* red = (float*)lds;
  u32 wv[32];
#pragma unroll
  for (int k = 0; k < 32; k++) wv[k] = *(const u32*)(wmod + (size_t)(ksl * 32 + k) * 3072 + col0 + cp * 2);
  float* sl_ = (float*)(lds + 2048);
  __syncthreads();
  {
    u32 cw = *(const u32*)(cvec + tid * 2);
    float c0 = bf_lo(cw), c1 = bf_hi(cw);
    sl_[tid * 2] = bfr(c0 / (1.f + __expf(-c0)));
    sl_[tid * 2 + 1] = bfr(c1 / (1.f + __expf(-c1)));
  }
  __syncthreads();
  float a0 = 0.f, a1 = 0.f;
#pragma unroll
  for (int k4 = 0; k4 < 8; k4++) {
    f32x4 sv = *(const f32x4*)(sl_ + ksl * 32 + k4 * 4);
#pragma unroll
    for (int j = 0; j < 4; j++) { a0 += sv[j] * bf_lo(wv[k4 * 4 + j]); a1 += sv[j] * bf_hi(wv[k4 * 4 + j]); }
  }
  red[ksl * 32 + cp * 2] = a0; red[ksl * 32 + cp * 2 + 1] = a1;
  __syncthreads();
  if (tid < 32) {
    float sacc = 0.f;
#pragma unroll
    for (int k = 0; k < 16; k++) sacc += red[k * 32 + tid];
    st4_sc1(f2u(bfr(sacc)), mkrsrc(ws + OFF_MOD), (u32)(col0 + tid) * 4u);
  }
}

__device__ __forceinline__ float row16_sum(float v) {
  v += u2f((u32)__builtin_amdgcn_mov_dpp((int)f2u(v), 0x128, 0xF, 0xF, true));
  v += u2f((u32)__builtin_amdgcn_mov_dpp((int)f2u(v), 0x124, 0xF, 0xF, true));
  v += u2f((u32)__builtin_amdgcn_mov_dpp((int)f2u(v), 0x122, 0xF, 0xF, true));
  v += u2f((u32)__builtin_amdgcn_mov_dpp((int)f2u(v), 0x121, 0xF, 0xF, true));
  return v;
}

__device__ __forceinline__ bool wait_progress(__amdgpu_buffer_rsrc_t rf, u32 off, u32 need, u32& seen) {
  u32 spins = 0;
  if (__all((int)seen >= (int)need)) return true;
  while (true) {
    asm volatile("" : "+v"(off) :: "memory");
    seen = ld4_sc1(rf, off);
    if (__all((int)seen >= (int)need)) return true;
    __builtin_amdgcn_s_sleep(2);
    if (++spins > 400000u) return false;
  }
}

__device__ __forceinline__ void px_task(char* ws, int s, int hg);
__device__ __forceinline__ void role_help(const u16* __restrict__ x, const u16* __restrict__ w1, const u16* __restrict__ w2,
                                          const u16* __restrict__ cvec, const u16* __restrict__ wmod, const u16* __restrict__ mem0,
                                          char* ws, char* lds, int a0) {
  const int tid = threadIdx.x, lane = tid & 63, w = tid >> 6, n = lane & 15, g = lane >> 4;
  u32x4 xpre[2][2][4];
#pragma unroll
  for (int sg = 0; sg < 2; sg++)
#pragma unroll
    for (int it = 0; it < 2; it++)
#pragma unroll
      for (int j = 0; j < 4; j++) xpre[sg][it][j] = *(const u32x4*)(x + (size_t)((a0 + 32 * sg) * 32 + it * 16 + w * 4 + g) * DM + (j * 16 + n) * 8);
  if (a0 == 31) {
    for (int i = tid; i < 512; i += 256) {
      u32x4 v = *(const u32x4*)(mem0 + (i & 7) * DM + (i >> 3) * 8);
      v.x = sane(v.x); v.y = sane(v.y); v.z = sane(v.z); v.w = sane(v.w);
      st16_sc1(v, mkrsrc(ws + OFF_MEMX), (u32)i * 16u);
    }
  }
  mod_cols(cvec, wmod, ws, lds, a0 * 32);
  asm volatile("s_waitcnt vmcnt(0)" ::: "memory");
  __syncthreads();
  if (tid == 0) st4_sc1(1u, mkrsrc(ws + OFF_FLAG), OFF_MDF + a0 * 16u);
  f32x2 fz[2][2][16];
#pragma unroll
  for (int sg = 0; sg < 2; sg++)
#pragma unroll
    for (int it = 0; it < 2; it++) {
      f32x2* f = fz[sg][it];
#pragma unroll
      for (int j = 0; j < 4; j++) {
        const u32x4 v = xpre[sg][it][j];
        f[4 * j + 0] = (f32x2){bf_lo(v.x), bf_hi(v.x)}; f[4 * j + 1] = (f32x2){bf_lo(v.y), bf_hi(v.y)};
        f[4 * j + 2] = (f32x2){bf_lo(v.z), bf_hi(v.z)}; f[4 * j + 3] = (f32x2){bf_lo(v.w), bf_hi(v.w)};
      }
      f32x2 s2 = {0.f, 0.f};
#pragma unroll
      for (int i = 0; i < 16; i++) s2 += f[i];
      const float m = row16_sum(s2.x + s2.y) * (1.f / DM);
      f32x2 q2 = {0.f, 0.f};
#pragma unroll
      for (int i = 0; i < 16; i++) { f[i] -= m; q2 += f[i] * f[i]; }
      const float rs = rsqrtf(row16_sum(q2.x + q2.y) * (1.f / DM) + 1e-5f);
#pragma unroll
      for (int i = 0; i < 16; i++) f[i] *= rs;
    }
  { u32 seenm = 0xFFFFFFFFu; if (!wait_progress(mkrsrc(ws + OFF_FLAG), OFF_MDF + (lane & 31) * 16u, 1u, seenm)) return; }
  float* modl = (float*)(lds + 8192);
  {
    u32x4 mv = ld16_sc1(mkrsrc(ws + OFF_MOD), (u32)tid * 16u);
    f32x4 mf = {u2f(mv.x), u2f(mv.y), u2f(mv.z), u2f(mv.w)};
    *(f32x4*)(modl + tid * 4) = tid >= 128 ? mf + 1.f : mf;
  }
  __syncthreads();
  const __amdgpu_buffer_rsrc_t rh = mkrsrc(ws + OFF_H);
#pragma unroll
  for (int sg = 0; sg < 2; sg++) {
    const int a = a0 + 32 * sg;
#pragma unroll
    for (int it = 0; it < 2; it++) {
      const f32x2* f = fz[sg][it];
#pragma unroll
      for (int j = 0; j < 4; j++) {
        const int c0 = (j * 16 + n) * 8;
        f32x4 c0v = *(const f32x4*)(modl + 512 + c0), c1v = *(const f32x4*)(modl + 512 + c0 + 4);
        f32x4 s0v = *(const f32x4*)(modl + c0), s1v = *(const f32x4*)(modl + c0 + 4);
        f32x2 o0 = f[4 * j + 0] * (f32x2){c0v[0], c0v[1]} + (f32x2){s0v[0], s0v[1]};
        f32x2 o1 = f[4 * j + 1] * (f32x2){c0v[2], c0v[3]} + (f32x2){s0v[2], s0v[3]};
        f32x2 o2 = f[4 * j + 2] * (f32x2){c1v[0], c1v[1]} + (f32x2){s1v[0], s1v[1]};
        f32x2 o3 = f[4 * j + 3] * (f32x2){c1v[2], c1v[3]} + (f32x2){s1v[2], s1v[3]};
        u32x4 r = {pk2(o0.x, o0.y), pk2(o1.x, o1.y), pk2(o2.x, o2.y), pk2(o3.x, o3.y)};
        st16_sc1(r, rh, (u32)((a * 32 + it * 16 + w * 4 + g) * DM + c0) * 2u);
      }
    }
    asm volatile("s_waitcnt vmcnt(0)" ::: "memory");
    __syncthreads();
    if (tid == 0) st4_sc1(1u, mkrsrc(ws + OFF_FLAG), OFF_DONE + a * 64u);
  }
  const int a = a0;
  mod_cols(cvec, wmod, ws, lds, 1024 + a * 64);
  mod_cols(cvec, wmod, ws, lds, 1024 + a * 64 + 32);
  __syncthreads();
  if (a < 28) {
    dma_tile<128>(lds, w1 + a * 64, DM, FF);
    dma_wait();
    u32x4* dst = (u32x4*)(ws + OFF_W1F);
    for (int ks = 0; ks < 16; ks++) {
      bf16x8 v = frag8(lds, 32 * ks + 8 * (lane >> 4), 16 * w, 128, lane);
      dst[((size_t)(a * 4 + w) * 16 + ks) * 64 + lane] = __builtin_bit_cast(u32x4, v);
    }
  }
  __syncthreads();
  {
    const int cb = a >> 2, kq = a & 3;
    dma_tile<128>(lds, w2 + (size_t)kq * 448 * DM + cb * 64, 448, DM);
    dma_wait();
    u32x4* dst = (u32x4*)(ws + OFF_W2F);
    for (int ks = 0; ks < 14; ks++) {
      u32x2 va = tr4(lds, 32 * ks + 4 * (lane >> 4), 16 * w, 128, lane), vb = tr4(lds, 32 * ks + 16 + 4 * (lane >> 4), 16 * w, 128, lane);
      dst[((size_t)(cb * 4 + w) * 56 + kq * 14 + ks) * 64 + lane] = (u32x4){va.x, va.y, vb.x, vb.y};
    }
  }
  {
    const __amdgpu_buffer_rsrc_t rflag = mkrsrc(ws + OFF_FLAG);
    const int hg = a & 7;
    for (int s = a >> 3; s < NSEG; s += 4) {
      px_task(ws, s, hg);
    }
  }
}

#define AD_DEC 3
#define AD_INC 60
#define ADAPT(D, n) do { if ((n) == 1) D -= AD_DEC; else D += AD_INC; if (D < 0) D = 0; } while (0)

__device__ __forceinline__ void role_score(const u16* __restrict__ Wq, const u16* __restrict__ Wk, char* ws,
                                           char* lds, int h) {
  const int tid = threadIdx.x, lane = tid & 63, w = tid >> 6;
  const int n = lane & 15, gb = lane >> 4;
  bf16x16 wf[4][8];
#pragma unroll
  for (int mat = 0; mat < 2; mat++) {
    __syncthreads();
    dma_tile<256>(lds, (mat ? Wk : Wq) + h * DH, DM, NH * DH);
    dma_wait();
#pragma unroll
    for (int tt = 0; tt < 2; tt++)
#pragma unroll
      for (int t = 0; t < 8; t++) {
        const int col = 32 * w + 16 * tt;
        const int ta = (t + 2 * w) & 7;
        u32x4 lo = frag8s(lds, 64 * ta + 16 * (gb >> 1) + 4 * (gb & 1), col, 256, lane);
        u32x4 hi = frag8s(lds, 64 * ta + 16 * (2 + (gb >> 1)) + 4 * (gb & 1), col, 256, lane);
        wf[mat * 2 + tt][t] = as_bf16(lo, hi);
      }
  }
  __syncthreads();
  char* memL = lds;
  char* qs = lds + 8192;
  char* ks_ = lds + 8192 + 2304;
  const int idx = (lane & 8) ? (int)0xEEEEEEEE : 0x44444444;
  const __amdgpu_buffer_rsrc_t rflag = mkrsrc(ws + OFF_FLAG);
  const __amdgpu_buffer_rsrc_t rkx = mkrsrc(ws + OFF_KX);
  const __amdgpu_buffer_rsrc_t rmem = mkrsrc(ws + OFF_MEMX);
  const __amdgpu_buffer_rsrc_t rpex = mkrsrc(ws + OFF_PEX);
  const int rho = lane & 15;
  const int keyp = 8 * ((rho >> 2) & 1) + 4 * (rho >> 3) + (rho & 3);
  const u32 prho = (u32)((n & 7) >> 2) * 10240u, pi = n & 3;
  const u32 pxoff = prho + (u32)(((h >> 2) * 64) + 16 * (2 * (h & 1) + (gb >> 1)) + 8 * (gb & 1) + 4 * ((h >> 1) & 1) + pi) * 16u + (n >> 3) * 8u;
  const u32 pmoff = prho + 8192u + (u32)(h * 4 + pi) * 16u + (gb & 1) * 8u + (gb >> 1) * 4u;
#define DS_OFF 350
#define DC_OFF 180
  int D1 = (w == 0 ? 3400 : (w == 1 ? 3300 : 4050)) - DS_OFF; u32 tref = now32() - 8000u, seen = 0xFFFFFFFFu;
  STAMP(1);
  for (int s = 0; s < NSEG; s++) {
    u32x4 kx[2][4];
    u32 nsw1;
    if (w == 0) {
      if (!wait_progress(rflag, 4096u + h * 128u, s + 1, seen)) return;
#pragma unroll
      for (int tt = 0; tt < 2; tt++)
#pragma unroll
        for (int k4 = 0; k4 < 4; k4++)
          kx[tt][k4] = ld16_sc1(rkx, (u32)(((32 * s + 16 * tt + keyp) * NH + h) * DH + 32 * k4 + 8 * gb) * 2u);
    }
    f32x4 acc[4];
#pragma unroll
    for (int i = 0; i < 4; i++) { acc[i] = (f32x4){0.f, 0.f, 0.f, 0.f}; asm volatile("" : "+v"(acc[i])); }
    u32 pxo = (u32)s * 20480u + pxoff, pmo = (u32)s * 20480u + pmoff;
    asm volatile("" : "+v"(pxo), "+v"(pmo));
    wait_until(tref + D1);
    {
      u32 off[2] = {(u32)s * 8192u + w * 2048u, (u32)s * 8192u + w * 2048u + 1024u};
      u32x4 a[2];
      nsw1 = poll_chunks<2>(rmem, off, a, lane * 16u);
      *(u32x4*)(memL + w * 2048 + lane * 16) = a[0];
      *(u32x4*)(memL + w * 2048 + 1024 + lane * 16) = a[1];
      __builtin_amdgcn_sched_barrier(0);
#pragma unroll
      for (int t = 0; t < 2; t++)
#pragma unroll
        for (int i = 0; i < 4; i++) acc[i] = SMF(as_bf8(a[t]), wf[i][t], acc[i], idx);
      __builtin_amdgcn_sched_barrier(0);
    }
    __syncthreads();
    STAMP(16 + 4 * s);
    bf16x8 af[8];
#pragma unroll
    for (int t = 2; t < 8; t++) af[t] = *(const bf16x8*)(memL + ((t + 2 * w) & 7) * 1024 + lane * 16);
    __builtin_amdgcn_sched_barrier(0);
    const int ecol = 32 * w + 16 * (lane >> 5) + n, er0 = 4 * ((lane >> 4) & 1);
#pragma unroll
    for (int t = 2; t < 8; t++) { acc[0] = SMF(af[t], wf[0][t], acc[0], idx); acc[1] = SMF(af[t], wf[1][t], acc[1], idx); }
    __builtin_amdgcn_sched_barrier(0);
#pragma unroll
    for (int t = 2; t < 8; t++) {
      acc[2] = SMF(af[t], wf[2][t], acc[2], idx); acc[3] = SMF(af[t], wf[3][t], acc[3], idx);
      if (t >= 3 && t < 7) {
        const int j = t - 3;
        auto r = __builtin_amdgcn_permlane32_swap(f2u(acc[0][j]), f2u(acc[1][j]), false, false);
        *(__bf16*)(qs + (er0 + j) * 272 + ecol * 2) = (__bf16)(u2f(r[0]) + u2f(r[1]));
      }
      __builtin_amdgcn_sched_barrier(0);
    }
#pragma unroll
    for (int j = 0; j < 4; j++) {
      auto r = __builtin_amdgcn_permlane32_swap(f2u(acc[2][j]), f2u(acc[3][j]), false, false);
      *(__bf16*)(ks_ + (er0 + j) * 272 + ecol * 2) = (__bf16)(u2f(r[0]) + u2f(r[1]));
    }
    __syncthreads();
    STAMP(17 + 4 * s);
    if (w == 0 && s < NSEG - 1) {
      f32x4 sc[3];
#pragma unroll
      for (int i = 0; i < 3; i++) { sc[i] = (f32x4){0.f, 0.f, 0.f, 0.f}; asm volatile("" : "+v"(sc[i])); }
      bf16x8 qf[4], kf[4];
#pragma unroll
      for (int k4 = 0; k4 < 4; k4++) {
        qf[k4] = *(const bf16x8*)(qs + (lane & 7) * 272 + (32 * k4 + 8 * gb) * 2);
        kf[k4] = *(const bf16x8*)(ks_ + (lane & 7) * 272 + (32 * k4 + 8 * gb) * 2);
      }
      __builtin_amdgcn_sched_barrier(0);
#pragma unroll
      for (int k4 = 0; k4 < 4; k4++) {
        asm volatile("s_nop 1\n\tv_mfma_f32_16x16x32_bf16 %0, %3, %6, %0\n\tv_mfma_f32_16x16x32_bf16 %1, %4, %6, %1\n\tv_mfma_f32_16x16x32_bf16 %2, %5, %6, %2"
                     : "+v"(sc[0]), "+v"(sc[1]), "+v"(sc[2]) : "v"(kx[0][k4]), "v"(kx[1][k4]), "v"(kf[k4]), "v"(qf[k4]));
      }
      asm volatile("s_nop 7\n\ts_nop 3" : "+v"(sc[0]), "+v"(sc[1]), "+v"(sc[2]));
      const float sl = 0.08838834764831845f * 1.4426950408889634f;
      const bool hiq = n >= 8, hig = gb >= 2;
      float p[6];
#pragma unroll
      for (int j = 0; j < 4; j++) p[j] = hiq ? sc[1][j] : sc[0][j];
      p[4] = hig ? sc[2][2] : sc[2][0];
      p[5] = hig ? sc[2][3] : sc[2][1];
      float mx = max2r(max3r(p[0], p[1], p[2]), max3r(p[3], p[4], p[5]));
      mx = max2r(mx, u2f((u32)__builtin_amdgcn_mov_dpp((int)f2u(mx), 0x128, 0xF, 0xF, true)));
      { auto r = __builtin_amdgcn_permlane16_swap(f2u(mx), f2u(mx), false, false);
        mx = max2r(u2f(r[0]), u2f(r[1])); }
      { auto r = __builtin_amdgcn_permlane32_swap(f2u(mx), f2u(mx), false, false);
        mx = max2r(u2f(r[0]), u2f(r[1])); }
      const float nb = -mx * sl;
#pragma unroll
      for (int j = 0; j < 6; j++) p[j] = __builtin_amdgcn_exp2f(__builtin_fmaf(p[j], sl, nb));
      float sum = (p[0] + p[1]) + (p[2] + p[3]) + (hiq ? 0.f : (p[4] + p[5]));
      sum += u2f((u32)__builtin_amdgcn_mov_dpp((int)f2u(sum), 0x128, 0xF, 0xF, true));
      { auto r = __builtin_amdgcn_permlane16_swap(f2u(sum), f2u(sum), false, false);
        sum = u2f(r[0]) + u2f(r[1]); }
      { auto r = __builtin_amdgcn_permlane32_swap(f2u(sum), f2u(sum), false, false);
        sum = u2f(r[0]) + u2f(r[1]); }
      const float inv = __builtin_amdgcn_rcpf(sum);
      u32x2 px = {sane(pk2(p[0] * inv, p[1] * inv)), sane(pk2(p[2] * inv, p[3] * inv))};
      st8_sc1(px, rpex, pxo);
      if (!hiq) st4_sc1(sane(pk2(p[4] * inv, p[5] * inv)), rpex, pmo);
      STAMP(18 + 4 * s);
    } else if (w == 1) {
      const __amdgpu_buffer_rsrc_t rkm = mkrsrc(ws + OFF_KMEM);
#pragma unroll
      for (int j = 0; j < 2; j++) {
        int u = j * 64 + lane, r = u >> 4, c = u & 15;
        st16_sc1(*(const u32x4*)(ks_ + r * 272 + c * 16), rkm, (u32)((s * NH + h) * 1024 + r * 128 + c * 8) * 2u);
      }
      asm volatile("s_waitcnt vmcnt(0)" ::: "memory");
      if (lane == 0) st4_sc1((u32)(s + 1), rflag, OFF_KMF + h * 64u);
    }
    tref = now32();
    if (!nsw1) return;
    if (s > 2) ADAPT(D1, nsw1);
  }
}

__device__ __forceinline__ void role_column(const u16* __restrict__ Wv, const u16* __restrict__ Wo, char* ws, char* lds, int c, int rho) {
  const int tid = threadIdx.x, lane = tid & 63, w = tid >> 6;
  const int n = lane & 15, gb = lane >> 4, cc = lane & 7;
  bf16x16 wf[4][8];
  wvo_task(Wv, Wo, ws, lds, c * 2 + rho);
  {
    u32 seenw = 0xFFFFFFFFu;
    if (!wait_progress(mkrsrc(ws + OFF_FLAG), OFF_WVF + (u32)((lane & 31) * 4 + (c >> 4)) * 16u, 1u, seenw)) return;
    const __amdgpu_buffer_rsrc_t rsrc = mkrsrc(ws + OFF_WVO + (size_t)(c * 4 + w) * 65536);
#pragma unroll
    for (int tl = 0; tl < 4; tl++)
#pragma unroll
      for (int t = 0; t < 8; t++)
        wf[tl][t] = as_bf16(ld16_sc1(rsrc, (u32)(((tl * 8 + t) * 2) * 64 + lane) * 16u), ld16_sc1(rsrc, (u32)(((tl * 8 + t) * 2 + 1) * 64 + lane) * 16u));
  }
  char* memL = lds;
  char* yl = lds + 8192 + w * 1024;
  float* red = (float*)(lds + 12288);
  const int idx = (lane & 8) ? (int)0xEEEEEEEE : 0x44444444;
  const __amdgpu_buffer_rsrc_t rflag = mkrsrc(ws + OFF_FLAG);
  const __amdgpu_buffer_rsrc_t rmem = mkrsrc(ws + OFF_MEMX);
  const __amdgpu_buffer_rsrc_t rpex = mkrsrc(ws + OFF_PEX);
  int D1 = w == 0 ? 750 : (w == 1 ? 850 : 1130), D2 = 2800 - DC_OFF; u32 tref = now32(), seen = 0xFFFFFFFFu;
  STAMP(1);
  for (int s = 0; s < NSEG; s++) {
    wait_until(tref + D1);
    {
      u32 off[2] = {(u32)s * 8192u + w * 2048u, (u32)s * 8192u + w * 2048u + 1024u};
      u32x4 a[2];
      u32 nsw = poll_chunks<2>(rmem, off, a, lane * 16u);
      if (!nsw) return;
      if (s > 2) ADAPT(D1, nsw);
      tref = now32();
      *(u32x4*)(memL + w * 2048 + lane * 16) = a[0];
      *(u32x4*)(memL + w * 2048 + 1024 + lane * 16) = a[1];
    }
    u32x4 uf[4][2];
    {
      if (!wait_progress(rflag, (u32)(8 * w + (lane & 7)) * 128u, s + 1, seen)) return;
      const __amdgpu_buffer_rsrc_t ru = mkrsrc(ws + OFF_UF + ((size_t)s * 64 + c) * 16384);
#pragma unroll
      for (int m = 0; m < 4; m++)
#pragma unroll
        for (int z = 0; z < 2; z++) uf[m][z] = ld16_sc1(ru, (u32)((((4 * w + m) * 2 + z) * 4 + gb) * 8 + cc) * 16u);
    }
    __syncthreads();
    STAMP(16 + 4 * s);
    bf16x16 yb;
    {
      f32x4 acc[4];
#pragma unroll
      for (int i = 0; i < 4; i++) acc[i] = (f32x4){0.f, 0.f, 0.f, 0.f};
      bf16x8 af[8];
#pragma unroll
      for (int t = 0; t < 8; t++) af[t] = *(const bf16x8*)(memL + t * 1024 + lane * 16);
      __builtin_amdgcn_sched_barrier(0);
#pragma unroll
      for (int t = 0; t < 8; t++)
#pragma unroll
        for (int i = 0; i < 4; i++) acc[i] = SMF(af[t], wf[i][t], acc[i], idx);
#pragma unroll
      for (int pr = 0; pr < 2; pr++) {
        float v[4];
#pragma unroll
        for (int j = 0; j < 4; j++) {
          auto r = __builtin_amdgcn_permlane32_swap(f2u(acc[2 * pr][j]), f2u(acc[2 * pr + 1][j]), false, false);
          v[j] = u2f(r[0]) + u2f(r[1]);
        }
        const int hl = 2 * (2 * pr + (lane >> 5)) + (n >> 3);
        u32x2 pv = {pk2(v[0], v[1]), pk2(v[2], v[3])};
        *(u32x2*)(yl + (hl * 8 + cc) * 16 + 8 * ((lane >> 4) & 1)) = pv;
      }
      asm volatile("s_waitcnt lgkmcnt(0)" ::: "memory");
      u32x4 hz[2];
#pragma unroll
      for (int z = 0; z < 2; z++) {
        const int ga = 2 * z + (gb >> 1);
        u32x2 a = *(const u32x2*)(yl + ((2 * ga) * 8 + cc) * 16 + 8 * (gb & 1));
        u32x2 b = *(const u32x2*)(yl + ((2 * ga + 1) * 8 + cc) * 16 + 8 * (gb & 1));
        hz[z] = (u32x4){a.x, b.x, a.y, b.y};
      }
      yb = as_bf16(hz[0], hz[1]);
      if (rho == 0) __builtin_amdgcn_raw_buffer_store_b128(*(const u32x4*)(yl + lane * 16), mkrsrc(ws + OFF_Y), (u32)(s * 64 + c) * 4096u + w * 1024u + lane * 16u, 0, 0);
    }
    STAMP(17 + 4 * s);
    if (s == NSEG - 1) break;
    bf16x16 ub16[4];
#pragma unroll
    for (int m = 0; m < 4; m++) { ub16[m] = as_bf16(uf[m][0], uf[m][1]); asm volatile("" : "+v"(ub16[m])); }
    asm volatile("" : "+v"(yb));
    f32x4 dA = {0.f, 0.f, 0.f, 0.f}, dB = {0.f, 0.f, 0.f, 0.f};
    u32 rdw = (u32)(12288 + ((s & 1) * 512 + cc * 16 + 4 * w + gb) * 4), rdr = (u32)(12288 + ((s & 1) * 512 + lane * 8) * 4);
    u32 mso = (u32)(s + 1) * 8192u + c * 128u + rho * 64u + (lane >> 2) * 4u;
    asm volatile("" : "+v"(dA), "+v"(dB), "+v"(rdw), "+v"(rdr), "+v"(mso));
    u32 nsw2;
    wait_until(tref + D2);
    {
      const u32 pb = (u32)s * 20480u + rho * 10240u;
      u32 off[3] = {pb + (2 * w) * 1024u + lane * 16u, pb + (2 * w + 1) * 1024u + lane * 16u,
                    pb + 8192u + (u32)((8 * w + 2 * gb + ((lane >> 3) & 1)) * 4 + (lane & 3)) * 16u};
      u32x4 g[3];
      nsw2 = poll_chunks<3>(rpex, off, g, 0u);
      dA = SMF(as_bf8(g[0]), ub16[0], dA, idx);
      dB = SMF(as_bf8(g[0]), ub16[1], dB, idx);
      dA = SMF(as_bf8(g[1]), ub16[2], dA, idx);
      dB = SMF(as_bf8(g[1]), ub16[3], dB, idx);
      dA = SMF(as_bf8(g[2]), yb, dA, idx);
      if (n < 8) {
#pragma unroll
        for (int j = 0; j < 4; j++) *(float*)(lds + rdw + j * 512) = (gb & 1) ? dB[j] : dA[j];
      }
    }
    __syncthreads();
    STAMP(18 + 4 * s);
    if (w == 0) {
      f32x4 a = *(const f32x4*)(lds + rdr), b = *(const f32x4*)(lds + rdr + 16);
      float v = ((a[0] + a[1]) + (a[2] + a[3])) + ((b[0] + b[1]) + (b[2] + b[3]));
      v += u2f((u32)__builtin_amdgcn_mov_dpp((int)f2u(v), 0xB1, 0xF, 0xF, true));
      float o = u2f((u32)__builtin_amdgcn_mov_dpp((int)f2u(v), 0x4E, 0xF, 0xF, true));
      if (!(lane & 3)) st4_sc1(sane(pk2(v, o)), rmem, mso);
      STAMP(19 + 4 * s);
    }
    tref = now32();
    if (!nsw2) return;
    if (s > 2) ADAPT(D2, nsw2);
  }
}

__device__ __forceinline__ void load_h(u32x4 (&r)[8], const char* ws, int s) {
  const __amdgpu_buffer_rsrc_t rh = mkrsrc(ws + OFF_H);
#pragma unroll
  for (int j = 0; j < 8; j++) r[j] = ld16_sc1(rh, (u32)s * 32768u + (j * 256 + threadIdx.x) * 16u);
}
__device__ __forceinline__ void put_h(char* dst, const u32x4 (&r)[8]) {
#pragma unroll
  for (int j = 0; j < 8; j++) {
    int u = j * 256 + threadIdx.x;
    *(u32x4*)(dst + (u >> 6) * 1040 + (u & 63) * 16) = r[j];
  }
}

template <bool SC1>
__device__ __forceinline__ void store_T_tile(const f32x16& acc, char* base, int tokrow, int h, int w, int lane) {
  const __amdgpu_buffer_rsrc_t r = mkrsrc(base);
#pragma unroll
  for (int qp = 0; qp < 2; qp++) {
    u32 ax = pk2(acc[8 * qp + 0], acc[8 * qp + 1]), ay = pk2(acc[8 * qp + 2], acc[8 * qp + 3]);
    u32 bx = pk2(acc[8 * qp + 4], acc[8 * qp + 5]), by = pk2(acc[8 * qp + 6], acc[8 * qp + 7]);
    auto r0 = __builtin_amdgcn_permlane32_swap(ax, bx, false, false);
    auto r1 = __builtin_amdgcn_permlane32_swap(ay, by, false, false);
    u32x4 v = {(u32)r0[0], (u32)r1[0], (u32)r0[1], (u32)r1[1]};
    u32 off = (u32)(((tokrow + (lane & 31)) * NH + h) * DH + 32 * w + 16 * qp + 8 * (lane >> 5)) * 2u;
    if (SC1) __builtin_amdgcn_raw_buffer_store_b128(v, r, off, 0, 16);
    else __builtin_amdgcn_raw_buffer_store_b128(v, r, off, 0, 0);
  }
}

__device__ __forceinline__ void role_vu(const u16* __restrict__ Wv, const u16* __restrict__ Wo,
                                        char* ws, char* lds, int h) {
  const int tid = threadIdx.x, lane = tid & 63, w = tid >> 6, x = lane & 31, hh = lane >> 5;
  bf16x8 wv[32], wo[4][8];
  dma_tile<256>(lds, Wv + h * DH, DM, NH * DH);
  dma_wait();
#pragma unroll
  for (int ks = 0; ks < 32; ks++) wv[ks] = frag8(lds, 16 * ks + 8 * hh, 32 * w + (x & 16), 256, lane);
  __syncthreads();
  dma_tile<1024>(lds, Wo + (size_t)h * DH * DM, DH, DM);
  dma_wait();
#pragma unroll
  for (int nt = 0; nt < 4; nt++)
#pragma unroll
    for (int ks = 0; ks < 8; ks++) wo[nt][ks] = frag8(lds, 16 * ks + 8 * hh, 128 * w + 32 * nt + (x & 16), 1024, lane);
  __syncthreads();
  char* vs = lds + 66560;
  const __amdgpu_buffer_rsrc_t rflag = mkrsrc(ws + OFF_FLAG);
  u32x4 hr[8];
  { u32 seenh = 0xFFFFFFFFu; if (!wait_progress(rflag, OFF_DONE + (lane & 31) * 64u, 1u, seenh)) return; }
  load_h(hr, ws, 0);
  put_h(lds, hr);
  load_h(hr, ws, 1);
  __syncthreads();
  for (int s = 0; s < NSEG; s++) {
    const char* hb = lds + (s & 1) * 33280;
    f32x16 av;
#pragma unroll
    for (int i = 0; i < 16; i++) av[i] = 0.f;
    {
      bf16x8 hf[2][8];
#pragma unroll
      for (int k = 0; k < 8; k++) hf[0][k] = *(const bf16x8*)(hb + x * 1040 + (16 * k + 8 * hh) * 2);
#pragma unroll
      for (int ch = 0; ch < 4; ch++) {
        if (ch < 3) {
#pragma unroll
          for (int k = 0; k < 8; k++) hf[(ch + 1) & 1][k] = *(const bf16x8*)(hb + x * 1040 + (16 * (8 * ch + 8 + k) + 8 * hh) * 2);
        }
        __builtin_amdgcn_sched_barrier(0);
#pragma unroll
        for (int k = 0; k < 8; k++) av = MFMA32(wv[8 * ch + k], hf[ch & 1][k], av);
        __builtin_amdgcn_sched_barrier(0);
      }
    }
#pragma unroll
    for (int q = 0; q < 4; q++) {
      u32x2 pv = {pk2(av[4 * q], av[4 * q + 1]), pk2(av[4 * q + 2], av[4 * q + 3])};
      *(u32x2*)(vs + x * 272 + (32 * w + 8 * q + 4 * hh) * 2) = pv;
    }
    put_h(lds + ((s + 1) & 1) * 33280, hr);
    asm volatile("s_waitcnt vmcnt(0)" ::: "memory");
    __syncthreads();
    if (tid == 0 && s > 4) st4_sc1((u32)s, rflag, h * 128u);
    bf16x8 vf[8];
#pragma unroll
    for (int ks = 0; ks < 8; ks++) vf[ks] = *(const bf16x8*)(vs + x * 272 + (16 * ks + 8 * hh) * 2);
    const __amdgpu_buffer_rsrc_t ru = mkrsrc(ws + OFF_UF + (size_t)s * 64 * 16384);
#pragma unroll
    for (int nt = 0; nt < 4; nt++) {
      f32x16 au;
#pragma unroll
      for (int i = 0; i < 16; i++) au[i] = 0.f;
#pragma unroll
      for (int ks = 0; ks < 8; ks++) au = MFMA32(vf[ks], wo[nt][ks], au);
      const int d = 128 * w + 32 * nt + x, c = d >> 3, cc = d & 7;
#pragma unroll
      for (int xb = 0; xb < 2; xb++)
        st16_sc1(PACK_S(au, 8 * xb), ru, (u32)((((c * 16 + (h >> 1)) * 2 + (h & 1)) * 4 + 2 * hh + xb) * 8 + cc) * 16u);
    }
    if (s < 4 || s == NSEG - 1) asm volatile("s_waitcnt vmcnt(0)" ::: "memory");
    if (s == 30) { u32 seenh = 0xFFFFFFFFu; if (!wait_progress(rflag, OFF_DONE + lane * 64u, 1u, seenh)) return; }
    load_h(hr, ws, s + 2 < NSEG ? s + 2 : NSEG - 1);
    __syncthreads();
    if (tid == 0 && (s < 4 || s == NSEG - 1)) st4_sc1((u32)(s + 1), rflag, h * 128u);
    STAMP(16 + 4 * s);
  }
}

__device__ __forceinline__ void role_qk(const u16* __restrict__ Wq, const u16* __restrict__ Wk, char* ws, char* lds, int h) {
  const int tid = threadIdx.x, lane = tid & 63, w = tid >> 6, x = lane & 31, hh = lane >> 5;
  bf16x8 wq[32], wk[32];
  dma_tile<256>(lds, Wk + h * DH, DM, NH * DH);
  dma_wait();
#pragma unroll
  for (int ks = 0; ks < 32; ks++) wk[ks] = frag8(lds, 16 * ks + 8 * hh, 32 * w + (x & 16), 256, lane);
  __syncthreads();
  dma_tile<256>(lds, Wq + h * DH, DM, NH * DH);
  dma_wait();
#pragma unroll
  for (int ks = 0; ks < 32; ks++) wq[ks] = frag8(lds, 16 * ks + 8 * hh, 32 * w + (x & 16), 256, lane);
  __syncthreads();
  const __amdgpu_buffer_rsrc_t rflag = mkrsrc(ws + OFF_FLAG);
  u32x4 hr[8];
  { u32 seenh = 0xFFFFFFFFu; if (!wait_progress(rflag, OFF_DONE + (lane & 31) * 64u, 1u, seenh)) return; }
  load_h(hr, ws, 0);
  put_h(lds, hr);
  load_h(hr, ws, 1);
  __syncthreads();
  for (int s = 0; s < NSEG; s++) {
    const char* hb = lds + (s & 1) * 33280;
    f32x16 aq, ak;
#pragma unroll
    for (int i = 0; i < 16; i++) { aq[i] = 0.f; ak[i] = 0.f; }
    {
      bf16x8 hf[2][8];
#pragma unroll
      for (int k = 0; k < 8; k++) hf[0][k] = *(const bf16x8*)(hb + x * 1040 + (16 * k + 8 * hh) * 2);
#pragma unroll
      for (int ch = 0; ch < 4; ch++) {
        if (ch < 3) {
#pragma unroll
          for (int k = 0; k < 8; k++) hf[(ch + 1) & 1][k] = *(const bf16x8*)(hb + x * 1040 + (16 * (8 * ch + 8 + k) + 8 * hh) * 2);
        }
        __builtin_amdgcn_sched_barrier(0);
#pragma unroll
        for (int k = 0; k < 8; k++) { ak = MFMA32(wk[8 * ch + k], hf[ch & 1][k], ak); aq = MFMA32(wq[8 * ch + k], hf[ch & 1][k], aq); }
        __builtin_amdgcn_sched_barrier(0);
      }
    }
    put_h(lds + ((s + 1) & 1) * 33280, hr);
    asm volatile("s_waitcnt vmcnt(0)" ::: "memory");
    __syncthreads();
    if (tid == 0 && s > 4) st4_sc1((u32)s, rflag, 4096u + h * 128u);
    store_T_tile<true>(ak, ws + OFF_KX, 32 * s, h, w, lane);
    store_T_tile<true>(aq, ws + OFF_QX, 32 * s, h, w, lane);
    if (s < 4 || s == NSEG - 1) {
      asm volatile("s_waitcnt vmcnt(0)" ::: "memory");
      __syncthreads();
      if (tid == 0) st4_sc1((u32)(s + 1), rflag, 4096u + h * 128u);
    }
    if (s == 30) { u32 seenh = 0xFFFFFFFFu; if (!wait_progress(rflag, OFF_DONE + lane * 64u, 1u, seenh)) return; }
    load_h(hr, ws, s + 2 < NSEG ? s + 2 : NSEG - 1);
    STAMP(16 + 4 * s);
  }
}

__device__ __forceinline__ float xg_max(float v) {
  auto r = __builtin_amdgcn_permlane16_swap(f2u(v), f2u(v), false, false);
  v = fmaxf(u2f(r[0]), u2f(r[1]));
  auto r2 = __builtin_amdgcn_permlane32_swap(f2u(v), f2u(v), false, false);
  return fmaxf(u2f(r2[0]), u2f(r2[1]));
}
__device__ __forceinline__ float xg_sum(float v) {
  auto r = __builtin_amdgcn_permlane16_swap(f2u(v), f2u(v), false, false);
  v = u2f(r[0]) + u2f(r[1]);
  auto r2 = __builtin_amdgcn_permlane32_swap(f2u(v), f2u(v), false, false);
  return u2f(r2[0]) + u2f(r2[1]);
}
__device__ __forceinline__ void px_task(char* ws, int s, int hg) {
  const int tid = threadIdx.x, lane = tid & 63, w = tid >> 6;
  const int hd = hg * 4 + w, hl = w;
  const int n = lane & 15, g = lane >> 4;
  const __amdgpu_buffer_rsrc_t rq = mkrsrc(ws + OFF_QX + (size_t)s * 32 * 8192);
  const __amdgpu_buffer_rsrc_t rk = mkrsrc(ws + OFF_KX + (size_t)s * 32 * 8192);
  const __amdgpu_buffer_rsrc_t rkm = mkrsrc(ws + OFF_KMEM + (size_t)s * 65536);
  const int keyrow = 16 * ((n >> 2) & 1) + 4 * (n >> 3) + (n & 3);
  const int keyb = 16 * (g & 1) + 4 * (g >> 1);
  const float sl = 0.08838834764831845f * 1.4426950408889634f;
  bf16x8 qf[2][4], kf[2][4], mf[4];
  const __amdgpu_buffer_rsrc_t rflag = mkrsrc(ws + OFF_FLAG);
  { u32 seenq = 0xFFFFFFFFu; wait_progress(rflag, 4096u + hd * 128u, s + 1, seenq); }
#pragma unroll
  for (int ks = 0; ks < 4; ks++) {
#pragma unroll
    for (int t = 0; t < 2; t++) {
      qf[t][ks] = as_bf8(ld16_sc1(rq, (u32)(((16 * t + n) * NH + hd) * DH + 32 * ks + 8 * g) * 2u));
      kf[t][ks] = as_bf8(ld16_sc1(rk, (u32)(((8 * t + keyrow) * NH + hd) * DH + 32 * ks + 8 * g) * 2u));
    }
  }
  {
    u32 spins = 0, off = OFF_KMF + (u32)hd * 64u;
    while (true) {
      asm volatile("" : "+v"(off) :: "memory");
      u32 v = ld4_sc1(rflag, off);
      if ((int)v > s) break;
      __builtin_amdgcn_s_sleep(6);
      if (++spins > 800000u) break;
    }
  }
#pragma unroll
  for (int ks = 0; ks < 4; ks++) mf[ks] = as_bf8(ld16_sc1(rkm, (u32)(hd * 1024 + (n & 7) * 128 + 32 * ks + 8 * g) * 2u));
  const bool mvalid = (g >> 1) == (hl >> 1);
  u32x4* PXx = (u32x4*)(ws + OFF_PX);
  char* PXm = ws + OFF_PX + (4ull << 20);
#pragma unroll
  for (int qt = 0; qt < 2; qt++) {
    f32x4 sc[3];
#pragma unroll
    for (int i = 0; i < 3; i++) sc[i] = (f32x4){0.f, 0.f, 0.f, 0.f};
#pragma unroll
    for (int ks = 0; ks < 4; ks++) {
      sc[0] = MFMA16(kf[0][ks], qf[qt][ks], sc[0]);
      sc[1] = MFMA16(kf[1][ks], qf[qt][ks], sc[1]);
      sc[2] = MFMA16(mf[ks], qf[qt][ks], sc[2]);
    }
    const int qi = 16 * qt + n;
    float mx = -3.0e38f;
#pragma unroll
    for (int j = 0; j < 4; j++) {
      if (keyb + j > qi) sc[0][j] = -3.0e38f;
      if (keyb + 8 + j > qi) sc[1][j] = -3.0e38f;
      if (!mvalid) sc[2][j] = -3.0e38f;
      mx = fmaxf(mx, fmaxf(fmaxf(sc[0][j], sc[1][j]), sc[2][j]));
    }
    mx = xg_max(mx);
    float sum = 0.f;
#pragma unroll
    for (int i = 0; i < 3; i++)
#pragma unroll
      for (int j = 0; j < 4; j++) {
        float p = sc[i][j] > -1.0e38f ? __builtin_amdgcn_exp2f((sc[i][j] - mx) * sl) : 0.f;
        sc[i][j] = p; sum += p;
      }
    const float inv = __builtin_amdgcn_rcpf(xg_sum(sum));
    u32x4 pa = {pk2(sc[0][0] * inv, sc[0][1] * inv), pk2(sc[1][0] * inv, sc[1][1] * inv),
                pk2(sc[0][2] * inv, sc[0][3] * inv), pk2(sc[1][2] * inv, sc[1][3] * inv)};
    PXx[(((size_t)s * NH + hd) * 2 + qt) * 64 + lane] = pa;
    if (mvalid) {
      u32x2 pm = {pk2(sc[2][0] * inv, sc[2][1] * inv), pk2(sc[2][2] * inv, sc[2][3] * inv)};
      *(u32x2*)(PXm + ((((size_t)s * 8 + hg) * 2 + qt) * 64 + lane) * 16 + (hl & 1) * 8) = pm;
    }
  }
}

__global__ void __launch_bounds__(256, 1) k2_rec(const u16* __restrict__ Wq, const u16* __restrict__ Wk, const u16* __restrict__ Wv,
                                                  const u16* __restrict__ Wo, const u16* __restrict__ x, const u16* __restrict__ w1,
                                                  const u16* __restrict__ w2, const u16* __restrict__ cvec,
                                                  const u16* __restrict__ wmod, const u16* __restrict__ mem0, char* ws) {
  extern __shared__ __attribute__((aligned(16))) char lds[];
  const int b = blockIdx.x;
  STAMP(0);
  if (b < 32) role_help(x, w1, w2, cvec, wmod, mem0, ws, lds, b);
  else if (b < 64) role_score(Wq, Wk, ws, lds, b - 32);
  else if (b < 192) { const int q = b - 64; role_column(Wv, Wo, ws, lds, (q & 7) | ((q >> 4) << 3), (q >> 3) & 1); }
  else if (b < 224) role_vu(Wv, Wo, ws, lds, b - 192);
  else role_qk(Wq, Wk, ws, lds, b - 224);
}

__device__ __forceinline__ void ph_oproj(const u16* __restrict__ x, char* ws, char* lds) {
  const int b = blockIdx.x, tid = threadIdx.x, lane = tid & 63, w = tid >> 6;
  const int s = (b & 7) * 8 + (b >> 5), nq = (b >> 3) & 3;
  const int n = lane & 15, g = lane >> 4, cc = lane & 7;
  const u32x4* UF = (const u32x4*)(ws + OFF_UF) + (size_t)s * 64 * 1024;
  const char* Yg = ws + OFF_Y + (size_t)s * 64 * 4096;
  const u32x4* PXx = (const u32x4*)(ws + OFF_PX) + (size_t)s * NH * 128;
  const u32x4* PXm = (const u32x4*)(ws + OFF_PX + (4ull << 20)) + (size_t)s * 8 * 128;
  f32x4 acc[2][8];
#pragma unroll
  for (int i = 0; i < 2; i++)
#pragma unroll
    for (int j = 0; j < 8; j++) acc[i][j] = (f32x4){0.f, 0.f, 0.f, 0.f};
  u32x4 ub[4][8], pa[4][2], pm[2], yf[8];
#pragma unroll
  for (int hl = 0; hl < 4; hl++) {
    const int hd = 4 * w + hl;
#pragma unroll
    for (int nt = 0; nt < 8; nt++) ub[hl][nt] = __builtin_nontemporal_load(UF + (size_t)(16 * nq + 2 * nt + (n >> 3)) * 1024 + (hd * 4 + g) * 8 + cc);
    pa[hl][0] = PXx[(hd * 2) * 64 + lane];
    pa[hl][1] = PXx[(hd * 2 + 1) * 64 + lane];
  }
  pm[0] = PXm[(w * 2) * 64 + lane];
  pm[1] = PXm[(w * 2 + 1) * 64 + lane];
  {
    const int ha = 4 * w + 2 * (g >> 1);
#pragma unroll
    for (int nt = 0; nt < 8; nt++) {
      const int c = 16 * nq + 2 * nt + (n >> 3);
      u32x2 ya = __builtin_nontemporal_load((const u32x2*)(Yg + (size_t)c * 4096 + (ha * 8 + cc) * 16 + 8 * (g & 1)));
      u32x2 yb = __builtin_nontemporal_load((const u32x2*)(Yg + (size_t)c * 4096 + ((ha + 1) * 8 + cc) * 16 + 8 * (g & 1)));
      yf[nt] = (u32x4){ya.x, ya.y, yb.x, yb.y};
    }
  }
  __builtin_amdgcn_sched_barrier(0);
#pragma unroll
  for (int hl = 0; hl < 4; hl++)
#pragma unroll
    for (int nt = 0; nt < 8; nt++) {
      acc[0][nt] = MFMA16(as_bf8(pa[hl][0]), as_bf8(ub[hl][nt]), acc[0][nt]);
      acc[1][nt] = MFMA16(as_bf8(pa[hl][1]), as_bf8(ub[hl][nt]), acc[1][nt]);
    }
#pragma unroll
  for (int nt = 0; nt < 8; nt++) {
    acc[0][nt] = MFMA16(as_bf8(pm[0]), as_bf8(yf[nt]), acc[0][nt]);
    acc[1][nt] = MFMA16(as_bf8(pm[1]), as_bf8(yf[nt]), acc[1][nt]);
  }
  float* red = (float*)lds;
#pragma unroll
  for (int qt = 0; qt < 2; qt++)
#pragma unroll
    for (int nt = 0; nt < 8; nt++)
#pragma unroll
      for (int j = 0; j < 4; j++) red[(w * 32 + 16 * qt + 4 * g + j) * 132 + 16 * nt + n] = acc[qt][nt][j];
  __syncthreads();
  const float* mod = (const float*)(ws + OFF_MOD);
  {
    const int r = tid >> 4, c8 = (tid & 15) * 8;
    float o[8];
#pragma unroll
    for (int j = 0; j < 8; j++) o[j] = 0.f;
#pragma unroll
    for (int ww = 0; ww < 8; ww++) {
      f32x4 a = *(const f32x4*)(red + (ww * 32 + r) * 132 + c8), bq = *(const f32x4*)(red + (ww * 32 + r) * 132 + c8 + 4);
#pragma unroll
      for (int j = 0; j < 4; j++) { o[j] += a[j]; o[4 + j] += bq[j]; }
    }
    const int col = 128 * nq + c8;
    const size_t go = (size_t)(32 * s + r) * DM + col;
    u32x4 xv = *(const u32x4*)(x + go);
    float xf[8] = {bf_lo(xv.x), bf_hi(xv.x), bf_lo(xv.y), bf_hi(xv.y), bf_lo(xv.z), bf_hi(xv.z), bf_lo(xv.w), bf_hi(xv.w)};
#pragma unroll
    for (int j = 0; j < 8; j++) o[j] = xf[j] + bfr(mod[1024 + col + j] * bfr(o[j]));
    u32x4 rv = {pk2(o[0], o[1]), pk2(o[2], o[3]), pk2(o[4], o[5]), pk2(o[6], o[7])};
    st16_sc1(rv, mkrsrc(ws + OFF_X1), (u32)go * 2u);
  }
  asm volatile("s_waitcnt vmcnt(0)" ::: "memory");
  __syncthreads();
  if (tid == 0) st4_sc1(1u, mkrsrc(ws + OFF_FLAG), OFF_X1F + (u32)(s * 4 + nq) * 4u);
}

__device__ __forceinline__ void ph_mlp1(const u16* __restrict__ b1, char* ws, char* lds) {
  const int b = blockIdx.x, tid = threadIdx.x, lane = tid & 63, w = tid >> 6;
  const int jq = b >> 3, mt0 = (b & 7) * 4 + jq / 7, nb = jq % 7;
  const float* mod = (const float*)(ws + OFF_MOD);
  const int n = lane & 15, g = lane >> 4;
  const f32x4 mv = *(const f32x4*)(mod + 1536 + (tid & 255) * 4);
  const u32x4* WF = (const u32x4*)(ws + OFF_W1F) + (size_t)(nb * 16 + w * 2) * 16 * 64 + lane;
  u32x4 bfm[2][16];
#pragma unroll
  for (int nt = 0; nt < 2; nt++)
#pragma unroll
    for (int ks = 0; ks < 8; ks++) bfm[nt][ks] = WF[(nt * 16 + ks) * 64];
  __builtin_amdgcn_sched_barrier(0);
  { u32 seenx = 0xFFFFFFFFu; wait_progress(mkrsrc(ws + OFF_FLAG), OFF_X1F + (u32)(mt0 * 8 + (lane & 7)) * 4u, 1u, seenx); }
  u32x4 xv[2][4];
#pragma unroll
  for (int it = 0; it < 2; it++)
#pragma unroll
    for (int j = 0; j < 4; j++) xv[it][j] = ld16_sc1(mkrsrc(ws + OFF_X1), (u32)((mt0 * 64 + it * 32 + w * 4 + g) * DM + (j * 16 + n) * 8) * 2u);
  __builtin_amdgcn_sched_barrier(0);
  float* modl = (float*)(lds + 66560);
  if (tid < 256) *(f32x4*)(modl + tid * 4) = tid >= 128 ? mv + 1.f : mv;
  __syncthreads();
#pragma unroll
  for (int it = 0; it < 2; it++) {
    f32x2 f[16];
#pragma unroll
    for (int j = 0; j < 4; j++) {
      f[4 * j + 0] = (f32x2){bf_lo(xv[it][j].x), bf_hi(xv[it][j].x)}; f[4 * j + 1] = (f32x2){bf_lo(xv[it][j].y), bf_hi(xv[it][j].y)};
      f[4 * j + 2] = (f32x2){bf_lo(xv[it][j].z), bf_hi(xv[it][j].z)}; f[4 * j + 3] = (f32x2){bf_lo(xv[it][j].w), bf_hi(xv[it][j].w)};
    }
    f32x2 s2 = {0.f, 0.f};
#pragma unroll
    for (int i = 0; i < 16; i++) s2 += f[i];
    float m = row16_sum(s2.x + s2.y) * (1.f / DM);
    f32x2 q2 = {0.f, 0.f};
#pragma unroll
    for (int i = 0; i < 16; i++) { f[i] -= m; q2 += f[i] * f[i]; }
    float rs = rsqrtf(row16_sum(q2.x + q2.y) * (1.f / DM) + 1e-5f);
    s2 = (f32x2){0.f, 0.f};
#pragma unroll
    for (int j = 0; j < 4; j++) {
      const int c0 = (j * 16 + n) * 8;
      f32x4 c0v = *(const f32x4*)(modl + 512 + c0), c1v = *(const f32x4*)(modl + 512 + c0 + 4);
      f32x4 s0v = *(const f32x4*)(modl + c0), s1v = *(const f32x4*)(modl + c0 + 4);
      f[4 * j + 0] = f[4 * j + 0] * rs * (f32x2){c0v[0], c0v[1]} + (f32x2){s0v[0], s0v[1]};
      f[4 * j + 1] = f[4 * j + 1] * rs * (f32x2){c0v[2], c0v[3]} + (f32x2){s0v[2], s0v[3]};
      f[4 * j + 2] = f[4 * j + 2] * rs * (f32x2){c1v[0], c1v[1]} + (f32x2){s1v[0], s1v[1]};
      f[4 * j + 3] = f[4 * j + 3] * rs * (f32x2){c1v[2], c1v[3]} + (f32x2){s1v[2], s1v[3]};
      s2 += (f[4 * j + 0] + f[4 * j + 1]) + (f[4 * j + 2] + f[4 * j + 3]);
    }
    m = row16_sum(s2.x + s2.y) * (1.f / DM);
    q2 = (f32x2){0.f, 0.f};
#pragma unroll
    for (int i = 0; i < 16; i++) { f[i] -= m; q2 += f[i] * f[i]; }
    rs = rsqrtf(row16_sum(q2.x + q2.y) * (1.f / DM) + 1e-5f);
#pragma unroll
    for (int j = 0; j < 4; j++) {
      f32x2 o0 = f[4 * j] * rs, o1 = f[4 * j + 1] * rs, o2 = f[4 * j + 2] * rs, o3 = f[4 * j + 3] * rs;
      u32x4 o = {pk2(o0.x, o0.y), pk2(o1.x, o1.y), pk2(o2.x, o2.y), pk2(o3.x, o3.y)};
      *(u32x4*)(lds + (it * 32 + w * 4 + g) * 1040 + (j * 16 + n) * 16) = o;
    }
    __builtin_amdgcn_sched_barrier(0);
  }
#pragma unroll
  for (int nt = 0; nt < 2; nt++)
#pragma unroll
    for (int ks = 8; ks < 16; ks++) bfm[nt][ks] = WF[(nt * 16 + ks) * 64];
  __builtin_amdgcn_sched_barrier(0);
  __syncthreads();
  f32x4 acc[4][2];
#pragma unroll
  for (int i = 0; i < 4; i++)
#pragma unroll
    for (int j = 0; j < 2; j++) acc[i][j] = (f32x4){0.f, 0.f, 0.f, 0.f};
#pragma unroll
  for (int ks = 0; ks < 16; ks++) {
    bf16x8 af[4];
#pragma unroll
    for (int mt = 0; mt < 4; mt++) af[mt] = *(const bf16x8*)(lds + (16 * mt + n) * 1040 + (32 * ks + 8 * g) * 2);
#pragma unroll
    for (int mt = 0; mt < 4; mt++)
#pragma unroll
      for (int nt = 0; nt < 2; nt++) acc[mt][nt] = MFMA16(as_bf8(bfm[nt][ks]), af[mt], acc[mt][nt]);
    if (ks & 1) __builtin_amdgcn_sched_barrier(0);
  }
  float bb[2][4];
#pragma unroll
  for (int nt = 0; nt < 2; nt++) {
    u32x2 bv = *(const u32x2*)(b1 + nb * 256 + w * 32 + 16 * nt + 4 * g);
    bb[nt][0] = bf_lo(bv.x); bb[nt][1] = bf_hi(bv.x); bb[nt][2] = bf_lo(bv.y); bb[nt][3] = bf_hi(bv.y);
  }
#pragma unroll
  for (int mt = 0; mt < 4; mt++) {
    float o[8];
#pragma unroll
    for (int nt = 0; nt < 2; nt++)
#pragma unroll
      for (int j = 0; j < 4; j++) {
        float u = bfr(bfr(acc[mt][nt][j]) + bb[nt][j]);
        float z = 0.7978845608028654f * (u + 0.044715f * u * u * u);
        float e = __builtin_amdgcn_exp2f(z * 2.8853900817779268f);
        float th = 1.f - 2.f * __builtin_amdgcn_rcpf(e + 1.f);
        o[4 * nt + j] = 0.5f * u * (1.f + th);
      }
    u32x4 ov = {pk2(o[0], o[1]), pk2(o[2], o[3]), pk2(o[4], o[5]), pk2(o[6], o[7])};
    st16_sc1(ov, mkrsrc(ws + OFF_HID), (u32)(((mt0 * 4 + mt) * 56 + nb * 8 + w) * 64 + lane) * 16u);
  }
  asm volatile("s_waitcnt vmcnt(0)" ::: "memory");
  __syncthreads();
  if (tid == 0) st4_sc1(1u, mkrsrc(ws + OFF_FLAG), OFF_HDF + (u32)(mt0 * 8 + nb) * 4u);
}

__device__ __forceinline__ void ph_mlp2(const u16* __restrict__ b2, char* ws, u16* __restrict__ out, char* lds) {
  const int b = blockIdx.x, tid = threadIdx.x, lane = tid & 63, w = tid >> 6;
  const int mt0 = (b & 7) * 4 + (b >> 6), nb = (b >> 3) & 7;
  const int n = lane & 15, g = lane >> 4;
  const __amdgpu_buffer_rsrc_t rhid = mkrsrc(ws + OFF_HID);
  const u32 hbase = (u32)(((mt0 * 4) * 56 + w * 7) * 64 + lane) * 16u;
  const u32x4* WF = (const u32x4*)(ws + OFF_W2F) + ((size_t)(nb * 4) * 56 + w * 7) * 64 + lane;
  f32x4 acc[4][4];
#pragma unroll
  for (int i = 0; i < 4; i++)
#pragma unroll
    for (int j = 0; j < 4; j++) acc[i][j] = (f32x4){0.f, 0.f, 0.f, 0.f};
  u32x4 bfm[7][4];
#pragma unroll
  for (int k = 0; k < 7; k++)
#pragma unroll
    for (int nt = 0; nt < 4; nt++) bfm[k][nt] = WF[(nt * 56 + k) * 64];
  __builtin_amdgcn_sched_barrier(0);
  { u32 seenh2 = 0xFFFFFFFFu; const int fl = (lane & 7) < 7 ? (lane & 7) : 0; wait_progress(mkrsrc(ws + OFF_FLAG), OFF_HDF + (u32)(mt0 * 8 + fl) * 4u, 1u, seenh2); }
#pragma unroll
  for (int hb = 0; hb < 2; hb++) {
    const int nk = hb ? 3 : 4;
    bf16x8 af[4][4];
#pragma unroll
    for (int k = 0; k < 4; k++) if (k < nk) {
#pragma unroll
      for (int mt = 0; mt < 4; mt++) af[k][mt] = as_bf8(ld16_sc1(rhid, hbase + (u32)((mt * 56 + hb * 4 + k) * 64) * 16u));
    }
    __builtin_amdgcn_sched_barrier(0);
#pragma unroll
    for (int k = 0; k < 4; k++) if (k < nk) {
#pragma unroll
      for (int mt = 0; mt < 4; mt++)
#pragma unroll
        for (int nt = 0; nt < 4; nt++) acc[mt][nt] = MFMA16(as_bf8(bfm[hb * 4 + k][nt]), af[k][mt], acc[mt][nt]);
    }
  }
  float* red = (float*)lds;
#pragma unroll
  for (int mt = 0; mt < 4; mt++)
#pragma unroll
    for (int nt = 0; nt < 4; nt++) *(f32x4*)(red + (w * 64 + 16 * mt + n) * 68 + 16 * nt + 4 * g) = acc[mt][nt];
  __syncthreads();
  const float* mod = (const float*)(ws + OFF_MOD);
  {
    const int r = tid >> 3, c8 = (tid & 7) * 8;
    float o[8];
#pragma unroll
    for (int j = 0; j < 8; j++) o[j] = 0.f;
#pragma unroll
    for (int ww = 0; ww < 8; ww++) {
      f32x4 a = *(const f32x4*)(red + (ww * 64 + r) * 68 + c8), bq = *(const f32x4*)(red + (ww * 64 + r) * 68 + c8 + 4);
#pragma unroll
      for (int j = 0; j < 4; j++) { o[j] += a[j]; o[4 + j] += bq[j]; }
    }
    const int col = 64 * nb + c8;
    const size_t go = (size_t)(mt0 * 64 + r) * DM + col;
    u32x4 xv = ld16_sc1(mkrsrc(ws + OFF_X1), (u32)go * 2u), bv = *(const u32x4*)(b2 + col);
    float xf[8] = {bf_lo(xv.x), bf_hi(xv.x), bf_lo(xv.y), bf_hi(xv.y), bf_lo(xv.z), bf_hi(xv.z), bf_lo(xv.w), bf_hi(xv.w)};
    float bb[8] = {bf_lo(bv.x), bf_hi(bv.x), bf_lo(bv.y), bf_hi(bv.y), bf_lo(bv.z), bf_hi(bv.z), bf_lo(bv.w), bf_hi(bv.w)};
#pragma unroll
    for (int j = 0; j < 8; j++) o[j] = xf[j] + bfr(mod[2560 + col + j] * bfr(bfr(o[j]) + bb[j]));
    u32x4 rv = {pk2(o[0], o[1]), pk2(o[2], o[3]), pk2(o[4], o[5]), pk2(o[6], o[7])};
    *(u32x4*)(out + go) = rv;
  }
}

__global__ void __launch_bounds__(512, 2) k3_post(const u16* __restrict__ x, const u16* __restrict__ b1, const u16* __restrict__ b2,
                                                   char* ws, u16* __restrict__ out) {
  extern __shared__ __attribute__((aligned(16))) char lds[];
  ph_oproj(x, ws, lds);
  __syncthreads();
  if (blockIdx.x < 224) ph_mlp1(b1, ws, lds);
  __syncthreads();
  ph_mlp2(b2, ws, out, lds);
}

extern "C" void kernel_launch(void* const* d_in, const int* in_sizes, int n_in, void* d_out, int out_size, void* d_ws,
                              size_t ws_size, hipStream_t stream) {
  const u16* x = (const u16*)d_in[0];
  const u16* mem0 = (const u16*)d_in[1];
  const u16* Wq = (const u16*)d_in[2];
  const u16* Wk = (const u16*)d_in[3];
  const u16* Wv = (const u16*)d_in[4];
  const u16* Wo = (const u16*)d_in[5];
  const u16* w1 = (const u16*)d_in[8];
  const u16* b1 = (const u16*)d_in[9];
  const u16* w2 = (const u16*)d_in[10];
  const u16* b2 = (const u16*)d_in[11];
  const u16* cvec = (const u16*)d_in[12];
  const u16* wmod = (const u16*)d_in[13];
  char* ws = (char*)d_ws;
  hipFuncSetAttribute((const void*)k2_rec, hipFuncAttributeMaxDynamicSharedMemorySize, 135168);
  hipMemsetAsync(ws, 0xFF, OFF_H, stream);
  k2_rec<<<256, 256, 135168, stream>>>(Wq, Wk, Wv, Wo, x, w1, w2, cvec, wmod, mem0, ws);
  hipFuncSetAttribute((const void*)k3_post, hipFuncAttributeMaxDynamicSharedMemorySize, 139264);
  k3_post<<<256, 512, 139264, stream>>>(x, b1, b2, ws, (u16*)d_out);
}
```
